```python
import torch

def _softmax(x, axis=-1):
    return torch.softmax(x, dim=axis)

def _gelu(x):
    return torch.nn.functional.gelu(x, approximate='tanh')

def _silu(x):
    return torch.nn.functional.silu(x)

def _sigmoid(x):
    return torch.sigmoid(x)

def _layernorm(x, axis=-1, eps=1e-05):
    m = x.mean(dim=axis, keepdim=True)
    v = x.var(dim=axis, keepdim=True, unbiased=False)
    return (x - m) / torch.sqrt(v + eps)

def _scatter_add(tgt, idx, src, axis=0):
    return tgt.clone().index_add_(axis, idx, src)

def _segment_sum(src, seg, n):
    out = torch.zeros((n,) + src.shape[1:], dtype=src.dtype)
    return out.index_add_(0, seg, src)

def _pad(x, pw):
    flat = tuple((v for pair in reversed(pw) for v in pair))
    return torch.nn.functional.pad(x, flat)
kernel_name = 'adaLN_cond__spectral_gfnet__glu_gated_bilinear__prompt'

def setup_inputs():

    _w = lambda a: (lambda t: t.to(torch.bfloat16) if t.is_floating_point() else t)(torch.as_tensor(a))

    def _A_setup():
        B, S, D = (1, 2048, 640)
        Df = D // 2 + 1
        return {'x': _w((lambda *s: torch.randn(*s, dtype=torch.bfloat16) * 0.05)(B, S, D)), 'w_r': _w((lambda *s: torch.randn(*s, dtype=torch.bfloat16) * 1.6)(S, Df) * 0.02), 'w_i': _w((lambda *s: torch.randn(*s, dtype=torch.bfloat16) * 1.6)(S, Df) * 0.02)}
    _A_in = _A_setup()

    def _B_setup():
        B, S, D, F = (1, 2048, 640, 1728)
        return {'x': _w((lambda *s: torch.randn(*s, dtype=torch.bfloat16) * 0.05)(B, S, D)), 'w_gate': _w((lambda *s: torch.randn(*s, dtype=torch.bfloat16) * 0.4)(D, F) * D ** (-0.5)), 'w_up': _w((lambda *s: torch.randn(*s, dtype=torch.bfloat16) * 0.4)(D, F) * D ** (-0.5)), 'w_down': _w((lambda *s: torch.randn(*s, dtype=torch.bfloat16) * 0.4)(F, D) * F ** (-0.5))}
    _B_in = _B_setup()
    return {'A_x': _A_in['x'], 'A_w_r': _A_in['w_r'], 'A_w_i': _A_in['w_i'], 'B_x': _B_in['x'], 'B_w_gate': _B_in['w_gate'], 'B_w_up': _B_in['w_up'], 'B_w_down': _B_in['w_down'], 'c': _w((lambda *s: torch.randn(*s, dtype=torch.bfloat16) * 0.1)(1, 640) * 8.0), 'w_mod': _w((lambda *s: torch.randn(*s, dtype=torch.bfloat16) * 0.1)(640, 6 * 640) * 640 ** (-0.5) * 8.0)}

def reference(A_x, A_w_r, A_w_i, B_x, B_w_gate, B_w_up, B_w_down, c, w_mod):

    def _A_ref(x, w_r, w_i):
        B, S, D = x.shape
        X = (lambda x, axes=(-2, -1): torch.fft.rfft2(x.float(), dim=axes))(x, axes=(-2, -1))
        W = w_r + 1j * w_i
        X = X * W
        y = (lambda x, s, axes=(-2, -1): torch.fft.irfft2(x, s=s, dim=axes).to(torch.bfloat16))(X, (S, D), axes=(-2, -1))
        return y

    def _B_ref(x, w_gate, w_up, w_down):
        g = torch.einsum('bsd,df->bsf', x, w_gate)
        u = torch.einsum('bsd,df->bsf', x, w_up)
        h = g * u
        return torch.einsum('bsf,fd->bsd', h, w_down)
    x = A_x
    mod = (_silu(c) @ w_mod).reshape(c.shape[0], 6, 1, -1)
    sa, ca, ga, sb, cb, gb = (mod[:, 0].clone(), mod[:, 1].clone(), mod[:, 2].clone(), mod[:, 3].clone(), mod[:, 4].clone(), mod[:, 5].clone())
    h = _layernorm(x, axis=-1) * (1 + ca) + sa
    x = x + ga * _A_ref(h, A_w_r, A_w_i)
    h = _layernorm(x, axis=-1) * (1 + cb) + sb
    x = x + gb * _B_ref(h, B_w_gate, B_w_up, B_w_down)
    return x
```

```cpp
#include <hip/hip_runtime.h>
#include <hip/hip_bf16.h>
#include <stdint.h>

typedef unsigned short u16;
typedef unsigned int u32;
typedef short s16x4 __attribute__((ext_vector_type(4)));
typedef short s16x8 __attribute__((ext_vector_type(8)));
typedef __bf16 bf2_t __attribute__((ext_vector_type(2)));
typedef float f32x2 __attribute__((ext_vector_type(2)));
typedef float f32x4 __attribute__((ext_vector_type(4)));
typedef float f32x16 __attribute__((ext_vector_type(16)));
typedef u32 u32x2 __attribute__((ext_vector_type(2)));
typedef u32 u32x4 __attribute__((ext_vector_type(4)));
#define DI __device__ __forceinline__
#define LDSP(T) __attribute__((address_space(3))) T*

constexpr int S_ = 2048, D_ = 640, F_ = 1728, DF = 321;

constexpr double kPi = 3.14159265358979323846264338327950288;
constexpr double c_sin0(double x) { double x2 = x * x, t = x, s = x; for (int i = 1; i < 11; ++i) { t *= -x2 / ((2.0 * i) * (2.0 * i + 1.0)); s += t; } return s; }
constexpr double c_cos0(double x) { double x2 = x * x, t = 1.0, s = 1.0; for (int i = 1; i < 11; ++i) { t *= -x2 / ((2.0 * i - 1.0) * (2.0 * i)); s += t; } return s; }
constexpr double c_cos(long n, long N) {
  n %= N; if (n < 0) n += N;
  if (2 * n > N) n = N - n;
  bool neg = false;
  if (4 * n > N) { neg = true; n = N - 2 * n; N = 2 * N; n = n;   }
  double r = 0;
  if (4 * n == N) r = 0.0;
  else if (8 * n > N) r = c_sin0(2 * kPi * (double)(N - 4 * n) / (double)(4 * N));
  else r = c_cos0(2 * kPi * (double)n / (double)N);
  return neg ? -r : r;
}
constexpr double c_sin(long n, long N) { return c_cos(4 * n - N, 4 * N); }
constexpr u16 c_bf16(double v) {
  float f = (float)v; u32 u = __builtin_bit_cast(u32, f);
  u += 0x7fffu + ((u >> 16) & 1u); return (u16)(u >> 16);
}

struct Tabs {
  u16 f32a[2][64][8];
  float twa[10][2][64][4];
  u16 f20b[3][2][64][8];
  u16 f20ai[3][2][64][8];
  float twc[3][64][4];
  float twc16[3][64][4];
  u16 f32bi[2][64][8];
};
constexpr Tabs make_tabs() {
  Tabs t{};
  for (int tt = 0; tt < 2; ++tt) for (int l = 0; l < 64; ++l) for (int j = 0; j < 8; ++j) {
    int o = 16 * tt + (l & 15), d1 = 8 * (l >> 4) + j; double v = 0;
    if (o == 0) v = 1.0; else if (o == 1) v = (d1 & 1) ? -1.0 : 1.0;
    else { int k1 = o >> 1; v = (o & 1) ? -c_sin(d1 * k1, 32) : c_cos(d1 * k1, 32); }
    t.f32a[tt][l][j] = c_bf16(v);
  }
  for (int j = 0; j < 10; ++j) for (int tt = 0; tt < 2; ++tt) for (int l = 0; l < 64; ++l) for (int rr = 0; rr < 2; ++rr) {
    int c = l & 15, g = l >> 4, Q = 4 * j + (c >> 2), d0 = 4 * (Q % 5) + (c & 3);
    int k1 = 8 * tt + 2 * g + rr; if (k1 == 0) k1 = 16;
    t.twa[j][tt][l][2 * rr] = (float)c_cos(d0 * k1, 640);
    t.twa[j][tt][l][2 * rr + 1] = (float)(-c_sin(d0 * k1, 640));
  }
  for (int nt = 0; nt < 3; ++nt) for (int ks = 0; ks < 2; ++ks) for (int l = 0; l < 64; ++l) for (int j = 0; j < 8; ++j) {
    int c = l & 15, g = l >> 4, k0 = 8 * nt + (c >> 1), part = c & 1, d0 = 16 * ks + 4 * g + (j >> 1), pp = j & 1; double v = 0;
    if (d0 < 20 && k0 < 20) {
      if (part == 0) v = pp == 0 ? c_cos(d0 * k0, 20) : c_sin(d0 * k0, 20);
      else v = pp == 0 ? -c_sin(d0 * k0, 20) : c_cos(d0 * k0, 20);
    }
    t.f20b[nt][ks][l][j] = c_bf16(v);
  }
  for (int mt = 0; mt < 3; ++mt) for (int ks = 0; ks < 2; ++ks) for (int l = 0; l < 64; ++l) for (int j = 0; j < 8; ++j) {
    int row = l & 15, g = l >> 4, d0 = 8 * mt + (row >> 1), part = row & 1, k0 = 16 * ks + 4 * g + (j >> 1), pp = j & 1; double v = 0;
    if (d0 < 20 && k0 < 20) {
      if (part == 0) v = pp == 0 ? c_cos(d0 * k0, 20) : -c_sin(d0 * k0, 20);
      else v = pp == 0 ? c_sin(d0 * k0, 20) : c_cos(d0 * k0, 20);
    }
    t.f20ai[mt][ks][l][j] = c_bf16(v);
  }
  for (int mt = 0; mt < 3; ++mt) for (int l = 0; l < 64; ++l) for (int rr = 0; rr < 2; ++rr) {
    int k1 = l & 15, g = l >> 4, d0 = 8 * mt + 2 * g + rr;
    t.twc[mt][l][2 * rr] = (float)c_cos(d0 * k1, 640); t.twc[mt][l][2 * rr + 1] = (float)c_sin(d0 * k1, 640);
    t.twc16[mt][l][2 * rr] = (float)c_cos(d0 * 16, 640); t.twc16[mt][l][2 * rr + 1] = (float)c_sin(d0 * 16, 640);
  }
  for (int nt = 0; nt < 2; ++nt) for (int l = 0; l < 64; ++l) for (int j = 0; j < 8; ++j) {
    int d1 = 16 * nt + (l & 15), K = 8 * (l >> 4) + j; double v = 0;
    if (K == 0) v = 1.0; else if (K == 1) v = (d1 & 1) ? -1.0 : 1.0;
    else { int k1 = K >> 1; v = (K & 1) ? -2.0 * c_sin(k1 * d1, 32) : 2.0 * c_cos(k1 * d1, 32); }
    t.f32bi[nt][l][j] = c_bf16(v);
  }
  return t;
}
static __device__ const Tabs g_tabs = make_tabs();

DI float bf_lo(u32 v) { return __uint_as_float(v << 16); }
DI float bf_hi(u32 v) { return __uint_as_float(v & 0xffff0000u); }
DI float bf2f(u16 v) { return __uint_as_float(((u32)v) << 16); }
DI u32 pk_bf(float a, float b) { f32x2 f = {a, b}; return __builtin_bit_cast(u32, __builtin_convertvector(f, bf2_t)); }
DI float rbf(float a) { return bf_lo(pk_bf(a, a)); }
DI float dpp_x1(float v) { return __int_as_float(__builtin_amdgcn_mov_dpp(__float_as_int(v), 0xB1, 0xf, 0xf, true)); }
DI float dpp_x2(float v) { return __int_as_float(__builtin_amdgcn_mov_dpp(__float_as_int(v), 0x4E, 0xf, 0xf, true)); }
DI float dpp_hm(float v) { return __int_as_float(__builtin_amdgcn_mov_dpp(__float_as_int(v), 0x141, 0xf, 0xf, true)); }
DI float dpp_rm(float v) { return __int_as_float(__builtin_amdgcn_mov_dpp(__float_as_int(v), 0x140, 0xf, 0xf, true)); }
DI f32x4 mfma16(s16x8 a, s16x8 b, f32x4 c) { return __builtin_amdgcn_mfma_f32_16x16x32_bf16(a, b, c, 0, 0, 0); }
DI f32x16 mfma32(s16x8 a, s16x8 b, f32x16 c) { return __builtin_amdgcn_mfma_f32_32x32x16_bf16(a, b, c, 0, 0, 0); }
DI s16x4 tr_read(const void* p) { return __builtin_amdgcn_ds_read_tr16_b64_v4i16((LDSP(s16x4))p); }
DI s16x8 cat8(s16x4 a, s16x4 b) { return __builtin_shufflevector(a, b, 0, 1, 2, 3, 4, 5, 6, 7); }
DI s16x8 as8(u32x4 v) { return __builtin_bit_cast(s16x8, v); }
DI float dot2bf(u32 a, u32 b, float c) { return __builtin_amdgcn_fdot2_f32_bf16(__builtin_bit_cast(bf2_t, a), __builtin_bit_cast(bf2_t, b), c, false); }

DI float reduce16(const float (&v)[16], int lane) {
  float a[8], b[4], c[2], d;
  const bool s3 = lane & 8, s2 = lane & 4, s1 = lane & 2, s0 = lane & 1;
#pragma unroll
  for (int i = 0; i < 8; ++i) { float keep = s3 ? v[8 + i] : v[i], send = s3 ? v[i] : v[8 + i]; a[i] = keep + dpp_rm(send); }
#pragma unroll
  for (int i = 0; i < 4; ++i) { float keep = s2 ? a[4 + i] : a[i], send = s2 ? a[i] : a[4 + i]; b[i] = keep + dpp_hm(send); }
#pragma unroll
  for (int i = 0; i < 2; ++i) { float keep = s1 ? b[2 + i] : b[i], send = s1 ? b[i] : b[2 + i]; c[i] = keep + dpp_x2(send); }
  { float keep = s0 ? c[1] : c[0], send = s0 ? c[0] : c[1]; d = keep + dpp_x1(send); }
  auto r16 = __builtin_amdgcn_permlane16_swap(__float_as_uint(d), __float_as_uint(d), false, false);
  d = __uint_as_float(r16[0]) + __uint_as_float(r16[1]);
  auto r32 = __builtin_amdgcn_permlane32_swap(__float_as_uint(d), __float_as_uint(d), false, false);
  return __uint_as_float(r32[0]) + __uint_as_float(r32[1]);
}

DI void ln_stats(const u32 (&xv)[8], float* part  , int tid, float (&mu)[8], float (&rs)[8]) {
  const int lane = tid & 63, w = tid >> 6;
  float v[16];
#pragma unroll
  for (int r = 0; r < 8; ++r) { v[r] = dot2bf(xv[r], 0x3f803f80u, 0.f); v[8 + r] = dot2bf(xv[r], xv[r], 0.f); }
  float t = reduce16(v, lane);
  if (lane < 16) part[w * 16 + lane] = t;
  __syncthreads();
  const int r = lane & 7;
  float s = 0.f, q = 0.f;
#pragma unroll
  for (int i = 0; i < 5; ++i) { s += part[i * 16 + r]; q += part[i * 16 + 8 + r]; }
  float mean = s * (1.0f / 640.0f);
  float var = fmaxf(q * (1.0f / 640.0f) - mean * mean, 0.f);
  float sd = rbf(sqrtf(rbf(rbf(var) + 1e-5f)));
  float m_ = rbf(mean), i_ = 1.0f / sd;
#pragma unroll
  for (int i = 0; i < 8; ++i) {
    mu[i] = __int_as_float(__builtin_amdgcn_readlane(__float_as_int(m_), i));
    rs[i] = __int_as_float(__builtin_amdgcn_readlane(__float_as_int(i_), i));
  }
}

__global__ __launch_bounds__(256) void k1_mod(const u16* __restrict__ c, const u16* __restrict__ wmod, float* __restrict__ mod) {
  __shared__ float red[4][32];
  const int t = threadIdx.x, cq = t & 3, rg = t >> 2, col0 = blockIdx.x * 32 + cq * 8;
  u32x4 w[10]; u16 cv[10];
#pragma unroll
  for (int i = 0; i < 10; ++i) w[i] = *(const u32x4*)(wmod + (size_t)(rg + 64 * i) * 3840 + col0);
#pragma unroll
  for (int i = 0; i < 10; ++i) cv[i] = c[rg + 64 * i];
  float acc[8] = {};
#pragma unroll
  for (int i = 0; i < 10; ++i) {
    float x = bf2f(cv[i]); float s = rbf(x / (1.0f + __expf(-x)));
#pragma unroll
    for (int j = 0; j < 4; ++j) { acc[2 * j] += s * bf_lo(w[i][j]); acc[2 * j + 1] += s * bf_hi(w[i][j]); }
  }
#pragma unroll
  for (int j = 0; j < 8; ++j) {
    float v = acc[j];
    v += __shfl_xor(v, 4); v += __shfl_xor(v, 8); v += __shfl_xor(v, 16); v += __shfl_xor(v, 32);
    acc[j] = v;
  }
  if ((t & 63) < 4) {
#pragma unroll
    for (int j = 0; j < 8; ++j) red[t >> 6][cq * 8 + j] = acc[j];
  }
  __syncthreads();
  if (t < 32) mod[blockIdx.x * 32 + t] = rbf(red[0][t] + red[1][t] + red[2][t] + red[3][t]);
}

constexpr int HT_RS = 1296;
__global__ __launch_bounds__(320) void k2_fwd(const u16* __restrict__ x, const float* __restrict__ mod, u32* __restrict__ XT) {
  __shared__ __attribute__((aligned(16))) char smem[8 * HT_RS + 8 * 17 * 80 + 5 * 16 * 4];
  char* hT = smem; char* U = smem + 8 * HT_RS; float* part = (float*)(smem + 8 * HT_RS + 8 * 17 * 80);
  const int tid = threadIdx.x, lane = tid & 63, w = tid >> 6, s0 = blockIdx.x;
  const int c = lane & 15, g = lane >> 4;
  u32 xv[8];
#pragma unroll
  for (int r = 0; r < 8; ++r) xv[r] = *(const u32*)(x + (size_t)(s0 + 256 * r) * D_ + 2 * tid);
  const f32x2 sa = *(const f32x2*)(mod + 2 * tid), ca = *(const f32x2*)(mod + 640 + 2 * tid);
  const s16x8 fa0 = *(const s16x8*)g_tabs.f32a[0][lane], fa1 = *(const s16x8*)g_tabs.f32a[1][lane];
  f32x4 tw[2][2];
#pragma unroll
  for (int jj = 0; jj < 2; ++jj)
#pragma unroll
    for (int tt = 0; tt < 2; ++tt) tw[jj][tt] = *(const f32x4*)g_tabs.twa[2 * w + jj][tt][lane];
  s16x8 fb[3][2];
#pragma unroll
  for (int nt = 0; nt < 3; ++nt)
#pragma unroll
    for (int ks = 0; ks < 2; ++ks) fb[nt][ks] = *(const s16x8*)g_tabs.f20b[nt][ks][lane];
  float mu[8], rs[8];
  ln_stats(xv, part, tid, mu, rs);
  const float g0 = rbf(1.0f + ca[0]), g1 = rbf(1.0f + ca[1]);
  float h0[8], h1[8];
#pragma unroll
  for (int r = 0; r < 8; ++r) {
    h0[r] = (bf_lo(xv[r]) - mu[r]) * rs[r] * g0 + sa[0];
    h1[r] = (bf_hi(xv[r]) - mu[r]) * rs[r] * g1 + sa[1];
  }
  float o0[8], o1[8];
  auto rad8 = [](const float (&h)[8], float (&o)[8]) {
    const float cc = 0.70710678118654752f;
    float a0 = h[0] + h[4], a1 = h[1] + h[5], a2 = h[2] + h[6], a3 = h[3] + h[7];
    float b0 = h[0] - h[4], b1 = h[1] - h[5], b2 = h[2] - h[6], b3 = h[3] - h[7];
    float e = a0 + a2, f = a1 + a3, u = cc * (b1 - b3), v = cc * (b1 + b3);
    o[0] = e + f; o[1] = e - f;
    o[2] = b0 + u; o[3] = -b2 - v;
    o[4] = a0 - a2; o[5] = a3 - a1;
    o[6] = b0 - u; o[7] = b2 - v;
  };
  rad8(h0, o0); rad8(h1, o1);
#pragma unroll
  for (int r = 0; r < 8; ++r) *(u32*)(hT + r * HT_RS + 4 * tid) = pk_bf(o0[r], o1[r]);
  __syncthreads();
  {
    const int q = c >> 2, p = c & 3;
#pragma unroll
    for (int jj = 0; jj < 2; ++jj) {
      const int j = 2 * w + jj;
      const int Qr = 4 * j + p, rho_r = Qr / 5, d0_r = 4 * (Qr % 5);
      const char* base = hT + rho_r * HT_RS + 2 * (20 * (8 * g + q) + d0_r);
      s16x8 b = cat8(tr_read(base), tr_read(base + 2 * 20 * 4));
      const int Qw = 4 * j + q, rho = Qw / 5, d0 = 4 * (Qw % 5) + p;
      f32x4 z = {0.f, 0.f, 0.f, 0.f};
      f32x4 a0 = mfma16(fa0, b, z), a1 = mfma16(fa1, b, z);
      u32* Ub = (u32*)U + rho * 17 * 20 + d0;
#pragma unroll
      for (int tt = 0; tt < 2; ++tt) {
        const f32x4 a = tt ? a1 : a0; const f32x4 t = tw[jj][tt];
        const int k1 = 8 * tt + 2 * g;
        float re0 = a[0] * t[0] - a[1] * t[1], im0 = a[0] * t[1] + a[1] * t[0];
        if (tt == 0 && g == 0) {
          Ub[16 * 20] = pk_bf(a[1] * t[0], a[1] * t[1]);
          re0 = a[0]; im0 = 0.f;
        }
        Ub[k1 * 20] = pk_bf(re0, im0);
        Ub[(k1 + 1) * 20] = pk_bf(a[2] * t[2] - a[3] * t[3], a[2] * t[3] + a[3] * t[2]);
      }
    }
  }
  __syncthreads();
  const int part_ = c & 1, k0l = c >> 1;
  const float sg = part_ ? 1.f : -1.f;
  u32* XTb = XT + (size_t)s0 * 16;
  auto put = [&](int m1d, int m1m, int k0, int k1, float re, float im) {
    const int k = k1 + 32 * k0;
    if (k0 < 20) {
      if (k <= 320) XTb[((size_t)(m1d * 21 + (k >> 4)) * 256) * 16 + (k & 15)] = pk_bf(re, im);
      else if (k1 != 0) { const int kf = 640 - k; XTb[((size_t)(m1m * 21 + (kf >> 4)) * 256) * 16 + (kf & 15)] = pk_bf(re, -im); }
    }
  };
  if (w < 4) {
    f32x4 acc[2][3];
#pragma unroll
    for (int rl = 0; rl < 2; ++rl) {
      const char* ab = U + ((2 * w + rl) * 17 + c) * 80;
      s16x8 a0 = *(const s16x8*)(ab + 16 * g), a1 = *(const s16x8*)(ab + 64);
#pragma unroll
      for (int nt = 0; nt < 3; ++nt) {
        f32x4 z = {0.f, 0.f, 0.f, 0.f};
        acc[rl][nt] = mfma16(a1, fb[nt][1], mfma16(a0, fb[nt][0], z));
      }
    }
    const int mp = w, mm = w == 0 ? 4 : 8 - w;
#pragma unroll
    for (int nt = 0; nt < 3; ++nt) {
      float Tp[4], Tm[4];
#pragma unroll
      for (int r = 0; r < 4; ++r) {
        float sw = dpp_x1(acc[1][nt][r]) * sg;
        Tp[r] = w == 0 ? acc[0][nt][r] : acc[0][nt][r] + sw;
        Tm[r] = w == 0 ? acc[1][nt][r] : acc[0][nt][r] - sw;
      }
      const int k0 = 8 * nt + k0l, k1a = 4 * g + 2 * part_;
#pragma unroll
      for (int z = 0; z < 2; ++z) {
        const float* X = z ? Tm : Tp;
        float r0 = dpp_x1(part_ ? X[0] : X[2]), r1 = dpp_x1(part_ ? X[1] : X[3]);
        float re0 = part_ ? r0 : X[0], im0 = part_ ? X[2] : r0, re1 = part_ ? r1 : X[1], im1 = part_ ? X[3] : r1;
        const int md = z ? mm : mp, mi = (w == 0) ? md : (z ? mp : mm);
        put(md, mi, k0, k1a, re0, im0);
        put(md, mi, k0, k1a + 1, re1, im1);
      }
    }
  } else {
    const char* ab = U + ((c & 7) * 17 + 16) * 80;
    s16x8 a0 = *(const s16x8*)(ab + 16 * g), a1 = *(const s16x8*)(ab + 64);
#pragma unroll
    for (int nt = 0; nt < 2; ++nt) {
      f32x4 z = {0.f, 0.f, 0.f, 0.f};
      f32x4 acc = mfma16(a1, fb[nt][1], mfma16(a0, fb[nt][0], z));
      const int k0 = 8 * nt + k0l;
#pragma unroll
      for (int pr = 0; pr < 2; ++pr) {
        const int P = 2 * g + pr;
        float sw = dpp_x1(acc[2 * pr + 1]) * sg;
        float Tp = P == 0 ? acc[2 * pr] : acc[2 * pr] + sw, Tm = P == 0 ? acc[2 * pr + 1] : acc[2 * pr] - sw;
        float rv = dpp_x1(part_ ? Tp : Tm);
        const int m1 = part_ ? (P == 0 ? 4 : 8 - P) : P;
        if (g < 2 && k0 <= 9) { const int k = 16 + 32 * k0; XTb[((size_t)(m1 * 21 + (k >> 4)) * 256) * 16] = part_ ? pk_bf(rv, Tm) : pk_bf(Tp, rv); }
      }
    }
  }
}

DI void sincos_rev(int n, float invN, float& s, float& c) { float a = (float)n * invN; s = __builtin_amdgcn_sinf(a); c = __builtin_amdgcn_cosf(a); }
__global__ __launch_bounds__(256) void k3_mid(const u32* __restrict__ XT, const u16* __restrict__ wr, const u16* __restrict__ wi, u32* __restrict__ ZT) {
  __shared__ __attribute__((aligned(16))) u32 ex[2][16 * 16 * 16];
  const int tid = threadIdx.x, lane = tid & 63, w = tid >> 6, c = lane & 15, g = lane >> 4;
  const int m1 = blockIdx.x & 7, jt = blockIdx.x >> 3;
  const u32* in = XT + (size_t)(m1 * 21 + jt) * 256 * 16;
  u32x4 bx[4];
#pragma unroll
  for (int j = 0; j < 4; ++j)
#pragma unroll
    for (int i = 0; i < 4; ++i) bx[j][i] = in[(16 * (4 * g + i) + 4 * w + j) * 16 + c];
  const int kk = min(16 * jt + c, 320);
  u16 wrv[4][4], wiv[4][4];
#pragma unroll
  for (int j = 0; j < 4; ++j)
#pragma unroll
    for (int r = 0; r < 4; ++r) {
      const int m = m1 + 8 * (4 * w + j) + 128 * (4 * g + r);
      wrv[j][r] = wr[m * DF + kk]; wiv[j][r] = wi[m * DF + kk];
    }
  u32x4 A0, A1;
#pragma unroll
  for (int i = 0; i < 4; ++i) { float s, co; sincos_rev(((4 * g + i) * (8 * c + m1)) & 127, 1.0f / 128, s, co); A0[i] = pk_bf(co, s); A1[i] = pk_bf(-s, co); }
  const f32x4 z4 = {0.f, 0.f, 0.f, 0.f};
  {
    f32x4 re[4], im[4];
#pragma unroll
    for (int j = 0; j < 4; ++j) { re[j] = mfma16(as8(A0), as8(bx[j]), z4); im[j] = mfma16(as8(A1), as8(bx[j]), z4); }
#pragma unroll
    for (int r = 0; r < 4; ++r) {
      u32x4 v;
#pragma unroll
      for (int j = 0; j < 4; ++j) v[j] = pk_bf(re[j][r], im[j][r]);
      *(u32x4*)&ex[0][((4 * g + r) * 16 + c) * 16 + 4 * w] = v;
    }
  }
  __syncthreads();
  const float scale = 1.0f / (2048.0f * 640.0f);
  f32x4 tre[4], tim[4];
#pragma unroll
  for (int j = 0; j < 4; ++j) {
    const int p = 4 * w + j;
    u32x4 b = *(const u32x4*)&ex[0][(p * 16 + c) * 16 + 4 * g];
    const int mq = m1 + 8 * p + 128 * c;
#pragma unroll
    for (int i = 0; i < 4; ++i) { float s, co; sincos_rev(((4 * g + i) * mq) & 2047, 1.0f / 2048, s, co); A0[i] = pk_bf(co, s); A1[i] = pk_bf(-s, co); }
    f32x4 xr = mfma16(as8(A0), as8(b), z4), xi = mfma16(as8(A1), as8(b), z4);
    float yr[4], yi[4];
#pragma unroll
    for (int r = 0; r < 4; ++r) {
      float a = bf2f(wrv[j][r]) * scale, bb = bf2f(wiv[j][r]) * scale;
      yr[r] = xr[r] * a - xi[r] * bb; yi[r] = xr[r] * bb + xi[r] * a;
    }
    u32x4 yb = {pk_bf(yr[0], yr[1]), pk_bf(yr[2], yr[3]), pk_bf(yi[0], yi[1]), pk_bf(yi[2], yi[3])};
    float cs[4], sn[4];
#pragma unroll
    for (int i = 0; i < 4; ++i) sincos_rev((c * (m1 + 8 * p + 128 * (4 * g + i))) & 2047, 1.0f / 2048, sn[i], cs[i]);
    A0 = u32x4{pk_bf(cs[0], cs[1]), pk_bf(cs[2], cs[3]), pk_bf(-sn[0], -sn[1]), pk_bf(-sn[2], -sn[3])};
    A1 = u32x4{pk_bf(sn[0], sn[1]), pk_bf(sn[2], sn[3]), pk_bf(cs[0], cs[1]), pk_bf(cs[2], cs[3])};
    tre[j] = mfma16(as8(A0), as8(yb), z4); tim[j] = mfma16(as8(A1), as8(yb), z4);
  }
#pragma unroll
  for (int r = 0; r < 4; ++r) {
    u32x4 v;
#pragma unroll
    for (int j = 0; j < 4; ++j) v[j] = pk_bf(tre[j][r], tim[j][r]);
    *(u32x4*)&ex[1][((4 * g + r) * 16 + c) * 16 + 4 * w] = v;
  }
#pragma unroll
  for (int i = 0; i < 4; ++i) { float s, co; sincos_rev((c * (8 * (4 * g + i) + m1)) & 127, 1.0f / 128, s, co); A0[i] = pk_bf(co, -s); A1[i] = pk_bf(s, co); }
  __syncthreads();
  u32* out = ZT + (size_t)(m1 * 21 + jt) * 256 * 16;
#pragma unroll
  for (int j = 0; j < 4; ++j) {
    const int bp = 4 * w + j;
    u32x4 b = *(const u32x4*)&ex[1][(bp * 16 + c) * 16 + 4 * g];
    f32x4 zr = mfma16(as8(A0), as8(b), z4), zi = mfma16(as8(A1), as8(b), z4);
#pragma unroll
    for (int r = 0; r < 4; ++r) out[(16 * (4 * g + r) + bp) * 16 + c] = pk_bf(zr[r], zi[r]);
  }
}

constexpr int Y_RS = 640 + 4;
__global__ __launch_bounds__(320) void k4_inv(const u32* __restrict__ ZT, const u16* __restrict__ x, const float* __restrict__ mod,
                                              u16* __restrict__ x1o, u16* __restrict__ h2o) {
  __shared__ __attribute__((aligned(16))) char smem[8 * 17 * 80 + 160 * 64 + 8 * Y_RS * 4 + 5 * 16 * 4];
  char* Zf = smem; char* Vs = smem + 8 * 17 * 80; float* Y = (float*)(Vs + 160 * 64); float* part = Y + 8 * Y_RS;
  const int tid = threadIdx.x, lane = tid & 63, w = tid >> 6, s0 = blockIdx.x, c = lane & 15, g = lane >> 4;
  const int P = tid / 80, qb = tid % 80;
  const int mA = P, mB = P == 0 ? 4 : 8 - P;
  const int kb = 4 * qb;
  const u32x4 za = *(const u32x4*)(ZT + ((size_t)(mA * 21 + (kb >> 4)) * 256 + s0) * 16 + (kb & 15));
  const u32x4 zb = *(const u32x4*)(ZT + ((size_t)(mB * 21 + (kb >> 4)) * 256 + s0) * 16 + (kb & 15));
  u32 ea = 0, eb = 0;
  if (tid < 4) {
    const int ma = tid, mb = tid == 0 ? 4 : 8 - tid;
    ea = ZT[((size_t)(ma * 21 + 20) * 256 + s0) * 16]; eb = ZT[((size_t)(mb * 21 + 20) * 256 + s0) * 16];
  }
  u32 xv[8];
#pragma unroll
  for (int r = 0; r < 8; ++r) xv[r] = *(const u32*)(x + (size_t)(s0 + 256 * r) * D_ + 2 * tid);
  const f32x2 ga = *(const f32x2*)(mod + 2 * 640 + 2 * tid), sb = *(const f32x2*)(mod + 3 * 640 + 2 * tid), cb = *(const f32x2*)(mod + 4 * 640 + 2 * tid);
  s16x8 fa[3][2];
#pragma unroll
  for (int mt = 0; mt < 3; ++mt)
#pragma unroll
    for (int ks = 0; ks < 2; ++ks) fa[mt][ks] = *(const s16x8*)g_tabs.f20ai[mt][ks][lane];
  f32x4 tw[3];
#pragma unroll
  for (int mt = 0; mt < 3; ++mt) tw[mt] = w < 4 ? *(const f32x4*)g_tabs.twc[mt][lane] : *(const f32x4*)g_tabs.twc16[mt][lane];
  const s16x8 fb0 = *(const s16x8*)g_tabs.f32bi[0][lane], fb1 = *(const s16x8*)g_tabs.f32bi[1][lane];
  auto scat = [&](int Pp, int k, u32 a, u32 b) {
    float ar = bf_lo(a), ai = bf_hi(a), br = bf_lo(b), bi = bf_hi(b);
    float p0r, p0i, p1r, p1i;
    if (Pp == 0) { p0r = ar; p0i = ai; p1r = br; p1i = bi; }
    else { p0r = ar + br; p0i = ai + bi; p1r = bi - ai; p1i = ar - br; }
    u32* z0 = (u32*)Zf + (2 * Pp) * 17 * 20; u32* z1 = z0 + 17 * 20;
    const int k1 = k & 31, k0 = k >> 5;
    if (k1 <= 16) { z0[k1 * 20 + k0] = pk_bf(p0r, p0i); z1[k1 * 20 + k0] = pk_bf(p1r, p1i); }
    if (k1 >= 16) { z0[(32 - k1) * 20 + 19 - k0] = pk_bf(p0r, -p0i); z1[(32 - k1) * 20 + 19 - k0] = pk_bf(p1r, -p1i); }
    if (k1 == 0 && k0 >= 1 && k0 <= 9) { z0[20 - k0] = pk_bf(p0r, -p0i); z1[20 - k0] = pk_bf(p1r, -p1i); }
  };
#pragma unroll
  for (int e = 0; e < 4; ++e) scat(P, kb + e, za[e], zb[e]);
  if (tid < 4) scat(tid, 320, ea, eb);
  __syncthreads();
  {
    const int nrl = w < 4 ? 2 : 1;
    for (int rl = 0; rl < nrl; ++rl) {
      const int rho = w < 4 ? 2 * w + rl : (c & 7);
      const char* bb = Zf + (rho * 17 + (w < 4 ? c : 16)) * 80;
      s16x8 b0 = *(const s16x8*)(bb + 16 * g), b1 = *(const s16x8*)(bb + 64);
#pragma unroll
      for (int mt = 0; mt < 3; ++mt) {
        f32x4 z = {0.f, 0.f, 0.f, 0.f};
        f32x4 a = mfma16(fa[mt][1], b1, mfma16(fa[mt][0], b0, z));
#pragma unroll
        for (int rr = 0; rr < 2; ++rr) {
          const int d0 = 8 * mt + 2 * g + rr;
          const float vr = a[2 * rr] * tw[mt][2 * rr] - a[2 * rr + 1] * tw[mt][2 * rr + 1];
          const float vi = a[2 * rr] * tw[mt][2 * rr + 1] + a[2 * rr + 1] * tw[mt][2 * rr];
          if (d0 < 20) {
            char* row = Vs + (rho * 20 + d0) * 64;
            if (w < 4) {
              if (c == 0) *(u16*)row = (u16)pk_bf(vr, vr);
              else *(u32*)(row + 4 * c) = pk_bf(vr, vi);
            } else if (c < 8) *(u16*)(row + 2) = (u16)pk_bf(vr, vr);
          }
        }
      }
    }
  }
  __syncthreads();
#pragma unroll
  for (int mm = 0; mm < 2; ++mm) {
    const int mt = 2 * w + mm;
    s16x8 a = *(const s16x8*)(Vs + (16 * mt + c) * 64 + 16 * g);
    f32x4 z = {0.f, 0.f, 0.f, 0.f};
    f32x4 y0 = mfma16(a, fb0, z), y1 = mfma16(a, fb1, z);
    const int R = 16 * mt + 4 * g, rho = R / 20, d0 = R % 20;
    *(f32x4*)(Y + rho * Y_RS + 20 * c + d0) = y0;
    *(f32x4*)(Y + rho * Y_RS + 20 * (16 + c) + d0) = y1;
  }
  __syncthreads();
  f32x2 R[8];
#pragma unroll
  for (int r = 0; r < 8; ++r) R[r] = *(const f32x2*)(Y + r * Y_RS + 2 * tid);
  u32 x1[8];
  {
    const float cc = 0.70710678118654752f;
    f32x2 G = R[0] + R[1], Fm = R[0] - R[1];
    f32x2 u = cc * (R[2] - R[6]), v = cc * (R[3] + R[7]);
    f32x2 c13 = R[2] + R[6], s13 = R[3] - R[7];
    f32x2 y[8];
    y[0] = G + R[4] + c13; y[4] = G + R[4] - c13; y[2] = G - R[4] + s13; y[6] = G - R[4] - s13;
    y[1] = Fm + u + v + R[5]; y[3] = Fm - u + v - R[5]; y[5] = Fm - u - v + R[5]; y[7] = Fm + u - v - R[5];
#pragma unroll
    for (int r = 0; r < 8; ++r) {
      u32 yb = pk_bf(y[r][0], y[r][1]);
      u32 gy = pk_bf(ga[0] * bf_lo(yb), ga[1] * bf_hi(yb));
      x1[r] = pk_bf(bf_lo(xv[r]) + bf_lo(gy), bf_hi(xv[r]) + bf_hi(gy));
    }
  }
  float mu[8], rs[8];
  ln_stats(x1, part, tid, mu, rs);
  const float g0 = rbf(1.0f + cb[0]), g1 = rbf(1.0f + cb[1]);
#pragma unroll
  for (int r = 0; r < 8; ++r) {
    const int tau = 8 * s0 + r;
    *(u32*)(x1o + (size_t)tau * D_ + 2 * tid) = x1[r];
    float n0 = rbf(rbf(bf_lo(x1[r]) - mu[r]) * rs[r]), n1 = rbf(rbf(bf_hi(x1[r]) - mu[r]) * rs[r]);
    u32 hv = pk_bf(rbf(n0 * g0) + sb[0], rbf(n1 * g1) + sb[1]);
    const int d = 2 * tid, T = tau >> 5, tok = tau & 31;
    *(u32*)(h2o + ((((size_t)T * 40 + (d >> 4)) * 2 + ((d >> 3) & 1)) * 32 + tok) * 8 + (d & 7)) = hv;
  }
}

constexpr int K5_IMG = 64 * 224;
constexpr int K5_SLAB = 2 * K5_IMG + 128;
__global__ __launch_bounds__(256) void k5_glu(const u16* __restrict__ h2, const u16* __restrict__ wg, const u16* __restrict__ wu, u16* __restrict__ hb) {
  __shared__ __attribute__((aligned(16))) char smem[2 * (K5_SLAB + 128)];
  const int tid = threadIdx.x, lane = tid & 63, w = __builtin_amdgcn_readfirstlane(tid >> 6);
  const int xcd = blockIdx.x & 7, li = blockIdx.x >> 3;
  const int tt = 8 * (xcd & 1) + (li & 7), fc = 4 * (xcd >> 1) + (li >> 3);
  const int u0 = fc < 12 ? 7 * fc : 84 + 6 * (fc - 12), U = fc < 12 ? 7 : 6;
  const int f0 = 16 * u0;
  const u16* wsrc = (w < 2 ? wg : wu);
  int goff[7];
#pragma unroll
  for (int i = 0; i < 7; ++i) { const int n = 448 * (w & 1) + 64 * i + lane, row = n / 14, ch = min(n % 14, 2 * U - 1); goff[i] = row * F_ + f0 + 8 * ch; }
  const int ldsw = (w < 2 ? 0 : K5_IMG + 128) + 448 * 16 * (w & 1);
  auto stage = [&](int ks, int buf) {
#pragma unroll
    for (int i = 0; i < 7; ++i)
      __builtin_amdgcn_global_load_lds((const u32*)(wsrc + (size_t)ks * 64 * F_ + goff[i]), (LDSP(u32))(smem + buf * (K5_SLAB + 128) + ldsw + i * 1024), 16, 0, 0);
  };
  f32x16 acc[7];
#pragma unroll
  for (int u = 0; u < 7; ++u)
#pragma unroll
    for (int i = 0; i < 16; ++i) acc[u][i] = 0.f;
  const int T = 4 * tt + w;
  const u16* hsrc = h2 + (size_t)T * 40 * 512 + lane * 8;
  const int hh = lane >> 5, blk = (lane >> 4) & 1, q = (lane & 15) >> 2, p = lane & 3;
  const int aoff = (blk ? K5_IMG + 128 : 0) + (8 * hh + q) * 224 + 8 * p;
  stage(0, 0);
  for (int ks = 0; ks < 10; ++ks) {
    u32x4 bfr[4];
#pragma unroll
    for (int t = 0; t < 4; ++t) bfr[t] = *(const u32x4*)(hsrc + (size_t)(4 * ks + t) * 512);
    asm volatile("s_waitcnt vmcnt(0)" ::: "memory");
    __syncthreads();
    if (ks + 1 < 10) stage(ks + 1, (ks + 1) & 1);
    const char* img = smem + (ks & 1) * (K5_SLAB + 128) + aoff;
#pragma unroll
    for (int t = 0; t < 4; ++t)
#pragma unroll
      for (int u = 0; u < 7; ++u) {
        const char* a = img + 16 * t * 224 + 32 * u;
        acc[u] = mfma32(cat8(tr_read(a), tr_read(a + 4 * 224)), as8(bfr[t]), acc[u]);
      }
  }
  const int tok = lane & 31, tau = 32 * T + tok, T16 = tau >> 4;
#pragma unroll
  for (int u = 0; u < 7; ++u) {
    if (u < U) {
      float h[8];
#pragma unroll
      for (int j = 0; j < 8; ++j) h[j] = rbf(acc[u][j]) * rbf(acc[u][8 + j]);
      const int un = u0 + u, ks = un >> 1, gg = 2 * (un & 1) + hh;
      u32x4 v = {pk_bf(h[0], h[1]), pk_bf(h[2], h[3]), pk_bf(h[4], h[5]), pk_bf(h[6], h[7])};
      *(u32x4*)(hb + ((((size_t)T16 * 54 + ks) * 4 + gg) * 16 + (tau & 15)) * 8) = v;
    }
  }
}

__global__ __launch_bounds__(256) void k6_down(const u16* __restrict__ hb, const u16* __restrict__ wd, const u16* __restrict__ x1, const float* __restrict__ mod, u16* __restrict__ out) {
  __shared__ __attribute__((aligned(16))) char smem[4 * 20480];
  const int tid = threadIdx.x, lane = tid & 63, w = __builtin_amdgcn_readfirstlane(tid >> 6), c = lane & 15, g = lane >> 4;
  const int xcd = blockIdx.x & 7, li = blockIdx.x >> 3;
  const int tt = 8 * (xcd >> 1) + (li & 7), nb = 4 * (xcd & 1) + (li >> 3);
  const int n0 = 80 * nb;
  const int ks0 = w < 2 ? 14 * w : 28 + 13 * (w - 2), nks = w < 2 ? 14 : 13;
  char* ring = smem + w * 20480;
  int goff[5];
#pragma unroll
  for (int i = 0; i < 5; ++i) { const int n = 64 * i + lane; goff[i] = (n / 10) * D_ + n0 + 8 * (n % 10); }
  auto stage = [&](int ks, int buf) {
#pragma unroll
    for (int i = 0; i < 5; ++i)
      __builtin_amdgcn_global_load_lds((const u32*)(wd + (size_t)ks * 32 * D_ + goff[i]), (LDSP(u32))(ring + buf * 5120 + i * 1024), 16, 0, 0);
  };
  f32x4 acc[5][4];
#pragma unroll
  for (int a = 0; a < 5; ++a)
#pragma unroll
    for (int b = 0; b < 4; ++b) acc[a][b] = f32x4{0.f, 0.f, 0.f, 0.f};
  const u16* hsrc = hb + (size_t)(4 * tt) * 54 * 512 + lane * 8;
  const int q = c >> 2, p = c & 3;
  const int aoff = (16 * (g >> 1) + 4 * (g & 1) + q) * 160 + 8 * p;
  stage(ks0, 0);
  for (int i = 0; i < nks; ++i) {
    const int ks = ks0 + i;
    u32x4 bfr[4];
#pragma unroll
    for (int t = 0; t < 4; ++t) bfr[t] = *(const u32x4*)(hsrc + ((size_t)t * 54 + ks) * 512);
    asm volatile("s_waitcnt vmcnt(0)" ::: "memory");
    if (i + 1 < nks) stage(ks + 1, (i + 1) & 1);
    const char* img = ring + (i & 1) * 5120 + aoff;
#pragma unroll
    for (int nt = 0; nt < 5; ++nt) {
      s16x8 a = cat8(tr_read(img + 32 * nt), tr_read(img + 32 * nt + 8 * 160));
#pragma unroll
      for (int t = 0; t < 4; ++t) acc[nt][t] = mfma16(a, as8(bfr[t]), acc[nt][t]);
    }
    asm volatile("s_waitcnt lgkmcnt(0)" ::: "memory");
  }
  __syncthreads();
  f32x4* red = (f32x4*)smem;
#pragma unroll
  for (int nt = 0; nt < 5; ++nt)
#pragma unroll
    for (int t = 0; t < 4; ++t) red[((w * 5 + nt) * 4 + t) * 64 + lane] = acc[nt][t];
  __syncthreads();
  const int tau = 64 * tt + 16 * w + c, srow = (tau >> 3) + 256 * (tau & 7);
#pragma unroll
  for (int nt = 0; nt < 5; ++nt) {
    f32x4 v = red[((0 * 5 + nt) * 4 + w) * 64 + lane];
#pragma unroll
    for (int ww = 1; ww < 4; ++ww) v += red[((ww * 5 + nt) * 4 + w) * 64 + lane];
    const int n = n0 + 16 * nt + 4 * g;
    const f32x4 gb = *(const f32x4*)(mod + 5 * 640 + n);
    const u32x2 xr = *(const u32x2*)(x1 + (size_t)tau * D_ + n);
    float o0 = bf_lo(xr[0]) + rbf(gb[0] * rbf(v[0])), o1 = bf_hi(xr[0]) + rbf(gb[1] * rbf(v[1]));
    float o2 = bf_lo(xr[1]) + rbf(gb[2] * rbf(v[2])), o3 = bf_hi(xr[1]) + rbf(gb[3] * rbf(v[3]));
    *(u32x2*)(out + (size_t)srow * D_ + n) = u32x2{pk_bf(o0, o1), pk_bf(o2, o3)};
  }
}

constexpr size_t WS_MOD = 0, WS_XT = 16384, WS_ZT = WS_XT + 8 * 21 * 256 * 16 * 4, WS_X1 = WS_ZT + 8 * 21 * 256 * 16 * 4,
                 WS_H2 = WS_X1 + 2048 * 640 * 2, WS_HB = WS_H2 + 2048 * 640 * 2;
extern "C" void kernel_launch(void* const* d_in, const int* in_sizes, int n_in, void* d_out, int out_size, void* d_ws, size_t ws_size,
                              hipStream_t stream) {
  const u16* x = (const u16*)d_in[0]; const u16* wr = (const u16*)d_in[1]; const u16* wi = (const u16*)d_in[2];
  const u16* wg = (const u16*)d_in[4]; const u16* wu = (const u16*)d_in[5]; const u16* wd = (const u16*)d_in[6];
  const u16* c = (const u16*)d_in[7]; const u16* wmod = (const u16*)d_in[8];
  char* ws = (char*)d_ws;
  float* mod = (float*)(ws + WS_MOD); u32* XT = (u32*)(ws + WS_XT); u32* ZT = (u32*)(ws + WS_ZT);
  u16* x1 = (u16*)(ws + WS_X1); u16* h2 = (u16*)(ws + WS_H2); u16* hb = (u16*)(ws + WS_HB);
  k1_mod<<<120, 256, 0, stream>>>(c, wmod, mod);
  k2_fwd<<<256, 320, 0, stream>>>(x, mod, XT);
  k3_mid<<<168, 256, 0, stream>>>(XT, wr, wi, ZT);
  k4_inv<<<256, 320, 0, stream>>>(ZT, x, mod, x1, h2);
  k5_glu<<<256, 256, 0, stream>>>(h2, wg, wu, hb);
  k6_down<<<256, 256, 0, stream>>>(hb, wd, x1, mod, (u16*)d_out);
}
```

```cpp
#include <hip/hip_runtime.h>
#include <hip/hip_bf16.h>
#include <stdint.h>
#include <utility>

typedef unsigned short u16;
typedef unsigned int u32;
typedef short s16x4 __attribute__((ext_vector_type(4)));
typedef short s16x8 __attribute__((ext_vector_type(8)));
typedef __bf16 bf2_t __attribute__((ext_vector_type(2)));
typedef float f32x2 __attribute__((ext_vector_type(2)));
typedef float f32x4 __attribute__((ext_vector_type(4)));
typedef float f32x16 __attribute__((ext_vector_type(16)));
typedef u32 u32x2 __attribute__((ext_vector_type(2)));
typedef u32 u32x4 __attribute__((ext_vector_type(4)));
#define DI __device__ __forceinline__
#define LDSP(T) __attribute__((address_space(3))) T*

constexpr int S_ = 2048, D_ = 640, F_ = 1728, DF = 321;

constexpr double kPi = 3.14159265358979323846264338327950288;
constexpr double c_sin0(double x) { double x2 = x * x, t = x, s = x; for (int i = 1; i < 11; ++i) { t *= -x2 / ((2.0 * i) * (2.0 * i + 1.0)); s += t; } return s; }
constexpr double c_cos0(double x) { double x2 = x * x, t = 1.0, s = 1.0; for (int i = 1; i < 11; ++i) { t *= -x2 / ((2.0 * i - 1.0) * (2.0 * i)); s += t; } return s; }
constexpr double c_cos(long n, long N) {
  n %= N; if (n < 0) n += N;
  if (2 * n > N) n = N - n;
  bool neg = false;
  if (4 * n > N) { neg = true; n = N - 2 * n; N = 2 * N; n = n;   }
  double r = 0;
  if (4 * n == N) r = 0.0;
  else if (8 * n > N) r = c_sin0(2 * kPi * (double)(N - 4 * n) / (double)(4 * N));
  else r = c_cos0(2 * kPi * (double)n / (double)N);
  return neg ? -r : r;
}
constexpr double c_sin(long n, long N) { return c_cos(4 * n - N, 4 * N); }
constexpr u16 c_bf16(double v) {
  float f = (float)v; u32 u = __builtin_bit_cast(u32, f);
  u += 0x7fffu + ((u >> 16) & 1u); return (u16)(u >> 16);
}

struct F8w { u16 v[2][64][8]; };
struct F8x6 { u16 v[3][2][64][8]; };
struct TwA { float v[2][64][4]; };
struct TwC { float v[3][64][4]; };
constexpr F8w mk_f32a() {
  F8w t{};
  for (int tt = 0; tt < 2; ++tt) for (int l = 0; l < 64; ++l) for (int j = 0; j < 8; ++j) {
    int o = 16 * tt + (l & 15), d1 = 8 * (l >> 4) + j; double v = 0;
    if (o == 0) v = 1.0; else if (o == 1) v = (d1 & 1) ? -1.0 : 1.0;
    else { int k1 = o >> 1; v = (o & 1) ? -c_sin(d1 * k1, 32) : c_cos(d1 * k1, 32); }
    t.v[tt][l][j] = c_bf16(v);
  }
  return t;
}
constexpr TwA mk_twa(int j) {
  TwA t{};
  for (int tt = 0; tt < 2; ++tt) for (int l = 0; l < 64; ++l) for (int rr = 0; rr < 2; ++rr) {
    int c = l & 15, g = l >> 4, Q = 4 * j + (c >> 2), d0 = 4 * (Q % 5) + (c & 3), rho = Q / 5;
    int k1 = 8 * tt + 2 * g + rr; if (k1 == 0) k1 = 16;
    double co = c_cos(d0 * k1, 640), si = -c_sin(d0 * k1, 640);
    if (rho >= 3 && (rho & 1)) { double x = co; co = -si; si = x; }
    t.v[tt][l][2 * rr] = (float)co; t.v[tt][l][2 * rr + 1] = (float)si;
  }
  return t;
}
constexpr F8x6 mk_f20(bool inv) {
  F8x6 t{};
  for (int mt = 0; mt < 3; ++mt) for (int ks = 0; ks < 2; ++ks) for (int l = 0; l < 64; ++l) for (int j = 0; j < 8; ++j) {
    int row = l & 15, g = l >> 4, k0 = 8 * mt + (row >> 1), part = row & 1, d0 = 16 * ks + 4 * g + (j >> 1), pp = j & 1; double v = 0;
    if (d0 < 20 && k0 < 20) {
      double co = c_cos(d0 * k0, 20), si = inv ? -c_sin(d0 * k0, 20) : c_sin(d0 * k0, 20);
      v = part == 0 ? (pp == 0 ? co : si) : (pp == 0 ? -si : co);
    }
    t.v[mt][ks][l][j] = c_bf16(v);
  }
  return t;
}
constexpr F8w mk_a1(int m1, bool inv) {
  F8w t{};
  for (int l = 0; l < 64; ++l) for (int i = 0; i < 4; ++i) {
    int c = l & 15, a = 4 * (l >> 4) + i, n = inv ? c * (8 * a + m1) : a * (8 * c + m1);
    double co = c_cos(n, 128), si = inv ? -c_sin(n, 128) : c_sin(n, 128);
    t.v[0][l][2 * i] = c_bf16(co); t.v[0][l][2 * i + 1] = c_bf16(si);
    t.v[1][l][2 * i] = c_bf16(-si); t.v[1][l][2 * i + 1] = c_bf16(co);
  }
  return t;
}
constexpr F8w mk_f16(bool inv) {
  F8w t{};
  for (int l = 0; l < 64; ++l) for (int i = 0; i < 4; ++i) {
    int c = l & 15, a = 4 * (l >> 4) + i;
    double co = c_cos(a * c, 16), si = c_sin(a * c, 16);
    if (!inv) { t.v[0][l][2 * i] = c_bf16(co); t.v[0][l][2 * i + 1] = c_bf16(si); t.v[1][l][2 * i] = c_bf16(-si); t.v[1][l][2 * i + 1] = c_bf16(co); }
    else { t.v[0][l][i] = c_bf16(co); t.v[0][l][4 + i] = c_bf16(-si); t.v[1][l][i] = c_bf16(si); t.v[1][l][4 + i] = c_bf16(co); }
  }
  return t;
}
constexpr TwC mk_twc(int kind) {
  TwC t{};
  const double sc = 1.0 / (2048.0 * 640.0);
  for (int mt = 0; mt < 3; ++mt) for (int l = 0; l < 64; ++l) for (int rr = 0; rr < 2; ++rr) {
    int k1 = kind == 2 ? 16 : (l & 15), g = l >> 4, d0 = 8 * mt + 2 * g + rr, rho = l & 7;
    double co = c_cos(d0 * k1, 640) * sc * (kind == 2 ? 2 : 1), si = c_sin(d0 * k1, 640) * sc * (kind == 2 ? 2 : 1);
    if (kind == 1 || (kind == 2 && rho >= 3 && (rho & 1))) { double x = co; co = -si; si = x; }
    t.v[mt][l][2 * rr] = (float)co; t.v[mt][l][2 * rr + 1] = (float)si;
  }
  return t;
}
constexpr F8w mk_f32bi() {
  F8w t{};
  for (int nt = 0; nt < 2; ++nt) for (int l = 0; l < 64; ++l) for (int j = 0; j < 8; ++j) {
    int d1 = 16 * nt + (l & 15), K = 8 * (l >> 4) + j; double v = 0;
    if (K == 0) v = 1.0; else if (K == 1) v = (d1 & 1) ? -1.0 : 1.0;
    else { int k1 = K >> 1; v = (K & 1) ? -2.0 * c_sin(k1 * d1, 32) : 2.0 * c_cos(k1 * d1, 32); }
    t.v[nt][l][j] = c_bf16(v);
  }
  return t;
}
static __device__ const F8w g_f32a = mk_f32a();
static __device__ const F8x6 g_f20a = mk_f20(false);
static __device__ const F8x6 g_f20ai = mk_f20(true);
static __device__ const F8w g_a1[8] = {mk_a1(0, false), mk_a1(1, false), mk_a1(2, false), mk_a1(3, false), mk_a1(4, false), mk_a1(5, false), mk_a1(6, false), mk_a1(7, false)};
static __device__ const F8w g_a1i[8] = {mk_a1(0, true), mk_a1(1, true), mk_a1(2, true), mk_a1(3, true), mk_a1(4, true), mk_a1(5, true), mk_a1(6, true), mk_a1(7, true)};
static __device__ const F8w g_f16f = mk_f16(false);
static __device__ const F8w g_f16i = mk_f16(true);
static __device__ const F8w g_f32bi = mk_f32bi();

#define WAITALL() do {} while (0)
#define STAMP_DECL
#define STAMP(k, i) do {} while (0)
#define SLAB(i) do {} while (0)
#define WSTAMP(j) do {} while (0)
DI float bf_lo(u32 v) { return __uint_as_float(v << 16); }
DI float bf_hi(u32 v) { return __uint_as_float(v & 0xffff0000u); }
DI float bf2f(u16 v) { return __uint_as_float(((u32)v) << 16); }
DI u32 pk_bf(float a, float b) { f32x2 f = {a, b}; return __builtin_bit_cast(u32, __builtin_convertvector(f, bf2_t)); }
DI float rbf(float a) { return bf_lo(pk_bf(a, a)); }
DI float dpp_x1(float v) { return __int_as_float(__builtin_amdgcn_mov_dpp(__float_as_int(v), 0xB1, 0xf, 0xf, true)); }
DI float dpp_x2(float v) { return __int_as_float(__builtin_amdgcn_mov_dpp(__float_as_int(v), 0x4E, 0xf, 0xf, true)); }
DI float dpp_hm(float v) { return __int_as_float(__builtin_amdgcn_mov_dpp(__float_as_int(v), 0x141, 0xf, 0xf, true)); }
DI float dpp_rm(float v) { return __int_as_float(__builtin_amdgcn_mov_dpp(__float_as_int(v), 0x140, 0xf, 0xf, true)); }
DI f32x4 mfma16(s16x8 a, s16x8 b, f32x4 c) { return __builtin_amdgcn_mfma_f32_16x16x32_bf16(a, b, c, 0, 0, 0); }
DI f32x16 mfma32(s16x8 a, s16x8 b, f32x16 c) { return __builtin_amdgcn_mfma_f32_32x32x16_bf16(a, b, c, 0, 0, 0); }
DI s16x4 tr_read(const void* p) { return __builtin_amdgcn_ds_read_tr16_b64_v4i16((LDSP(s16x4))p); }
template <class T> DI void pin(T& v) { asm volatile("" : "+v"(v)); }
DI void l2_touch(const void* base, u32 voff, u32 lds) {
  asm volatile("s_mov_b32 m0, %0\n\ts_nop 0\n\tglobal_load_lds_dword %1, %2" :: "s"(lds), "v"(voff), "s"(base) : "memory");
}
DI u32 lds_addr(const void* p) { return (u32)(size_t)(LDSP(const char))p; }
DI s16x8 cat8(s16x4 a, s16x4 b) { return __builtin_shufflevector(a, b, 0, 1, 2, 3, 4, 5, 6, 7); }
DI s16x8 as8(u32x4 v) { return __builtin_bit_cast(s16x8, v); }
DI float dot2bf(u32 a, u32 b, float c) { return __builtin_amdgcn_fdot2_f32_bf16(__builtin_bit_cast(bf2_t, a), __builtin_bit_cast(bf2_t, b), c, false); }

DI float reduce16(const float (&v)[16], int lane) {
  float a[8], b[4], c[2], d;
  const bool s3 = lane & 8, s2 = lane & 4, s1 = lane & 2, s0 = lane & 1;
#pragma unroll
  for (int i = 0; i < 8; ++i) { float keep = s3 ? v[8 + i] : v[i], send = s3 ? v[i] : v[8 + i]; a[i] = keep + dpp_rm(send); }
#pragma unroll
  for (int i = 0; i < 4; ++i) { float keep = s2 ? a[4 + i] : a[i], send = s2 ? a[i] : a[4 + i]; b[i] = keep + dpp_hm(send); }
#pragma unroll
  for (int i = 0; i < 2; ++i) { float keep = s1 ? b[2 + i] : b[i], send = s1 ? b[i] : b[2 + i]; c[i] = keep + dpp_x2(send); }
  { float keep = s0 ? c[1] : c[0], send = s0 ? c[0] : c[1]; d = keep + dpp_x1(send); }
  auto r16 = __builtin_amdgcn_permlane16_swap(__float_as_uint(d), __float_as_uint(d), false, false);
  d = __uint_as_float(r16[0]) + __uint_as_float(r16[1]);
  auto r32 = __builtin_amdgcn_permlane32_swap(__float_as_uint(d), __float_as_uint(d), false, false);
  return __uint_as_float(r32[0]) + __uint_as_float(r32[1]);
}

DI void ln_finish(float s, float q, float& m_, float& i_) {
  const float mean = s * (1.0f / 640.0f);
  const float var = fmaxf(q * (1.0f / 640.0f) - mean * mean, 0.f);
  const float sd = rbf(__builtin_amdgcn_sqrtf(rbf(rbf(var) + 1e-5f)));
  m_ = rbf(mean); i_ = __builtin_amdgcn_rcpf(sd);
}
DI void ln_stats5(const u32 (&xv)[8], float* part  , int tid, float (&a_)[8], float (&b_)[8]) {
  const int lane = tid & 63, w = tid >> 6;
  float v[16];
#pragma unroll
  for (int r = 0; r < 8; ++r) { v[r] = dot2bf(xv[r], 0x3f803f80u, 0.f); v[8 + r] = dot2bf(xv[r], xv[r], 0.f); }
  float t = reduce16(v, lane);
  if (lane < 16) part[w * 16 + lane] = t;
  __syncthreads();
  const int r = lane & 7;
  float s = 0.f, q = 0.f;
#pragma unroll
  for (int i = 0; i < 5; ++i) { s += part[i * 16 + r]; q += part[i * 16 + 8 + r]; }
  float m_, i_; ln_finish(s, q, m_, i_);
  const float nb = -m_ * i_;
#pragma unroll
  for (int i = 0; i < 8; ++i) {
    a_[i] = __int_as_float(__builtin_amdgcn_readlane(__float_as_int(i_), i));
    b_[i] = __int_as_float(__builtin_amdgcn_readlane(__float_as_int(nb), i));
  }
}
DI float reduce8(const float (&v)[8], int lane) {
  float b[4], c[2], d;
  const bool s2 = lane & 4, s1 = lane & 2, s0 = lane & 1;
#pragma unroll
  for (int i = 0; i < 4; ++i) { float keep = s2 ? v[4 + i] : v[i], send = s2 ? v[i] : v[4 + i]; b[i] = keep + dpp_hm(send); }
#pragma unroll
  for (int i = 0; i < 2; ++i) { float keep = s1 ? b[2 + i] : b[i], send = s1 ? b[i] : b[2 + i]; c[i] = keep + dpp_x2(send); }
  { float keep = s0 ? c[1] : c[0], send = s0 ? c[0] : c[1]; d = keep + dpp_x1(send); }
  d += __int_as_float(__builtin_amdgcn_mov_dpp(__float_as_int(d), 0x128, 0xf, 0xf, true));
  auto r16 = __builtin_amdgcn_permlane16_swap(__float_as_uint(d), __float_as_uint(d), false, false);
  d = __uint_as_float(r16[0]) + __uint_as_float(r16[1]);
  auto r32 = __builtin_amdgcn_permlane32_swap(__float_as_uint(d), __float_as_uint(d), false, false);
  return __uint_as_float(r32[0]) + __uint_as_float(r32[1]);
}

template <int NKS> DI void prep_tile(const u16* __restrict__ src, int ld, u16* __restrict__ dst, int jstride, int kstride, char* lds) {
  const int t = threadIdx.x, lane = t & 63, w = t >> 6;
  u32x4 v[NKS];
#pragma unroll
  for (int i = 0; i < NKS; ++i) v[i] = *(const u32x4*)(src + (size_t)(32 * i + (t >> 3)) * ld + 8 * (t & 7));
#pragma unroll
  for (int i = 0; i < NKS; ++i) {
    const int row = 32 * i + (t >> 3), ch = t & 7;
    *(u32x4*)(lds + row * 128 + (((ch >> 1) ^ ((row >> 1) & 3)) * 32) + (ch & 1) * 16) = v[i];
  }
  __syncthreads();
  const int c = lane & 15, g = lane >> 4, q = c >> 2, p = c & 3, r = 4 * g + q;
  const char* b = lds + r * 128 + ((w ^ ((r >> 1) & 3)) * 32) + 8 * p;
#pragma unroll
  for (int i = 0; i < NKS; ++i) {
    s16x8 f = cat8(tr_read(b + 32 * i * 128), tr_read(b + (32 * i + 16) * 128));
    *(s16x8*)(dst + (size_t)w * jstride + (size_t)i * kstride + lane * 8) = f;
  }
}
DI void prep_gu(int task, const u16* wg, const u16* wu, u16* Wgu, char* lds) {
  const int mat = task >= 135, tk = task - 135 * mat, cg = tk / 5, rg = tk % 5;
  prep_tile<4>((mat ? wu : wg) + (size_t)rg * 128 * F_ + 64 * cg, F_, Wgu + ((size_t)(4 * cg * 20 + 4 * rg) * 2 + mat) * 512, 20 * 2 * 512, 2 * 512, lds);
}
DI void prep_d(int task, const u16* wd, u16* Wd, char* lds) {
  const int cg = task / 18, rg = task % 18;
  prep_tile<3>(wd + (size_t)rg * 96 * D_ + 64 * cg, D_, Wd + (size_t)(4 * cg * 54 + 3 * rg) * 512, 54 * 512, 512, lds);
}

DI void prep_w(int task, const u16* __restrict__ wr, const u16* __restrict__ wi, u32x4* __restrict__ Wp);
__global__ __launch_bounds__(256) void k1_mod(const u16* __restrict__ c, const u16* __restrict__ wmod, float* __restrict__ mod,
                                              const u16* __restrict__ wg, const u16* __restrict__ wu, const u16* __restrict__ wd, u16* __restrict__ Wgu, u16* __restrict__ Wd,
                                              const u16* __restrict__ wr, const u16* __restrict__ wi, u32x4* __restrict__ Wp) {
  STAMP_DECL; STAMP(0, 0);
  __shared__ __attribute__((aligned(16))) char plds[128 * 128];
  if (blockIdx.x >= 120) {
    const int task = blockIdx.x - 120;
    if (task < 336) prep_w(task, wr, wi, Wp); else if (task < 336 + 270) prep_gu(task - 336, wg, wu, Wgu, plds); else prep_d(task - 606, wd, Wd, plds);
    STAMP(0, 6);
    return;
  }
  float (*red)[32] = (float (*)[32])plds;
  const int t = threadIdx.x, cq = t & 3, rg = t >> 2, col0 = blockIdx.x * 32 + cq * 8;
  u32x4 w[10]; u16 cv[10];
#pragma unroll
  for (int i = 0; i < 10; ++i) w[i] = *(const u32x4*)(wmod + (size_t)(rg + 64 * i) * 3840 + col0);
#pragma unroll
  for (int i = 0; i < 10; ++i) cv[i] = c[rg + 64 * i];
  float acc[8] = {};
  asm volatile("s_waitcnt vmcnt(0)" ::: "memory");
  STAMP(0, 1);
#pragma unroll
  for (int i = 0; i < 10; ++i) {
    float x = bf2f(cv[i]); float s = rbf(x / (1.0f + __expf(-x)));
#pragma unroll
    for (int j = 0; j < 4; ++j) { acc[2 * j] += s * bf_lo(w[i][j]); acc[2 * j + 1] += s * bf_hi(w[i][j]); }
  }
#pragma unroll
  for (int j = 0; j < 8; ++j) {
    float v = acc[j];
    v += __shfl_xor(v, 4); v += __shfl_xor(v, 8); v += __shfl_xor(v, 16); v += __shfl_xor(v, 32);
    acc[j] = v;
  }
  if ((t & 63) < 4) {
#pragma unroll
    for (int j = 0; j < 8; ++j) red[t >> 6][cq * 8 + j] = acc[j];
  }
  __syncthreads();
  if (t < 32) mod[blockIdx.x * 32 + t] = rbf(red[0][t] + red[1][t] + red[2][t] + red[3][t]);
  STAMP(0, 7);
}

DI int row_group(int b) { return 2 * (8 * (b >> 4) + (b & 7)) + ((b >> 3) & 1); }
DI void sincos_rev(float a, float& s, float& c) { s = __builtin_amdgcn_sinf(a); c = __builtin_amdgcn_cosf(a); }
DI void gram_part(const u32x4 (&f)[4], float* part  , int i, int lane) {
  f32x4 acc = {0.f, 0.f, 0.f, 0.f};
#pragma unroll
  for (int k = 0; k < 4; ++k) acc = mfma16(as8(f[k]), as8(f[k]), acc);
  const int c = lane & 15, g = lane >> 4;
  const float dg = (c & 2) ? ((c & 1) ? acc[3] : acc[2]) : ((c & 1) ? acc[1] : acc[0]);
  if (c < 8 && g == (c >> 2)) part[i * 16 + 8 + c] = dg;
  if (c < 8 && g == 2) part[i * 16 + c] = acc[0];
}
DI void ln_bcast8(const float* part, int lane, float (&a_)[8], float (&b_)[8]) {
  const int r = lane & 7;
  float s = 0.f, q = 0.f;
#pragma unroll
  for (int i = 0; i < 5; ++i) { s += part[i * 16 + r]; q += part[i * 16 + 8 + r]; }
  float m_, i_; ln_finish(s, q, m_, i_);
  const float nb = -m_ * i_;
#pragma unroll
  for (int i = 0; i < 8; ++i) {
    a_[i] = __int_as_float(__builtin_amdgcn_readlane(__float_as_int(i_), i));
    b_[i] = __int_as_float(__builtin_amdgcn_readlane(__float_as_int(nb), i));
  }
}
constexpr int HT_RS = 1296;
__global__ __launch_bounds__(640) void k2_fwd(const u16* __restrict__ x, const float* __restrict__ mod, u32* __restrict__ XT, const u32x4* __restrict__ Wp) {
  STAMP_DECL; STAMP(1, 0);
  __shared__ __attribute__((aligned(16))) char smem[8 * HT_RS + 8 * 17 * 80 + 5 * 16 * 4 + 8 * 1024 + 256];
  char* hT = smem; char* U = smem + 8 * HT_RS; float* part = (float*)(smem + 8 * HT_RS + 8 * 17 * 80);
  char* tab = smem + 8 * HT_RS + 8 * 17 * 80 + 5 * 16 * 4;
  const int tid = threadIdx.x, lane = tid & 63, w = __builtin_amdgcn_readfirstlane(tid >> 6);
  const int s0 = row_group(blockIdx.x);
  const int c = lane & 15, g = lane >> 4, q = c >> 2, p = c & 3;
  u32 xv[8]; f32x2 sa, gg;
  if (w < 5) {
#pragma unroll
    for (int r = 0; r < 8; ++r) xv[r] = *(const u32*)(x + (size_t)(s0 + 256 * r) * D_ + 2 * tid);
    sa = *(const f32x2*)(mod + 2 * tid); const f32x2 ca = *(const f32x2*)(mod + 640 + 2 * tid);
    gg = f32x2{rbf(1.0f + ca[0]), rbf(1.0f + ca[1])};
  } else {
    const int i = w - 5;
    u32x4 f[4];
    const u16* xr = x + (size_t)(s0 + 256 * (c & 7)) * D_ + 128 * i + 8 * g;
#pragma unroll
    for (int k = 0; k < 4; ++k) f[k] = *(const u32x4*)(xr + 32 * k);
    const s16x8 t0 = i < 2 ? *(const s16x8*)g_f32a.v[i][lane] : *(const s16x8*)&g_f20a.v[0][0][0][(i - 2) * 512 + lane * 8];
    const s16x8 t1 = *(const s16x8*)&g_f20a.v[0][0][0][(i < 3 ? i + 3 : 5) * 512 + lane * 8];
    if (c >= 8) {
      const u32 fill = c == 8 ? 0x3f803f80u : 0u;
#pragma unroll
      for (int k = 0; k < 4; ++k) f[k] = u32x4{fill, fill, fill, fill};
    }
    gram_part(f, part, i, lane);
    *(s16x8*)(tab + i * 1024 + lane * 16) = t0;
    if (i < 3) *(s16x8*)(tab + (i + 5) * 1024 + lane * 16) = t1;
    if (i < 2) {
      const int li = blockIdx.x >> 3, ln = li * 84 + i * 64;
      const int l = ln + lane, tl = l >> 7;
      if (i * 64 + lane < 84) l2_touch(Wp, (u32)((tl * 8 + (blockIdx.x & 7)) * 16384 + (l & 127) * 128), lds_addr(smem) + sizeof(smem) - 256);
    }
  }
  const int Qw = 4 * w + q, rho = Qw / 5, d0 = 4 * (Qw % 5) + p;
  const bool rot = rho >= 3 && (rho & 1);
  f32x4 tw0, tw1;
  {
    const float fd = (float)d0 * (1.0f / 640.0f);
    const int k10 = g == 0 ? 16 : 2 * g;
    float s_[4], c_[4];
    sincos_rev(fd * (float)k10, s_[0], c_[0]); sincos_rev(fd * (float)(2 * g + 1), s_[1], c_[1]);
    sincos_rev(fd * (float)(8 + 2 * g), s_[2], c_[2]); sincos_rev(fd * (float)(9 + 2 * g), s_[3], c_[3]);
    tw0 = f32x4{rot ? s_[0] : c_[0], rot ? c_[0] : -s_[0], rot ? s_[1] : c_[1], rot ? c_[1] : -s_[1]};
    tw1 = f32x4{rot ? s_[2] : c_[2], rot ? c_[2] : -s_[2], rot ? s_[3] : c_[3], rot ? c_[3] : -s_[3]};
  }
  const int Qr = 4 * w + p;
  u32 ard = lds_addr(hT) + (Qr / 5) * HT_RS + 2 * (20 * (8 * g + q) + 4 * (Qr % 5));
  u32 awr = lds_addr(U) + 4 * (rho * 17 * 20 + d0 + 2 * g * 20);
  u32 aw0 = g == 0 ? awr + 4 * 16 * 20 : awr;
  float z0r = rot ? 0.f : 1.f, z0i = rot ? 1.f : 0.f;
  pin(tw0); pin(tw1); pin(ard); pin(awr); pin(aw0); pin(z0r); pin(z0i);
  const int P = w >> 1, mp = P, mm = P == 0 ? 4 : 8 - P;
  u32 acr = lds_addr(U) + (w < 8 ? ((2 * P) * 17 + c) * 80 : ((c & 7) * 17 + 16) * 80);
  pin(acr);
  STAMP(1, 1);
  WAITALL();
  STAMP(1, 5);
  __syncthreads();
  if (w < 5) {
    float na[8], nb[8];
    ln_bcast8(part, lane, na, nb);
    STAMP(1, 2);
    f32x2 h[8];
#pragma unroll
    for (int r = 0; r < 8; ++r) { f32x2 v = {bf_lo(xv[r]), bf_hi(xv[r])}; h[r] = (v * na[r] + nb[r]) * gg + sa; }
    const float cc = 0.70710678118654752f;
    f32x2 a0 = h[0] + h[4], a1 = h[1] + h[5], a2 = h[2] + h[6], a3 = h[3] + h[7];
    f32x2 b0 = h[0] - h[4], b1 = h[1] - h[5], b2 = h[2] - h[6], b3 = h[3] - h[7];
    f32x2 e = a0 + a2, f = a1 + a3, u = cc * (b1 - b3), v = cc * (b1 + b3);
    f32x2 o[8] = {e + f, e - f, b0 + u, -b2 - v, a0 - a2, a3 - a1, b0 - u, b2 - v};
#pragma unroll
    for (int r = 0; r < 8; ++r) *(u32*)(hT + r * HT_RS + 4 * tid) = pk_bf(o[r][0], o[r][1]);
  }
  __syncthreads();
  STAMP(1, 3);
  {
    const LDSP(const char) base = (LDSP(const char))(size_t)ard;
    s16x8 b = cat8(__builtin_amdgcn_ds_read_tr16_b64_v4i16((LDSP(s16x4))base), __builtin_amdgcn_ds_read_tr16_b64_v4i16((LDSP(s16x4))(base + 160)));
    const s16x8 fa0 = *(const s16x8*)(tab + lane * 16), fa1 = *(const s16x8*)(tab + 1024 + lane * 16);
    const f32x4 z = {0.f, 0.f, 0.f, 0.f};
    f32x4 a0 = mfma16(fa0, b, z), a1 = mfma16(fa1, b, z);
    LDSP(u32) Ub = (LDSP(u32))(size_t)awr;
    const float x0 = g == 0 ? a0[1] : a0[0], x1 = g == 0 ? 0.f : a0[1];
    *(LDSP(u32))(size_t)aw0 = pk_bf(x0 * tw0[0] - x1 * tw0[1], x0 * tw0[1] + x1 * tw0[0]);
    if (g == 0) Ub[0] = pk_bf(a0[0] * z0r, a0[0] * z0i);
    Ub[20] = pk_bf(a0[2] * tw0[2] - a0[3] * tw0[3], a0[2] * tw0[3] + a0[3] * tw0[2]);
    Ub[8 * 20] = pk_bf(a1[0] * tw1[0] - a1[1] * tw1[1], a1[0] * tw1[1] + a1[1] * tw1[0]);
    Ub[9 * 20] = pk_bf(a1[2] * tw1[2] - a1[3] * tw1[3], a1[2] * tw1[3] + a1[3] * tw1[2]);
  }
  __syncthreads();
  STAMP(1, 4);
  const f32x4 z4 = {0.f, 0.f, 0.f, 0.f};
  u32* XTb = XT + (size_t)s0 * 16;
  const char* ft = tab + 2048 + lane * 16;
  const LDSP(const char) be = (LDSP(const char))(size_t)acr;
  if (w < 8) {
    const s16x8 e0 = *(const LDSP(s16x8))(be + 16 * g), e1 = *(const LDSP(s16x8))(be + 64), o0 = *(const LDSP(s16x8))(be + 1360 + 16 * g), o1 = *(const LDSP(s16x8))(be + 1360 + 64);
    u32* dp = XTb + (size_t)(mp * 21 + 2 * g) * 4096 + c; u32* dm = XTb + (size_t)(mm * 21 + 2 * g) * 4096 + c;
    if (!(w & 1)) {
#pragma unroll
      for (int mt = 0; mt < 2; ++mt) {
        const s16x8 f0 = *(const s16x8*)(ft + (2 * mt) * 1024), f1 = *(const s16x8*)(ft + (2 * mt + 1) * 1024);
        f32x4 ae = mfma16(f1, e1, mfma16(f0, e0, z4)), ao = mfma16(f1, o1, mfma16(f0, o0, z4));
        f32x4 tp = ae + ao, tm = ae - ao;
        if (P == 0) { tp = ae; tm = ao; }
        u32* ep = dp + (size_t)(8 * mt) * 4096; u32* em = dm + (size_t)(8 * mt) * 4096;
        ep[0] = pk_bf(tp[0], tp[1]); ep[4096] = pk_bf(tp[2], tp[3]);
        em[0] = pk_bf(tm[0], tm[1]); em[4096] = pk_bf(tm[2], tm[3]);
      }
    } else {
      const s16x8 f0 = *(const s16x8*)(ft + 4 * 1024), f1 = *(const s16x8*)(ft + 5 * 1024);
      f32x4 ae = mfma16(f1, e1, mfma16(f0, e0, z4)), ao = mfma16(f1, o1, mfma16(f0, o0, z4));
      f32x4 tp = ae + ao, tm = ae - ao;
      if (P == 0) { tp = ae; tm = ao; }
      if (g < 2) {
        u32* ep = dp + (size_t)16 * 4096; u32* em = dm + (size_t)16 * 4096;
        ep[0] = pk_bf(tp[0], tp[1]); ep[4096] = pk_bf(tp[2], tp[3]);
        em[0] = pk_bf(tm[0], tm[1]); em[4096] = pk_bf(tm[2], tm[3]);
      }
    }
  } else {
    const int rh = c & 7, mt = w - 8;
    const s16x8 b0 = *(const LDSP(s16x8))(be + 16 * g), b1 = *(const LDSP(s16x8))(be + 64);
    const s16x8 f0 = *(const s16x8*)(ft + (2 * mt) * 1024), f1 = *(const s16x8*)(ft + (2 * mt + 1) * 1024);
    f32x4 a = mfma16(f1, b1, mfma16(f0, b0, z4));
    const float ca_ = (rh >= 2 && (rh & 1)) ? -1.f : 1.f, cb_ = rh >= 2 ? 1.f : 0.f;
    f32x4 t;
#pragma unroll
    for (int r = 0; r < 4; ++r) t[r] = ca_ * a[r] + cb_ * dpp_x1(a[r]);
    const int m1 = rh == 0 ? 0 : rh == 1 ? 4 : (rh & 1) ? 8 - (rh >> 1) : (rh >> 1);
    const int k0 = 8 * mt + 2 * g;
    u32* d = XTb + (size_t)(m1 * 21 + 20) * 4096;
    if (c < 8 && k0 < 10) *(u32x2*)(d + k0) = u32x2{pk_bf(t[0], t[1]), pk_bf(t[2], t[3])};
  }
  if (w == 5 || w == 6) asm volatile("s_waitcnt vmcnt(0)" ::: "memory");
  STAMP(1, 7);
}

DI void prep_w(int task, const u16* __restrict__ wr, const u16* __restrict__ wi, u32x4* __restrict__ Wp) {
  const int t = threadIdx.x, blk = task >> 1, w = 4 * (task & 1) + (t >> 6), lane = t & 63, c = lane & 15, g = lane >> 4;
  const int m1 = blk & 7, tile = blk >> 3;
  const int k = tile < 20 ? c + 32 * tile : 16 + 32 * c;
  const bool valid = tile < 20 || c < 10, mir = k > 320;
  const int kk = valid ? (mir ? 640 - k : k) : 0;
  u16 a[2][4], b[2][4];
#pragma unroll
  for (int j = 0; j < 2; ++j)
#pragma unroll
    for (int r = 0; r < 4; ++r) {
      const int m = m1 + 8 * (2 * w + j) + 128 * (4 * g + r), mm = mir ? ((2048 - m) & 2047) : m;
      a[j][r] = wr[mm * DF + kk]; b[j][r] = wi[mm * DF + kk];
    }
#pragma unroll
  for (int j = 0; j < 2; ++j) {
    const u32 sg = mir ? 0x80008000u : 0u, ok = valid ? 0xffffffffu : 0u;
    u32x4 v = {(a[j][0] | (u32)a[j][1] << 16) & ok, (a[j][2] | (u32)a[j][3] << 16) & ok, ((b[j][0] | (u32)b[j][1] << 16) ^ sg) & ok, ((b[j][2] | (u32)b[j][3] << 16) ^ sg) & ok};
    Wp[((size_t)(blk * 8 + w) * 2 + j) * 64 + lane] = v;
  }
}

constexpr int EX_RS = 20;
__global__ __launch_bounds__(512) void k3_mid(const u32* __restrict__ XT, const u32x4* __restrict__ Wp, u32* __restrict__ ZT) {
  STAMP_DECL; STAMP(2, 0);
  __shared__ __attribute__((aligned(16))) u32 ex[2][256 * EX_RS];
  __shared__ __attribute__((aligned(16))) char tab[8 * 1024];
  const int tid = threadIdx.x, lane = tid & 63, w = __builtin_amdgcn_readfirstlane(tid >> 6), c = lane & 15, g = lane >> 4;
  const int m1 = blockIdx.x & 7, tile = blockIdx.x >> 3;
  const F8w* tsrc = w < 2 ? &g_a1[m1] : w < 4 ? &g_f16f : w < 6 ? &g_f16i : &g_a1i[m1];
  const s16x8 tfrag = *(const s16x8*)tsrc->v[w & 1][lane];
  const u32* in = XT + (size_t)(m1 * 21 + tile) * 4096 + (64 * g + 2 * w) * 16 + c;
  u32x4 bx[2];
#pragma unroll
  for (int j = 0; j < 2; ++j)
#pragma unroll
    for (int i = 0; i < 4; ++i) bx[j][i] = in[(16 * i + j) * 16];
  u32x4 wv[2];
#pragma unroll
  for (int j = 0; j < 2; ++j) wv[j] = Wp[((size_t)(blockIdx.x * 8 + w) * 2 + j) * 64 + lane];
  *(s16x8*)(tab + w * 1024 + lane * 16) = tfrag;
  f32x4 fcs[2], fsn[2], ics[2], isn[2];
#pragma unroll
  for (int j = 0; j < 2; ++j)
#pragma unroll
    for (int r = 0; r < 4; ++r) {
      float s_, c_;
      sincos_rev((float)((2 * w + j) * (m1 + 8 * (4 * g + r))) * (1.0f / 2048), s_, c_); fcs[j][r] = c_; fsn[j][r] = s_;
      sincos_rev((float)((4 * g + r) * (m1 + 8 * (2 * w + j))) * (1.0f / 2048), s_, c_); ics[j][r] = c_; isn[j][r] = s_;
    }
  u32 awr = lds_addr(ex) + 4 * ((4 * g * 16 + c) * EX_RS + 2 * w);
  u32 ard = lds_addr(ex) + 4 * ((2 * w * 16 + c) * EX_RS + 4 * g);
#pragma unroll
  for (int j = 0; j < 2; ++j) { pin(fcs[j]); pin(fsn[j]); pin(ics[j]); pin(isn[j]); }
  pin(awr); pin(ard);
  STAMP(2, 1);
  WAITALL();
  __syncthreads();
  STAMP(2, 2);
  const f32x4 z4 = {0.f, 0.f, 0.f, 0.f};
  const char* tb = tab + lane * 16;
  const LDSP(char) wr0 = (LDSP(char))(size_t)awr; const LDSP(const char) rd0 = (LDSP(const char))(size_t)ard;
  constexpr int RB = 16 * EX_RS * 4, XB = 256 * EX_RS * 4;
  {
    const s16x8 A10 = *(const s16x8*)tb, A11 = *(const s16x8*)(tb + 1024);
    u32 pk[2][4];
#pragma unroll
    for (int j = 0; j < 2; ++j) {
      f32x4 re = mfma16(A10, as8(bx[j]), z4), im = mfma16(A11, as8(bx[j]), z4);
      f32x4 tr = re * fcs[j] + im * fsn[j], ti = im * fcs[j] - re * fsn[j];
#pragma unroll
      for (int r = 0; r < 4; ++r) pk[j][r] = pk_bf(tr[r], ti[r]);
    }
#pragma unroll
    for (int r = 0; r < 4; ++r) *(LDSP(u32x2))(wr0 + r * RB) = u32x2{pk[0][r], pk[1][r]};
  }
  __syncthreads();
  STAMP(2, 3);
  {
    const s16x8 F0 = *(const s16x8*)(tb + 2048), F1 = *(const s16x8*)(tb + 3072), G0 = *(const s16x8*)(tb + 4096), G1 = *(const s16x8*)(tb + 5120);
    u32 pk[2][4];
#pragma unroll
    for (int j = 0; j < 2; ++j) {
      const u32x4 b = *(const LDSP(u32x4))(rd0 + j * RB);
      f32x4 xr = mfma16(F0, as8(b), z4), xi = mfma16(F1, as8(b), z4);
      const f32x4 a = {bf_lo(wv[j][0]), bf_hi(wv[j][0]), bf_lo(wv[j][1]), bf_hi(wv[j][1])};
      const f32x4 bb = {bf_lo(wv[j][2]), bf_hi(wv[j][2]), bf_lo(wv[j][3]), bf_hi(wv[j][3])};
      const f32x4 yr = xr * a - xi * bb, yi = xr * bb + xi * a;
      const u32x4 yb = {pk_bf(yr[0], yr[1]), pk_bf(yr[2], yr[3]), pk_bf(yi[0], yi[1]), pk_bf(yi[2], yi[3])};
      f32x4 re = mfma16(G0, as8(yb), z4), im = mfma16(G1, as8(yb), z4);
      f32x4 tr = re * ics[j] - im * isn[j], ti = im * ics[j] + re * isn[j];
#pragma unroll
      for (int r = 0; r < 4; ++r) pk[j][r] = pk_bf(tr[r], ti[r]);
    }
#pragma unroll
    for (int r = 0; r < 4; ++r) *(LDSP(u32x2))(wr0 + XB + r * RB) = u32x2{pk[0][r], pk[1][r]};
  }
  __syncthreads();
  STAMP(2, 4);
  u32* out = ZT + (size_t)(m1 * 21 + tile) * 4096 + (64 * g + 2 * w) * 16 + c;
  const s16x8 I0 = *(const s16x8*)(tb + 6144), I1 = *(const s16x8*)(tb + 7168);
#pragma unroll
  for (int j = 0; j < 2; ++j) {
    const u32x4 b = *(const LDSP(u32x4))(rd0 + XB + j * RB);
    f32x4 zr = mfma16(I0, as8(b), z4), zi = mfma16(I1, as8(b), z4);
#pragma unroll
    for (int r = 0; r < 4; ++r) out[(16 * r + j) * 16] = pk_bf(zr[r], zi[r]);
  }
  STAMP(2, 7);
}

constexpr int Y_RS = 640 + 4;
__global__ __launch_bounds__(640) void k4_inv(const u32* __restrict__ ZT, const u16* __restrict__ x, const float* __restrict__ mod,
                                              u16* __restrict__ x1o, u16* __restrict__ h2o, const u16* __restrict__ Wgu) {
  STAMP_DECL; STAMP(3, 0);
  __shared__ __attribute__((aligned(16))) char smem[162 * 64 + 8 * Y_RS * 4 + 10 * 8 * 4 + 8 * 1024 + 256];
  char* Vs = smem; float* Y = (float*)(Vs + 162 * 64); float* part = Y + 8 * Y_RS; char* tab = (char*)(part + 80);
  const int tid = threadIdx.x, lane = tid & 63, w = __builtin_amdgcn_readfirstlane(tid >> 6), c = lane & 15, g = lane >> 4;
  const int s0 = row_group(blockIdx.x);
  const u32* Zb = ZT + (size_t)s0 * 16;
  const s16x8 tfrag = w < 6 ? *(const s16x8*)&g_f20ai.v[0][0][0][w * 512 + lane * 8] : *(const s16x8*)g_f32bi.v[(w - 6) & 1][lane];
  u32 za[8], zb[8];
  const int rho = w < 8 ? w : (c & 7), P = rho >> 1, mA = rho == 1 ? 4 : P, mB = P == 0 ? 4 : 8 - P;
  if (w < 8) {
    const u32* pa = Zb + (size_t)(mA * 21) * 4096 + c; const u32* pb = Zb + (size_t)(mB * 21) * 4096 + c;
#pragma unroll
    for (int i = 0; i < 4; ++i) { za[i] = pa[(size_t)(4 * g + i) * 4096]; za[4 + i] = pa[(size_t)(16 + i) * 4096]; }
    if (w >= 2) {
#pragma unroll
      for (int i = 0; i < 4; ++i) { zb[i] = pb[(size_t)(4 * g + i) * 4096]; zb[4 + i] = pb[(size_t)(16 + i) * 4096]; }
    }
  } else if (w == 8) {
    const int gq = g < 3 ? g : 0;
    const u32x4 a = *(const u32x4*)(Zb + (size_t)(mA * 21 + 20) * 4096 + 4 * gq), b = *(const u32x4*)(Zb + (size_t)(mB * 21 + 20) * 4096 + 4 * gq);
#pragma unroll
    for (int i = 0; i < 4; ++i) { const bool ok = 4 * g + i < 10; za[i] = ok ? a[i] : 0u; zb[i] = ok ? b[i] : 0u; za[4 + i] = 0u; zb[4 + i] = 0u; }
  }
  const int hf = w >= 5, cp = tid - 320 * hf;
  u32 xv[4];
#pragma unroll
  for (int r = 0; r < 4; ++r) xv[r] = *(const u32*)(x + (size_t)(s0 + 256 * (4 * hf + r)) * D_ + 2 * cp);
  const f32x2 ga = *(const f32x2*)(mod + 2 * 640 + 2 * cp), sb = *(const f32x2*)(mod + 3 * 640 + 2 * cp), cb = *(const f32x2*)(mod + 4 * 640 + 2 * cp);
  if (w < 8) *(s16x8*)(tab + w * 1024 + lane * 16) = tfrag;
  f32x4 tw[3]; u32 aw[3];
  {
    const bool rot = rho >= 3 && (rho & 1);
    const float sc = w < 8 ? 1.0f / (2048.0f * 640.0f) : 2.0f / (2048.0f * 640.0f), fk = (float)(w < 8 ? c : 16) * (1.0f / 640.0f);
#pragma unroll
    for (int mt = 0; mt < 3; ++mt) {
#pragma unroll
      for (int rr = 0; rr < 2; ++rr) {
        float s_, c_; sincos_rev(fk * (float)(8 * mt + 2 * g + rr), s_, c_);
        tw[mt][2 * rr] = (rot ? -s_ : c_) * sc; tw[mt][2 * rr + 1] = (rot ? c_ : s_) * sc;
      }
      const int d0 = 8 * mt + 2 * g;
      const bool ok = d0 < 20 && (w < 8 || c < 8);
      aw[mt] = lds_addr(Vs) + (ok ? (rho * 20 + d0) * 64 : 160 * 64) + (w < 8 ? 4 * c : 2);
      pin(tw[mt]); pin(aw[mt]);
    }
  }
  STAMP(3, 1);
  WAITALL();
  u32x4 b0, b1;
  if (w < 2) {
#pragma unroll
    for (int i = 0; i < 4; ++i) { b0[i] = za[i]; b1[i] = za[4 + i]; }
  } else {
    const float be = rho < 2 ? 0.f : (rho & 1) ? -1.f : 1.f;
#pragma unroll
    for (int i = 0; i < 4; ++i) {
      b0[i] = pk_bf(bf_lo(za[i]) + be * bf_lo(zb[i]), bf_hi(za[i]) + be * bf_hi(zb[i]));
      b1[i] = pk_bf(bf_lo(za[4 + i]) + be * bf_lo(zb[4 + i]), bf_hi(za[4 + i]) + be * bf_hi(zb[4 + i]));
    }
  }
  __syncthreads();
  STAMP(3, 2);
  if (w < 9) {
    f32x4 a[3];
#pragma unroll
    for (int mt = 0; mt < 3; ++mt) {
      const f32x4 z = {0.f, 0.f, 0.f, 0.f};
      const s16x8 f0 = *(const s16x8*)(tab + (2 * mt) * 1024 + lane * 16), f1 = *(const s16x8*)(tab + (2 * mt + 1) * 1024 + lane * 16);
      a[mt] = mfma16(f1, as8(b1), mfma16(f0, as8(b0), z));
    }
    if (w < 8) {
      const u32 hi_skip = c == 0 ? 1u : 0u;
#pragma unroll
      for (int mt = 0; mt < 3; ++mt)
#pragma unroll
        for (int rr = 0; rr < 2; ++rr) {
          const u32 pv = pk_bf(a[mt][2 * rr] * tw[mt][2 * rr] - a[mt][2 * rr + 1] * tw[mt][2 * rr + 1], a[mt][2 * rr] * tw[mt][2 * rr + 1] + a[mt][2 * rr + 1] * tw[mt][2 * rr]);
          LDSP(char) dst = (LDSP(char))(size_t)aw[mt] + 64 * rr;
          if (hi_skip) *(LDSP(u16))dst = (u16)pv; else *(LDSP(u32))dst = pv;
        }
    } else {
#pragma unroll
      for (int mt = 0; mt < 3; ++mt)
#pragma unroll
        for (int rr = 0; rr < 2; ++rr)
          *(LDSP(u16))((LDSP(char))(size_t)aw[mt] + 64 * rr) = (u16)pk_bf(a[mt][2 * rr] * tw[mt][2 * rr] - a[mt][2 * rr + 1] * tw[mt][2 * rr + 1], 0.f);
    }
  }
  __syncthreads();
  STAMP(3, 3);
  {
    const s16x8 a = *(const s16x8*)(Vs + (16 * w + c) * 64 + 16 * g);
    const s16x8 fb0 = *(const s16x8*)(tab + 6144 + lane * 16), fb1 = *(const s16x8*)(tab + 7168 + lane * 16);
    const f32x4 z = {0.f, 0.f, 0.f, 0.f};
    const f32x4 y0 = mfma16(a, fb0, z), y1 = mfma16(a, fb1, z);
    const int R = 16 * w + 4 * g, rr = R / 20, d0 = R % 20;
    *(f32x4*)(Y + rr * Y_RS + 20 * c + d0) = y0;
    *(f32x4*)(Y + rr * Y_RS + 20 * (16 + c) + d0) = y1;
  }
  __syncthreads();
  STAMP(3, 4);
  {
    f32x2 R[8];
#pragma unroll
    for (int r = 0; r < 8; ++r) R[r] = *(const f32x2*)(Y + r * Y_RS + 2 * cp);
    const float cc = 0.70710678118654752f;
    const f32x2 G = R[0] + R[1], Fm = R[0] - R[1];
    const f32x2 u = cc * (R[2] - R[6]), v = cc * (R[3] + R[7]);
    const f32x2 c13 = R[2] + R[6], s13 = R[3] - R[7];
    const float sg = hf ? -1.f : 1.f;
    f32x2 y[4];
    y[0] = G + R[4] + sg * c13;
    y[2] = G - R[4] + sg * s13;
    y[1] = Fm + R[5] + sg * (u + v);
    y[3] = Fm - R[5] + sg * (v - u);
    u32 x1[4];
    float sv[8];
    u16* x1d = x1o + (size_t)(8 * s0 + 4 * hf) * D_ + 2 * cp;
#pragma unroll
    for (int r = 0; r < 4; ++r) {
      const f32x2 xx = {bf_lo(xv[r]), bf_hi(xv[r])};
      const f32x2 t = xx + ga * y[r];
      x1[r] = pk_bf(t[0], t[1]);
      *(u32*)(x1d + r * D_) = x1[r];
      sv[r] = dot2bf(x1[r], 0x3f803f80u, 0.f); sv[4 + r] = dot2bf(x1[r], x1[r], 0.f);
    }
    const float tot = reduce8(sv, lane);
    if (lane < 8) part[w * 8 + lane] = tot;
    const f32x2 gg = {rbf(1.0f + cb[0]), rbf(1.0f + cb[1])};
    const int d = 2 * cp, wi = d & 31;
    u16* hd = h2o + ((((size_t)(s0 >> 1) * 20 + (d >> 5)) * 4 + ((wi & 15) >> 2)) * 16 + 8 * (s0 & 1) + 4 * hf) * 8 + 4 * (wi >> 4) + (wi & 3);
    __syncthreads();
    STAMP(3, 5);
    const int r = lane & 3;
    float s = 0.f, qq = 0.f;
#pragma unroll
    for (int i = 0; i < 5; ++i) { s += part[(5 * hf + i) * 8 + r]; qq += part[(5 * hf + i) * 8 + 4 + r]; }
    float m_, i_; ln_finish(s, qq, m_, i_);
    const float nb_ = -m_ * i_;
#pragma unroll
    for (int i = 0; i < 4; ++i) {
      const float na = __int_as_float(__builtin_amdgcn_readlane(__float_as_int(i_), i)), nb = __int_as_float(__builtin_amdgcn_readlane(__float_as_int(nb_), i));
      const f32x2 v2 = {bf_lo(x1[i]), bf_hi(x1[i])};
      const f32x2 hv = (v2 * na + nb) * gg + sb;
      *(u32*)(hd + i * 8) = pk_bf(hv[0], hv[1]);
    }
  }
  if (w == 9) asm volatile("s_waitcnt vmcnt(0)" ::: "memory");
  STAMP(3, 7);
}

constexpr int K5_SLOT = 28 * 1024;
constexpr int K5_NS = 4;
constexpr int K5_PD = 4;
template <int N> DI void wait_vm() { asm volatile("s_waitcnt vmcnt(%0)" ::"n"(N) : "memory"); }
template <int OFF> DI void gload16_s(u32x4& d, const void* sbase, u32 voff) {
  asm volatile("global_load_dwordx4 %0, %1, %2 offset:%3" : "=v"(d) : "v"(voff), "s"(sbase), "n"(OFF) : "memory");
}
template <int OFF> DI void glds16_s(const void* sbase, u32 voff) {
  asm volatile("global_load_lds_dwordx4 %0, %1 offset:%2" :: "v"(voff), "s"(sbase), "n"(OFF) : "memory");
}
DI void set_m0(u32 v) { asm volatile("s_mov_b32 m0, %0\n\ts_nop 0" :: "s"(v) : "memory"); }
DI void tie4(u32x4 (&t)[4]) { asm volatile("" : "+v"(t[0]), "+v"(t[1]), "+v"(t[2]), "+v"(t[3])); }
struct K5Ctx { const u16* wdp; int pfl; u32 dump; const u16* wsrc; const u16* hsrc; u32 ldsw; u32 goff[7]; u32 hoff; u32 abase[2]; };
template <int s, int i> DI void k5_w1(const K5Ctx& c) {
  if constexpr (i == 0) set_m0(c.ldsw + (s % K5_NS) * K5_SLOT + 3072);
  glds16_s<1024 * i - 3072>((const char*)(c.wsrc + s * 4 * 512) - 4096, c.goff[i]);
}
template <int s, int i> DI void k5_t1(const K5Ctx& c, u32x4 (&t)[4]) {
  gload16_s<(i >> 1) * 1024>(t[i], c.hsrc + ((i & 1) * 20 + 2 * s) * 512, c.hoff);
}
template <int s> DI void k5_wall(const K5Ctx& c) { k5_w1<s, 0>(c); k5_w1<s, 1>(c); k5_w1<s, 2>(c); k5_w1<s, 3>(c); k5_w1<s, 4>(c); k5_w1<s, 5>(c); k5_w1<s, 6>(c); }
template <int s> DI void k5_tall(const K5Ctx& c, u32x4 (&t)[4]) { k5_t1<s, 0>(c, t); k5_t1<s, 1>(c, t); k5_t1<s, 2>(c, t); k5_t1<s, 3>(c, t); }
template <int n> DI s16x8 k5_rd(const K5Ctx& c) {
  constexpr int s = n / 28, t = (n / 14) & 1, j = n % 14, sl = s % K5_NS;
  return *(const LDSP(const s16x8))((LDSP(const char))(size_t)c.abase[sl >> 1] + (sl & 1) * K5_SLOT + ((j >> 1) * 4 + t * 2 + (j & 1)) * 1024);
}
template <int n> DI void k5_sub(const K5Ctx& c, f32x4 (&acc)[14][2], s16x8 (&fr)[K5_PD + 1], u32x4 (&tok)[10][4]) {
  constexpr int s = n / 28, t = (n / 14) & 1, j = n % 14;
  constexpr int nb = n + K5_PD;
  if constexpr (nb < 280 && nb % 28 == 0) {
    wait_vm<(s + 2 <= 9) ? 11 : 0>();
    __builtin_amdgcn_s_barrier();
    SLAB(s + 1);
    tie4(tok[s + 1]);
  }
  if constexpr (nb < 280) fr[nb % (K5_PD + 1)] = k5_rd<nb>(c);
  {
    constexpr int q = nb / 28, pos = nb % 28 - (q == 0 ? K5_PD : 0);
    constexpr int idx = (pos % 2 == 0 && pos < 22) ? pos / 2 : -1;
    if constexpr (idx >= 0 && idx < 4 && q + 2 <= 9) k5_t1<q + 2, idx>(c, tok[q + 2]);
    if constexpr (idx >= 4 && q + 2 <= 9) k5_w1<q + 2, idx - 4>(c);
  }
  if constexpr (false) {
    constexpr int i = (n - 224) / 2;
    l2_touch(c.wdp, (u32)(c.pfl + i * 64) * 128u, c.dump);
  }
  const s16x8 a = fr[n % (K5_PD + 1)];
  acc[j][0] = mfma16(a, as8(tok[s][2 * t]), acc[j][0]);
  acc[j][1] = mfma16(a, as8(tok[s][2 * t + 1]), acc[j][1]);
  __builtin_amdgcn_sched_barrier(0);
}
template <int... I> DI void k5_all(const K5Ctx& c, f32x4 (&acc)[14][2], s16x8 (&fr)[K5_PD + 1], u32x4 (&tok)[10][4], std::integer_sequence<int, I...>) { (k5_sub<I>(c, acc, fr, tok), ...); }
__global__ __launch_bounds__(256, 2) void k5_glu(const u16* __restrict__ h2, const u16* __restrict__ Wgu, u16* __restrict__ hb, const u16* __restrict__ Wd) {
  STAMP_DECL; STAMP(4, 0);
  extern __shared__ __attribute__((aligned(16))) char smem[];
  const int tid = threadIdx.x, lane = tid & 63, w = __builtin_amdgcn_readfirstlane(tid >> 6);
  const int xcd = blockIdx.x & 7, li = blockIdx.x >> 3;
  const int tt = 8 * (xcd & 1) + (li & 7), fc = 4 * (xcd >> 1) + (li >> 3);
  const int u0 = fc < 12 ? 7 * fc : 84 + 6 * (fc - 12), U = fc < 12 ? 7 : 6;
  K5Ctx c;
  c.wsrc = Wgu + (size_t)u0 * 20 * 2 * 512;
  c.dump = lds_addr(smem) + 2 * K5_SLOT + w * 256;
  c.wdp = Wd + (size_t)(20 * (xcd >> 2)) * 54 * 512; c.pfl = min(li * 270 + w * 68 + min(lane, 63), 8640 - 65);
#pragma unroll
  for (int i = 0; i < 7; ++i) { const int f = 7 * w + i, u = min(f >> 2, U - 1); c.goff[i] = 2 * (u * 20 * 2 * 512 + (f & 3) * 512) + lane * 16 + 4096 - (1024 * i - 3072); }
  c.ldsw = lds_addr(smem) + 7 * w * 1024;
  const int T16 = 8 * tt + 2 * w;
  c.hsrc = h2 + (size_t)T16 * 20 * 512; c.hoff = lane * 16;
#pragma unroll
  for (int j = 0; j < 2; ++j) { c.abase[j] = lds_addr(smem) + 2 * j * K5_SLOT + lane * 16; asm volatile("" : "+v"(c.abase[j])); }
  u32x4 tok[10][4];
  k5_tall<0>(c, tok[0]); k5_wall<0>(c);
  k5_tall<1>(c, tok[1]); k5_wall<1>(c);
  f32x4 acc[14][2];
#pragma unroll
  for (int u = 0; u < 14; ++u) { acc[u][0] = f32x4{0.f, 0.f, 0.f, 0.f}; acc[u][1] = f32x4{0.f, 0.f, 0.f, 0.f}; }
  STAMP(4, 3);
  wait_vm<11>();
  __builtin_amdgcn_s_barrier();
  tie4(tok[0]);
  STAMP(4, 4); SLAB(0);
  s16x8 fr[K5_PD + 1];
  fr[0] = k5_rd<0>(c); fr[1] = k5_rd<1>(c); fr[2] = k5_rd<2>(c); fr[3] = k5_rd<3>(c);
  __builtin_amdgcn_sched_barrier(0);
  k5_all(c, acc, fr, tok, std::make_integer_sequence<int, 280>{});
  STAMP(4, 5); SLAB(10);
#pragma unroll
  for (int tl = 0; tl < 2; ++tl) {
    u16* hdst = hb + ((size_t)(T16 + tl) * 54 * 64 + lane) * 8;
#pragma unroll
    for (int u = 0; u < 7; ++u) {
      if (u < U) {
        const f32x4 a = acc[2 * u][tl], b = acc[2 * u + 1][tl];
        const int un = u0 + u;
        *(u32x2*)(hdst + (un >> 1) * 512 + (un & 1) * 4) = u32x2{pk_bf(a[0] * b[0], a[1] * b[1]), pk_bf(a[2] * b[2], a[3] * b[3])};
      }
    }
  }
  STAMP(4, 7);
}

constexpr int K6_NS = 4;
struct K6Ctx { const u16* wsrc; const u16* hsrc; u32 hoff; };
struct K6Grp { u32x4 a[5]; u32x4 t[4]; };
template <int i> DI void k6_v1(const K6Ctx& c, int ks, K6Grp& gr) {
  if constexpr (i < 5) gload16_s<0>(gr.a[i], c.wsrc + (ks + i * 54) * 512, c.hoff);
  else gload16_s<0>(gr.t[i - 5], c.hsrc + (ks + (i - 5) * 54) * 512, c.hoff);
}
DI void k6_issue(const K6Ctx& c, int ks, K6Grp& gr) {
  k6_v1<0>(c, ks, gr); k6_v1<5>(c, ks, gr); k6_v1<6>(c, ks, gr); k6_v1<7>(c, ks, gr); k6_v1<8>(c, ks, gr);
  k6_v1<1>(c, ks, gr); k6_v1<2>(c, ks, gr); k6_v1<3>(c, ks, gr); k6_v1<4>(c, ks, gr);
}
DI void k6_tie(K6Grp& g) { asm volatile("" : "+v"(g.a[0]), "+v"(g.a[1]), "+v"(g.a[2]), "+v"(g.a[3]), "+v"(g.a[4]), "+v"(g.t[0]), "+v"(g.t[1]), "+v"(g.t[2]), "+v"(g.t[3])); }
template <int i, int NK> DI void k6_step(const K6Ctx& c, int ks0, f32x4 (&acc)[5][4], K6Grp (&gr)[K6_NS]) {
  constexpr int later = (NK - 1 - i) < 2 ? (NK - 1 - i) : 2;
  wait_vm<9 * later>();
  K6Grp& g = gr[i % K6_NS];
  k6_tie(g);
  K6Grp& nx = gr[(i + 3) % K6_NS];
#pragma unroll
  for (int nt = 0; nt < 5; ++nt) {
    if constexpr (i + 3 < NK) {
      if (nt == 0) { k6_v1<0>(c, ks0 + i + 3, nx); k6_v1<5>(c, ks0 + i + 3, nx); }
      if (nt == 1) { k6_v1<6>(c, ks0 + i + 3, nx); k6_v1<7>(c, ks0 + i + 3, nx); }
      if (nt == 2) { k6_v1<8>(c, ks0 + i + 3, nx); k6_v1<1>(c, ks0 + i + 3, nx); }
      if (nt == 3) { k6_v1<2>(c, ks0 + i + 3, nx); k6_v1<3>(c, ks0 + i + 3, nx); }
      if (nt == 4) { k6_v1<4>(c, ks0 + i + 3, nx); }
    }
#pragma unroll
    for (int t = 0; t < 4; ++t) acc[nt][t] = mfma16(as8(g.a[nt]), as8(g.t[t]), acc[nt][t]);
    __builtin_amdgcn_sched_barrier(0);
  }
}
template <int i, int NK> struct K6Loop { static DI void run(const K6Ctx& c, int ks0, f32x4 (&acc)[5][4], K6Grp (&gr)[K6_NS]) { k6_step<i, NK>(c, ks0, acc, gr); K6Loop<i + 1, NK>::run(c, ks0, acc, gr); } };
template <int NK> struct K6Loop<NK, NK> { static DI void run(const K6Ctx&, int, f32x4 (&)[5][4], K6Grp (&)[K6_NS]) {} };
template <int NK> DI void k6_main(const K6Ctx& cx, int ks0, f32x4 (&acc)[5][4]) {
  K6Grp gr[K6_NS];
  k6_issue(cx, ks0, gr[0]); k6_issue(cx, ks0 + 1, gr[1]); k6_issue(cx, ks0 + 2, gr[2]);
  WSTAMP(0);
  K6Loop<0, NK>::run(cx, ks0, acc, gr);
  WSTAMP(1);
}
__global__ __launch_bounds__(256, 2) void k6_down(const u16* __restrict__ hb, const u16* __restrict__ Wd, const u16* __restrict__ x1, const float* __restrict__ mod, u16* __restrict__ out) {
  STAMP_DECL; STAMP(5, 0);
  extern __shared__ __attribute__((aligned(16))) char smem[];
  const int tid = threadIdx.x, lane = tid & 63, w = __builtin_amdgcn_readfirstlane(tid >> 6), c = lane & 15, g = lane >> 4;
  const int xcd = blockIdx.x & 7, li = blockIdx.x >> 3;
  const int tt = 8 * (xcd & 3) + (li & 7), nb = 4 * (xcd >> 2) + (li >> 3);
  const int n0 = 80 * nb;
  const int ks0 = w < 2 ? 14 * w : 28 + 13 * (w - 2);
  const int tau = 64 * tt + 16 * w + c, srow = (tau >> 3) + 256 * (tau & 7);
  f32x4 gb[5]; u32x2 xr[5];
#pragma unroll
  for (int nt = 0; nt < 5; ++nt) { const int n = n0 + 16 * nt + 4 * g; gb[nt] = *(const f32x4*)(mod + 5 * 640 + n); xr[nt] = *(const u32x2*)(x1 + (size_t)tau * D_ + n); }
  K6Ctx cx;
  cx.wsrc = Wd + (size_t)(5 * nb) * 54 * 512; cx.hsrc = hb + (size_t)(4 * tt) * 54 * 512; cx.hoff = lane * 16;
  f32x4 acc[5][4];
#pragma unroll
  for (int a = 0; a < 5; ++a)
#pragma unroll
    for (int b = 0; b < 4; ++b) acc[a][b] = f32x4{0.f, 0.f, 0.f, 0.f};
  if (w < 2) k6_main<14>(cx, ks0, acc); else k6_main<13>(cx, ks0, acc);
  STAMP(5, 2);
  f32x4* red = (f32x4*)smem;
#pragma unroll
  for (int nt = 0; nt < 5; ++nt)
#pragma unroll
    for (int t = 0; t < 4; ++t) if (t != w) red[((w * 5 + nt) * 4 + t) * 64 + lane] = acc[nt][t];
  __syncthreads();
#pragma unroll
  for (int nt = 0; nt < 5; ++nt) {
    f32x4 v = w == 0 ? acc[nt][0] : w == 1 ? acc[nt][1] : w == 2 ? acc[nt][2] : acc[nt][3];
#pragma unroll
    for (int ww = 1; ww < 4; ++ww) v += red[((((w + ww) & 3) * 5 + nt) * 4 + w) * 64 + lane];
    const int n = n0 + 16 * nt + 4 * g;
    float o0 = bf_lo(xr[nt][0]) + rbf(gb[nt][0] * rbf(v[0])), o1 = bf_hi(xr[nt][0]) + rbf(gb[nt][1] * rbf(v[1]));
    float o2 = bf_lo(xr[nt][1]) + rbf(gb[nt][2] * rbf(v[2])), o3 = bf_hi(xr[nt][1]) + rbf(gb[nt][3] * rbf(v[3]));
    *(u32x2*)(out + (size_t)srow * D_ + n) = u32x2{pk_bf(o0, o1), pk_bf(o2, o3)};
  }
  STAMP(5, 7);
}

constexpr size_t WS_MOD = 0, WS_XT = 16384, WS_ZT = WS_XT + 8 * 21 * 256 * 16 * 4, WS_X1 = WS_ZT + 8 * 21 * 256 * 16 * 4,
                 WS_H2 = WS_X1 + 2048 * 640 * 2, WS_HB = WS_H2 + 2048 * 640 * 2, WS_WGU = WS_HB + 2048 * 1728 * 2, WS_WD = WS_WGU + 2 * 640 * 1728 * 2, WS_WP = WS_WD + 640 * 1728 * 2;
extern "C" void kernel_launch(void* const* d_in, const int* in_sizes, int n_in, void* d_out, int out_size, void* d_ws, size_t ws_size,
                              hipStream_t stream) {
  const u16* x = (const u16*)d_in[0]; const u16* wr = (const u16*)d_in[1]; const u16* wi = (const u16*)d_in[2];
  const u16* wg = (const u16*)d_in[4]; const u16* wu = (const u16*)d_in[5]; const u16* wd = (const u16*)d_in[6];
  const u16* c = (const u16*)d_in[7]; const u16* wmod = (const u16*)d_in[8];
  char* ws = (char*)d_ws;
  float* mod = (float*)(ws + WS_MOD); u32* XT = (u32*)(ws + WS_XT); u32* ZT = (u32*)(ws + WS_ZT);
  u16* x1 = (u16*)(ws + WS_X1); u16* h2 = (u16*)(ws + WS_H2); u16* hb = (u16*)(ws + WS_HB);
  u16* Wgu = (u16*)(ws + WS_WGU); u16* Wd = (u16*)(ws + WS_WD); u32x4* Wp = (u32x4*)(ws + WS_WP);
  k1_mod<<<120 + 336 + 450, 256, 0, stream>>>(c, wmod, mod, wg, wu, wd, Wgu, Wd, wr, wi, Wp);
  k2_fwd<<<256, 640, 0, stream>>>(x, mod, XT, Wp);
  k3_mid<<<168, 512, 0, stream>>>(XT, Wp, ZT);
  k4_inv<<<256, 640, 0, stream>>>(ZT, x, mod, x1, h2, Wgu);
  hipFuncSetAttribute((const void*)k5_glu, hipFuncAttributeMaxDynamicSharedMemorySize, K5_NS * K5_SLOT);
  k5_glu<<<256, 256, K5_NS * K5_SLOT, stream>>>(h2, Wgu, hb, Wd);
  hipFuncSetAttribute((const void*)k6_down, hipFuncAttributeMaxDynamicSharedMemorySize, 81920);
  k6_down<<<256, 256, 81920, stream>>>(hb, Wd, x1, mod, (u16*)d_out);
}
```

```cpp
#include <hip/hip_runtime.h>
#include <hip/hip_bf16.h>
#include <stdint.h>
#include <utility>

typedef unsigned short u16;
typedef unsigned int u32;
typedef short s16x4 __attribute__((ext_vector_type(4)));
typedef short s16x8 __attribute__((ext_vector_type(8)));
typedef __bf16 bf2_t __attribute__((ext_vector_type(2)));
typedef float f32x2 __attribute__((ext_vector_type(2)));
typedef float f32x4 __attribute__((ext_vector_type(4)));
typedef float f32x16 __attribute__((ext_vector_type(16)));
typedef u32 u32x2 __attribute__((ext_vector_type(2)));
typedef u32 u32x4 __attribute__((ext_vector_type(4)));
#define DI __device__ __forceinline__
#define LDSP(T) __attribute__((address_space(3))) T*

constexpr int S_ = 2048, D_ = 640, F_ = 1728, DF = 321;

constexpr double kPi = 3.14159265358979323846264338327950288;
constexpr double c_sin0(double x) { double x2 = x * x, t = x, s = x; for (int i = 1; i < 11; ++i) { t *= -x2 / ((2.0 * i) * (2.0 * i + 1.0)); s += t; } return s; }
constexpr double c_cos0(double x) { double x2 = x * x, t = 1.0, s = 1.0; for (int i = 1; i < 11; ++i) { t *= -x2 / ((2.0 * i - 1.0) * (2.0 * i)); s += t; } return s; }
constexpr double c_cos(long n, long N) {
  n %= N; if (n < 0) n += N;
  if (2 * n > N) n = N - n;
  bool neg = false;
  if (4 * n > N) { neg = true; n = N - 2 * n; N = 2 * N; n = n;   }
  double r = 0;
  if (4 * n == N) r = 0.0;
  else if (8 * n > N) r = c_sin0(2 * kPi * (double)(N - 4 * n) / (double)(4 * N));
  else r = c_cos0(2 * kPi * (double)n / (double)N);
  return neg ? -r : r;
}
constexpr double c_sin(long n, long N) { return c_cos(4 * n - N, 4 * N); }
constexpr u16 c_bf16(double v) {
  float f = (float)v; u32 u = __builtin_bit_cast(u32, f);
  u += 0x7fffu + ((u >> 16) & 1u); return (u16)(u >> 16);
}

struct F8w { u16 v[2][64][8]; };
struct F8x6 { u16 v[3][2][64][8]; };
struct TwA { float v[2][64][4]; };
struct TwC { float v[3][64][4]; };
constexpr F8w mk_f32a() {
  F8w t{};
  for (int tt = 0; tt < 2; ++tt) for (int l = 0; l < 64; ++l) for (int j = 0; j < 8; ++j) {
    int o = 16 * tt + (l & 15), d1 = 8 * (l >> 4) + j; double v = 0;
    if (o == 0) v = 1.0; else if (o == 1) v = (d1 & 1) ? -1.0 : 1.0;
    else { int k1 = o >> 1; v = (o & 1) ? -c_sin(d1 * k1, 32) : c_cos(d1 * k1, 32); }
    t.v[tt][l][j] = c_bf16(v);
  }
  return t;
}
constexpr TwA mk_twa(int j) {
  TwA t{};
  for (int tt = 0; tt < 2; ++tt) for (int l = 0; l < 64; ++l) for (int rr = 0; rr < 2; ++rr) {
    int c = l & 15, g = l >> 4, Q = 4 * j + (c >> 2), d0 = 4 * (Q % 5) + (c & 3), rho = Q / 5;
    int k1 = 8 * tt + 2 * g + rr; if (k1 == 0) k1 = 16;
    double co = c_cos(d0 * k1, 640), si = -c_sin(d0 * k1, 640);
    if (rho >= 3 && (rho & 1)) { double x = co; co = -si; si = x; }
    t.v[tt][l][2 * rr] = (float)co; t.v[tt][l][2 * rr + 1] = (float)si;
  }
  return t;
}
constexpr F8x6 mk_f20(bool inv) {
  F8x6 t{};
  for (int mt = 0; mt < 3; ++mt) for (int ks = 0; ks < 2; ++ks) for (int l = 0; l < 64; ++l) for (int j = 0; j < 8; ++j) {
    int row = l & 15, g = l >> 4, k0 = 8 * mt + (row >> 1), part = row & 1, d0 = 16 * ks + 4 * g + (j >> 1), pp = j & 1; double v = 0;
    if (d0 < 20 && k0 < 20) {
      double co = c_cos(d0 * k0, 20), si = inv ? -c_sin(d0 * k0, 20) : c_sin(d0 * k0, 20);
      v = part == 0 ? (pp == 0 ? co : si) : (pp == 0 ? -si : co);
    }
    t.v[mt][ks][l][j] = c_bf16(v);
  }
  return t;
}
constexpr F8w mk_a1(int m1, bool inv) {
  F8w t{};
  for (int l = 0; l < 64; ++l) for (int i = 0; i < 4; ++i) {
    int c = l & 15, a = 4 * (l >> 4) + i, n = inv ? c * (8 * a + m1) : a * (8 * c + m1);
    double co = c_cos(n, 128), si = inv ? -c_sin(n, 128) : c_sin(n, 128);
    t.v[0][l][2 * i] = c_bf16(co); t.v[0][l][2 * i + 1] = c_bf16(si);
    t.v[1][l][2 * i] = c_bf16(-si); t.v[1][l][2 * i + 1] = c_bf16(co);
  }
  return t;
}
constexpr F8w mk_f16(bool inv) {
  F8w t{};
  for (int l = 0; l < 64; ++l) for (int i = 0; i < 4; ++i) {
    int c = l & 15, a = 4 * (l >> 4) + i;
    double co = c_cos(a * c, 16), si = c_sin(a * c, 16);
    if (!inv) { t.v[0][l][2 * i] = c_bf16(co); t.v[0][l][2 * i + 1] = c_bf16(si); t.v[1][l][2 * i] = c_bf16(-si); t.v[1][l][2 * i + 1] = c_bf16(co); }
    else { t.v[0][l][i] = c_bf16(co); t.v[0][l][4 + i] = c_bf16(-si); t.v[1][l][i] = c_bf16(si); t.v[1][l][4 + i] = c_bf16(co); }
  }
  return t;
}
constexpr TwC mk_twc(int kind) {
  TwC t{};
  const double sc = 1.0 / (2048.0 * 640.0);
  for (int mt = 0; mt < 3; ++mt) for (int l = 0; l < 64; ++l) for (int rr = 0; rr < 2; ++rr) {
    int k1 = kind == 2 ? 16 : (l & 15), g = l >> 4, d0 = 8 * mt + 2 * g + rr, rho = l & 7;
    double co = c_cos(d0 * k1, 640) * sc * (kind == 2 ? 2 : 1), si = c_sin(d0 * k1, 640) * sc * (kind == 2 ? 2 : 1);
    if (kind == 1 || (kind == 2 && rho >= 3 && (rho & 1))) { double x = co; co = -si; si = x; }
    t.v[mt][l][2 * rr] = (float)co; t.v[mt][l][2 * rr + 1] = (float)si;
  }
  return t;
}
constexpr F8w mk_f32bi() {
  F8w t{};
  for (int nt = 0; nt < 2; ++nt) for (int l = 0; l < 64; ++l) for (int j = 0; j < 8; ++j) {
    int d1 = 16 * nt + (l & 15), K = 8 * (l >> 4) + j; double v = 0;
    if (K == 0) v = 1.0; else if (K == 1) v = (d1 & 1) ? -1.0 : 1.0;
    else { int k1 = K >> 1; v = (K & 1) ? -2.0 * c_sin(k1 * d1, 32) : 2.0 * c_cos(k1 * d1, 32); }
    t.v[nt][l][j] = c_bf16(v);
  }
  return t;
}
static __device__ const F8w g_f32a = mk_f32a();
static __device__ const F8x6 g_f20a = mk_f20(false);
static __device__ const F8x6 g_f20ai = mk_f20(true);
static __device__ const F8w g_a1[8] = {mk_a1(0, false), mk_a1(1, false), mk_a1(2, false), mk_a1(3, false), mk_a1(4, false), mk_a1(5, false), mk_a1(6, false), mk_a1(7, false)};
static __device__ const F8w g_a1i[8] = {mk_a1(0, true), mk_a1(1, true), mk_a1(2, true), mk_a1(3, true), mk_a1(4, true), mk_a1(5, true), mk_a1(6, true), mk_a1(7, true)};
static __device__ const F8w g_f16f = mk_f16(false);
static __device__ const F8w g_f16i = mk_f16(true);
static __device__ const F8w g_f32bi = mk_f32bi();

#define STAMPS 1
__device__ unsigned long long g_stamps[8][1024][8];
__device__ unsigned long long g_cyc[8][1024][8];
__device__ unsigned long long g_slab[1024][16];
__device__ unsigned g_xcc[8][1024];
__device__ unsigned long long g_wv[1024][8][2];
#define STAMP_DECL unsigned long long st_c[8] = {0, 0, 0, 0, 0, 0, 0, 0}, st_r0 = 0
#define STAMP(k, i) do { \
    if (i == 0) asm volatile("s_memrealtime %0" : "=s"(st_r0)); \
    asm volatile("s_memtime %0" : "=s"(st_c[i])); \
    if (i == 7 || i == 6) { unsigned long long r1_; asm volatile("s_memrealtime %0\n\ts_waitcnt lgkmcnt(0)" : "=s"(r1_) :: "memory"); \
      if (threadIdx.x == 0) { g_stamps[k][blockIdx.x][0] = st_r0; g_stamps[k][blockIdx.x][i] = r1_; \
        for (int q_ = 0; q_ < 8; ++q_) g_cyc[k][blockIdx.x][q_] = st_c[q_]; } } } while (0)
#define SLAB(i) do {} while (0)
#define WSTAMP(j) do {} while (0)
#define WAITALL() asm volatile("s_waitcnt vmcnt(0)" ::: "memory")
DI float bf_lo(u32 v) { return __uint_as_float(v << 16); }
DI float bf_hi(u32 v) { return __uint_as_float(v & 0xffff0000u); }
DI float bf2f(u16 v) { return __uint_as_float(((u32)v) << 16); }
DI u32 pk_bf(float a, float b) { f32x2 f = {a, b}; return __builtin_bit_cast(u32, __builtin_convertvector(f, bf2_t)); }
DI float rbf(float a) { return bf_lo(pk_bf(a, a)); }
DI float dpp_x1(float v) { return __int_as_float(__builtin_amdgcn_mov_dpp(__float_as_int(v), 0xB1, 0xf, 0xf, true)); }
DI float dpp_x2(float v) { return __int_as_float(__builtin_amdgcn_mov_dpp(__float_as_int(v), 0x4E, 0xf, 0xf, true)); }
DI float dpp_hm(float v) { return __int_as_float(__builtin_amdgcn_mov_dpp(__float_as_int(v), 0x141, 0xf, 0xf, true)); }
DI float dpp_rm(float v) { return __int_as_float(__builtin_amdgcn_mov_dpp(__float_as_int(v), 0x140, 0xf, 0xf, true)); }
DI f32x4 mfma16(s16x8 a, s16x8 b, f32x4 c) { return __builtin_amdgcn_mfma_f32_16x16x32_bf16(a, b, c, 0, 0, 0); }
DI f32x16 mfma32(s16x8 a, s16x8 b, f32x16 c) { return __builtin_amdgcn_mfma_f32_32x32x16_bf16(a, b, c, 0, 0, 0); }
DI s16x4 tr_read(const void* p) { return __builtin_amdgcn_ds_read_tr16_b64_v4i16((LDSP(s16x4))p); }
template <class T> DI void pin(T& v) { asm volatile("" : "+v"(v)); }
DI void l2_touch(const void* base, u32 voff, u32 lds) {
  asm volatile("s_mov_b32 m0, %0\n\ts_nop 0\n\tglobal_load_lds_dword %1, %2" :: "s"(lds), "v"(voff), "s"(base) : "memory");
}
DI void side_delay(int ticks10ns) {
  const unsigned long long t0 = __builtin_amdgcn_s_memrealtime();
  while (__builtin_amdgcn_s_memrealtime() - t0 < (unsigned long long)ticks10ns) __builtin_amdgcn_s_sleep(4);
}
#define GSP(T) __attribute__((address_space(1))) T*
template <class T> DI T ldg(const void* sbase, u32 voff) { GSP(const char) p = (GSP(const char))sbase; asm("" : "+s"(p)); return *(GSP(const T))(p + voff); }
template <class T> DI void stg(void* sbase, u32 voff, T v) { GSP(char) p = (GSP(char))sbase; asm("" : "+s"(p)); *(GSP(T))(p + voff) = v; }
DI u32 lds_addr(const void* p) { return (u32)(size_t)(LDSP(const char))p; }
DI s16x8 cat8(s16x4 a, s16x4 b) { return __builtin_shufflevector(a, b, 0, 1, 2, 3, 4, 5, 6, 7); }
DI s16x8 as8(u32x4 v) { return __builtin_bit_cast(s16x8, v); }
DI float dot2bf(u32 a, u32 b, float c) { return __builtin_amdgcn_fdot2_f32_bf16(__builtin_bit_cast(bf2_t, a), __builtin_bit_cast(bf2_t, b), c, false); }

DI float reduce16(const float (&v)[16], int lane) {
  float a[8], b[4], c[2], d;
  const bool s3 = lane & 8, s2 = lane & 4, s1 = lane & 2, s0 = lane & 1;
#pragma unroll
  for (int i = 0; i < 8; ++i) { float keep = s3 ? v[8 + i] : v[i], send = s3 ? v[i] : v[8 + i]; a[i] = keep + dpp_rm(send); }
#pragma unroll
  for (int i = 0; i < 4; ++i) { float keep = s2 ? a[4 + i] : a[i], send = s2 ? a[i] : a[4 + i]; b[i] = keep + dpp_hm(send); }
#pragma unroll
  for (int i = 0; i < 2; ++i) { float keep = s1 ? b[2 + i] : b[i], send = s1 ? b[i] : b[2 + i]; c[i] = keep + dpp_x2(send); }
  { float keep = s0 ? c[1] : c[0], send = s0 ? c[0] : c[1]; d = keep + dpp_x1(send); }
  auto r16 = __builtin_amdgcn_permlane16_swap(__float_as_uint(d), __float_as_uint(d), false, false);
  d = __uint_as_float(r16[0]) + __uint_as_float(r16[1]);
  auto r32 = __builtin_amdgcn_permlane32_swap(__float_as_uint(d), __float_as_uint(d), false, false);
  return __uint_as_float(r32[0]) + __uint_as_float(r32[1]);
}

DI void ln_finish(float s, float q, float& m_, float& i_) {
  const float mean = s * (1.0f / 640.0f);
  const float var = fmaxf(q * (1.0f / 640.0f) - mean * mean, 0.f);
  const float sd = rbf(__builtin_amdgcn_sqrtf(rbf(rbf(var) + 1e-5f)));
  m_ = rbf(mean); i_ = __builtin_amdgcn_rcpf(sd);
}
DI void ln_stats5(const u32 (&xv)[8], float* part  , int tid, float (&a_)[8], float (&b_)[8]) {
  const int lane = tid & 63, w = tid >> 6;
  float v[16];
#pragma unroll
  for (int r = 0; r < 8; ++r) { v[r] = dot2bf(xv[r], 0x3f803f80u, 0.f); v[8 + r] = dot2bf(xv[r], xv[r], 0.f); }
  float t = reduce16(v, lane);
  if (lane < 16) part[w * 16 + lane] = t;
  __syncthreads();
  const int r = lane & 7;
  float s = 0.f, q = 0.f;
#pragma unroll
  for (int i = 0; i < 5; ++i) { s += part[i * 16 + r]; q += part[i * 16 + 8 + r]; }
  float m_, i_; ln_finish(s, q, m_, i_);
  const float nb = -m_ * i_;
#pragma unroll
  for (int i = 0; i < 8; ++i) {
    a_[i] = __int_as_float(__builtin_amdgcn_readlane(__float_as_int(i_), i));
    b_[i] = __int_as_float(__builtin_amdgcn_readlane(__float_as_int(nb), i));
  }
}
DI float reduce8(const float (&v)[8], int lane) {
  float b[4], c[2], d;
  const bool s2 = lane & 4, s1 = lane & 2, s0 = lane & 1;
#pragma unroll
  for (int i = 0; i < 4; ++i) { float keep = s2 ? v[4 + i] : v[i], send = s2 ? v[i] : v[4 + i]; b[i] = keep + dpp_hm(send); }
#pragma unroll
  for (int i = 0; i < 2; ++i) { float keep = s1 ? b[2 + i] : b[i], send = s1 ? b[i] : b[2 + i]; c[i] = keep + dpp_x2(send); }
  { float keep = s0 ? c[1] : c[0], send = s0 ? c[0] : c[1]; d = keep + dpp_x1(send); }
  d += __int_as_float(__builtin_amdgcn_mov_dpp(__float_as_int(d), 0x128, 0xf, 0xf, true));
  auto r16 = __builtin_amdgcn_permlane16_swap(__float_as_uint(d), __float_as_uint(d), false, false);
  d = __uint_as_float(r16[0]) + __uint_as_float(r16[1]);
  auto r32 = __builtin_amdgcn_permlane32_swap(__float_as_uint(d), __float_as_uint(d), false, false);
  return __uint_as_float(r32[0]) + __uint_as_float(r32[1]);
}

DI void prep_load(u32x4 (&v)[4], const u16* __restrict__ src, int ld, int lane) {
  const u32 vo = (u32)((lane >> 3) * ld + 8 * (lane & 7)) * 2u;
#pragma unroll
  for (int i = 0; i < 4; ++i) v[i] = ldg<u32x4>(src + (size_t)8 * i * ld, vo);
}
DI void prep_store(const u32x4 (&v)[4], u16* __restrict__ dst, int jstride, char* lds, int lane) {
#pragma unroll
  for (int i = 0; i < 4; ++i) {
    const int row = 8 * i + (lane >> 3), ch = lane & 7;
    *(u32x4*)(lds + row * 128 + (((ch >> 1) ^ ((row >> 1) & 3)) * 32) + (ch & 1) * 16) = v[i];
  }
  const int c = lane & 15, g = lane >> 4, q = c >> 2, p = c & 3, r = 4 * g + q;
#pragma unroll
  for (int j = 0; j < 4; ++j) {
    const char* b = lds + r * 128 + ((j ^ ((r >> 1) & 3)) * 32) + 8 * p;
    const s16x8 f = cat8(tr_read(b), tr_read(b + 16 * 128));
    stg(dst + (size_t)j * jstride, (u32)lane * 16u, f);
  }
}
DI void prep_wave(const u16* __restrict__ src, int ld, u16* __restrict__ dst, int jstride, char* lds, int lane) {
  u32x4 v[4]; prep_load(v, src, ld, lane); prep_store(v, dst, jstride, lds, lane);
}
DI void prep_gu(int task, const u16* wg, const u16* wu, u16* Wgu, char* lds, int lane) {
  const int mat = task >= 540, tk = task - 540 * mat, cg = tk / 20, ks = tk % 20;
  prep_wave((mat ? wu : wg) + (size_t)ks * 32 * F_ + 64 * cg, F_, Wgu + ((size_t)(4 * cg * 20 + ks) * 2 + mat) * 512, 20 * 2 * 512, lds, lane);
}
DI void prep_d3(int blk, const u16* wd, u16* Wd, char* lds, int lane) {
  u32x4 v0[4], v1[4], v2[4];
  const int t0 = blk, t1 = blk + 256, t2 = min(blk + 512, 539);
  prep_load(v0, wd + (size_t)(t0 % 54) * 32 * D_ + 64 * (t0 / 54), D_, lane);
  prep_load(v1, wd + (size_t)(t1 % 54) * 32 * D_ + 64 * (t1 / 54), D_, lane);
  const bool third = blk + 512 < 540;
  if (third) prep_load(v2, wd + (size_t)(t2 % 54) * 32 * D_ + 64 * (t2 / 54), D_, lane);
  prep_store(v0, Wd + (size_t)(4 * (t0 / 54) * 54 + t0 % 54) * 512, 54 * 512, lds, lane);
  prep_store(v1, Wd + (size_t)(4 * (t1 / 54) * 54 + t1 % 54) * 512, 54 * 512, lds + 4096, lane);
  if (third) prep_store(v2, Wd + (size_t)(4 * (t2 / 54) * 54 + t2 % 54) * 512, 54 * 512, lds + 8192, lane);
}

DI void prep_w20(int task  , const u16* __restrict__ wr, const u16* __restrict__ wi, u32x4* __restrict__ Wp20, int lane) {
  const int m1 = task >> 3, w = task & 7, c = lane & 15, g = lane >> 4, k = 16 + 32 * min(c, 9);
  u16 a[2][4], b[2][4];
#pragma unroll
  for (int j = 0; j < 2; ++j)
#pragma unroll
    for (int r = 0; r < 4; ++r) { const u32 vo = (u32)((m1 + 8 * (2 * w + j) + 128 * (4 * g + r)) * DF + k) * 2u; a[j][r] = ldg<u16>(wr, vo); b[j][r] = ldg<u16>(wi, vo); }
#pragma unroll
  for (int j = 0; j < 2; ++j)
    stg(Wp20 + (size_t)(task * 2 + j) * 64, (u32)lane * 16u, u32x4{a[j][0] | (u32)a[j][1] << 16, a[j][2] | (u32)a[j][3] << 16, b[j][0] | (u32)b[j][1] << 16, b[j][2] | (u32)b[j][3] << 16});
}

typedef __attribute__((address_space(1))) u32 gu32;
typedef __attribute__((address_space(1))) unsigned long long gu64;
DI u32 ld_agent(const u32* p) { return __hip_atomic_load((const gu32*)p, __ATOMIC_RELAXED, __HIP_MEMORY_SCOPE_AGENT); }
DI void st_agent(u32* p, u32 v) { __hip_atomic_store((gu32*)p, v, __ATOMIC_RELAXED, __HIP_MEMORY_SCOPE_AGENT); }
DI void st_agent64(unsigned long long* p, unsigned long long v) { __hip_atomic_store((gu64*)p, v, __ATOMIC_RELAXED, __HIP_MEMORY_SCOPE_AGENT); }
#define K2_SIDE_DELAY 100
#define K4_SIDE_DELAY 60
constexpr int SPIN_LIMIT = 1 << 16;

template <int NR> DI void gemv_part(int col0, int row0, u32 tagv, bool publish, unsigned long long* gran, int gidx0, const u16* __restrict__ c, const u16* __restrict__ wmod,
                                    float* __restrict__ mod, char* lds) {
  float* sl = (float*)lds; float* part = sl + 640;
  const int t = threadIdx.x, lane = t & 63, w = t >> 6, cq = t & 7, rg = t >> 3;
  const u16 cv = ldg<u16>(c, 2 * t);
  u32x4 wv[NR];
  const u32 wo = (u32)((row0 + rg) * 3840 + col0 + 8 * cq) * 2u;
#pragma unroll
  for (int i = 0; i < NR; ++i) wv[i] = ldg<u32x4>(wmod + (size_t)80 * i * 3840, wo);
  { const float x = bf2f(cv); sl[t] = rbf(x * __builtin_amdgcn_rcpf(1.0f + __expf(-x))); }
  __syncthreads();
  float acc[8] = {};
#pragma unroll
  for (int i = 0; i < NR; ++i) {
    const float s = sl[row0 + rg + 80 * i];
#pragma unroll
    for (int k = 0; k < 4; ++k) { acc[2 * k] += s * bf_lo(wv[i][k]); acc[2 * k + 1] += s * bf_hi(wv[i][k]); }
  }
  float u[4];
  const bool b3 = lane & 8;
#pragma unroll
  for (int i = 0; i < 4; ++i) { const float keep = b3 ? acc[4 + i] : acc[i], send = b3 ? acc[i] : acc[4 + i]; u[i] = keep + __int_as_float(__builtin_amdgcn_mov_dpp(__float_as_int(send), 0x128, 0xf, 0xf, true)); }
  float r2[2];
#pragma unroll
  for (int i = 0; i < 2; ++i) { auto x = __builtin_amdgcn_permlane16_swap(__float_as_uint(u[i]), __float_as_uint(u[i + 2]), false, false); r2[i] = __uint_as_float(x[0]) + __uint_as_float(x[1]); }
  auto y = __builtin_amdgcn_permlane32_swap(__float_as_uint(r2[0]), __float_as_uint(r2[1]), false, false);
  part[w * 64 + 8 * cq + 4 * ((lane >> 3) & 1) + 2 * ((lane >> 4) & 1) + (lane >> 5)] = __uint_as_float(y[0]) + __uint_as_float(y[1]);
  __syncthreads();
  if (t < 64) {
    float s = 0.f;
#pragma unroll
    for (int i = 0; i < 10; ++i) s += part[i * 64 + t];
    if (publish) st_agent64(gran + gidx0 + t, ((unsigned long long)(tagv + 1u) << 32) | __float_as_uint(s));
    else mod[col0 + t] = rbf(s);
  }
}

DI int row_group(int b) { return 2 * (8 * (b >> 4) + (b & 7)) + ((b >> 3) & 1); }
DI void sincos_rev(float a, float& s, float& c) { s = __builtin_amdgcn_sinf(a); c = __builtin_amdgcn_cosf(a); }
DI void gram_part(const u32x4 (&f)[4], float* part  , int i, int lane) {
  f32x4 acc = {0.f, 0.f, 0.f, 0.f};
#pragma unroll
  for (int k = 0; k < 4; ++k) acc = mfma16(as8(f[k]), as8(f[k]), acc);
  const int c = lane & 15, g = lane >> 4;
  const float dg = (c & 2) ? ((c & 1) ? acc[3] : acc[2]) : ((c & 1) ? acc[1] : acc[0]);
  if (c < 8 && g == (c >> 2)) part[i * 16 + 8 + c] = dg;
  if (c < 8 && g == 2) part[i * 16 + c] = acc[0];
}
DI void ln_bcast8(const float* part, int lane, float (&a_)[8], float (&b_)[8]) {
  const int r = lane & 7;
  float s = 0.f, q = 0.f;
#pragma unroll
  for (int i = 0; i < 5; ++i) { s += part[i * 16 + r]; q += part[i * 16 + 8 + r]; }
  float m_, i_; ln_finish(s, q, m_, i_);
  const float nb = -m_ * i_;
#pragma unroll
  for (int i = 0; i < 8; ++i) {
    a_[i] = __int_as_float(__builtin_amdgcn_readlane(__float_as_int(i_), i));
    b_[i] = __int_as_float(__builtin_amdgcn_readlane(__float_as_int(nb), i));
  }
}
constexpr int HT_RS = 1296;
struct K2Args { const u16* x; const u16* c; const u16* wmod; float* mod; u32* XT; const u16* wr; const u16* wi; const u16* wg; const u16* wu; u16* Wgu; u32x4* Wp20; u32* gen; u32* arr; unsigned long long* gran; };
constexpr int K2_MAIN0 = 88, K2_GRID = 344;
__global__ __launch_bounds__(640) void k2_fwd(const K2Args A) {
  STAMP_DECL; STAMP(1, 0);
  __shared__ __attribute__((aligned(16))) char smem[32768 + 5 * 4096 + 256];
  const int bid = blockIdx.x;
  if (bid >= 40 && bid < 80) {
    side_delay(K2_SIDE_DELAY);
    gemv_part<8>(1280 + 64 * (bid - 40), 0, 0u, false, nullptr, 0, A.c, A.wmod, A.mod, smem); STAMP(0, 6); return;
  }
  if (bid >= 80 && bid < 87) return;
  if (bid < 40) {
    const u32 tagv = ld_agent(A.gen);
    gemv_part<4>(64 * (bid >> 1), 320 * (bid & 1), tagv, true, A.gran, 1280 * (bid & 1) + 64 * (bid >> 1), A.c, A.wmod, A.mod, smem);
    if (threadIdx.x == 0) st_agent(A.arr + bid, tagv + 1u);
    STAMP(0, 7); return;
  }
  if (bid == 87) {
    const u32 tag = ld_agent(A.gen) + 1u;
    const int t = threadIdx.x, who = t < 40 ? t : t + 48;
    for (int spin = 0; spin < SPIN_LIMIT; ++spin) {
      const bool ok = t >= 296 || ld_agent(A.arr + who) == tag;
      if (__syncthreads_and(ok)) break;
      __builtin_amdgcn_s_sleep(8);
    }
    if (t == 0) st_agent(A.gen, tag);
    return;
  }
  u32* __restrict__ XT = A.XT;
  char* hT = smem; char* U = smem + 8 * HT_RS; float* part = (float*)(smem + 8 * HT_RS + 8 * 17 * 80);
  char* tab = smem + 8 * HT_RS + 8 * 17 * 80 + 5 * 16 * 4;
  const int tid = threadIdx.x, lane = tid & 63, w = __builtin_amdgcn_readfirstlane(tid >> 6);
  const int s0 = row_group(bid - K2_MAIN0);
  const int c = lane & 15, g = lane >> 4, q = c >> 2, p = c & 3;
  const u16* xrow = A.x + (size_t)s0 * D_;
  f32x4 tw0, tw1; u32 ard, awr, aw0, acr; float z0r, z0i;
  auto precompute = [&]() {
    const int Qw = 4 * w + q, rho = Qw / 5, d0 = 4 * (Qw % 5) + p;
    const bool rot = rho >= 3 && (rho & 1);
    const float fd = (float)d0 * (1.0f / 640.0f);
    const int k10 = g == 0 ? 16 : 2 * g;
    float s_[4], c_[4];
    sincos_rev(fd * (float)k10, s_[0], c_[0]); sincos_rev(fd * (float)(2 * g + 1), s_[1], c_[1]);
    sincos_rev(fd * (float)(8 + 2 * g), s_[2], c_[2]); sincos_rev(fd * (float)(9 + 2 * g), s_[3], c_[3]);
    tw0 = f32x4{rot ? s_[0] : c_[0], rot ? c_[0] : -s_[0], rot ? s_[1] : c_[1], rot ? c_[1] : -s_[1]};
    tw1 = f32x4{rot ? s_[2] : c_[2], rot ? c_[2] : -s_[2], rot ? s_[3] : c_[3], rot ? c_[3] : -s_[3]};
    const int Qr = 4 * w + p;
    ard = lds_addr(hT) + (Qr / 5) * HT_RS + 2 * (20 * (8 * g + q) + 4 * (Qr % 5));
    awr = lds_addr(U) + 4 * (rho * 17 * 20 + d0 + 2 * g * 20);
    aw0 = g == 0 ? awr + 4 * 16 * 20 : awr;
    z0r = rot ? 0.f : 1.f; z0i = rot ? 1.f : 0.f;
    acr = lds_addr(U) + (w < 8 ? ((2 * (w >> 1)) * 17 + c) * 80 : ((c & 7) * 17 + 16) * 80);
    pin(tw0); pin(tw1); pin(ard); pin(awr); pin(aw0); pin(z0r); pin(z0i); pin(acr);
  };
  if (w < 5) {
    u32 xv[8];
#pragma unroll
    for (int r = 0; r < 8; ++r) xv[r] = ldg<u32>(xrow + (size_t)r * 256 * D_, 4 * tid);
    const u32 tagv = ld_agent(A.gen);
    precompute();
    STAMP(1, 1);
    WAITALL();
    STAMP(1, 5);
    __syncthreads();
    float na[8], nb[8];
    ln_bcast8(part, lane, na, nb);
    f32x2 o[8];
    {
      f32x2 h[8];
#pragma unroll
      for (int r = 0; r < 8; ++r) { f32x2 v = {bf_lo(xv[r]), bf_hi(xv[r])}; h[r] = v * na[r] + nb[r]; }
      const float cc = 0.70710678118654752f;
      f32x2 a0 = h[0] + h[4], a1 = h[1] + h[5], a2 = h[2] + h[6], a3 = h[3] + h[7];
      f32x2 b0 = h[0] - h[4], b1 = h[1] - h[5], b2 = h[2] - h[6], b3 = h[3] - h[7];
      f32x2 e = a0 + a2, f = a1 + a3, u = cc * (b1 - b3), v = cc * (b1 + b3);
      o[0] = e + f; o[1] = e - f; o[2] = b0 + u; o[3] = -b2 - v; o[4] = a0 - a2; o[5] = a3 - a1; o[6] = b0 - u; o[7] = b2 - v;
#pragma unroll
      for (int r = 0; r < 8; ++r) pin(o[r]);
    }
    const u32 tag = __builtin_amdgcn_readfirstlane(tagv) + 1u;
    if (tid == 0) st_agent(A.arr + bid, tag);
    f32x2 sa, gg;
    {
      const unsigned long long* ga_ = A.gran + 2 * tid;
      u32x4 va, vb, vc, vd;
      for (int spin = 0; spin < SPIN_LIMIT; ++spin) {
        asm volatile("global_load_dwordx4 %0, %4, off sc1\n\tglobal_load_dwordx4 %1, %5, off sc1\n\tglobal_load_dwordx4 %2, %6, off sc1\n\tglobal_load_dwordx4 %3, %7, off sc1\n\ts_waitcnt vmcnt(0)"
                     : "=&v"(va), "=&v"(vb), "=&v"(vc), "=&v"(vd) : "v"(ga_), "v"(ga_ + 1280), "v"(ga_ + 640), "v"(ga_ + 1920) : "memory");
        const bool ok = va[1] == tag && va[3] == tag && vb[1] == tag && vb[3] == tag && vc[1] == tag && vc[3] == tag && vd[1] == tag && vd[3] == tag;
        if (__all(ok)) break;
      }
      sa = f32x2{rbf(__uint_as_float(va[0]) + __uint_as_float(vb[0])), rbf(__uint_as_float(va[2]) + __uint_as_float(vb[2]))};
      gg = f32x2{rbf(1.0f + rbf(__uint_as_float(vc[0]) + __uint_as_float(vd[0]))), rbf(1.0f + rbf(__uint_as_float(vc[2]) + __uint_as_float(vd[2])))};
    }
    STAMP(1, 2);
    o[0] = o[0] * gg + 8.0f * sa;
    *(u32*)(hT + 4 * tid) = pk_bf(o[0][0], o[0][1]);
#pragma unroll
    for (int r = 1; r < 8; ++r) { const f32x2 v = o[r] * gg; *(u32*)(hT + r * HT_RS + 4 * tid) = pk_bf(v[0], v[1]); }
  } else {
    const int i = w - 5;
    u32x4 f[4];
    const u32 xo = (u32)((c & 7) * 256 * D_ + 128 * i + 8 * g) * 2u;
#pragma unroll
    for (int k = 0; k < 4; ++k) f[k] = ldg<u32x4>(xrow, xo + 64 * k);
    const s16x8 t0 = i < 2 ? ldg<s16x8>(g_f32a.v[i < 2 ? i : 0], lane * 16) : ldg<s16x8>(&g_f20a.v[0][0][0][0] + (i - 2) * 512, lane * 16);
    const s16x8 t1 = ldg<s16x8>(&g_f20a.v[0][0][0][0] + (i < 3 ? i + 3 : 5) * 512, lane * 16);
    precompute();
    if (c >= 8) {
      const u32 fill = c == 8 ? 0x3f803f80u : 0u;
#pragma unroll
      for (int k = 0; k < 4; ++k) f[k] = u32x4{fill, fill, fill, fill};
    }
    gram_part(f, part, i, lane);
    *(s16x8*)(tab + i * 1024 + lane * 16) = t0;
    if (i < 3) *(s16x8*)(tab + (i + 5) * 1024 + lane * 16) = t1;
    __syncthreads();
    const int task = (bid - K2_MAIN0) * 5 + i;
    if (task < 1080) prep_gu(task, A.wg, A.wu, A.Wgu, smem + 32768 + 4096 * i, lane);
    else if (task < 1080 + 64) prep_w20(task - 1080, A.wr, A.wi, A.Wp20, lane);
    if (i == 4) {
      const int xc = bid & 7, li = (bid - K2_MAIN0) >> 3;
#pragma unroll
      for (int k = 0; k < 4; ++k) {
        const int e = (k & 1) * 64 + lane, id = li * 96 + e;
        const int ln = id % 6, row = 8 * ((id / 6) & 255) + (id < 1536 ? xc : ((8 - xc) & 7));
        const u32 off = min((u32)(row * 642 + ln * 128), (u32)(2048 * 642 - 4)) & ~3u;
        if (e < 96) l2_touch((k >> 1) ? A.wi : A.wr, off, lds_addr(smem) + sizeof(smem) - 256);
      }
    }
    asm volatile("s_waitcnt vmcnt(0) lgkmcnt(0)" ::: "memory");
  }
  __syncthreads();
  STAMP(1, 3);
  {
    const LDSP(const char) base = (LDSP(const char))(size_t)ard;
    s16x8 b = cat8(__builtin_amdgcn_ds_read_tr16_b64_v4i16((LDSP(s16x4))base), __builtin_amdgcn_ds_read_tr16_b64_v4i16((LDSP(s16x4))(base + 160)));
    const s16x8 fa0 = *(const s16x8*)(tab + lane * 16), fa1 = *(const s16x8*)(tab + 1024 + lane * 16);
    const f32x4 z = {0.f, 0.f, 0.f, 0.f};
    f32x4 a0 = mfma16(fa0, b, z), a1 = mfma16(fa1, b, z);
    LDSP(u32) Ub = (LDSP(u32))(size_t)awr;
    const float x0 = g == 0 ? a0[1] : a0[0], x1 = g == 0 ? 0.f : a0[1];
    *(LDSP(u32))(size_t)aw0 = pk_bf(x0 * tw0[0] - x1 * tw0[1], x0 * tw0[1] + x1 * tw0[0]);
    if (g == 0) Ub[0] = pk_bf(a0[0] * z0r, a0[0] * z0i);
    Ub[20] = pk_bf(a0[2] * tw0[2] - a0[3] * tw0[3], a0[2] * tw0[3] + a0[3] * tw0[2]);
    Ub[8 * 20] = pk_bf(a1[0] * tw1[0] - a1[1] * tw1[1], a1[0] * tw1[1] + a1[1] * tw1[0]);
    Ub[9 * 20] = pk_bf(a1[2] * tw1[2] - a1[3] * tw1[3], a1[2] * tw1[3] + a1[3] * tw1[2]);
  }
  __syncthreads();
  STAMP(1, 4);
  const f32x4 z4 = {0.f, 0.f, 0.f, 0.f};
  u32* XTb = XT + (size_t)s0 * 16;
  const char* ft = tab + 2048 + lane * 16;
  const LDSP(const char) be = (LDSP(const char))(size_t)acr;
  const int P = w >> 1, mp = P, mm = P == 0 ? 4 : 8 - P;
  if (w < 8) {
    const s16x8 e0 = *(const LDSP(s16x8))(be + 16 * g), e1 = *(const LDSP(s16x8))(be + 64), o0 = *(const LDSP(s16x8))(be + 1360 + 16 * g), o1 = *(const LDSP(s16x8))(be + 1360 + 64);
    char* dp = (char*)(XTb + (size_t)(mp * 21) * 4096); char* dm = (char*)(XTb + (size_t)(mm * 21) * 4096);
    const u32 vo = (u32)(2 * g * 4096 + c) * 4u;
    if (!(w & 1)) {
#pragma unroll
      for (int mt = 0; mt < 2; ++mt) {
        const s16x8 f0 = *(const s16x8*)(ft + (2 * mt) * 1024), f1 = *(const s16x8*)(ft + (2 * mt + 1) * 1024);
        f32x4 ae = mfma16(f1, e1, mfma16(f0, e0, z4)), ao = mfma16(f1, o1, mfma16(f0, o0, z4));
        f32x4 tp = ae + ao, tm = ae - ao;
        if (P == 0) { tp = ae; tm = ao; }
        char* ep = dp + (size_t)(8 * mt) * 16384; char* em = dm + (size_t)(8 * mt) * 16384;
        stg(ep, vo, pk_bf(tp[0], tp[1])); stg(ep + 16384, vo, pk_bf(tp[2], tp[3]));
        stg(em, vo, pk_bf(tm[0], tm[1])); stg(em + 16384, vo, pk_bf(tm[2], tm[3]));
      }
    } else {
      const s16x8 f0 = *(const s16x8*)(ft + 4 * 1024), f1 = *(const s16x8*)(ft + 5 * 1024);
      f32x4 ae = mfma16(f1, e1, mfma16(f0, e0, z4)), ao = mfma16(f1, o1, mfma16(f0, o0, z4));
      f32x4 tp = ae + ao, tm = ae - ao;
      if (P == 0) { tp = ae; tm = ao; }
      if (g < 2) {
        char* ep = dp + (size_t)16 * 16384; char* em = dm + (size_t)16 * 16384;
        stg(ep, vo, pk_bf(tp[0], tp[1])); stg(ep + 16384, vo, pk_bf(tp[2], tp[3]));
        stg(em, vo, pk_bf(tm[0], tm[1])); stg(em + 16384, vo, pk_bf(tm[2], tm[3]));
      }
    }
  } else {
    const int rh = c & 7, mt = w - 8;
    const s16x8 b0 = *(const LDSP(s16x8))(be + 16 * g), b1 = *(const LDSP(s16x8))(be + 64);
    const s16x8 f0 = *(const s16x8*)(ft + (2 * mt) * 1024), f1 = *(const s16x8*)(ft + (2 * mt + 1) * 1024);
    f32x4 a = mfma16(f1, b1, mfma16(f0, b0, z4));
    const float ca_ = (rh >= 2 && (rh & 1)) ? -1.f : 1.f, cb_ = rh >= 2 ? 1.f : 0.f;
    f32x4 t;
#pragma unroll
    for (int r = 0; r < 4; ++r) t[r] = ca_ * a[r] + cb_ * dpp_x1(a[r]);
    const int m1 = rh == 0 ? 0 : rh == 1 ? 4 : (rh & 1) ? 8 - (rh >> 1) : (rh >> 1);
    const int k0 = 8 * mt + 2 * g;
    u32* d = XTb + (size_t)(m1 * 21 + 20) * 4096;
    if (c < 8 && k0 < 10) *(u32x2*)(d + k0) = u32x2{pk_bf(t[0], t[1]), pk_bf(t[2], t[3])};
  }
  STAMP(1, 7);
}

constexpr int EX_RS = 20;
__global__ __launch_bounds__(512) void k3_mid(const u32* __restrict__ XT, const u16* __restrict__ wr, const u16* __restrict__ wi, const u32x4* __restrict__ Wp20, u32* __restrict__ ZT) {
  STAMP_DECL; STAMP(2, 0);
  __shared__ __attribute__((aligned(16))) u32 ex[2][256 * EX_RS];
  __shared__ __attribute__((aligned(16))) char tab[8 * 1024];
  const int tid = threadIdx.x, lane = tid & 63, w = __builtin_amdgcn_readfirstlane(tid >> 6), c = lane & 15, g = lane >> 4;
  const int m1 = blockIdx.x & 7, tile = blockIdx.x >> 3;
  const char* in = (const char*)(XT + (size_t)(m1 * 21 + tile) * 4096 + 2 * w * 16);
  const u32 vin = (u32)(64 * g * 16 + c) * 4u;
  u32x4 bx[2];
#pragma unroll
  for (int j = 0; j < 2; ++j)
#pragma unroll
    for (int i = 0; i < 4; ++i) bx[j][i] = ldg<u32>(in + (16 * i + j) * 64, vin);
  const F8w* tsrc = w < 2 ? &g_a1[m1] : w < 4 ? &g_f16f : w < 6 ? &g_f16i : &g_a1i[m1];
  const s16x8 tfrag = ldg<s16x8>(tsrc->v[w & 1], lane * 16);
  u32 wra[2][4], wia[2][4];
  const int kb = c + 32 * tile;
  const bool mir = tile < 20 && kb > 320;
  if (tile < 20) {
    const int kk = mir ? 640 - kb : kb;
#pragma unroll
    for (int j = 0; j < 2; ++j)
#pragma unroll
      for (int r = 0; r < 4; ++r) {
        const int m = m1 + 8 * (2 * w + j) + 128 * (4 * g + r), mm = mir ? ((2048 - m) & 2047) : m;
        const u32 vo = (u32)(mm * DF + kk) * 2u;
        wra[j][r] = (u32)ldg<u16>(wr, vo) << 16; wia[j][r] = (u32)ldg<u16>(wi, vo) << 16;
      }
  } else {
#pragma unroll
    for (int j = 0; j < 2; ++j) {
      const u32x4 v = ldg<u32x4>(Wp20 + (size_t)((m1 * 8 + w) * 2 + j) * 64, (u32)lane * 16u);
      wra[j][0] = v[0] << 16; wra[j][1] = v[0] & 0xffff0000u; wra[j][2] = v[1] << 16; wra[j][3] = v[1] & 0xffff0000u;
      wia[j][0] = v[2] << 16; wia[j][1] = v[2] & 0xffff0000u; wia[j][2] = v[3] << 16; wia[j][3] = v[3] & 0xffff0000u;
    }
  }
  *(s16x8*)(tab + w * 1024 + lane * 16) = tfrag;
  f32x4 fcs[2], fsn[2], ics[2], isn[2];
#pragma unroll
  for (int j = 0; j < 2; ++j)
#pragma unroll
    for (int r = 0; r < 4; ++r) {
      float s_, c_;
      sincos_rev((float)((2 * w + j) * (m1 + 8 * (4 * g + r))) * (1.0f / 2048), s_, c_); fcs[j][r] = c_; fsn[j][r] = s_;
      sincos_rev((float)((4 * g + r) * (m1 + 8 * (2 * w + j))) * (1.0f / 2048), s_, c_); ics[j][r] = c_; isn[j][r] = s_;
    }
  u32 awr = lds_addr(ex) + 4 * ((4 * g * 16 + c) * EX_RS + 2 * w);
  u32 ard = lds_addr(ex) + 4 * ((2 * w * 16 + c) * EX_RS + 4 * g);
#pragma unroll
  for (int j = 0; j < 2; ++j) { pin(fcs[j]); pin(fsn[j]); pin(ics[j]); pin(isn[j]); }
  pin(awr); pin(ard);
  STAMP(2, 1);
  WAITALL();
  __syncthreads();
  STAMP(2, 2);
  const f32x4 z4 = {0.f, 0.f, 0.f, 0.f};
  const char* tb = tab + lane * 16;
  const LDSP(char) wr0 = (LDSP(char))(size_t)awr; const LDSP(const char) rd0 = (LDSP(const char))(size_t)ard;
  constexpr int RB = 16 * EX_RS * 4, XB = 256 * EX_RS * 4;
  {
    const s16x8 A10 = *(const s16x8*)tb, A11 = *(const s16x8*)(tb + 1024);
    u32 pk[2][4];
#pragma unroll
    for (int j = 0; j < 2; ++j) {
      f32x4 re = mfma16(A10, as8(bx[j]), z4), im = mfma16(A11, as8(bx[j]), z4);
      f32x4 tr = re * fcs[j] + im * fsn[j], ti = im * fcs[j] - re * fsn[j];
#pragma unroll
      for (int r = 0; r < 4; ++r) pk[j][r] = pk_bf(tr[r], ti[r]);
    }
#pragma unroll
    for (int r = 0; r < 4; ++r) *(LDSP(u32x2))(wr0 + r * RB) = u32x2{pk[0][r], pk[1][r]};
  }
  __syncthreads();
  STAMP(2, 3);
  {
    const s16x8 F0 = *(const s16x8*)(tb + 2048), F1 = *(const s16x8*)(tb + 3072), G0 = *(const s16x8*)(tb + 4096), G1 = *(const s16x8*)(tb + 5120);
    u32 pk[2][4];
#pragma unroll
    for (int j = 0; j < 2; ++j) {
      const u32x4 b = *(const LDSP(u32x4))(rd0 + j * RB);
      f32x4 xr = mfma16(F0, as8(b), z4), xi = mfma16(F1, as8(b), z4);
      const f32x4 a = {__uint_as_float(wra[j][0]), __uint_as_float(wra[j][1]), __uint_as_float(wra[j][2]), __uint_as_float(wra[j][3])};
      const u32 sg = mir ? 0x80000000u : 0u;
      const f32x4 bb = {__uint_as_float(wia[j][0] ^ sg), __uint_as_float(wia[j][1] ^ sg), __uint_as_float(wia[j][2] ^ sg), __uint_as_float(wia[j][3] ^ sg)};
      const f32x4 yr = xr * a - xi * bb, yi = xr * bb + xi * a;
      const u32x4 yb = {pk_bf(yr[0], yr[1]), pk_bf(yr[2], yr[3]), pk_bf(yi[0], yi[1]), pk_bf(yi[2], yi[3])};
      f32x4 re = mfma16(G0, as8(yb), z4), im = mfma16(G1, as8(yb), z4);
      f32x4 tr = re * ics[j] - im * isn[j], ti = im * ics[j] + re * isn[j];
#pragma unroll
      for (int r = 0; r < 4; ++r) pk[j][r] = pk_bf(tr[r], ti[r]);
    }
#pragma unroll
    for (int r = 0; r < 4; ++r) *(LDSP(u32x2))(wr0 + XB + r * RB) = u32x2{pk[0][r], pk[1][r]};
  }
  __syncthreads();
  STAMP(2, 4);
  char* out = (char*)(ZT + (size_t)(m1 * 21 + tile) * 4096 + 2 * w * 16);
  const s16x8 I0 = *(const s16x8*)(tb + 6144), I1 = *(const s16x8*)(tb + 7168);
#pragma unroll
  for (int j = 0; j < 2; ++j) {
    const u32x4 b = *(const LDSP(u32x4))(rd0 + XB + j * RB);
    f32x4 zr = mfma16(I0, as8(b), z4), zi = mfma16(I1, as8(b), z4);
#pragma unroll
    for (int r = 0; r < 4; ++r) stg(out + (16 * r + j) * 64, vin, pk_bf(zr[r], zi[r]));
  }
  STAMP(2, 7);
}

constexpr int Y_RS = 640 + 4;
__global__ __launch_bounds__(640) void k4_inv(const u32* __restrict__ ZT, const u16* __restrict__ x, const float* __restrict__ mod,
                                              u16* __restrict__ x1o, u16* __restrict__ h2o, const u16* __restrict__ Wgu,
                                              const u16* __restrict__ wd, u16* __restrict__ Wd) {
  STAMP_DECL; STAMP(3, 0);
  __shared__ __attribute__((aligned(16))) char smem[162 * 64 + 8 * Y_RS * 4 + 10 * 8 * 4 + 8 * 1024 + 12288 + 256];
  char* Vs = smem; float* Y = (float*)(Vs + 162 * 64); float* part = Y + 8 * Y_RS; char* tab = (char*)(part + 80);
  const int tid = threadIdx.x, lane = tid & 63, w = __builtin_amdgcn_readfirstlane(tid >> 6), c = lane & 15, g = lane >> 4;
  const int s0 = row_group(blockIdx.x);
  const char* Zb = (const char*)(ZT + (size_t)s0 * 16);
  u32 za[8], zb[8];
  const int rho = w < 8 ? w : (c & 7), P = rho >> 1, mA = rho == 1 ? 4 : P, mB = P == 0 ? 4 : 8 - P;
  if (w < 8) {
    const int Pu = w >> 1, mAu = w == 1 ? 4 : Pu, mBu = Pu == 0 ? 4 : 8 - Pu;
    const char* pa = Zb + (size_t)(mAu * 21) * 16384; const char* pb = Zb + (size_t)(mBu * 21) * 16384;
    const u32 v0 = (u32)(4 * g * 4096 + c) * 4u, v1 = (u32)c * 4u;
#pragma unroll
    for (int i = 0; i < 4; ++i) { za[i] = ldg<u32>(pa + (size_t)i * 16384, v0); za[4 + i] = ldg<u32>(pa + (size_t)(16 + i) * 16384, v1); }
    if (w >= 2) {
#pragma unroll
      for (int i = 0; i < 4; ++i) { zb[i] = ldg<u32>(pb + (size_t)i * 16384, v0); zb[4 + i] = ldg<u32>(pb + (size_t)(16 + i) * 16384, v1); }
    }
  } else if (w == 8) {
    const int gq = g < 3 ? g : 0;
    const u32x4 a = ldg<u32x4>(Zb, (u32)((mA * 21 + 20) * 4096 + 4 * gq) * 4u), b = ldg<u32x4>(Zb, (u32)((mB * 21 + 20) * 4096 + 4 * gq) * 4u);
#pragma unroll
    for (int i = 0; i < 4; ++i) { const bool ok = 4 * g + i < 10; za[i] = ok ? a[i] : 0u; zb[i] = ok ? b[i] : 0u; za[4 + i] = 0u; zb[4 + i] = 0u; }
  }
  const s16x8 tfrag = w < 6 ? ldg<s16x8>(&g_f20ai.v[0][0][0][0] + w * 512, lane * 16) : ldg<s16x8>(g_f32bi.v[(w - 6) & 1], lane * 16);
  const int hf = w >= 5, cp = tid - 320 * hf;
  u32 xv[4];
#pragma unroll
  for (int r = 0; r < 4; ++r) xv[r] = ldg<u32>(x + (size_t)(s0 + 256 * (4 * hf + r)) * D_, 4 * cp);
  const f32x2 ga = ldg<f32x2>(mod + 2 * 640, 8 * cp), sb = ldg<f32x2>(mod + 3 * 640, 8 * cp), cb = ldg<f32x2>(mod + 4 * 640, 8 * cp);
  if (w < 8) *(s16x8*)(tab + w * 1024 + lane * 16) = tfrag;
  f32x4 tw[3]; u32 aw[3];
  {
    const bool rot = rho >= 3 && (rho & 1);
    const float sc = w < 8 ? 1.0f / (2048.0f * 640.0f) : 2.0f / (2048.0f * 640.0f), fk = (float)(w < 8 ? c : 16) * (1.0f / 640.0f);
#pragma unroll
    for (int mt = 0; mt < 3; ++mt) {
#pragma unroll
      for (int rr = 0; rr < 2; ++rr) {
        float s_, c_; sincos_rev(fk * (float)(8 * mt + 2 * g + rr), s_, c_);
        tw[mt][2 * rr] = (rot ? -s_ : c_) * sc; tw[mt][2 * rr + 1] = (rot ? c_ : s_) * sc;
      }
      const int d0 = 8 * mt + 2 * g;
      const bool ok = d0 < 20 && (w < 8 || c < 8);
      aw[mt] = lds_addr(Vs) + (ok ? (rho * 20 + d0) * 64 : 160 * 64) + (w < 8 ? 4 * c : 2);
      pin(tw[mt]); pin(aw[mt]);
    }
  }
  if (w == 9) prep_d3(blockIdx.x, wd, Wd, tab + 8 * 1024, lane);
  STAMP(3, 1);
  WAITALL();
  u32x4 b0, b1;
  if (w < 2) {
#pragma unroll
    for (int i = 0; i < 4; ++i) { b0[i] = za[i]; b1[i] = za[4 + i]; }
  } else {
    const float be = rho < 2 ? 0.f : (rho & 1) ? -1.f : 1.f;
#pragma unroll
    for (int i = 0; i < 4; ++i) {
      b0[i] = pk_bf(bf_lo(za[i]) + be * bf_lo(zb[i]), bf_hi(za[i]) + be * bf_hi(zb[i]));
      b1[i] = pk_bf(bf_lo(za[4 + i]) + be * bf_lo(zb[4 + i]), bf_hi(za[4 + i]) + be * bf_hi(zb[4 + i]));
    }
  }
  __syncthreads();
  STAMP(3, 2);
  if (w < 9) {
    f32x4 a[3];
#pragma unroll
    for (int mt = 0; mt < 3; ++mt) {
      const f32x4 z = {0.f, 0.f, 0.f, 0.f};
      const s16x8 f0 = *(const s16x8*)(tab + (2 * mt) * 1024 + lane * 16), f1 = *(const s16x8*)(tab + (2 * mt + 1) * 1024 + lane * 16);
      a[mt] = mfma16(f1, as8(b1), mfma16(f0, as8(b0), z));
    }
    if (w < 8) {
      const u32 hi_skip = c == 0 ? 1u : 0u;
#pragma unroll
      for (int mt = 0; mt < 3; ++mt)
#pragma unroll
        for (int rr = 0; rr < 2; ++rr) {
          const u32 pv = pk_bf(a[mt][2 * rr] * tw[mt][2 * rr] - a[mt][2 * rr + 1] * tw[mt][2 * rr + 1], a[mt][2 * rr] * tw[mt][2 * rr + 1] + a[mt][2 * rr + 1] * tw[mt][2 * rr]);
          LDSP(char) dst = (LDSP(char))(size_t)aw[mt] + 64 * rr;
          if (hi_skip) *(LDSP(u16))dst = (u16)pv; else *(LDSP(u32))dst = pv;
        }
    } else {
#pragma unroll
      for (int mt = 0; mt < 3; ++mt)
#pragma unroll
        for (int rr = 0; rr < 2; ++rr)
          *(LDSP(u16))((LDSP(char))(size_t)aw[mt] + 64 * rr) = (u16)pk_bf(a[mt][2 * rr] * tw[mt][2 * rr] - a[mt][2 * rr + 1] * tw[mt][2 * rr + 1], 0.f);
    }
  }
  __syncthreads();
  STAMP(3, 3);
  {
    const s16x8 a = *(const s16x8*)(Vs + (16 * w + c) * 64 + 16 * g);
    const s16x8 fb0 = *(const s16x8*)(tab + 6144 + lane * 16), fb1 = *(const s16x8*)(tab + 7168 + lane * 16);
    const f32x4 z = {0.f, 0.f, 0.f, 0.f};
    const f32x4 y0 = mfma16(a, fb0, z), y1 = mfma16(a, fb1, z);
    const int R = 16 * w + 4 * g, rr = R / 20, d0 = R % 20;
    *(f32x4*)(Y + rr * Y_RS + 20 * c + d0) = y0;
    *(f32x4*)(Y + rr * Y_RS + 20 * (16 + c) + d0) = y1;
  }
  __syncthreads();
  STAMP(3, 4);
  {
    f32x2 R[8];
#pragma unroll
    for (int r = 0; r < 8; ++r) R[r] = *(const f32x2*)(Y + r * Y_RS + 2 * cp);
    const float cc = 0.70710678118654752f;
    const f32x2 G = R[0] + R[1], Fm = R[0] - R[1];
    const f32x2 u = cc * (R[2] - R[6]), v = cc * (R[3] + R[7]);
    const f32x2 c13 = R[2] + R[6], s13 = R[3] - R[7];
    const float sg = hf ? -1.f : 1.f;
    f32x2 y[4];
    y[0] = G + R[4] + sg * c13;
    y[2] = G - R[4] + sg * s13;
    y[1] = Fm + R[5] + sg * (u + v);
    y[3] = Fm - R[5] + sg * (v - u);
    u32 x1[4];
    float sv[8];
    u16* x1d = x1o + (size_t)(8 * s0 + 4 * hf) * D_ + 2 * cp;
#pragma unroll
    for (int r = 0; r < 4; ++r) {
      const f32x2 xx = {bf_lo(xv[r]), bf_hi(xv[r])};
      const f32x2 t = xx + ga * y[r];
      x1[r] = pk_bf(t[0], t[1]);
      *(u32*)(x1d + r * D_) = x1[r];
      sv[r] = dot2bf(x1[r], 0x3f803f80u, 0.f); sv[4 + r] = dot2bf(x1[r], x1[r], 0.f);
    }
    const float tot = reduce8(sv, lane);
    if (lane < 8) part[w * 8 + lane] = tot;
    const f32x2 gg = {rbf(1.0f + cb[0]), rbf(1.0f + cb[1])};
    const int d = 2 * cp, wi = d & 31;
    u16* hd = h2o + ((((size_t)(s0 >> 1) * 20 + (d >> 5)) * 4 + ((wi & 15) >> 2)) * 16 + 8 * (s0 & 1) + 4 * hf) * 8 + 4 * (wi >> 4) + (wi & 3);
    __syncthreads();
    STAMP(3, 5);
    const int r = lane & 3;
    float s = 0.f, qq = 0.f;
#pragma unroll
    for (int i = 0; i < 5; ++i) { s += part[(5 * hf + i) * 8 + r]; qq += part[(5 * hf + i) * 8 + 4 + r]; }
    float m_, i_; ln_finish(s, qq, m_, i_);
    const float nb_ = -m_ * i_;
#pragma unroll
    for (int i = 0; i < 4; ++i) {
      const float na = __int_as_float(__builtin_amdgcn_readlane(__float_as_int(i_), i)), nb = __int_as_float(__builtin_amdgcn_readlane(__float_as_int(nb_), i));
      const f32x2 v2 = {bf_lo(x1[i]), bf_hi(x1[i])};
      const f32x2 hv = (v2 * na + nb) * gg + sb;
      *(u32*)(hd + i * 8) = pk_bf(hv[0], hv[1]);
    }
  }
  if (w == 9) asm volatile("s_waitcnt vmcnt(0)" ::: "memory");
  STAMP(3, 7);
}

constexpr int K5_SLOT = 28 * 1024;
constexpr int K5_NS = 4;
constexpr int K5_PD = 4;
template <int N> DI void wait_vm() { asm volatile("s_waitcnt vmcnt(%0)" ::"n"(N) : "memory"); }
template <int OFF> DI void gload16_s(u32x4& d, const void* sbase, u32 voff) {
  asm volatile("global_load_dwordx4 %0, %1, %2 offset:%3" : "=v"(d) : "v"(voff), "s"(sbase), "n"(OFF) : "memory");
}
template <int OFF> DI void glds16_s(const void* sbase, u32 voff) {
  asm volatile("global_load_lds_dwordx4 %0, %1 offset:%2" :: "v"(voff), "s"(sbase), "n"(OFF) : "memory");
}
DI void set_m0(u32 v) { asm volatile("s_mov_b32 m0, %0\n\ts_nop 0" :: "s"(v) : "memory"); }
DI void tie4(u32x4 (&t)[4]) { asm volatile("" : "+v"(t[0]), "+v"(t[1]), "+v"(t[2]), "+v"(t[3])); }
struct K5Ctx { const u16* wdp; int pfl; u32 dump; const u16* wsrc; const u16* hsrc; u32 ldsw; u32 goff[7]; u32 hoff; u32 abase[2]; };
template <int s, int i> DI void k5_w1(const K5Ctx& c) {
  if constexpr (i == 0) set_m0(c.ldsw + (s % K5_NS) * K5_SLOT + 3072);
  glds16_s<1024 * i - 3072>((const char*)(c.wsrc + s * 4 * 512) - 4096, c.goff[i]);
}
template <int s, int i> DI void k5_t1(const K5Ctx& c, u32x4 (&t)[4]) {
  gload16_s<(i >> 1) * 1024>(t[i], c.hsrc + ((i & 1) * 20 + 2 * s) * 512, c.hoff);
}
template <int s> DI void k5_wall(const K5Ctx& c) { k5_w1<s, 0>(c); k5_w1<s, 1>(c); k5_w1<s, 2>(c); k5_w1<s, 3>(c); k5_w1<s, 4>(c); k5_w1<s, 5>(c); k5_w1<s, 6>(c); }
template <int s> DI void k5_tall(const K5Ctx& c, u32x4 (&t)[4]) { k5_t1<s, 0>(c, t); k5_t1<s, 1>(c, t); k5_t1<s, 2>(c, t); k5_t1<s, 3>(c, t); }
template <int n> DI s16x8 k5_rd(const K5Ctx& c) {
  constexpr int s = n / 28, t = (n / 14) & 1, j = n % 14, sl = s % K5_NS;
  return *(const LDSP(const s16x8))((LDSP(const char))(size_t)c.abase[sl >> 1] + (sl & 1) * K5_SLOT + ((j >> 1) * 4 + t * 2 + (j & 1)) * 1024);
}
template <int n> DI void k5_sub(const K5Ctx& c, f32x4 (&acc)[14][2], s16x8 (&fr)[K5_PD + 1], u32x4 (&tok)[10][4]) {
  constexpr int s = n / 28, t = (n / 14) & 1, j = n % 14;
  constexpr int nb = n + K5_PD;
  if constexpr (nb < 280 && nb % 28 == 0) {
    wait_vm<(s + 2 <= 9) ? 11 : 0>();
    __builtin_amdgcn_s_barrier();
    SLAB(s + 1);
    tie4(tok[s + 1]);
  }
  if constexpr (nb < 280) fr[nb % (K5_PD + 1)] = k5_rd<nb>(c);
  {
    constexpr int q = nb / 28, pos = nb % 28 - (q == 0 ? K5_PD : 0);
    constexpr int idx = (pos % 2 == 0 && pos < 22) ? pos / 2 : -1;
    if constexpr (idx >= 0 && idx < 4 && q + 2 <= 9) k5_t1<q + 2, idx>(c, tok[q + 2]);
    if constexpr (idx >= 4 && q + 2 <= 9) k5_w1<q + 2, idx - 4>(c);
  }
  if constexpr (false) {
    constexpr int i = (n - 224) / 2;
    l2_touch(c.wdp, (u32)(c.pfl + i * 64) * 128u, c.dump);
  }
  const s16x8 a = fr[n % (K5_PD + 1)];
  acc[j][0] = mfma16(a, as8(tok[s][2 * t]), acc[j][0]);
  acc[j][1] = mfma16(a, as8(tok[s][2 * t + 1]), acc[j][1]);
  __builtin_amdgcn_sched_barrier(0);
}
template <int... I> DI void k5_all(const K5Ctx& c, f32x4 (&acc)[14][2], s16x8 (&fr)[K5_PD + 1], u32x4 (&tok)[10][4], std::integer_sequence<int, I...>) { (k5_sub<I>(c, acc, fr, tok), ...); }
__global__ __launch_bounds__(256, 2) void k5_glu(const u16* __restrict__ h2, const u16* __restrict__ Wgu, u16* __restrict__ hb, const u16* __restrict__ Wd) {
  STAMP_DECL; STAMP(4, 0);
  extern __shared__ __attribute__((aligned(16))) char smem[];
  const int tid = threadIdx.x, lane = tid & 63, w = __builtin_amdgcn_readfirstlane(tid >> 6);
  const int xcd = blockIdx.x & 7, li = blockIdx.x >> 3;
  const int tt = 8 * (xcd & 1) + (li & 7), fc = 4 * (xcd >> 1) + (li >> 3);
  const int u0 = fc < 12 ? 7 * fc : 84 + 6 * (fc - 12), U = fc < 12 ? 7 : 6;
  K5Ctx c;
  c.wsrc = Wgu + (size_t)u0 * 20 * 2 * 512;
  c.dump = lds_addr(smem) + 2 * K5_SLOT + w * 256;
  c.wdp = Wd + (size_t)(20 * (xcd >> 2)) * 54 * 512;
#pragma unroll
  for (int i = 0; i < 7; ++i) { const int f = 7 * w + i, u = min(f >> 2, U - 1); c.goff[i] = 2 * (u * 20 * 2 * 512 + (f & 3) * 512) + lane * 16 + 4096 - (1024 * i - 3072); }
  c.ldsw = lds_addr(smem) + 7 * w * 1024;
  const int T16 = 8 * tt + 2 * w;
  c.hsrc = h2 + (size_t)T16 * 20 * 512; c.hoff = lane * 16;
#pragma unroll
  for (int j = 0; j < 2; ++j) { c.abase[j] = lds_addr(smem) + 2 * j * K5_SLOT + lane * 16; asm volatile("" : "+v"(c.abase[j])); }
  u32x4 tok[10][4];
  k5_tall<0>(c, tok[0]); k5_wall<0>(c);
  k5_tall<1>(c, tok[1]); k5_wall<1>(c);
  f32x4 acc[14][2];
#pragma unroll
  for (int u = 0; u < 14; ++u) { acc[u][0] = f32x4{0.f, 0.f, 0.f, 0.f}; acc[u][1] = f32x4{0.f, 0.f, 0.f, 0.f}; }
  STAMP(4, 3);
  wait_vm<11>();
  __builtin_amdgcn_s_barrier();
  tie4(tok[0]);
  STAMP(4, 4); SLAB(0);
  s16x8 fr[K5_PD + 1];
  fr[0] = k5_rd<0>(c); fr[1] = k5_rd<1>(c); fr[2] = k5_rd<2>(c); fr[3] = k5_rd<3>(c);
  __builtin_amdgcn_sched_barrier(0);
  k5_all(c, acc, fr, tok, std::make_integer_sequence<int, 280>{});
  STAMP(4, 5); SLAB(10);
#pragma unroll
  for (int tl = 0; tl < 2; ++tl) {
    u16* hdst = hb + ((size_t)(T16 + tl) * 54 * 64 + lane) * 8;
#pragma unroll
    for (int u = 0; u < 7; ++u) {
      if (u < U) {
        const f32x4 a = acc[2 * u][tl], b = acc[2 * u + 1][tl];
        const int un = u0 + u;
        *(u32x2*)(hdst + (un >> 1) * 512 + (un & 1) * 4) = u32x2{pk_bf(a[0] * b[0], a[1] * b[1]), pk_bf(a[2] * b[2], a[3] * b[3])};
      }
    }
  }
  STAMP(4, 7);
}

constexpr int K6_NS = 4;
struct K6Ctx { const u16* wsrc; const u16* hsrc; u32 hoff; };
struct K6Grp { u32x4 a[5]; u32x4 t[4]; };
template <int i> DI void k6_v1(const K6Ctx& c, int ks, K6Grp& gr) {
  if constexpr (i < 5) gload16_s<0>(gr.a[i], c.wsrc + (ks + i * 54) * 512, c.hoff);
  else gload16_s<0>(gr.t[i - 5], c.hsrc + (ks + (i - 5) * 54) * 512, c.hoff);
}
DI void k6_issue(const K6Ctx& c, int ks, K6Grp& gr) {
  k6_v1<0>(c, ks, gr); k6_v1<5>(c, ks, gr); k6_v1<6>(c, ks, gr); k6_v1<7>(c, ks, gr); k6_v1<8>(c, ks, gr);
  k6_v1<1>(c, ks, gr); k6_v1<2>(c, ks, gr); k6_v1<3>(c, ks, gr); k6_v1<4>(c, ks, gr);
}
DI void k6_tie(K6Grp& g) { asm volatile("" : "+v"(g.a[0]), "+v"(g.a[1]), "+v"(g.a[2]), "+v"(g.a[3]), "+v"(g.a[4]), "+v"(g.t[0]), "+v"(g.t[1]), "+v"(g.t[2]), "+v"(g.t[3])); }
template <int i, int NK> DI void k6_step(const K6Ctx& c, int ks0, f32x4 (&acc)[5][4], K6Grp (&gr)[K6_NS]) {
  constexpr int later = (NK - 1 - i) < 2 ? (NK - 1 - i) : 2;
  wait_vm<9 * later>();
  K6Grp& g = gr[i % K6_NS];
  k6_tie(g);
  K6Grp& nx = gr[(i + 3) % K6_NS];
#pragma unroll
  for (int nt = 0; nt < 5; ++nt) {
    if constexpr (i + 3 < NK) {
      if (nt == 0) { k6_v1<0>(c, ks0 + i + 3, nx); k6_v1<5>(c, ks0 + i + 3, nx); }
      if (nt == 1) { k6_v1<6>(c, ks0 + i + 3, nx); k6_v1<7>(c, ks0 + i + 3, nx); }
      if (nt == 2) { k6_v1<8>(c, ks0 + i + 3, nx); k6_v1<1>(c, ks0 + i + 3, nx); }
      if (nt == 3) { k6_v1<2>(c, ks0 + i + 3, nx); k6_v1<3>(c, ks0 + i + 3, nx); }
      if (nt == 4) { k6_v1<4>(c, ks0 + i + 3, nx); }
    }
#pragma unroll
    for (int t = 0; t < 4; ++t) acc[nt][t] = mfma16(as8(g.a[nt]), as8(g.t[t]), acc[nt][t]);
    __builtin_amdgcn_sched_barrier(0);
  }
}
template <int i, int NK> struct K6Loop { static DI void run(const K6Ctx& c, int ks0, f32x4 (&acc)[5][4], K6Grp (&gr)[K6_NS]) { k6_step<i, NK>(c, ks0, acc, gr); K6Loop<i + 1, NK>::run(c, ks0, acc, gr); } };
template <int NK> struct K6Loop<NK, NK> { static DI void run(const K6Ctx&, int, f32x4 (&)[5][4], K6Grp (&)[K6_NS]) {} };
template <int NK> DI void k6_main(const K6Ctx& cx, int ks0, f32x4 (&acc)[5][4]) {
  K6Grp gr[K6_NS];
  k6_issue(cx, ks0, gr[0]); k6_issue(cx, ks0 + 1, gr[1]); k6_issue(cx, ks0 + 2, gr[2]);
  WSTAMP(0);
  K6Loop<0, NK>::run(cx, ks0, acc, gr);
  WSTAMP(1);
}
__global__ __launch_bounds__(256, 2) void k6_down(const u16* __restrict__ hb, const u16* __restrict__ Wd, const u16* __restrict__ x1, const float* __restrict__ mod, u16* __restrict__ out) {
  STAMP_DECL; STAMP(5, 0);
  extern __shared__ __attribute__((aligned(16))) char smem[];
  const int tid = threadIdx.x, lane = tid & 63, w = __builtin_amdgcn_readfirstlane(tid >> 6), c = lane & 15, g = lane >> 4;
  const int xcd = blockIdx.x & 7, li = blockIdx.x >> 3;
  const int tt = 8 * (xcd & 3) + (li & 7), nb = 4 * (xcd >> 2) + (li >> 3);
  const int n0 = 80 * nb;
  const int ks0 = w < 2 ? 14 * w : 28 + 13 * (w - 2);
  const int tau = 64 * tt + 16 * w + c, srow = (tau >> 3) + 256 * (tau & 7);
  f32x4 gb[5]; u32x2 xr[5];
#pragma unroll
  for (int nt = 0; nt < 5; ++nt) { const int n = n0 + 16 * nt + 4 * g; gb[nt] = *(const f32x4*)(mod + 5 * 640 + n); xr[nt] = *(const u32x2*)(x1 + (size_t)tau * D_ + n); }
  K6Ctx cx;
  cx.wsrc = Wd + (size_t)(5 * nb) * 54 * 512; cx.hsrc = hb + (size_t)(4 * tt) * 54 * 512; cx.hoff = lane * 16;
  f32x4 acc[5][4];
#pragma unroll
  for (int a = 0; a < 5; ++a)
#pragma unroll
    for (int b = 0; b < 4; ++b) acc[a][b] = f32x4{0.f, 0.f, 0.f, 0.f};
  if (w < 2) k6_main<14>(cx, ks0, acc); else k6_main<13>(cx, ks0, acc);
  STAMP(5, 2);
  f32x4* red = (f32x4*)smem;
#pragma unroll
  for (int nt = 0; nt < 5; ++nt)
#pragma unroll
    for (int t = 0; t < 4; ++t) if (t != w) red[((w * 5 + nt) * 4 + t) * 64 + lane] = acc[nt][t];
  __syncthreads();
#pragma unroll
  for (int nt = 0; nt < 5; ++nt) {
    f32x4 v = w == 0 ? acc[nt][0] : w == 1 ? acc[nt][1] : w == 2 ? acc[nt][2] : acc[nt][3];
#pragma unroll
    for (int ww = 1; ww < 4; ++ww) v += red[((((w + ww) & 3) * 5 + nt) * 4 + w) * 64 + lane];
    const int n = n0 + 16 * nt + 4 * g;
    float o0 = bf_lo(xr[nt][0]) + rbf(gb[nt][0] * rbf(v[0])), o1 = bf_hi(xr[nt][0]) + rbf(gb[nt][1] * rbf(v[1]));
    float o2 = bf_lo(xr[nt][1]) + rbf(gb[nt][2] * rbf(v[2])), o3 = bf_hi(xr[nt][1]) + rbf(gb[nt][3] * rbf(v[3]));
    *(u32x2*)(out + (size_t)srow * D_ + n) = u32x2{pk_bf(o0, o1), pk_bf(o2, o3)};
  }
  STAMP(5, 7);
}

__global__ void dbg_print() {
  unsigned long long t0 = ~0ull;
  for (int k = 0; k < 6; ++k) for (int b = 0; b < 512; ++b) if (g_stamps[k][b][0]) t0 = min(t0, g_stamps[k][b][0]);
  for (int k = 0; k < 6; ++k) {
    unsigned long long s0 = ~0ull, s1 = 0, e0 = ~0ull, e1 = 0; double ph[8]; int n = 0; for (int i = 0; i < 8; ++i) ph[i] = 0;
    for (int b = 0; b < 512; ++b) if (g_stamps[k][b][0] && g_stamps[k][b][7]) { s0 = min(s0, g_stamps[k][b][0]); s1 = max(s1, g_stamps[k][b][0]); e0 = min(e0, g_stamps[k][b][7]); e1 = max(e1, g_stamps[k][b][7]); ++n;
      for (int i = 1; i < 8; ++i) if (g_cyc[k][b][i]) ph[i] += (double)(g_cyc[k][b][i] - g_cyc[k][b][0]); }
    if (n) printf("DBG k%d n=%d start %.2f..%.2f end %.2f..%.2f | cum cycles %.0f %.0f %.0f %.0f %.0f %.0f %.0f\n", k + 1, n, (s0 - t0) / 100.0, (s1 - t0) / 100.0, (e0 - t0) / 100.0, (e1 - t0) / 100.0, ph[1] / n, ph[2] / n, ph[3] / n, ph[4] / n, ph[5] / n, ph[6] / n, ph[7] / n);
  }
  for (int k = 0; k < 6; ++k) for (int b = 0; b < 512; ++b) for (int i = 0; i < 8; ++i) { g_stamps[k][b][i] = 0; g_cyc[k][b][i] = 0; }
}
constexpr size_t WS_MOD = 0, WS_XT = 16384, WS_ZT = WS_XT + 8 * 21 * 256 * 16 * 4, WS_X1 = WS_ZT + 8 * 21 * 256 * 16 * 4,
                 WS_H2 = WS_X1 + 2048 * 640 * 2, WS_HB = WS_H2 + 2048 * 640 * 2, WS_WGU = WS_HB + 2048 * 1728 * 2, WS_WD = WS_WGU + 2 * 640 * 1728 * 2, WS_WP = WS_WD + 640 * 1728 * 2, WS_SYNC = WS_WP + 168 * 16384;
extern "C" void kernel_launch(void* const* d_in, const int* in_sizes, int n_in, void* d_out, int out_size, void* d_ws, size_t ws_size,
                              hipStream_t stream) {
  const u16* x = (const u16*)d_in[0]; const u16* wr = (const u16*)d_in[1]; const u16* wi = (const u16*)d_in[2];
  const u16* wg = (const u16*)d_in[4]; const u16* wu = (const u16*)d_in[5]; const u16* wd = (const u16*)d_in[6];
  const u16* c = (const u16*)d_in[7]; const u16* wmod = (const u16*)d_in[8];
  char* ws = (char*)d_ws;
  float* mod = (float*)(ws + WS_MOD); u32* XT = (u32*)(ws + WS_XT); u32* ZT = (u32*)(ws + WS_ZT);
  u16* x1 = (u16*)(ws + WS_X1); u16* h2 = (u16*)(ws + WS_H2); u16* hb = (u16*)(ws + WS_HB);
  u16* Wgu = (u16*)(ws + WS_WGU); u16* Wd = (u16*)(ws + WS_WD);
  K2Args A;
  A.x = x; A.c = c; A.wmod = wmod; A.mod = mod; A.XT = XT; A.wr = wr; A.wi = wi; A.wg = wg; A.wu = wu; A.Wgu = Wgu; A.Wp20 = (u32x4*)(ws + WS_WP);
  A.gen = (u32*)(ws + WS_SYNC); A.arr = (u32*)(ws + WS_SYNC + 4096); A.gran = (unsigned long long*)(ws + WS_SYNC + 8192);
  k2_fwd<<<K2_GRID, 640, 0, stream>>>(A);
  k3_mid<<<168, 512, 0, stream>>>(XT, wr, wi, A.Wp20, ZT);
  k4_inv<<<256, 640, 0, stream>>>(ZT, x, mod, x1, h2, Wgu, wd, Wd);
  hipFuncSetAttribute((const void*)k5_glu, hipFuncAttributeMaxDynamicSharedMemorySize, K5_NS * K5_SLOT);
  k5_glu<<<256, 256, K5_NS * K5_SLOT, stream>>>(h2, Wgu, hb, Wd);
  hipFuncSetAttribute((const void*)k6_down, hipFuncAttributeMaxDynamicSharedMemorySize, 81920);
  k6_down<<<256, 256, 81920, stream>>>(hb, Wd, x1, mod, (u16*)d_out);
  dbg_print<<<1, 1, 0, stream>>>();
}
```

```cpp
#include <hip/hip_runtime.h>
#include <hip/hip_bf16.h>
#include <stdint.h>
#include <utility>

typedef unsigned short u16;
typedef unsigned int u32;
typedef short s16x4 __attribute__((ext_vector_type(4)));
typedef short s16x8 __attribute__((ext_vector_type(8)));
typedef __bf16 bf2_t __attribute__((ext_vector_type(2)));
typedef float f32x2 __attribute__((ext_vector_type(2)));
typedef float f32x4 __attribute__((ext_vector_type(4)));
typedef float f32x16 __attribute__((ext_vector_type(16)));
typedef u32 u32x2 __attribute__((ext_vector_type(2)));
typedef u32 u32x4 __attribute__((ext_vector_type(4)));
#define DI __device__ __forceinline__
#define LDSP(T) __attribute__((address_space(3))) T*

constexpr int S_ = 2048, D_ = 640, F_ = 1728, DF = 321;

constexpr double kPi = 3.14159265358979323846264338327950288;
constexpr double c_sin0(double x) { double x2 = x * x, t = x, s = x; for (int i = 1; i < 11; ++i) { t *= -x2 / ((2.0 * i) * (2.0 * i + 1.0)); s += t; } return s; }
constexpr double c_cos0(double x) { double x2 = x * x, t = 1.0, s = 1.0; for (int i = 1; i < 11; ++i) { t *= -x2 / ((2.0 * i - 1.0) * (2.0 * i)); s += t; } return s; }
constexpr double c_cos(long n, long N) {
  n %= N; if (n < 0) n += N;
  if (2 * n > N) n = N - n;
  bool neg = false;
  if (4 * n > N) { neg = true; n = N - 2 * n; N = 2 * N; n = n;   }
  double r = 0;
  if (4 * n == N) r = 0.0;
  else if (8 * n > N) r = c_sin0(2 * kPi * (double)(N - 4 * n) / (double)(4 * N));
  else r = c_cos0(2 * kPi * (double)n / (double)N);
  return neg ? -r : r;
}
constexpr double c_sin(long n, long N) { return c_cos(4 * n - N, 4 * N); }
constexpr u16 c_bf16(double v) {
  float f = (float)v; u32 u = __builtin_bit_cast(u32, f);
  u += 0x7fffu + ((u >> 16) & 1u); return (u16)(u >> 16);
}

struct F8w { u16 v[2][64][8]; };
struct F8x6 { u16 v[3][2][64][8]; };
struct TwA { float v[2][64][4]; };
struct TwC { float v[3][64][4]; };
constexpr F8w mk_f32a() {
  F8w t{};
  for (int tt = 0; tt < 2; ++tt) for (int l = 0; l < 64; ++l) for (int j = 0; j < 8; ++j) {
    int o = 16 * tt + (l & 15), d1 = 8 * (l >> 4) + j; double v = 0;
    if (o == 0) v = 1.0; else if (o == 1) v = (d1 & 1) ? -1.0 : 1.0;
    else { int k1 = o >> 1; v = (o & 1) ? -c_sin(d1 * k1, 32) : c_cos(d1 * k1, 32); }
    t.v[tt][l][j] = c_bf16(v);
  }
  return t;
}
constexpr TwA mk_twa(int j) {
  TwA t{};
  for (int tt = 0; tt < 2; ++tt) for (int l = 0; l < 64; ++l) for (int rr = 0; rr < 2; ++rr) {
    int c = l & 15, g = l >> 4, Q = 4 * j + (c >> 2), d0 = 4 * (Q % 5) + (c & 3), rho = Q / 5;
    int k1 = 8 * tt + 2 * g + rr; if (k1 == 0) k1 = 16;
    double co = c_cos(d0 * k1, 640), si = -c_sin(d0 * k1, 640);
    if (rho >= 3 && (rho & 1)) { double x = co; co = -si; si = x; }
    t.v[tt][l][2 * rr] = (float)co; t.v[tt][l][2 * rr + 1] = (float)si;
  }
  return t;
}
constexpr F8x6 mk_f20(bool inv) {
  F8x6 t{};
  for (int mt = 0; mt < 3; ++mt) for (int ks = 0; ks < 2; ++ks) for (int l = 0; l < 64; ++l) for (int j = 0; j < 8; ++j) {
    int row = l & 15, g = l >> 4, k0 = 8 * mt + (row >> 1), part = row & 1, d0 = 16 * ks + 4 * g + (j >> 1), pp = j & 1; double v = 0;
    if (d0 < 20 && k0 < 20) {
      double co = c_cos(d0 * k0, 20), si = inv ? -c_sin(d0 * k0, 20) : c_sin(d0 * k0, 20);
      v = part == 0 ? (pp == 0 ? co : si) : (pp == 0 ? -si : co);
    }
    t.v[mt][ks][l][j] = c_bf16(v);
  }
  return t;
}
constexpr F8w mk_a1(int m1, bool inv) {
  F8w t{};
  for (int l = 0; l < 64; ++l) for (int i = 0; i < 4; ++i) {
    int c = l & 15, a = 4 * (l >> 4) + i, n = inv ? c * (8 * a + m1) : a * (8 * c + m1);
    double co = c_cos(n, 128), si = inv ? -c_sin(n, 128) : c_sin(n, 128);
    t.v[0][l][2 * i] = c_bf16(co); t.v[0][l][2 * i + 1] = c_bf16(si);
    t.v[1][l][2 * i] = c_bf16(-si); t.v[1][l][2 * i + 1] = c_bf16(co);
  }
  return t;
}
constexpr F8w mk_f16(bool inv) {
  F8w t{};
  for (int l = 0; l < 64; ++l) for (int i = 0; i < 4; ++i) {
    int c = l & 15, a = 4 * (l >> 4) + i;
    double co = c_cos(a * c, 16), si = c_sin(a * c, 16);
    if (!inv) { t.v[0][l][2 * i] = c_bf16(co); t.v[0][l][2 * i + 1] = c_bf16(si); t.v[1][l][2 * i] = c_bf16(-si); t.v[1][l][2 * i + 1] = c_bf16(co); }
    else { t.v[0][l][i] = c_bf16(co); t.v[0][l][4 + i] = c_bf16(-si); t.v[1][l][i] = c_bf16(si); t.v[1][l][4 + i] = c_bf16(co); }
  }
  return t;
}
constexpr TwC mk_twc(int kind) {
  TwC t{};
  const double sc = 1.0 / (2048.0 * 640.0);
  for (int mt = 0; mt < 3; ++mt) for (int l = 0; l < 64; ++l) for (int rr = 0; rr < 2; ++rr) {
    int k1 = kind == 2 ? 16 : (l & 15), g = l >> 4, d0 = 8 * mt + 2 * g + rr, rho = l & 7;
    double co = c_cos(d0 * k1, 640) * sc * (kind == 2 ? 2 : 1), si = c_sin(d0 * k1, 640) * sc * (kind == 2 ? 2 : 1);
    if (kind == 1 || (kind == 2 && rho >= 3 && (rho & 1))) { double x = co; co = -si; si = x; }
    t.v[mt][l][2 * rr] = (float)co; t.v[mt][l][2 * rr + 1] = (float)si;
  }
  return t;
}
constexpr F8w mk_f32bi() {
  F8w t{};
  for (int nt = 0; nt < 2; ++nt) for (int l = 0; l < 64; ++l) for (int j = 0; j < 8; ++j) {
    int d1 = 16 * nt + (l & 15), K = 8 * (l >> 4) + j; double v = 0;
    if (K == 0) v = 1.0; else if (K == 1) v = (d1 & 1) ? -1.0 : 1.0;
    else { int k1 = K >> 1; v = (K & 1) ? -2.0 * c_sin(k1 * d1, 32) : 2.0 * c_cos(k1 * d1, 32); }
    t.v[nt][l][j] = c_bf16(v);
  }
  return t;
}
static __device__ const F8w g_f32a = mk_f32a();
static __device__ const F8x6 g_f20a = mk_f20(false);
static __device__ const F8x6 g_f20ai = mk_f20(true);
static __device__ const F8w g_a1[8] = {mk_a1(0, false), mk_a1(1, false), mk_a1(2, false), mk_a1(3, false), mk_a1(4, false), mk_a1(5, false), mk_a1(6, false), mk_a1(7, false)};
static __device__ const F8w g_a1i[8] = {mk_a1(0, true), mk_a1(1, true), mk_a1(2, true), mk_a1(3, true), mk_a1(4, true), mk_a1(5, true), mk_a1(6, true), mk_a1(7, true)};
static __device__ const F8w g_f16f = mk_f16(false);
static __device__ const F8w g_f16i = mk_f16(true);
static __device__ const F8w g_f32bi = mk_f32bi();

#define WAITALL() do {} while (0)
#define STAMP_DECL
#define STAMP(k, i) do {} while (0)
#define SLAB(i) do {} while (0)
#define WSTAMP(j) do {} while (0)
DI float bf_lo(u32 v) { return __uint_as_float(v << 16); }
DI float bf_hi(u32 v) { return __uint_as_float(v & 0xffff0000u); }
DI float bf2f(u16 v) { return __uint_as_float(((u32)v) << 16); }
DI u32 pk_bf(float a, float b) { f32x2 f = {a, b}; return __builtin_bit_cast(u32, __builtin_convertvector(f, bf2_t)); }
DI float rbf(float a) { return bf_lo(pk_bf(a, a)); }
DI float dpp_x1(float v) { return __int_as_float(__builtin_amdgcn_mov_dpp(__float_as_int(v), 0xB1, 0xf, 0xf, true)); }
DI float dpp_x2(float v) { return __int_as_float(__builtin_amdgcn_mov_dpp(__float_as_int(v), 0x4E, 0xf, 0xf, true)); }
DI float dpp_hm(float v) { return __int_as_float(__builtin_amdgcn_mov_dpp(__float_as_int(v), 0x141, 0xf, 0xf, true)); }
DI float dpp_rm(float v) { return __int_as_float(__builtin_amdgcn_mov_dpp(__float_as_int(v), 0x140, 0xf, 0xf, true)); }
DI f32x4 mfma16(s16x8 a, s16x8 b, f32x4 c) { return __builtin_amdgcn_mfma_f32_16x16x32_bf16(a, b, c, 0, 0, 0); }
DI f32x16 mfma32(s16x8 a, s16x8 b, f32x16 c) { return __builtin_amdgcn_mfma_f32_32x32x16_bf16(a, b, c, 0, 0, 0); }
DI s16x4 tr_read(const void* p) { return __builtin_amdgcn_ds_read_tr16_b64_v4i16((LDSP(s16x4))p); }
template <class T> DI void pin(T& v) { asm volatile("" : "+v"(v)); }
DI void l2_touch(const void* base, u32 voff, u32 lds) {
  asm volatile("s_mov_b32 m0, %0\n\ts_nop 0\n\tglobal_load_lds_dword %1, %2" :: "s"(lds), "v"(voff), "s"(base) : "memory");
}
DI void side_delay(int ticks10ns) {
  const unsigned long long t0 = __builtin_amdgcn_s_memrealtime();
  while (__builtin_amdgcn_s_memrealtime() - t0 < (unsigned long long)ticks10ns) __builtin_amdgcn_s_sleep(4);
}
#define GSP(T) __attribute__((address_space(1))) T*
template <class T> DI T ldg(const void* sbase, u32 voff) { GSP(const char) p = (GSP(const char))sbase; asm("" : "+s"(p)); return *(GSP(const T))(p + voff); }
template <class T> DI void stg(void* sbase, u32 voff, T v) { GSP(char) p = (GSP(char))sbase; asm("" : "+s"(p)); *(GSP(T))(p + voff) = v; }
DI void stg_wt(void* sbase, u32 voff, u32x4 v) { asm volatile("global_store_dwordx4 %0, %1, %2 sc1\n\ts_nop 1" :: "v"(voff), "v"(v), "s"(sbase) : "memory"); }
DI void stg_wt(void* sbase, u32 voff, u32x2 v) { asm volatile("global_store_dwordx2 %0, %1, %2 sc1\n\ts_nop 1" :: "v"(voff), "v"(v), "s"(sbase) : "memory"); }
DI void stg_wt(void* sbase, u32 voff, u32 v) { asm volatile("global_store_dword %0, %1, %2 sc1" :: "v"(voff), "v"(v), "s"(sbase) : "memory"); }
#define WT_MASK 127
template <int BIT, class T> DI void stg_sel(void* sbase, u32 voff, T v) { if constexpr ((WT_MASK & BIT) != 0) stg_wt(sbase, voff, v); else stg(sbase, voff, v); }
DI u32 lds_addr(const void* p) { return (u32)(size_t)(LDSP(const char))p; }
DI s16x8 cat8(s16x4 a, s16x4 b) { return __builtin_shufflevector(a, b, 0, 1, 2, 3, 4, 5, 6, 7); }
DI s16x8 as8(u32x4 v) { return __builtin_bit_cast(s16x8, v); }
DI float dot2bf(u32 a, u32 b, float c) { return __builtin_amdgcn_fdot2_f32_bf16(__builtin_bit_cast(bf2_t, a), __builtin_bit_cast(bf2_t, b), c, false); }

DI float reduce16(const float (&v)[16], int lane) {
  float a[8], b[4], c[2], d;
  const bool s3 = lane & 8, s2 = lane & 4, s1 = lane & 2, s0 = lane & 1;
#pragma unroll
  for (int i = 0; i < 8; ++i) { float keep = s3 ? v[8 + i] : v[i], send = s3 ? v[i] : v[8 + i]; a[i] = keep + dpp_rm(send); }
#pragma unroll
  for (int i = 0; i < 4; ++i) { float keep = s2 ? a[4 + i] : a[i], send = s2 ? a[i] : a[4 + i]; b[i] = keep + dpp_hm(send); }
#pragma unroll
  for (int i = 0; i < 2; ++i) { float keep = s1 ? b[2 + i] : b[i], send = s1 ? b[i] : b[2 + i]; c[i] = keep + dpp_x2(send); }
  { float keep = s0 ? c[1] : c[0], send = s0 ? c[0] : c[1]; d = keep + dpp_x1(send); }
  auto r16 = __builtin_amdgcn_permlane16_swap(__float_as_uint(d), __float_as_uint(d), false, false);
  d = __uint_as_float(r16[0]) + __uint_as_float(r16[1]);
  auto r32 = __builtin_amdgcn_permlane32_swap(__float_as_uint(d), __float_as_uint(d), false, false);
  return __uint_as_float(r32[0]) + __uint_as_float(r32[1]);
}

DI void ln_finish(float s, float q, float& m_, float& i_) {
  const float mean = s * (1.0f / 640.0f);
  const float var = fmaxf(q * (1.0f / 640.0f) - mean * mean, 0.f);
  const float sd = rbf(__builtin_amdgcn_sqrtf(rbf(rbf(var) + 1e-5f)));
  m_ = rbf(mean); i_ = __builtin_amdgcn_rcpf(sd);
}
DI void ln_stats5(const u32 (&xv)[8], float* part  , int tid, float (&a_)[8], float (&b_)[8]) {
  const int lane = tid & 63, w = tid >> 6;
  float v[16];
#pragma unroll
  for (int r = 0; r < 8; ++r) { v[r] = dot2bf(xv[r], 0x3f803f80u, 0.f); v[8 + r] = dot2bf(xv[r], xv[r], 0.f); }
  float t = reduce16(v, lane);
  if (lane < 16) part[w * 16 + lane] = t;
  __syncthreads();
  const int r = lane & 7;
  float s = 0.f, q = 0.f;
#pragma unroll
  for (int i = 0; i < 5; ++i) { s += part[i * 16 + r]; q += part[i * 16 + 8 + r]; }
  float m_, i_; ln_finish(s, q, m_, i_);
  const float nb = -m_ * i_;
#pragma unroll
  for (int i = 0; i < 8; ++i) {
    a_[i] = __int_as_float(__builtin_amdgcn_readlane(__float_as_int(i_), i));
    b_[i] = __int_as_float(__builtin_amdgcn_readlane(__float_as_int(nb), i));
  }
}
DI float reduce8(const float (&v)[8], int lane) {
  float b[4], c[2], d;
  const bool s2 = lane & 4, s1 = lane & 2, s0 = lane & 1;
#pragma unroll
  for (int i = 0; i < 4; ++i) { float keep = s2 ? v[4 + i] : v[i], send = s2 ? v[i] : v[4 + i]; b[i] = keep + dpp_hm(send); }
#pragma unroll
  for (int i = 0; i < 2; ++i) { float keep = s1 ? b[2 + i] : b[i], send = s1 ? b[i] : b[2 + i]; c[i] = keep + dpp_x2(send); }
  { float keep = s0 ? c[1] : c[0], send = s0 ? c[0] : c[1]; d = keep + dpp_x1(send); }
  d += __int_as_float(__builtin_amdgcn_mov_dpp(__float_as_int(d), 0x128, 0xf, 0xf, true));
  auto r16 = __builtin_amdgcn_permlane16_swap(__float_as_uint(d), __float_as_uint(d), false, false);
  d = __uint_as_float(r16[0]) + __uint_as_float(r16[1]);
  auto r32 = __builtin_amdgcn_permlane32_swap(__float_as_uint(d), __float_as_uint(d), false, false);
  return __uint_as_float(r32[0]) + __uint_as_float(r32[1]);
}

template <int N> DI void wait_vm() { asm volatile("s_waitcnt vmcnt(%0)" ::"n"(N) : "memory"); }
template <int OFF> DI void gload16_s(u32x4& d, const void* sbase, u32 voff) {
  asm volatile("global_load_dwordx4 %0, %1, %2 offset:%3" : "=v"(d) : "v"(voff), "s"(sbase), "n"(OFF) : "memory");
}
template <int OFF> DI void glds16_s(const void* sbase, u32 voff) {
  asm volatile("global_load_lds_dwordx4 %0, %1 offset:%2" :: "v"(voff), "s"(sbase), "n"(OFF) : "memory");
}
DI void dma1k(const void* sbase, u32 voff, u32 lds) {
  asm volatile("s_mov_b32 m0, %0\n\ts_nop 0\n\tglobal_load_lds_dwordx4 %1, %2" :: "s"(lds), "v"(voff), "s"(sbase) : "memory");
}
DI void set_m0(u32 v) { asm volatile("s_mov_b32 m0, %0\n\ts_nop 0" :: "s"(v) : "memory"); }
DI void tie4(u32x4 (&t)[4]) { asm volatile("" : "+v"(t[0]), "+v"(t[1]), "+v"(t[2]), "+v"(t[3])); }

DI void prep_load(u32x4 (&v)[4], const u16* __restrict__ src, int ld, int lane) {
  const u32 vo = (u32)((lane >> 3) * ld + 8 * (lane & 7)) * 2u;
#pragma unroll
  for (int i = 0; i < 4; ++i) v[i] = ldg<u32x4>(src + (size_t)8 * i * ld, vo);
}
DI void prep_store(const u32x4 (&v)[4], u16* __restrict__ dst, int jstride, char* lds, int lane) {
#pragma unroll
  for (int i = 0; i < 4; ++i) {
    const int row = 8 * i + (lane >> 3), ch = lane & 7;
    *(u32x4*)(lds + row * 128 + (((ch >> 1) ^ ((row >> 1) & 3)) * 32) + (ch & 1) * 16) = v[i];
  }
  const int c = lane & 15, g = lane >> 4, q = c >> 2, p = c & 3, r = 4 * g + q;
#pragma unroll
  for (int j = 0; j < 4; ++j) {
    const char* b = lds + r * 128 + ((j ^ ((r >> 1) & 3)) * 32) + 8 * p;
    const s16x8 f = cat8(tr_read(b), tr_read(b + 16 * 128));
    stg_sel<1>(dst + (size_t)j * jstride, (u32)lane * 16u, __builtin_bit_cast(u32x4, f));
  }
}
DI void prep_wave(const u16* __restrict__ src, int ld, u16* __restrict__ dst, int jstride, char* lds, int lane) {
  u32x4 v[4]; prep_load(v, src, ld, lane); prep_store(v, dst, jstride, lds, lane);
}
DI void prep_gu(int task, const u16* wg, const u16* wu, u16* Wgu, char* lds, int lane) {
  const int mat = task >= 540, tk = task - 540 * mat, cg = tk / 20, ks = tk % 20;
  prep_wave((mat ? wu : wg) + (size_t)ks * 32 * F_ + 64 * cg, F_, Wgu + ((size_t)(4 * cg * 20 + ks) * 2 + mat) * 512, 20 * 2 * 512, lds, lane);
}
DI void prep_d(int task, const u16* wd, u16* Wd, char* lds, int lane) {
  const int cg = task / 54, ks = task % 54;
  prep_wave(wd + (size_t)ks * 32 * D_ + 64 * cg, D_, Wd + (size_t)(4 * cg * 54 + ks) * 512, 54 * 512, lds, lane);
}

typedef __attribute__((address_space(1))) u32 gu32;
typedef __attribute__((address_space(1))) unsigned long long gu64;
DI u32 ld_agent(const u32* p) { return __hip_atomic_load((const gu32*)p, __ATOMIC_RELAXED, __HIP_MEMORY_SCOPE_AGENT); }
DI void st_agent(u32* p, u32 v) { __hip_atomic_store((gu32*)p, v, __ATOMIC_RELAXED, __HIP_MEMORY_SCOPE_AGENT); }
DI void st_agent64(unsigned long long* p, unsigned long long v) { __hip_atomic_store((gu64*)p, v, __ATOMIC_RELAXED, __HIP_MEMORY_SCOPE_AGENT); }
#define K2_SIDE_DELAY 80
#define K4_SIDE_DELAY 60
constexpr int SPIN_LIMIT = 1 << 16;

template <int NR> DI void gemv_part(int col0, int row0, u32 tagv, bool publish, unsigned long long* gran, int gidx0, const u16* __restrict__ c, const u16* __restrict__ wmod,
                                    float* __restrict__ mod, char* lds) {
  float* sl = (float*)lds; float* part = sl + 640;
  const int t = threadIdx.x, lane = t & 63, w = t >> 6, cq = t & 7, rg = t >> 3;
  const u16 cv = ldg<u16>(c, 2 * t);
  u32x4 wv[NR];
  const u32 wo = (u32)((row0 + rg) * 3840 + col0 + 8 * cq) * 2u;
#pragma unroll
  for (int i = 0; i < NR; ++i) wv[i] = ldg<u32x4>(wmod + (size_t)80 * i * 3840, wo);
  { const float x = bf2f(cv); sl[t] = rbf(x * __builtin_amdgcn_rcpf(1.0f + __expf(-x))); }
  __syncthreads();
  float acc[8] = {};
#pragma unroll
  for (int i = 0; i < NR; ++i) {
    const float s = sl[row0 + rg + 80 * i];
#pragma unroll
    for (int k = 0; k < 4; ++k) { acc[2 * k] += s * bf_lo(wv[i][k]); acc[2 * k + 1] += s * bf_hi(wv[i][k]); }
  }
  float u[4];
  const bool b3 = lane & 8;
#pragma unroll
  for (int i = 0; i < 4; ++i) { const float keep = b3 ? acc[4 + i] : acc[i], send = b3 ? acc[i] : acc[4 + i]; u[i] = keep + __int_as_float(__builtin_amdgcn_mov_dpp(__float_as_int(send), 0x128, 0xf, 0xf, true)); }
  float r2[2];
#pragma unroll
  for (int i = 0; i < 2; ++i) { auto x = __builtin_amdgcn_permlane16_swap(__float_as_uint(u[i]), __float_as_uint(u[i + 2]), false, false); r2[i] = __uint_as_float(x[0]) + __uint_as_float(x[1]); }
  auto y = __builtin_amdgcn_permlane32_swap(__float_as_uint(r2[0]), __float_as_uint(r2[1]), false, false);
  part[w * 64 + 8 * cq + 4 * ((lane >> 3) & 1) + 2 * ((lane >> 4) & 1) + (lane >> 5)] = __uint_as_float(y[0]) + __uint_as_float(y[1]);
  __syncthreads();
  if (t < 64) {
    float s = 0.f;
#pragma unroll
    for (int i = 0; i < 10; ++i) s += part[i * 64 + t];
    if (publish) st_agent64(gran + gidx0 + t, ((unsigned long long)(tagv + 1u) << 32) | __float_as_uint(s));
    else mod[col0 + t] = rbf(s);
  }
}

DI int row_group(int b) { return 2 * (8 * (b >> 4) + (b & 7)) + ((b >> 3) & 1); }
DI void sincos_rev(float a, float& s, float& c) { s = __builtin_amdgcn_sinf(a); c = __builtin_amdgcn_cosf(a); }
DI void gram_part(const u32x4 (&f)[4], float* part  , int i, int lane) {
  f32x4 acc = {0.f, 0.f, 0.f, 0.f};
#pragma unroll
  for (int k = 0; k < 4; ++k) acc = mfma16(as8(f[k]), as8(f[k]), acc);
  const int c = lane & 15, g = lane >> 4;
  const float dg = (c & 2) ? ((c & 1) ? acc[3] : acc[2]) : ((c & 1) ? acc[1] : acc[0]);
  if (c < 8 && g == (c >> 2)) part[i * 16 + 8 + c] = dg;
  if (c < 8 && g == 2) part[i * 16 + c] = acc[0];
}
DI void ln_bcast8(const float* part, int lane, float (&a_)[8], float (&b_)[8]) {
  const int r = lane & 7;
  float s = 0.f, q = 0.f;
#pragma unroll
  for (int i = 0; i < 5; ++i) { s += part[i * 16 + r]; q += part[i * 16 + 8 + r]; }
  float m_, i_; ln_finish(s, q, m_, i_);
  const float nb = -m_ * i_;
#pragma unroll
  for (int i = 0; i < 8; ++i) {
    a_[i] = __int_as_float(__builtin_amdgcn_readlane(__float_as_int(i_), i));
    b_[i] = __int_as_float(__builtin_amdgcn_readlane(__float_as_int(nb), i));
  }
}
constexpr int HT_RS = 1296;
struct K2Args { const u16* x; const u16* c; const u16* wmod; float* mod; u32* XT; const u16* wr; const u16* wi; const u16* wg; const u16* wu; u16* Wgu; const u16* wd; u16* Wd; u32* gen; u32* arr; unsigned long long* gran; };
constexpr int K2_MAIN0 = 88, K2_SIDE0 = 344, K2_GRID = 344 + 54;
__global__ __launch_bounds__(640) void k2_fwd(const K2Args A) {
  STAMP_DECL; STAMP(1, 0);
  __shared__ __attribute__((aligned(16))) char smem[32768 + 5 * 4096 + 256];
  const int bid = blockIdx.x;
  if (bid >= K2_SIDE0) {
    side_delay(K2_SIDE_DELAY);
    const int wv = __builtin_amdgcn_readfirstlane(threadIdx.x >> 6);
    prep_d((bid - K2_SIDE0) * 10 + wv, A.wd, A.Wd, smem + 4096 * wv, threadIdx.x & 63);
    STAMP(1, 6);
    return;
  }
  if (bid >= 40 && bid < 80) {
    side_delay(K2_SIDE_DELAY);
    gemv_part<8>(1280 + 64 * (bid - 40), 0, 0u, false, nullptr, 0, A.c, A.wmod, A.mod, smem); STAMP(0, 6); return;
  }
  if (bid >= 80 && bid < 87) return;
  if (bid < 40) {
    const u32 tagv = ld_agent(A.gen);
    gemv_part<4>(64 * (bid >> 1), 320 * (bid & 1), tagv, true, A.gran, 1280 * (bid & 1) + 64 * (bid >> 1), A.c, A.wmod, A.mod, smem);
    if (threadIdx.x == 0) st_agent(A.arr + bid, tagv + 1u);
    STAMP(0, 7); return;
  }
  if (bid == 87) {
    const u32 tag = ld_agent(A.gen) + 1u;
    const int t = threadIdx.x, who = t < 40 ? t : t + 48;
    for (int spin = 0; spin < SPIN_LIMIT; ++spin) {
      const bool ok = t >= 296 || ld_agent(A.arr + who) == tag;
      if (__syncthreads_and(ok)) break;
      __builtin_amdgcn_s_sleep(8);
    }
    if (t == 0) st_agent(A.gen, tag);
    return;
  }
  u32* __restrict__ XT = A.XT;
  char* hT = smem; char* U = smem + 8 * HT_RS; float* part = (float*)(smem + 8 * HT_RS + 8 * 17 * 80);
  char* tab = smem + 8 * HT_RS + 8 * 17 * 80 + 5 * 16 * 4;
  const int tid = threadIdx.x, lane = tid & 63, w = __builtin_amdgcn_readfirstlane(tid >> 6);
  const int s0 = row_group(bid - K2_MAIN0);
  const int c = lane & 15, g = lane >> 4, q = c >> 2, p = c & 3;
  const u16* xrow = A.x + (size_t)s0 * D_;
  f32x4 tw0, tw1; u32 ard, awr, aw0, acr; float z0r, z0i;
  auto precompute = [&]() {
    const int Qw = 4 * w + q, rho = Qw / 5, d0 = 4 * (Qw % 5) + p;
    const bool rot = rho >= 3 && (rho & 1);
    const float fd = (float)d0 * (1.0f / 640.0f);
    const int k10 = g == 0 ? 16 : 2 * g;
    float s_[4], c_[4];
    sincos_rev(fd * (float)k10, s_[0], c_[0]); sincos_rev(fd * (float)(2 * g + 1), s_[1], c_[1]);
    sincos_rev(fd * (float)(8 + 2 * g), s_[2], c_[2]); sincos_rev(fd * (float)(9 + 2 * g), s_[3], c_[3]);
    tw0 = f32x4{rot ? s_[0] : c_[0], rot ? c_[0] : -s_[0], rot ? s_[1] : c_[1], rot ? c_[1] : -s_[1]};
    tw1 = f32x4{rot ? s_[2] : c_[2], rot ? c_[2] : -s_[2], rot ? s_[3] : c_[3], rot ? c_[3] : -s_[3]};
    const int Qr = 4 * w + p;
    ard = lds_addr(hT) + (Qr / 5) * HT_RS + 2 * (20 * (8 * g + q) + 4 * (Qr % 5));
    awr = lds_addr(U) + 4 * (rho * 17 * 20 + d0 + 2 * g * 20);
    aw0 = g == 0 ? awr + 4 * 16 * 20 : awr;
    z0r = rot ? 0.f : 1.f; z0i = rot ? 1.f : 0.f;
    acr = lds_addr(U) + (w < 8 ? ((2 * (w >> 1)) * 17 + c) * 80 : ((c & 7) * 17 + 16) * 80);
    pin(tw0); pin(tw1); pin(ard); pin(awr); pin(aw0); pin(z0r); pin(z0i); pin(acr);
  };
  if (w < 5) {
    u32 xv[8];
#pragma unroll
    for (int r = 0; r < 8; ++r) xv[r] = ldg<u32>(xrow + (size_t)r * 256 * D_, 4 * tid);
    const u32 tagv = ld_agent(A.gen);
    precompute();
    STAMP(1, 1);
    WAITALL();
    STAMP(1, 5);
    __syncthreads();
    float na[8], nb[8];
    ln_bcast8(part, lane, na, nb);
    f32x2 o[8];
    {
      f32x2 h[8];
#pragma unroll
      for (int r = 0; r < 8; ++r) { f32x2 v = {bf_lo(xv[r]), bf_hi(xv[r])}; h[r] = v * na[r] + nb[r]; }
      const float cc = 0.70710678118654752f;
      f32x2 a0 = h[0] + h[4], a1 = h[1] + h[5], a2 = h[2] + h[6], a3 = h[3] + h[7];
      f32x2 b0 = h[0] - h[4], b1 = h[1] - h[5], b2 = h[2] - h[6], b3 = h[3] - h[7];
      f32x2 e = a0 + a2, f = a1 + a3, u = cc * (b1 - b3), v = cc * (b1 + b3);
      o[0] = e + f; o[1] = e - f; o[2] = b0 + u; o[3] = -b2 - v; o[4] = a0 - a2; o[5] = a3 - a1; o[6] = b0 - u; o[7] = b2 - v;
#pragma unroll
      for (int r = 0; r < 8; ++r) pin(o[r]);
    }
    const u32 tag = __builtin_amdgcn_readfirstlane(tagv) + 1u;
    if (tid == 0) st_agent(A.arr + bid, tag);
    f32x2 sa, gg;
    {
      const unsigned long long* ga_ = A.gran + 2 * tid;
      u32x4 va, vb, vc, vd;
      for (int spin = 0; spin < SPIN_LIMIT; ++spin) {
        asm volatile("global_load_dwordx4 %0, %4, off sc1\n\tglobal_load_dwordx4 %1, %5, off sc1\n\tglobal_load_dwordx4 %2, %6, off sc1\n\tglobal_load_dwordx4 %3, %7, off sc1\n\ts_waitcnt vmcnt(0)"
                     : "=&v"(va), "=&v"(vb), "=&v"(vc), "=&v"(vd) : "v"(ga_), "v"(ga_ + 1280), "v"(ga_ + 640), "v"(ga_ + 1920) : "memory");
        const bool ok = va[1] == tag && va[3] == tag && vb[1] == tag && vb[3] == tag && vc[1] == tag && vc[3] == tag && vd[1] == tag && vd[3] == tag;
        if (__all(ok)) break;
      }
      sa = f32x2{rbf(__uint_as_float(va[0]) + __uint_as_float(vb[0])), rbf(__uint_as_float(va[2]) + __uint_as_float(vb[2]))};
      gg = f32x2{rbf(1.0f + rbf(__uint_as_float(vc[0]) + __uint_as_float(vd[0]))), rbf(1.0f + rbf(__uint_as_float(vc[2]) + __uint_as_float(vd[2])))};
    }
    STAMP(1, 2);
    o[0] = o[0] * gg + 8.0f * sa;
    *(u32*)(hT + 4 * tid) = pk_bf(o[0][0], o[0][1]);
#pragma unroll
    for (int r = 1; r < 8; ++r) { const f32x2 v = o[r] * gg; *(u32*)(hT + r * HT_RS + 4 * tid) = pk_bf(v[0], v[1]); }
  } else {
    const int i = w - 5;
    u32x4 f[4];
    const u32 xo = (u32)((c & 7) * 256 * D_ + 128 * i + 8 * g) * 2u;
#pragma unroll
    for (int k = 0; k < 4; ++k) f[k] = ldg<u32x4>(xrow, xo + 64 * k);
    const s16x8 t0 = i < 2 ? ldg<s16x8>(g_f32a.v[i < 2 ? i : 0], lane * 16) : ldg<s16x8>(&g_f20a.v[0][0][0][0] + (i - 2) * 512, lane * 16);
    const s16x8 t1 = ldg<s16x8>(&g_f20a.v[0][0][0][0] + (i < 3 ? i + 3 : 5) * 512, lane * 16);
    precompute();
    if (c >= 8) {
      const u32 fill = c == 8 ? 0x3f803f80u : 0u;
#pragma unroll
      for (int k = 0; k < 4; ++k) f[k] = u32x4{fill, fill, fill, fill};
    }
    gram_part(f, part, i, lane);
    *(s16x8*)(tab + i * 1024 + lane * 16) = t0;
    if (i < 3) *(s16x8*)(tab + (i + 5) * 1024 + lane * 16) = t1;
    __syncthreads();
    const int task = (bid - K2_MAIN0) * 5 + i;
    if (task < 1080) prep_gu(task, A.wg, A.wu, A.Wgu, smem + 32768 + 4096 * i, lane);
    if (i == 4) {
      const int xc = bid & 7, li = (bid - K2_MAIN0) >> 3;
#pragma unroll
      for (int k = 0; k < 4; ++k) {
        const int e = (k & 1) * 64 + lane, id = li * 96 + e;
        const int ln = id % 6, row = 8 * ((id / 6) & 255) + (id < 1536 ? xc : ((8 - xc) & 7));
        const u32 off = min((u32)(row * 642 + ln * 128), (u32)(2048 * 642 - 4)) & ~3u;
        if (e < 96) l2_touch((k >> 1) ? A.wi : A.wr, off, lds_addr(smem) + sizeof(smem) - 256);
      }
    }
  }
  __syncthreads();
  STAMP(1, 3);
  {
    const LDSP(const char) base = (LDSP(const char))(size_t)ard;
    s16x8 b = cat8(__builtin_amdgcn_ds_read_tr16_b64_v4i16((LDSP(s16x4))base), __builtin_amdgcn_ds_read_tr16_b64_v4i16((LDSP(s16x4))(base + 160)));
    const s16x8 fa0 = *(const s16x8*)(tab + lane * 16), fa1 = *(const s16x8*)(tab + 1024 + lane * 16);
    const f32x4 z = {0.f, 0.f, 0.f, 0.f};
    f32x4 a0 = mfma16(fa0, b, z), a1 = mfma16(fa1, b, z);
    LDSP(u32) Ub = (LDSP(u32))(size_t)awr;
    const float x0 = g == 0 ? a0[1] : a0[0], x1 = g == 0 ? 0.f : a0[1];
    *(LDSP(u32))(size_t)aw0 = pk_bf(x0 * tw0[0] - x1 * tw0[1], x0 * tw0[1] + x1 * tw0[0]);
    if (g == 0) Ub[0] = pk_bf(a0[0] * z0r, a0[0] * z0i);
    Ub[20] = pk_bf(a0[2] * tw0[2] - a0[3] * tw0[3], a0[2] * tw0[3] + a0[3] * tw0[2]);
    Ub[8 * 20] = pk_bf(a1[0] * tw1[0] - a1[1] * tw1[1], a1[0] * tw1[1] + a1[1] * tw1[0]);
    Ub[9 * 20] = pk_bf(a1[2] * tw1[2] - a1[3] * tw1[3], a1[2] * tw1[3] + a1[3] * tw1[2]);
  }
  __syncthreads();
  STAMP(1, 4);
  const f32x4 z4 = {0.f, 0.f, 0.f, 0.f};
  u32* XTb = XT + (size_t)s0 * 16;
  const char* ft = tab + 2048 + lane * 16;
  const LDSP(const char) be = (LDSP(const char))(size_t)acr;
  const int P = w >> 1, mp = P, mm = P == 0 ? 4 : 8 - P;
  if (w < 8) {
    const s16x8 e0 = *(const LDSP(s16x8))(be + 16 * g), e1 = *(const LDSP(s16x8))(be + 64), o0 = *(const LDSP(s16x8))(be + 1360 + 16 * g), o1 = *(const LDSP(s16x8))(be + 1360 + 64);
    char* dp = (char*)(XTb + (size_t)(mp * 21) * 4096); char* dm = (char*)(XTb + (size_t)(mm * 21) * 4096);
    const u32 vo = (u32)(2 * g * 4096 + c) * 4u;
    if (!(w & 1)) {
#pragma unroll
      for (int mt = 0; mt < 2; ++mt) {
        const s16x8 f0 = *(const s16x8*)(ft + (2 * mt) * 1024), f1 = *(const s16x8*)(ft + (2 * mt + 1) * 1024);
        f32x4 ae = mfma16(f1, e1, mfma16(f0, e0, z4)), ao = mfma16(f1, o1, mfma16(f0, o0, z4));
        f32x4 tp = ae + ao, tm = ae - ao;
        if (P == 0) { tp = ae; tm = ao; }
        char* ep = dp + (size_t)(8 * mt) * 16384; char* em = dm + (size_t)(8 * mt) * 16384;
        const int k0 = 8 * mt + 2 * g;
        { const float d0_ = (c == 0 && k0 >= 1 && k0 <= 9) ? 2.f : 1.f, d1_ = (c == 0 && k0 + 1 <= 9) ? 2.f : 1.f;
          const f32x4 dd = {d0_, d0_, d1_, d1_}; tp = tp * dd; tm = tm * dd; }
        if (mt == 0 || c != 0 || k0 <= 10) { stg_sel<4>(ep, vo, pk_bf(tp[0], tp[1])); stg_sel<4>(em, vo, pk_bf(tm[0], tm[1])); }
        if (mt == 0 || c != 0 || k0 + 1 <= 10) { stg_sel<4>(ep + 16384, vo, pk_bf(tp[2], tp[3])); stg_sel<4>(em + 16384, vo, pk_bf(tm[2], tm[3])); }
      }
    } else {
      const s16x8 f0 = *(const s16x8*)(ft + 4 * 1024), f1 = *(const s16x8*)(ft + 5 * 1024);
      f32x4 ae = mfma16(f1, e1, mfma16(f0, e0, z4)), ao = mfma16(f1, o1, mfma16(f0, o0, z4));
      f32x4 tp = ae + ao, tm = ae - ao;
      if (P == 0) { tp = ae; tm = ao; }
      if (g < 2 && c != 0) {
        char* ep = dp + (size_t)16 * 16384; char* em = dm + (size_t)16 * 16384;
        stg_sel<4>(ep, vo, pk_bf(tp[0], tp[1])); stg_sel<4>(ep + 16384, vo, pk_bf(tp[2], tp[3]));
        stg_sel<4>(em, vo, pk_bf(tm[0], tm[1])); stg_sel<4>(em + 16384, vo, pk_bf(tm[2], tm[3]));
      }
    }
  } else {
    const int rh = c & 7, mt = w - 7;
    const s16x8 b0 = *(const LDSP(s16x8))(be + 16 * g), b1 = *(const LDSP(s16x8))(be + 64);
    const s16x8 f0 = *(const s16x8*)(ft + (2 * mt) * 1024), f1 = *(const s16x8*)(ft + (2 * mt + 1) * 1024);
    f32x4 a = mfma16(f1, b1, mfma16(f0, b0, z4));
    const float ca_ = (rh >= 2 && (rh & 1)) ? -1.f : 1.f, cb_ = rh >= 2 ? 1.f : 0.f;
    f32x4 t;
#pragma unroll
    for (int r = 0; r < 4; ++r) t[r] = ca_ * a[r] + cb_ * dpp_x1(a[r]);
    const int m1 = rh == 0 ? 0 : rh == 1 ? 4 : (rh & 1) ? 8 - (rh >> 1) : (rh >> 1);
    const int k0 = 8 * mt + 2 * g;
    const u32 o0 = (u32)((m1 * 21 + (k0 == 10 ? 20 : k0)) * 4096) * 4u, o1 = (u32)((m1 * 21 + k0 + 1) * 4096) * 4u;
    if (c < 8 && k0 >= 10 && k0 < 20) { stg_sel<4>(XTb, o0, pk_bf(t[0], t[1])); stg_sel<4>(XTb, o1, pk_bf(t[2], t[3])); }
    if (w == 9) asm volatile("s_waitcnt vmcnt(0)" ::: "memory");
  }
  STAMP(1, 7);
}

constexpr int EX_RS = 20;
__global__ __launch_bounds__(512) void k3_mid(const u32* __restrict__ XT, const u16* __restrict__ wr, const u16* __restrict__ wi, u32* __restrict__ ZT) {
  STAMP_DECL; STAMP(2, 0);
  __shared__ __attribute__((aligned(16))) u32 ex[2][256 * EX_RS];
  __shared__ __attribute__((aligned(16))) char tab[8 * 1024];
  const int tid = threadIdx.x, lane = tid & 63, w = __builtin_amdgcn_readfirstlane(tid >> 6), c = lane & 15, g = lane >> 4;
  const int m1 = blockIdx.x & 7, tile = blockIdx.x >> 3;
  const char* in = (const char*)(XT + (size_t)(m1 * 21 + tile) * 4096);
  dma1k(in + 2048 * w, (u32)lane * 16u, lds_addr(ex[1]) + 2048 * w);
  dma1k(in + 2048 * w + 1024, (u32)lane * 16u, lds_addr(ex[1]) + 2048 * w + 1024);
  const F8w* tsrc = w < 2 ? &g_a1[m1] : w < 4 ? &g_f16f : w < 6 ? &g_f16i : &g_a1i[m1];
  const s16x8 tfrag = ldg<s16x8>(tsrc->v[w & 1], lane * 16);
  u32 wra[2][4], wia[2][4];
  const int kb = tile == 20 ? 336 : (c == 0 && tile >= 11) ? 32 * tile + 16 : 32 * tile + c;
  const bool mir = kb > 320;
  {
    const int kk = mir ? 640 - kb : kb;
#pragma unroll
    for (int j = 0; j < 2; ++j)
#pragma unroll
      for (int r = 0; r < 4; ++r) {
        const int m = m1 + 8 * (2 * w + j) + 128 * (4 * g + r), mm = mir ? ((2048 - m) & 2047) : m;
        const u32 vo = (u32)(mm * DF + kk) * 2u;
        wra[j][r] = (u32)ldg<u16>(wr, vo) << 16; wia[j][r] = (u32)ldg<u16>(wi, vo) << 16;
      }
  }
  f32x4 fcs[2], fsn[2], ics[2], isn[2];
#pragma unroll
  for (int j = 0; j < 2; ++j)
#pragma unroll
    for (int r = 0; r < 4; ++r) {
      float s_, c_;
      sincos_rev((float)((2 * w + j) * (m1 + 8 * (4 * g + r))) * (1.0f / 2048), s_, c_); fcs[j][r] = c_; fsn[j][r] = s_;
      sincos_rev((float)((4 * g + r) * (m1 + 8 * (2 * w + j))) * (1.0f / 2048), s_, c_); ics[j][r] = c_; isn[j][r] = s_;
    }
  u32 awr = lds_addr(ex) + 4 * ((4 * g * 16 + c) * EX_RS + 2 * w);
  u32 ard = lds_addr(ex) + 4 * ((2 * w * 16 + c) * EX_RS + 4 * g);
#pragma unroll
  for (int j = 0; j < 2; ++j) { pin(fcs[j]); pin(fsn[j]); pin(ics[j]); pin(isn[j]); }
  pin(awr); pin(ard);
  *(s16x8*)(tab + w * 1024 + lane * 16) = tfrag;
  STAMP(2, 1);
  wait_vm<16>();
  __syncthreads();
  STAMP(2, 2);
  u32x4 bx[2];
#pragma unroll
  for (int j = 0; j < 2; ++j)
#pragma unroll
    for (int i = 0; i < 4; ++i) bx[j][i] = ex[1][(16 * (4 * g + i) + 2 * w + j) * 16 + c];
  const f32x4 z4 = {0.f, 0.f, 0.f, 0.f};
  const char* tb = tab + lane * 16;
  const LDSP(char) wr0 = (LDSP(char))(size_t)awr; const LDSP(const char) rd0 = (LDSP(const char))(size_t)ard;
  constexpr int RB = 16 * EX_RS * 4, XB = 256 * EX_RS * 4;
  {
    const s16x8 A10 = *(const s16x8*)tb, A11 = *(const s16x8*)(tb + 1024);
    u32 pk[2][4];
#pragma unroll
    for (int j = 0; j < 2; ++j) {
      f32x4 re = mfma16(A10, as8(bx[j]), z4), im = mfma16(A11, as8(bx[j]), z4);
      f32x4 tr = re * fcs[j] + im * fsn[j], ti = im * fcs[j] - re * fsn[j];
#pragma unroll
      for (int r = 0; r < 4; ++r) pk[j][r] = pk_bf(tr[r], ti[r]);
    }
#pragma unroll
    for (int r = 0; r < 4; ++r) *(LDSP(u32x2))(wr0 + r * RB) = u32x2{pk[0][r], pk[1][r]};
  }
  __syncthreads();
  STAMP(2, 3);
  {
    const s16x8 F0 = *(const s16x8*)(tb + 2048), F1 = *(const s16x8*)(tb + 3072), G0 = *(const s16x8*)(tb + 4096), G1 = *(const s16x8*)(tb + 5120);
    u32 pk[2][4];
#pragma unroll
    for (int j = 0; j < 2; ++j) {
      const u32x4 b = *(const LDSP(u32x4))(rd0 + j * RB);
      f32x4 xr = mfma16(F0, as8(b), z4), xi = mfma16(F1, as8(b), z4);
      const f32x4 a = {__uint_as_float(wra[j][0]), __uint_as_float(wra[j][1]), __uint_as_float(wra[j][2]), __uint_as_float(wra[j][3])};
      const u32 sg = mir ? 0x80000000u : 0u;
      const f32x4 bb = {__uint_as_float(wia[j][0] ^ sg), __uint_as_float(wia[j][1] ^ sg), __uint_as_float(wia[j][2] ^ sg), __uint_as_float(wia[j][3] ^ sg)};
      const f32x4 yr = xr * a - xi * bb, yi = xr * bb + xi * a;
      const u32x4 yb = {pk_bf(yr[0], yr[1]), pk_bf(yr[2], yr[3]), pk_bf(yi[0], yi[1]), pk_bf(yi[2], yi[3])};
      f32x4 re = mfma16(G0, as8(yb), z4), im = mfma16(G1, as8(yb), z4);
      f32x4 tr = re * ics[j] - im * isn[j], ti = im * ics[j] + re * isn[j];
#pragma unroll
      for (int r = 0; r < 4; ++r) pk[j][r] = pk_bf(tr[r], ti[r]);
    }
#pragma unroll
    for (int r = 0; r < 4; ++r) *(LDSP(u32x2))(wr0 + XB + r * RB) = u32x2{pk[0][r], pk[1][r]};
  }
  __syncthreads();
  STAMP(2, 4);
  char* out = (char*)(ZT + (size_t)(2 * w) * 8 * 21 * 16 + (m1 * 21 + tile) * 16);
  const u32 vout = (u32)(64 * g * 10752 + c * 4);
  const s16x8 I0 = *(const s16x8*)(tb + 6144), I1 = *(const s16x8*)(tb + 7168);
#pragma unroll
  for (int j = 0; j < 2; ++j) {
    const u32x4 b = *(const LDSP(u32x4))(rd0 + XB + j * RB);
    f32x4 zr = mfma16(I0, as8(b), z4), zi = mfma16(I1, as8(b), z4);
#pragma unroll
    for (int r = 0; r < 4; ++r) stg_sel<8>(out + (16 * r + j) * 10752, vout, pk_bf(zr[r], zi[r]));
  }
  STAMP(2, 7);
}

constexpr int Y_RS = 640 + 4;
__global__ __launch_bounds__(640) void k4_inv(const u32* __restrict__ ZT, const u16* __restrict__ x, const float* __restrict__ mod,
                                              u16* __restrict__ x1o, u16* __restrict__ h2o, const u16* __restrict__ Wgu) {
  STAMP_DECL; STAMP(3, 0);
  __shared__ __attribute__((aligned(16))) char smem[162 * 64 + 8 * Y_RS * 4 + 10 * 8 * 4 + 8 * 1024 + 10752 + 256];
  char* Vs = smem; float* Y = (float*)(Vs + 162 * 64); float* part = Y + 8 * Y_RS; char* tab = (char*)(part + 80);
  const int tid = threadIdx.x, lane = tid & 63, w = __builtin_amdgcn_readfirstlane(tid >> 6), c = lane & 15, g = lane >> 4;
  const int s0 = row_group(blockIdx.x);
  u32* Zs = (u32*)(tab + 8 * 1024);
  {
    const char* Zb = (const char*)(ZT + (size_t)s0 * 8 * 21 * 16);
    dma1k(Zb + 1024 * w, (u32)lane * 16u, lds_addr(Zs) + 1024 * w);
    if (w == 0 && lane < 32) dma1k(Zb + 10240, (u32)lane * 16u, lds_addr(Zs) + 10240);
  }
  const int rho = w < 8 ? w : (c & 7), P = rho >> 1, mA = rho == 1 ? 4 : P, mB = P == 0 ? 4 : 8 - P;
  const s16x8 tfrag = w < 6 ? ldg<s16x8>(&g_f20ai.v[0][0][0][0] + w * 512, lane * 16) : ldg<s16x8>(g_f32bi.v[(w - 6) & 1], lane * 16);
  const int hf = w >= 5, cp = tid - 320 * hf;
  u32 xv[4];
#pragma unroll
  for (int r = 0; r < 4; ++r) xv[r] = ldg<u32>(x + (size_t)(s0 + 256 * (4 * hf + r)) * D_, 4 * cp);
  const f32x2 ga = ldg<f32x2>(mod + 2 * 640, 8 * cp), sb = ldg<f32x2>(mod + 3 * 640, 8 * cp), cb = ldg<f32x2>(mod + 4 * 640, 8 * cp);
  f32x4 tw[3]; u32 aw[3];
  {
    const bool rot = rho >= 3 && (rho & 1);
    const float sc = w < 8 ? 1.0f / (2048.0f * 640.0f) : 2.0f / (2048.0f * 640.0f), fk = (float)(w < 8 ? c : 16) * (1.0f / 640.0f);
#pragma unroll
    for (int mt = 0; mt < 3; ++mt) {
#pragma unroll
      for (int rr = 0; rr < 2; ++rr) {
        float s_, c_; sincos_rev(fk * (float)(8 * mt + 2 * g + rr), s_, c_);
        tw[mt][2 * rr] = (rot ? -s_ : c_) * sc; tw[mt][2 * rr + 1] = (rot ? c_ : s_) * sc;
      }
      const int d0 = 8 * mt + 2 * g;
      const bool ok = d0 < 20 && (w < 8 || c < 8);
      aw[mt] = lds_addr(Vs) + (ok ? (rho * 20 + d0) * 64 : 160 * 64) + (w < 8 ? 4 * c : 2);
      pin(tw[mt]); pin(aw[mt]);
    }
  }
  float be = rho < 2 ? 0.f : (rho & 1) ? -1.f : 1.f;
  u32 msk[8], zoa[8];
#pragma unroll
  for (int i = 0; i < 8; ++i) {
    const int k0 = i < 4 ? 4 * g + i : 16 + (i - 4);
    const bool dead = w < 8 ? (c == 0 && k0 > 10) : (k0 < 10 || (i >= 4 && g != 0));
    msk[i] = dead ? 0u : 0xffffffffu;
    zoa[i] = w < 8 ? k0 * 16 + c : (k0 < 10 ? 11 : k0 == 10 ? 20 : k0) * 16;
    pin(msk[i]); pin(zoa[i]);
  }
  u32 zba = lds_addr(Zs) + mA * 21 * 64, zbb = lds_addr(Zs) + mB * 21 * 64;
  pin(be); pin(zba); pin(zbb);
  STAMP(3, 1);
  if (w < 8) *(s16x8*)(tab + w * 1024 + lane * 16) = tfrag;
  wait_vm<4>();
  __syncthreads();
  u32x4 b0, b1;
  if (w < 9) {
#pragma unroll
    for (int i = 0; i < 8; ++i) {
      const u32 zz = *(const LDSP(u32))(size_t)(zba + 4 * zoa[i]) & msk[i], zy = *(const LDSP(u32))(size_t)(zbb + 4 * zoa[i]) & msk[i];
      const f32x2 a = {bf_lo(zz), bf_hi(zz)}, b = {bf_lo(zy), bf_hi(zy)};
      const f32x2 v = a + be * b;
      if (i < 4) b0[i] = pk_bf(v[0], v[1]); else b1[i - 4] = pk_bf(v[0], v[1]);
    }
  }
  STAMP(3, 2);
  if (w < 9) {
    f32x4 a[3];
#pragma unroll
    for (int mt = 0; mt < 3; ++mt) {
      const f32x4 z = {0.f, 0.f, 0.f, 0.f};
      const s16x8 f0 = *(const s16x8*)(tab + (2 * mt) * 1024 + lane * 16), f1 = *(const s16x8*)(tab + (2 * mt + 1) * 1024 + lane * 16);
      a[mt] = mfma16(f1, as8(b1), mfma16(f0, as8(b0), z));
    }
    if (w < 8) {
      const u32 hi_skip = c == 0 ? 1u : 0u;
#pragma unroll
      for (int mt = 0; mt < 3; ++mt)
#pragma unroll
        for (int rr = 0; rr < 2; ++rr) {
          const u32 pv = pk_bf(a[mt][2 * rr] * tw[mt][2 * rr] - a[mt][2 * rr + 1] * tw[mt][2 * rr + 1], a[mt][2 * rr] * tw[mt][2 * rr + 1] + a[mt][2 * rr + 1] * tw[mt][2 * rr]);
          LDSP(char) dst = (LDSP(char))(size_t)aw[mt] + 64 * rr;
          if (hi_skip) *(LDSP(u16))dst = (u16)pv; else *(LDSP(u32))dst = pv;
        }
    } else {
#pragma unroll
      for (int mt = 0; mt < 3; ++mt)
#pragma unroll
        for (int rr = 0; rr < 2; ++rr)
          *(LDSP(u16))((LDSP(char))(size_t)aw[mt] + 64 * rr) = (u16)pk_bf(a[mt][2 * rr] * tw[mt][2 * rr] - a[mt][2 * rr + 1] * tw[mt][2 * rr + 1], 0.f);
    }
  }
  __syncthreads();
  STAMP(3, 3);
  {
    const s16x8 a = *(const s16x8*)(Vs + (16 * w + c) * 64 + 16 * g);
    const s16x8 fb0 = *(const s16x8*)(tab + 6144 + lane * 16), fb1 = *(const s16x8*)(tab + 7168 + lane * 16);
    const f32x4 z = {0.f, 0.f, 0.f, 0.f};
    const f32x4 y0 = mfma16(a, fb0, z), y1 = mfma16(a, fb1, z);
    const int R = 16 * w + 4 * g, rr = R / 20, d0 = R % 20;
    *(f32x4*)(Y + rr * Y_RS + 20 * c + d0) = y0;
    *(f32x4*)(Y + rr * Y_RS + 20 * (16 + c) + d0) = y1;
  }
  __syncthreads();
  STAMP(3, 4);
  {
    f32x2 R[8];
#pragma unroll
    for (int r = 0; r < 8; ++r) R[r] = *(const f32x2*)(Y + r * Y_RS + 2 * cp);
    const float cc = 0.70710678118654752f;
    const f32x2 G = R[0] + R[1], Fm = R[0] - R[1];
    const f32x2 u = cc * (R[2] - R[6]), v = cc * (R[3] + R[7]);
    const f32x2 c13 = R[2] + R[6], s13 = R[3] - R[7];
    const float sg = hf ? -1.f : 1.f;
    f32x2 y[4];
    y[0] = G + R[4] + sg * c13;
    y[2] = G - R[4] + sg * s13;
    y[1] = Fm + R[5] + sg * (u + v);
    y[3] = Fm - R[5] + sg * (v - u);
    u32 x1[4];
    float sv[8];
#pragma unroll
    for (int r = 0; r < 4; ++r) {
      const f32x2 xx = {bf_lo(xv[r]), bf_hi(xv[r])};
      const f32x2 t = xx + ga * y[r];
      x1[r] = pk_bf(t[0], t[1]);
      stg_sel<2>(x1o + (size_t)(8 * s0 + 4 * hf + r) * D_, (u32)cp * 4u, x1[r]);
      sv[r] = dot2bf(x1[r], 0x3f803f80u, 0.f); sv[4 + r] = dot2bf(x1[r], x1[r], 0.f);
    }
    const float tot = reduce8(sv, lane);
    if (lane < 8) part[w * 8 + lane] = tot;
    const f32x2 gg = {rbf(1.0f + cb[0]), rbf(1.0f + cb[1])};
    const int d = 2 * cp, wi = d & 31;
    char* stage = (char*)Y;
    const int so = (((d >> 5) * 4 + ((wi & 15) >> 2)) * 8 + 4 * hf) * 16 + (4 * (wi >> 4) + (wi & 3)) * 2;
    __syncthreads();
    STAMP(3, 5);
    const int r = lane & 3;
    float s = 0.f, qq = 0.f;
#pragma unroll
    for (int i = 0; i < 5; ++i) { s += part[(5 * hf + i) * 8 + r]; qq += part[(5 * hf + i) * 8 + 4 + r]; }
    float m_, i_; ln_finish(s, qq, m_, i_);
    const float nb_ = -m_ * i_;
#pragma unroll
    for (int i = 0; i < 4; ++i) {
      const float na = __int_as_float(__builtin_amdgcn_readlane(__float_as_int(i_), i)), nb = __int_as_float(__builtin_amdgcn_readlane(__float_as_int(nb_), i));
      const f32x2 v2 = {bf_lo(x1[i]), bf_hi(x1[i])};
      const f32x2 hv = (v2 * na + nb) * gg + sb;
      *(u32*)(stage + so + 16 * i) = pk_bf(hv[0], hv[1]);
    }
    __syncthreads();
    const u32x4 piece = *(const u32x4*)(stage + 16 * tid);
    stg_sel<16>(h2o + ((size_t)(s0 >> 1) * 20 * 64 + 8 * (s0 & 1)) * 8, (u32)((tid >> 3) * 16 + (tid & 7)) * 16u, piece);
  }
  STAMP(3, 7);
}

constexpr int K5_SLOT = 28 * 1024;
constexpr int K5_NS = 4;
constexpr int K5_PD = 4;
struct K5Ctx { const u16* wdp; int pfl; u32 dump; const u16* wsrc; const u16* hsrc; u32 ldsw; u32 goff[7]; u32 hoff; u32 abase[2]; };
template <int s, int i> DI void k5_w1(const K5Ctx& c) {
  if constexpr (i == 0) set_m0(c.ldsw + (s % K5_NS) * K5_SLOT + 3072);
  glds16_s<1024 * i - 3072>((const char*)(c.wsrc + s * 4 * 512) - 4096, c.goff[i]);
}
template <int s, int i> DI void k5_t1(const K5Ctx& c, u32x4 (&t)[4]) {
  gload16_s<(i >> 1) * 1024>(t[i], c.hsrc + ((i & 1) * 20 + 2 * s) * 512, c.hoff);
}
template <int s> DI void k5_wall(const K5Ctx& c) { k5_w1<s, 0>(c); k5_w1<s, 1>(c); k5_w1<s, 2>(c); k5_w1<s, 3>(c); k5_w1<s, 4>(c); k5_w1<s, 5>(c); k5_w1<s, 6>(c); }
template <int s> DI void k5_tall(const K5Ctx& c, u32x4 (&t)[4]) { k5_t1<s, 0>(c, t); k5_t1<s, 1>(c, t); k5_t1<s, 2>(c, t); k5_t1<s, 3>(c, t); }
template <int n> DI s16x8 k5_rd(const K5Ctx& c) {
  constexpr int s = n / 28, t = (n / 14) & 1, j = n % 14, sl = s % K5_NS;
  return *(const LDSP(const s16x8))((LDSP(const char))(size_t)c.abase[sl >> 1] + (sl & 1) * K5_SLOT + ((j >> 1) * 4 + t * 2 + (j & 1)) * 1024);
}
template <int n> DI void k5_sub(const K5Ctx& c, f32x4 (&acc)[14][2], s16x8 (&fr)[K5_PD + 1], u32x4 (&tok)[10][4]) {
  constexpr int s = n / 28, t = (n / 14) & 1, j = n % 14;
  constexpr int nb = n + K5_PD;
  if constexpr (nb < 280 && nb % 28 == 0) {
    wait_vm<(s + 2 <= 9) ? 11 : 0>();
    __builtin_amdgcn_s_barrier();
    SLAB(s + 1);
    tie4(tok[s + 1]);
  }
  if constexpr (nb < 280) fr[nb % (K5_PD + 1)] = k5_rd<nb>(c);
  {
    constexpr int q = nb / 28, pos = nb % 28 - (q == 0 ? K5_PD : 0);
    constexpr int idx = (pos % 2 == 0 && pos < 22) ? pos / 2 : -1;
    if constexpr (idx >= 0 && idx < 4 && q + 2 <= 9) k5_t1<q + 2, idx>(c, tok[q + 2]);
    if constexpr (idx >= 4 && q + 2 <= 9) k5_w1<q + 2, idx - 4>(c);
  }
  if constexpr (false) {
    constexpr int i = (n - 224) / 2;
    l2_touch(c.wdp, (u32)(c.pfl + i * 64) * 128u, c.dump);
  }
  const s16x8 a = fr[n % (K5_PD + 1)];
  acc[j][0] = mfma16(a, as8(tok[s][2 * t]), acc[j][0]);
  acc[j][1] = mfma16(a, as8(tok[s][2 * t + 1]), acc[j][1]);
  __builtin_amdgcn_sched_barrier(0);
}
template <int... I> DI void k5_all(const K5Ctx& c, f32x4 (&acc)[14][2], s16x8 (&fr)[K5_PD + 1], u32x4 (&tok)[10][4], std::integer_sequence<int, I...>) { (k5_sub<I>(c, acc, fr, tok), ...); }
__global__ __launch_bounds__(256, 2) void k5_glu(const u16* __restrict__ h2, const u16* __restrict__ Wgu, u16* __restrict__ hb, const u16* __restrict__ Wd) {
  STAMP_DECL; STAMP(4, 0);
  extern __shared__ __attribute__((aligned(16))) char smem[];
  const int tid = threadIdx.x, lane = tid & 63, w = __builtin_amdgcn_readfirstlane(tid >> 6);
  const int xcd = blockIdx.x & 7, li = blockIdx.x >> 3;
  const int tt = 8 * (xcd & 1) + (li & 7), fc = 4 * (xcd >> 1) + (li >> 3);
  const int u0 = fc < 12 ? 7 * fc : 84 + 6 * (fc - 12), U = fc < 12 ? 7 : 6;
  K5Ctx c;
  c.wsrc = Wgu + (size_t)u0 * 20 * 2 * 512;
  c.dump = lds_addr(smem) + 2 * K5_SLOT + w * 256;
  c.wdp = Wd + (size_t)(20 * (xcd >> 2)) * 54 * 512;
#pragma unroll
  for (int i = 0; i < 7; ++i) { const int f = 7 * w + i, u = min(f >> 2, U - 1); c.goff[i] = 2 * (u * 20 * 2 * 512 + (f & 3) * 512) + lane * 16 + 4096 - (1024 * i - 3072); }
  c.ldsw = lds_addr(smem) + 7 * w * 1024;
  const int T16 = 8 * tt + 2 * w;
  c.hsrc = h2 + (size_t)T16 * 20 * 512; c.hoff = lane * 16;
#pragma unroll
  for (int j = 0; j < 2; ++j) { c.abase[j] = lds_addr(smem) + 2 * j * K5_SLOT + lane * 16; asm volatile("" : "+v"(c.abase[j])); }
  u32x4 tok[10][4];
  k5_tall<0>(c, tok[0]); k5_wall<0>(c);
  k5_tall<1>(c, tok[1]); k5_wall<1>(c);
  f32x4 acc[14][2];
#pragma unroll
  for (int u = 0; u < 14; ++u) { acc[u][0] = f32x4{0.f, 0.f, 0.f, 0.f}; acc[u][1] = f32x4{0.f, 0.f, 0.f, 0.f}; }
  STAMP(4, 3);
  wait_vm<11>();
  __builtin_amdgcn_s_barrier();
  tie4(tok[0]);
  STAMP(4, 4); SLAB(0);
  s16x8 fr[K5_PD + 1];
  fr[0] = k5_rd<0>(c); fr[1] = k5_rd<1>(c); fr[2] = k5_rd<2>(c); fr[3] = k5_rd<3>(c);
  __builtin_amdgcn_sched_barrier(0);
  k5_all(c, acc, fr, tok, std::make_integer_sequence<int, 280>{});
  STAMP(4, 5); SLAB(10);
  auto hv = [&](int u, int tl) { const f32x4 a = acc[2 * u][tl] * acc[2 * u + 1][tl]; return u32x2{pk_bf(a[0], a[1]), pk_bf(a[2], a[3])}; };
#pragma unroll
  for (int tl = 0; tl < 2; ++tl) {
    u16* hdst = hb + (size_t)(T16 + tl) * 54 * 512 + (u0 >> 1) * 512;
    const u32 vo = (u32)lane * 16u;
    if (!(u0 & 1)) {
#pragma unroll
      for (int k = 0; k < 3; ++k) { const u32x2 a = hv(2 * k, tl), b = hv(2 * k + 1, tl); stg_sel<32>(hdst + k * 512, vo, u32x4{a[0], a[1], b[0], b[1]}); }
      if (U == 7) stg_sel<32>(hdst + 3 * 512, vo, hv(6, tl));
    } else {
      stg_sel<32>(hdst + 4, vo, hv(0, tl));
#pragma unroll
      for (int k = 0; k < 3; ++k) { const u32x2 a = hv(2 * k + 1, tl), b = hv(2 * k + 2, tl); stg_sel<32>(hdst + (k + 1) * 512, vo, u32x4{a[0], a[1], b[0], b[1]}); }
    }
  }
  STAMP(4, 7);
}

constexpr int K6_NS = 4;
struct K6Ctx { const u16* wsrc; const u16* hsrc; u32 hoff; const float* gbp; u32 gbo; const u16* xp; u32 xo; const u16* tbase; u32 toff; u32 dump; };
struct K6Grp { u32x4 a[5]; u32x4 t[4]; };
template <int i> DI void k6_v1(const K6Ctx& c, int ks, K6Grp& gr) {
  if constexpr (i < 5) gload16_s<0>(gr.a[i], c.wsrc + (ks + i * 54) * 512, c.hoff);
  else gload16_s<0>(gr.t[i - 5], c.hsrc + (ks + (i - 5) * 54) * 512, c.hoff);
}
DI void k6_issue(const K6Ctx& c, int ks, K6Grp& gr) {
  k6_v1<0>(c, ks, gr); k6_v1<5>(c, ks, gr); k6_v1<6>(c, ks, gr); k6_v1<7>(c, ks, gr); k6_v1<8>(c, ks, gr);
  k6_v1<1>(c, ks, gr); k6_v1<2>(c, ks, gr); k6_v1<3>(c, ks, gr); k6_v1<4>(c, ks, gr);
}
DI void k6_tie(K6Grp& g) { asm volatile("" : "+v"(g.a[0]), "+v"(g.a[1]), "+v"(g.a[2]), "+v"(g.a[3]), "+v"(g.a[4]), "+v"(g.t[0]), "+v"(g.t[1]), "+v"(g.t[2]), "+v"(g.t[3])); }
struct K6Epi { u32x4 gb[5]; u32x2 xr[5]; };
template <int OFF> DI void gload8_s(u32x2& d, const void* sbase, u32 voff) {
  asm volatile("global_load_dwordx2 %0, %1, %2 offset:%3" : "=v"(d) : "v"(voff), "s"(sbase), "n"(OFF) : "memory");
}
#define K6_TOUCH 0
#define K6_TDV 5
constexpr int K6_TD = K6_TDV, K6_TLAST = K6_TOUCH ? 12 : -1;
template <int j> DI void k6_touch(const K6Ctx& c, int lane) { if (lane < 13) l2_touch(c.tbase + j * 512, c.toff, c.dump); }
constexpr int k6_after(int i, int NK) {
  int n = 0; bool seen = false;
  for (int g = 0; g < 3; ++g) { if (seen) n += 9; if (g == i) seen = true; }
  for (int j = 3; j < K6_TD; ++j) if (seen && K6_TOUCH) n += 1;
  for (int st = 0; st < i; ++st) {
    if (st + K6_TD <= K6_TLAST && seen) n += 1;
    if (st == NK - 3 && seen) n += 10;
    if (st + 3 < NK) { if (seen) n += 9; if (st + 3 == i) seen = true; }
  }
  return n;
}
template <int i, int NK> DI void k6_step(const K6Ctx& c, int ks0, f32x4 (&acc)[5][4], K6Grp (&gr)[K6_NS], K6Epi& ep, f32x4* red, int w, int lane) {
  wait_vm<k6_after(i, NK)>();
  K6Grp& g = gr[i % K6_NS];
  k6_tie(g);
  K6Grp& nx = gr[(i + 3) % K6_NS];
  if constexpr (i + K6_TD <= K6_TLAST) k6_touch<i + K6_TD>(c, lane);
#pragma unroll
  for (int nt = 0; nt < 5; ++nt) {
    if constexpr (i + 3 < NK) {
      if (nt == 0) { k6_v1<0>(c, ks0 + i + 3, nx); k6_v1<5>(c, ks0 + i + 3, nx); }
      if (nt == 1) { k6_v1<6>(c, ks0 + i + 3, nx); k6_v1<7>(c, ks0 + i + 3, nx); }
      if (nt == 2) { k6_v1<8>(c, ks0 + i + 3, nx); k6_v1<1>(c, ks0 + i + 3, nx); }
      if (nt == 3) { k6_v1<2>(c, ks0 + i + 3, nx); k6_v1<3>(c, ks0 + i + 3, nx); }
      if (nt == 4) { k6_v1<4>(c, ks0 + i + 3, nx); }
    }
    if constexpr (i == NK - 3) {
      gload16_s<0>(ep.gb[nt], c.gbp + 16 * nt, c.gbo); gload8_s<0>(ep.xr[nt], c.xp + 16 * nt, c.xo);
    }
#pragma unroll
    for (int t = 0; t < 4; ++t) acc[nt][t] = mfma16(as8(g.a[nt]), as8(g.t[t]), acc[nt][t]);
    if constexpr (i == NK - 1) {
      if (nt >= 1) {
#pragma unroll
        for (int t = 0; t < 4; ++t) if (t != w) red[((w * 5 + nt - 1) * 4 + t) * 64 + lane] = acc[nt - 1][t];
      }
    }
    __builtin_amdgcn_sched_barrier(0);
  }
}
template <int i, int NK> struct K6Loop { static DI void run(const K6Ctx& c, int ks0, f32x4 (&acc)[5][4], K6Grp (&gr)[K6_NS], K6Epi& ep, f32x4* red, int w, int lane) {
  k6_step<i, NK>(c, ks0, acc, gr, ep, red, w, lane); K6Loop<i + 1, NK>::run(c, ks0, acc, gr, ep, red, w, lane); } };
template <int NK> struct K6Loop<NK, NK> { static DI void run(const K6Ctx&, int, f32x4 (&)[5][4], K6Grp (&)[K6_NS], K6Epi&, f32x4*, int, int) {} };
template <int NK> DI void k6_main(const K6Ctx& cx, int ks0, f32x4* red, int w, int lane, u16* __restrict__ out, u32 oo) {
  K6Grp gr[K6_NS]; K6Epi ep;
  k6_issue(cx, ks0, gr[0]); k6_issue(cx, ks0 + 1, gr[1]); k6_issue(cx, ks0 + 2, gr[2]);
  if constexpr (K6_TOUCH) { k6_touch<3>(cx, lane); k6_touch<4>(cx, lane); if constexpr (K6_TD > 5) k6_touch<5>(cx, lane); if constexpr (K6_TD > 6) k6_touch<6>(cx, lane); }
  f32x4 acc[5][4];
#pragma unroll
  for (int a = 0; a < 5; ++a)
#pragma unroll
    for (int b = 0; b < 4; ++b) acc[a][b] = f32x4{0.f, 0.f, 0.f, 0.f};
  K6Loop<0, NK>::run(cx, ks0, acc, gr, ep, red, w, lane);
#pragma unroll
  for (int t = 0; t < 4; ++t) if (t != w) red[((w * 5 + 4) * 4 + t) * 64 + lane] = acc[4][t];
  wait_vm<0>();
  asm volatile("" : "+v"(ep.gb[0]), "+v"(ep.gb[1]), "+v"(ep.gb[2]), "+v"(ep.gb[3]), "+v"(ep.gb[4]), "+v"(ep.xr[0]), "+v"(ep.xr[1]), "+v"(ep.xr[2]), "+v"(ep.xr[3]), "+v"(ep.xr[4]));
  __syncthreads();
#pragma unroll
  for (int nt = 0; nt < 5; ++nt) {
    f32x4 v = w == 0 ? acc[nt][0] : w == 1 ? acc[nt][1] : w == 2 ? acc[nt][2] : acc[nt][3];
#pragma unroll
    for (int ww = 1; ww < 4; ++ww) v += red[((((w + ww) & 3) * 5 + nt) * 4 + w) * 64 + lane];
    const f32x4 gb = __builtin_bit_cast(f32x4, ep.gb[nt]);
    const f32x4 x4 = {bf_lo(ep.xr[nt][0]), bf_hi(ep.xr[nt][0]), bf_lo(ep.xr[nt][1]), bf_hi(ep.xr[nt][1])};
    const f32x4 o = x4 + gb * v;
    stg_sel<64>(out + 16 * nt, oo, u32x2{pk_bf(o[0], o[1]), pk_bf(o[2], o[3])});
  }
}
__global__ __launch_bounds__(256, 2) void k6_down(const u16* __restrict__ hb, const u16* __restrict__ Wd, const u16* __restrict__ x1, const float* __restrict__ mod, u16* __restrict__ out) {
  STAMP_DECL; STAMP(5, 0);
  extern __shared__ __attribute__((aligned(16))) char smem[];
  const int tid = threadIdx.x, lane = tid & 63, w = __builtin_amdgcn_readfirstlane(tid >> 6), c = lane & 15, g = lane >> 4;
  const int xcd = blockIdx.x & 7, li = blockIdx.x >> 3;
  const int tt = 8 * (xcd & 3) + (li & 7), nb = 4 * (xcd >> 2) + (li >> 3);
  const int n0 = 80 * nb;
  const int ks0 = w < 2 ? 14 * w : 28 + 13 * (w - 2);
  K6Ctx cx;
  cx.wsrc = Wd + (size_t)(5 * nb) * 54 * 512; cx.hsrc = hb + (size_t)(4 * tt) * 54 * 512; cx.hoff = lane * 16;
  cx.gbp = mod + 5 * 640 + n0; cx.gbo = 16 * g;
  cx.xp = x1 + (size_t)(64 * tt + 16 * w) * D_ + n0; cx.xo = (u32)(c * D_ + 4 * g) * 2u;
  {
    const int L = min(13 * (4 * li + w) + lane, 1663), wp = L / 416, r = L % 416, k0p = wp < 2 ? 14 * wp : 28 + 13 * (wp - 2);
    const u32 wdelta = (u32)((const char*)Wd - (const char*)hb);
    const u32 oh = (u32)(((4 * (8 * (xcd & 3) + (r >> 5)) + ((r >> 3) & 3)) * 54 + k0p) * 1024 + (r & 7) * 128);
    const int r2 = r - 256;
    const u32 ow = wdelta + (u32)(((5 * (4 * (xcd >> 2) + r2 / 40) + (r2 % 40) / 8) * 54 + k0p) * 1024 + (r2 & 7) * 128);
    cx.tbase = hb; cx.toff = r < 256 ? oh : ow; cx.dump = lds_addr(smem) + 81920;
  }
  const int tau = 64 * tt + 16 * w + c, srow = (tau >> 3) + 256 * (tau & 7);
  const u32 oo = (u32)(srow * D_ + 4 * g) * 2u;
  if (w < 2) k6_main<14>(cx, ks0, (f32x4*)smem, w, lane, out + n0, oo); else k6_main<13>(cx, ks0, (f32x4*)smem, w, lane, out + n0, oo);
  STAMP(5, 7);
}

constexpr size_t WS_MOD = 0, WS_XT = 16384, WS_ZT = WS_XT + 8 * 21 * 256 * 16 * 4, WS_X1 = WS_ZT + 8 * 21 * 256 * 16 * 4,
                 WS_H2 = WS_X1 + 2048 * 640 * 2, WS_HB = WS_H2 + 2048 * 640 * 2, WS_WGU = WS_HB + 2048 * 1728 * 2, WS_WD = WS_WGU + 2 * 640 * 1728 * 2, WS_WP = WS_WD + 640 * 1728 * 2, WS_SYNC = WS_WP + 168 * 16384;
extern "C" void kernel_launch(void* const* d_in, const int* in_sizes, int n_in, void* d_out, int out_size, void* d_ws, size_t ws_size,
                              hipStream_t stream) {
  const u16* x = (const u16*)d_in[0]; const u16* wr = (const u16*)d_in[1]; const u16* wi = (const u16*)d_in[2];
  const u16* wg = (const u16*)d_in[4]; const u16* wu = (const u16*)d_in[5]; const u16* wd = (const u16*)d_in[6];
  const u16* c = (const u16*)d_in[7]; const u16* wmod = (const u16*)d_in[8];
  char* ws = (char*)d_ws;
  float* mod = (float*)(ws + WS_MOD); u32* XT = (u32*)(ws + WS_XT); u32* ZT = (u32*)(ws + WS_ZT);
  u16* x1 = (u16*)(ws + WS_X1); u16* h2 = (u16*)(ws + WS_H2); u16* hb = (u16*)(ws + WS_HB);
  u16* Wgu = (u16*)(ws + WS_WGU); u16* Wd = (u16*)(ws + WS_WD);
  K2Args A;
  A.x = x; A.c = c; A.wmod = wmod; A.mod = mod; A.XT = XT; A.wr = wr; A.wi = wi; A.wg = wg; A.wu = wu; A.Wgu = Wgu; A.wd = wd; A.Wd = Wd;
  A.gen = (u32*)(ws + WS_SYNC); A.arr = (u32*)(ws + WS_SYNC + 4096); A.gran = (unsigned long long*)(ws + WS_SYNC + 8192);
  k2_fwd<<<K2_GRID, 640, 0, stream>>>(A);
  k3_mid<<<168, 512, 0, stream>>>(XT, wr, wi, ZT);
  k4_inv<<<256, 640, 0, stream>>>(ZT, x, mod, x1, h2, Wgu);
  hipFuncSetAttribute((const void*)k5_glu, hipFuncAttributeMaxDynamicSharedMemorySize, K5_NS * K5_SLOT);
  k5_glu<<<256, 256, K5_NS * K5_SLOT, stream>>>(h2, Wgu, hb, Wd);
  hipFuncSetAttribute((const void*)k6_down, hipFuncAttributeMaxDynamicSharedMemorySize, 81920 + 256);
  k6_down<<<256, 256, 81920 + 256, stream>>>(hb, Wd, x1, mod, (u16*)d_out);
}
```

```cpp
#include <hip/hip_runtime.h>
#include <hip/hip_bf16.h>
#include <stdint.h>
#include <utility>

typedef unsigned short u16;
typedef unsigned int u32;
typedef short s16x4 __attribute__((ext_vector_type(4)));
typedef short s16x8 __attribute__((ext_vector_type(8)));
typedef __bf16 bf2_t __attribute__((ext_vector_type(2)));
typedef float f32x2 __attribute__((ext_vector_type(2)));
typedef float f32x4 __attribute__((ext_vector_type(4)));
typedef float f32x16 __attribute__((ext_vector_type(16)));
typedef u32 u32x2 __attribute__((ext_vector_type(2)));
typedef u32 u32x4 __attribute__((ext_vector_type(4)));
#define DI __device__ __forceinline__
#define LDSP(T) __attribute__((address_space(3))) T*

constexpr int S_ = 2048, D_ = 640, F_ = 1728, DF = 321;

constexpr double kPi = 3.14159265358979323846264338327950288;
constexpr double c_sin0(double x) { double x2 = x * x, t = x, s = x; for (int i = 1; i < 11; ++i) { t *= -x2 / ((2.0 * i) * (2.0 * i + 1.0)); s += t; } return s; }
constexpr double c_cos0(double x) { double x2 = x * x, t = 1.0, s = 1.0; for (int i = 1; i < 11; ++i) { t *= -x2 / ((2.0 * i - 1.0) * (2.0 * i)); s += t; } return s; }
constexpr double c_cos(long n, long N) {
  n %= N; if (n < 0) n += N;
  if (2 * n > N) n = N - n;
  bool neg = false;
  if (4 * n > N) { neg = true; n = N - 2 * n; N = 2 * N; n = n;   }
  double r = 0;
  if (4 * n == N) r = 0.0;
  else if (8 * n > N) r = c_sin0(2 * kPi * (double)(N - 4 * n) / (double)(4 * N));
  else r = c_cos0(2 * kPi * (double)n / (double)N);
  return neg ? -r : r;
}
constexpr double c_sin(long n, long N) { return c_cos(4 * n - N, 4 * N); }
constexpr u16 c_bf16(double v) {
  float f = (float)v; u32 u = __builtin_bit_cast(u32, f);
  u += 0x7fffu + ((u >> 16) & 1u); return (u16)(u >> 16);
}

struct F8w { u16 v[2][64][8]; };
struct F8x6 { u16 v[3][2][64][8]; };
struct TwA { float v[2][64][4]; };
struct TwC { float v[3][64][4]; };
constexpr F8w mk_f32a() {
  F8w t{};
  for (int tt = 0; tt < 2; ++tt) for (int l = 0; l < 64; ++l) for (int j = 0; j < 8; ++j) {
    int o = 16 * tt + (l & 15), d1 = 8 * (l >> 4) + j; double v = 0;
    if (o == 0) v = 1.0; else if (o == 1) v = (d1 & 1) ? -1.0 : 1.0;
    else { int k1 = o >> 1; v = (o & 1) ? -c_sin(d1 * k1, 32) : c_cos(d1 * k1, 32); }
    t.v[tt][l][j] = c_bf16(v);
  }
  return t;
}
constexpr TwA mk_twa(int j) {
  TwA t{};
  for (int tt = 0; tt < 2; ++tt) for (int l = 0; l < 64; ++l) for (int rr = 0; rr < 2; ++rr) {
    int c = l & 15, g = l >> 4, Q = 4 * j + (c >> 2), d0 = 4 * (Q % 5) + (c & 3), rho = Q / 5;
    int k1 = 8 * tt + 2 * g + rr; if (k1 == 0) k1 = 16;
    double co = c_cos(d0 * k1, 640), si = -c_sin(d0 * k1, 640);
    if (rho >= 3 && (rho & 1)) { double x = co; co = -si; si = x; }
    t.v[tt][l][2 * rr] = (float)co; t.v[tt][l][2 * rr + 1] = (float)si;
  }
  return t;
}
constexpr F8x6 mk_f20(bool inv) {
  F8x6 t{};
  for (int mt = 0; mt < 3; ++mt) for (int ks = 0; ks < 2; ++ks) for (int l = 0; l < 64; ++l) for (int j = 0; j < 8; ++j) {
    int row = l & 15, g = l >> 4, k0 = 8 * mt + (row >> 1), part = row & 1, d0 = 16 * ks + 4 * g + (j >> 1), pp = j & 1; double v = 0;
    if (d0 < 20 && k0 < 20) {
      double co = c_cos(d0 * k0, 20), si = inv ? -c_sin(d0 * k0, 20) : c_sin(d0 * k0, 20);
      v = part == 0 ? (pp == 0 ? co : si) : (pp == 0 ? -si : co);
    }
    t.v[mt][ks][l][j] = c_bf16(v);
  }
  return t;
}
constexpr F8w mk_a1(int m1, bool inv) {
  F8w t{};
  for (int l = 0; l < 64; ++l) for (int i = 0; i < 4; ++i) {
    int c = l & 15, a = 4 * (l >> 4) + i, n = inv ? c * (8 * a + m1) : a * (8 * c + m1);
    double co = c_cos(n, 128), si = inv ? -c_sin(n, 128) : c_sin(n, 128);
    t.v[0][l][2 * i] = c_bf16(co); t.v[0][l][2 * i + 1] = c_bf16(si);
    t.v[1][l][2 * i] = c_bf16(-si); t.v[1][l][2 * i + 1] = c_bf16(co);
  }
  return t;
}
constexpr F8w mk_f16(bool inv) {
  F8w t{};
  for (int l = 0; l < 64; ++l) for (int i = 0; i < 4; ++i) {
    int c = l & 15, a = 4 * (l >> 4) + i;
    double co = c_cos(a * c, 16), si = c_sin(a * c, 16);
    if (!inv) { t.v[0][l][2 * i] = c_bf16(co); t.v[0][l][2 * i + 1] = c_bf16(si); t.v[1][l][2 * i] = c_bf16(-si); t.v[1][l][2 * i + 1] = c_bf16(co); }
    else { t.v[0][l][i] = c_bf16(co); t.v[0][l][4 + i] = c_bf16(-si); t.v[1][l][i] = c_bf16(si); t.v[1][l][4 + i] = c_bf16(co); }
  }
  return t;
}
constexpr TwC mk_twc(int kind) {
  TwC t{};
  const double sc = 1.0 / (2048.0 * 640.0);
  for (int mt = 0; mt < 3; ++mt) for (int l = 0; l < 64; ++l) for (int rr = 0; rr < 2; ++rr) {
    int k1 = kind == 2 ? 16 : (l & 15), g = l >> 4, d0 = 8 * mt + 2 * g + rr, rho = l & 7;
    double co = c_cos(d0 * k1, 640) * sc * (kind == 2 ? 2 : 1), si = c_sin(d0 * k1, 640) * sc * (kind == 2 ? 2 : 1);
    if (kind == 1 || (kind == 2 && rho >= 3 && (rho & 1))) { double x = co; co = -si; si = x; }
    t.v[mt][l][2 * rr] = (float)co; t.v[mt][l][2 * rr + 1] = (float)si;
  }
  return t;
}
constexpr F8w mk_f32bi() {
  F8w t{};
  for (int nt = 0; nt < 2; ++nt) for (int l = 0; l < 64; ++l) for (int j = 0; j < 8; ++j) {
    int d1 = 16 * nt + (l & 15), K = 8 * (l >> 4) + j; double v = 0;
    if (K == 0) v = 1.0; else if (K == 1) v = (d1 & 1) ? -1.0 : 1.0;
    else { int k1 = K >> 1; v = (K & 1) ? -2.0 * c_sin(k1 * d1, 32) : 2.0 * c_cos(k1 * d1, 32); }
    t.v[nt][l][j] = c_bf16(v);
  }
  return t;
}
static __device__ const F8w g_f32a = mk_f32a();
static __device__ const F8x6 g_f20a = mk_f20(false);
static __device__ const F8x6 g_f20ai = mk_f20(true);
static __device__ const F8w g_a1[8] = {mk_a1(0, false), mk_a1(1, false), mk_a1(2, false), mk_a1(3, false), mk_a1(4, false), mk_a1(5, false), mk_a1(6, false), mk_a1(7, false)};
static __device__ const F8w g_a1i[8] = {mk_a1(0, true), mk_a1(1, true), mk_a1(2, true), mk_a1(3, true), mk_a1(4, true), mk_a1(5, true), mk_a1(6, true), mk_a1(7, true)};
static __device__ const F8w g_f16f = mk_f16(false);
static __device__ const F8w g_f16i = mk_f16(true);
static __device__ const F8w g_f32bi = mk_f32bi();

#define STAMPS 1
__device__ unsigned long long g_stamps[8][1024][8];
__device__ unsigned long long g_cyc[8][1024][8];
__device__ unsigned long long g_slab[1024][16];
__device__ unsigned g_xcc[8][1024];
__device__ unsigned long long g_wv[1024][8][2];
#define STAMP_DECL unsigned long long st_c[8] = {0, 0, 0, 0, 0, 0, 0, 0}, st_r0 = 0
#define STAMP(k, i) do { \
    if (i == 0) asm volatile("s_memrealtime %0" : "=s"(st_r0)); \
    asm volatile("s_memtime %0" : "=s"(st_c[i])); \
    if (i == 7 || i == 6) { unsigned long long r1_; asm volatile("s_memrealtime %0\n\ts_waitcnt lgkmcnt(0)" : "=s"(r1_) :: "memory"); \
      if (threadIdx.x == 0) { g_stamps[k][blockIdx.x][0] = st_r0; g_stamps[k][blockIdx.x][i] = r1_; \
        for (int q_ = 0; q_ < 8; ++q_) g_cyc[k][blockIdx.x][q_] = st_c[q_]; } } } while (0)
#define SLAB(i) do {} while (0)
#define WSTAMP(j) do {} while (0)
#define WAITALL() asm volatile("s_waitcnt vmcnt(0)" ::: "memory")
DI float bf_lo(u32 v) { return __uint_as_float(v << 16); }
DI float bf_hi(u32 v) { return __uint_as_float(v & 0xffff0000u); }
DI float bf2f(u16 v) { return __uint_as_float(((u32)v) << 16); }
DI u32 pk_bf(float a, float b) { f32x2 f = {a, b}; return __builtin_bit_cast(u32, __builtin_convertvector(f, bf2_t)); }
DI float rbf(float a) { return bf_lo(pk_bf(a, a)); }
DI float dpp_x1(float v) { return __int_as_float(__builtin_amdgcn_mov_dpp(__float_as_int(v), 0xB1, 0xf, 0xf, true)); }
DI float dpp_x2(float v) { return __int_as_float(__builtin_amdgcn_mov_dpp(__float_as_int(v), 0x4E, 0xf, 0xf, true)); }
DI float dpp_hm(float v) { return __int_as_float(__builtin_amdgcn_mov_dpp(__float_as_int(v), 0x141, 0xf, 0xf, true)); }
DI float dpp_rm(float v) { return __int_as_float(__builtin_amdgcn_mov_dpp(__float_as_int(v), 0x140, 0xf, 0xf, true)); }
DI f32x4 mfma16(s16x8 a, s16x8 b, f32x4 c) { return __builtin_amdgcn_mfma_f32_16x16x32_bf16(a, b, c, 0, 0, 0); }
DI f32x16 mfma32(s16x8 a, s16x8 b, f32x16 c) { return __builtin_amdgcn_mfma_f32_32x32x16_bf16(a, b, c, 0, 0, 0); }
DI s16x4 tr_read(const void* p) { return __builtin_amdgcn_ds_read_tr16_b64_v4i16((LDSP(s16x4))p); }
template <class T> DI void pin(T& v) { asm volatile("" : "+v"(v)); }
DI void l2_touch(const void* base, u32 voff, u32 lds) {
  asm volatile("s_mov_b32 m0, %0\n\ts_nop 0\n\tglobal_load_lds_dword %1, %2" :: "s"(lds), "v"(voff), "s"(base) : "memory");
}
DI void side_delay(int ticks10ns) {
  const unsigned long long t0 = __builtin_amdgcn_s_memrealtime();
  while (__builtin_amdgcn_s_memrealtime() - t0 < (unsigned long long)ticks10ns) __builtin_amdgcn_s_sleep(4);
}
#define GSP(T) __attribute__((address_space(1))) T*
template <class T> DI T ldg(const void* sbase, u32 voff) { GSP(const char) p = (GSP(const char))sbase; asm("" : "+s"(p)); return *(GSP(const T))(p + voff); }
template <class T> DI void stg(void* sbase, u32 voff, T v) { GSP(char) p = (GSP(char))sbase; asm("" : "+s"(p)); *(GSP(T))(p + voff) = v; }
DI void stg_wt(void* sbase, u32 voff, u32x4 v) { asm volatile("global_store_dwordx4 %0, %1, %2 sc1\n\ts_nop 1" :: "v"(voff), "v"(v), "s"(sbase) : "memory"); }
DI void stg_wt(void* sbase, u32 voff, u32x2 v) { asm volatile("global_store_dwordx2 %0, %1, %2 sc1\n\ts_nop 1" :: "v"(voff), "v"(v), "s"(sbase) : "memory"); }
DI void stg_wt(void* sbase, u32 voff, u32 v) { asm volatile("global_store_dword %0, %1, %2 sc1" :: "v"(voff), "v"(v), "s"(sbase) : "memory"); }
#define WT_MASK 127
template <int BIT, class T> DI void stg_sel(void* sbase, u32 voff, T v) { if constexpr ((WT_MASK & BIT) != 0) stg_wt(sbase, voff, v); else stg(sbase, voff, v); }
DI u32 lds_addr(const void* p) { return (u32)(size_t)(LDSP(const char))p; }
DI s16x8 cat8(s16x4 a, s16x4 b) { return __builtin_shufflevector(a, b, 0, 1, 2, 3, 4, 5, 6, 7); }
DI s16x8 as8(u32x4 v) { return __builtin_bit_cast(s16x8, v); }
DI float dot2bf(u32 a, u32 b, float c) { return __builtin_amdgcn_fdot2_f32_bf16(__builtin_bit_cast(bf2_t, a), __builtin_bit_cast(bf2_t, b), c, false); }

DI float reduce16(const float (&v)[16], int lane) {
  float a[8], b[4], c[2], d;
  const bool s3 = lane & 8, s2 = lane & 4, s1 = lane & 2, s0 = lane & 1;
#pragma unroll
  for (int i = 0; i < 8; ++i) { float keep = s3 ? v[8 + i] : v[i], send = s3 ? v[i] : v[8 + i]; a[i] = keep + dpp_rm(send); }
#pragma unroll
  for (int i = 0; i < 4; ++i) { float keep = s2 ? a[4 + i] : a[i], send = s2 ? a[i] : a[4 + i]; b[i] = keep + dpp_hm(send); }
#pragma unroll
  for (int i = 0; i < 2; ++i) { float keep = s1 ? b[2 + i] : b[i], send = s1 ? b[i] : b[2 + i]; c[i] = keep + dpp_x2(send); }
  { float keep = s0 ? c[1] : c[0], send = s0 ? c[0] : c[1]; d = keep + dpp_x1(send); }
  auto r16 = __builtin_amdgcn_permlane16_swap(__float_as_uint(d), __float_as_uint(d), false, false);
  d = __uint_as_float(r16[0]) + __uint_as_float(r16[1]);
  auto r32 = __builtin_amdgcn_permlane32_swap(__float_as_uint(d), __float_as_uint(d), false, false);
  return __uint_as_float(r32[0]) + __uint_as_float(r32[1]);
}

DI void ln_finish(float s, float q, float& m_, float& i_) {
  const float mean = s * (1.0f / 640.0f);
  const float var = fmaxf(q * (1.0f / 640.0f) - mean * mean, 0.f);
  const float sd = rbf(__builtin_amdgcn_sqrtf(rbf(rbf(var) + 1e-5f)));
  m_ = rbf(mean); i_ = __builtin_amdgcn_rcpf(sd);
}
DI void ln_stats5(const u32 (&xv)[8], float* part  , int tid, float (&a_)[8], float (&b_)[8]) {
  const int lane = tid & 63, w = tid >> 6;
  float v[16];
#pragma unroll
  for (int r = 0; r < 8; ++r) { v[r] = dot2bf(xv[r], 0x3f803f80u, 0.f); v[8 + r] = dot2bf(xv[r], xv[r], 0.f); }
  float t = reduce16(v, lane);
  if (lane < 16) part[w * 16 + lane] = t;
  __syncthreads();
  const int r = lane & 7;
  float s = 0.f, q = 0.f;
#pragma unroll
  for (int i = 0; i < 5; ++i) { s += part[i * 16 + r]; q += part[i * 16 + 8 + r]; }
  float m_, i_; ln_finish(s, q, m_, i_);
  const float nb = -m_ * i_;
#pragma unroll
  for (int i = 0; i < 8; ++i) {
    a_[i] = __int_as_float(__builtin_amdgcn_readlane(__float_as_int(i_), i));
    b_[i] = __int_as_float(__builtin_amdgcn_readlane(__float_as_int(nb), i));
  }
}
DI float reduce8(const float (&v)[8], int lane) {
  float b[4], c[2], d;
  const bool s2 = lane & 4, s1 = lane & 2, s0 = lane & 1;
#pragma unroll
  for (int i = 0; i < 4; ++i) { float keep = s2 ? v[4 + i] : v[i], send = s2 ? v[i] : v[4 + i]; b[i] = keep + dpp_hm(send); }
#pragma unroll
  for (int i = 0; i < 2; ++i) { float keep = s1 ? b[2 + i] : b[i], send = s1 ? b[i] : b[2 + i]; c[i] = keep + dpp_x2(send); }
  { float keep = s0 ? c[1] : c[0], send = s0 ? c[0] : c[1]; d = keep + dpp_x1(send); }
  d += __int_as_float(__builtin_amdgcn_mov_dpp(__float_as_int(d), 0x128, 0xf, 0xf, true));
  auto r16 = __builtin_amdgcn_permlane16_swap(__float_as_uint(d), __float_as_uint(d), false, false);
  d = __uint_as_float(r16[0]) + __uint_as_float(r16[1]);
  auto r32 = __builtin_amdgcn_permlane32_swap(__float_as_uint(d), __float_as_uint(d), false, false);
  return __uint_as_float(r32[0]) + __uint_as_float(r32[1]);
}

template <int N> DI void wait_vm() { asm volatile("s_waitcnt vmcnt(%0)" ::"n"(N) : "memory"); }
template <int OFF> DI void gload16_s(u32x4& d, const void* sbase, u32 voff) {
  asm volatile("global_load_dwordx4 %0, %1, %2 offset:%3" : "=v"(d) : "v"(voff), "s"(sbase), "n"(OFF) : "memory");
}
template <int OFF> DI void glds16_s(const void* sbase, u32 voff) {
  asm volatile("global_load_lds_dwordx4 %0, %1 offset:%2" :: "v"(voff), "s"(sbase), "n"(OFF) : "memory");
}
DI void dma1k(const void* sbase, u32 voff, u32 lds) {
  asm volatile("s_mov_b32 m0, %0\n\ts_nop 0\n\tglobal_load_lds_dwordx4 %1, %2" :: "s"(lds), "v"(voff), "s"(sbase) : "memory");
}
DI void set_m0(u32 v) { asm volatile("s_mov_b32 m0, %0\n\ts_nop 0" :: "s"(v) : "memory"); }
DI void tie4(u32x4 (&t)[4]) { asm volatile("" : "+v"(t[0]), "+v"(t[1]), "+v"(t[2]), "+v"(t[3])); }

DI void prep_load(u32x4 (&v)[4], const u16* __restrict__ src, int ld, int lane) {
  const u32 vo = (u32)((lane >> 3) * ld + 8 * (lane & 7)) * 2u;
#pragma unroll
  for (int i = 0; i < 4; ++i) v[i] = ldg<u32x4>(src + (size_t)8 * i * ld, vo);
}
DI void prep_store(const u32x4 (&v)[4], u16* __restrict__ dst, int jstride, char* lds, int lane) {
#pragma unroll
  for (int i = 0; i < 4; ++i) {
    const int row = 8 * i + (lane >> 3), ch = lane & 7;
    *(u32x4*)(lds + row * 128 + (((ch >> 1) ^ ((row >> 1) & 3)) * 32) + (ch & 1) * 16) = v[i];
  }
  const int c = lane & 15, g = lane >> 4, q = c >> 2, p = c & 3, r = 4 * g + q;
#pragma unroll
  for (int j = 0; j < 4; ++j) {
    const char* b = lds + r * 128 + ((j ^ ((r >> 1) & 3)) * 32) + 8 * p;
    const s16x8 f = cat8(tr_read(b), tr_read(b + 16 * 128));
    stg_sel<1>(dst + (size_t)j * jstride, (u32)lane * 16u, __builtin_bit_cast(u32x4, f));
  }
}
DI void prep_wave(const u16* __restrict__ src, int ld, u16* __restrict__ dst, int jstride, char* lds, int lane) {
  u32x4 v[4]; prep_load(v, src, ld, lane); prep_store(v, dst, jstride, lds, lane);
}
DI void prep_gu(int task, const u16* wg, const u16* wu, u16* Wgu, char* lds, int lane) {
  const int mat = task >= 540, tk = task - 540 * mat, cg = tk / 20, ks = tk % 20;
  prep_wave((mat ? wu : wg) + (size_t)ks * 32 * F_ + 64 * cg, F_, Wgu + ((size_t)(4 * cg * 20 + ks) * 2 + mat) * 512, 20 * 2 * 512, lds, lane);
}
DI void prep_d(int task, const u16* wd, u16* Wd, char* lds, int lane) {
  const int cg = task / 54, ks = task % 54;
  prep_wave(wd + (size_t)ks * 32 * D_ + 64 * cg, D_, Wd + (size_t)(4 * cg * 54 + ks) * 512, 54 * 512, lds, lane);
}

typedef __attribute__((address_space(1))) u32 gu32;
typedef __attribute__((address_space(1))) unsigned long long gu64;
DI u32 ld_agent(const u32* p) { return __hip_atomic_load((const gu32*)p, __ATOMIC_RELAXED, __HIP_MEMORY_SCOPE_AGENT); }
DI void st_agent(u32* p, u32 v) { __hip_atomic_store((gu32*)p, v, __ATOMIC_RELAXED, __HIP_MEMORY_SCOPE_AGENT); }
DI void st_agent64(unsigned long long* p, unsigned long long v) { __hip_atomic_store((gu64*)p, v, __ATOMIC_RELAXED, __HIP_MEMORY_SCOPE_AGENT); }
#define K2_SIDE_DELAY 80
#define K4_SIDE_DELAY 60
constexpr int SPIN_LIMIT = 1 << 16;

#define GST_ARG , unsigned long long (&st_c)[8], unsigned long long& st_r0
#define GST_PASS , st_c, st_r0
template <int NR> DI void gemv_part(int col0, int row0, u32 tagv, bool publish, unsigned long long* gran, int gidx0, const u16* __restrict__ c, const u16* __restrict__ wmod,
                                    float* __restrict__ mod, char* lds GST_ARG) {
  float* sl = (float*)lds; float* part = sl + 640;
  const int t = threadIdx.x, lane = t & 63, w = t >> 6, cq = t & 7, rg = t >> 3;
  const u16 cv = ldg<u16>(c, 2 * t);
  u32x4 wv[NR];
  const u32 wo = (u32)((row0 + rg) * 3840 + col0 + 8 * cq) * 2u;
#pragma unroll
  for (int i = 0; i < NR; ++i) wv[i] = ldg<u32x4>(wmod + (size_t)80 * i * 3840, wo);
  STAMP(0, 1);
  { const float x = bf2f(cv); sl[t] = rbf(x * __builtin_amdgcn_rcpf(1.0f + __expf(-x))); }
  STAMP(0, 2);
  __syncthreads();
  STAMP(0, 3);
  WAITALL();
  STAMP(0, 4);
  float acc[8] = {};
#pragma unroll
  for (int i = 0; i < NR; ++i) {
    const float s = sl[row0 + rg + 80 * i];
#pragma unroll
    for (int k = 0; k < 4; ++k) { acc[2 * k] += s * bf_lo(wv[i][k]); acc[2 * k + 1] += s * bf_hi(wv[i][k]); }
  }
  float u[4];
  const bool b3 = lane & 8;
#pragma unroll
  for (int i = 0; i < 4; ++i) { const float keep = b3 ? acc[4 + i] : acc[i], send = b3 ? acc[i] : acc[4 + i]; u[i] = keep + __int_as_float(__builtin_amdgcn_mov_dpp(__float_as_int(send), 0x128, 0xf, 0xf, true)); }
  float r2[2];
#pragma unroll
  for (int i = 0; i < 2; ++i) { auto x = __builtin_amdgcn_permlane16_swap(__float_as_uint(u[i]), __float_as_uint(u[i + 2]), false, false); r2[i] = __uint_as_float(x[0]) + __uint_as_float(x[1]); }
  auto y = __builtin_amdgcn_permlane32_swap(__float_as_uint(r2[0]), __float_as_uint(r2[1]), false, false);
  part[w * 64 + 8 * cq + 4 * ((lane >> 3) & 1) + 2 * ((lane >> 4) & 1) + (lane >> 5)] = __uint_as_float(y[0]) + __uint_as_float(y[1]);
  __syncthreads();
  if (t < 64) {
    float s = 0.f;
#pragma unroll
    for (int i = 0; i < 10; ++i) s += part[i * 64 + t];
    if (publish) st_agent64(gran + gidx0 + t, ((unsigned long long)(tagv + 1u) << 32) | __float_as_uint(s));
    else mod[col0 + t] = rbf(s);
  }
}

DI int row_group(int b) { return 2 * (8 * (b >> 4) + (b & 7)) + ((b >> 3) & 1); }
DI void sincos_rev(float a, float& s, float& c) { s = __builtin_amdgcn_sinf(a); c = __builtin_amdgcn_cosf(a); }
DI void gram_part(const u32x4 (&f)[4], float* part  , int i, int lane) {
  f32x4 acc = {0.f, 0.f, 0.f, 0.f};
#pragma unroll
  for (int k = 0; k < 4; ++k) acc = mfma16(as8(f[k]), as8(f[k]), acc);
  const int c = lane & 15, g = lane >> 4;
  const float dg = (c & 2) ? ((c & 1) ? acc[3] : acc[2]) : ((c & 1) ? acc[1] : acc[0]);
  if (c < 8 && g == (c >> 2)) part[i * 16 + 8 + c] = dg;
  if (c < 8 && g == 2) part[i * 16 + c] = acc[0];
}
DI void ln_bcast8(const float* part, int lane, float (&a_)[8], float (&b_)[8]) {
  const int r = lane & 7;
  float s = 0.f, q = 0.f;
#pragma unroll
  for (int i = 0; i < 5; ++i) { s += part[i * 16 + r]; q += part[i * 16 + 8 + r]; }
  float m_, i_; ln_finish(s, q, m_, i_);
  const float nb = -m_ * i_;
#pragma unroll
  for (int i = 0; i < 8; ++i) {
    a_[i] = __int_as_float(__builtin_amdgcn_readlane(__float_as_int(i_), i));
    b_[i] = __int_as_float(__builtin_amdgcn_readlane(__float_as_int(nb), i));
  }
}
constexpr int HT_RS = 1296;
struct K2Args { const u16* x; const u16* c; const u16* wmod; float* mod; u32* XT; const u16* wr; const u16* wi; const u16* wg; const u16* wu; u16* Wgu; const u16* wd; u16* Wd; u32* gen; u32* arr; unsigned long long* gran; };
constexpr int K2_MAIN0 = 88, K2_SIDE0 = 344, K2_GRID = 344 + 54;
__global__ __launch_bounds__(640) void k2_fwd(const K2Args A) {
  STAMP_DECL; STAMP(1, 0);
  __shared__ __attribute__((aligned(16))) char smem[32768 + 5 * 4096 + 256];
  const int bid = blockIdx.x;
  if (bid >= K2_SIDE0) {
    side_delay(K2_SIDE_DELAY);
    const int wv = __builtin_amdgcn_readfirstlane(threadIdx.x >> 6);
    prep_d((bid - K2_SIDE0) * 10 + wv, A.wd, A.Wd, smem + 4096 * wv, threadIdx.x & 63);
    STAMP(1, 6);
    return;
  }
  if (bid >= 40 && bid < 80) {
    side_delay(K2_SIDE_DELAY);
    gemv_part<8>(1280 + 64 * (bid - 40), 0, 0u, false, nullptr, 0, A.c, A.wmod, A.mod, smem GST_PASS); STAMP(0, 6); return;
  }
  if (bid >= 80 && bid < 87) return;
  if (bid < 40) {
    const u32 tagv = ld_agent(A.gen);
    gemv_part<4>(64 * (bid >> 1), 320 * (bid & 1), tagv, true, A.gran, 1280 * (bid & 1) + 64 * (bid >> 1), A.c, A.wmod, A.mod, smem GST_PASS);
    if (threadIdx.x == 0) st_agent(A.arr + bid, tagv + 1u);
    STAMP(0, 7); return;
  }
  if (bid == 87) {
    const u32 tag = ld_agent(A.gen) + 1u;
    const int t = threadIdx.x, who = t < 40 ? t : t + 48;
    for (int spin = 0; spin < SPIN_LIMIT; ++spin) {
      const bool ok = t >= 296 || ld_agent(A.arr + who) == tag;
      if (__syncthreads_and(ok)) break;
      __builtin_amdgcn_s_sleep(8);
    }
    if (t == 0) st_agent(A.gen, tag);
    return;
  }
  u32* __restrict__ XT = A.XT;
  char* hT = smem; char* U = smem + 8 * HT_RS; float* part = (float*)(smem + 8 * HT_RS + 8 * 17 * 80);
  char* tab = smem + 8 * HT_RS + 8 * 17 * 80 + 5 * 16 * 4;
  const int tid = threadIdx.x, lane = tid & 63, w = __builtin_amdgcn_readfirstlane(tid >> 6);
  const int s0 = row_group(bid - K2_MAIN0);
  const int c = lane & 15, g = lane >> 4, q = c >> 2, p = c & 3;
  const u16* xrow = A.x + (size_t)s0 * D_;
  f32x4 tw0, tw1; u32 ard, awr, aw0, acr; float z0r, z0i;
  auto precompute = [&]() {
    const int Qw = 4 * w + q, rho = Qw / 5, d0 = 4 * (Qw % 5) + p;
    const bool rot = rho >= 3 && (rho & 1);
    const float fd = (float)d0 * (1.0f / 640.0f);
    const int k10 = g == 0 ? 16 : 2 * g;
    float s_[4], c_[4];
    sincos_rev(fd * (float)k10, s_[0], c_[0]); sincos_rev(fd * (float)(2 * g + 1), s_[1], c_[1]);
    sincos_rev(fd * (float)(8 + 2 * g), s_[2], c_[2]); sincos_rev(fd * (float)(9 + 2 * g), s_[3], c_[3]);
    tw0 = f32x4{rot ? s_[0] : c_[0], rot ? c_[0] : -s_[0], rot ? s_[1] : c_[1], rot ? c_[1] : -s_[1]};
    tw1 = f32x4{rot ? s_[2] : c_[2], rot ? c_[2] : -s_[2], rot ? s_[3] : c_[3], rot ? c_[3] : -s_[3]};
    const int Qr = 4 * w + p;
    ard = lds_addr(hT) + (Qr / 5) * HT_RS + 2 * (20 * (8 * g + q) + 4 * (Qr % 5));
    awr = lds_addr(U) + 4 * (rho * 17 * 20 + d0 + 2 * g * 20);
    aw0 = g == 0 ? awr + 4 * 16 * 20 : awr;
    z0r = rot ? 0.f : 1.f; z0i = rot ? 1.f : 0.f;
    acr = lds_addr(U) + (w < 8 ? ((2 * (w >> 1)) * 17 + c) * 80 : ((c & 7) * 17 + 16) * 80);
    pin(tw0); pin(tw1); pin(ard); pin(awr); pin(aw0); pin(z0r); pin(z0i); pin(acr);
  };
  if (w < 5) {
    u32 xv[8];
#pragma unroll
    for (int r = 0; r < 8; ++r) xv[r] = ldg<u32>(xrow + (size_t)r * 256 * D_, 4 * tid);
    const u32 tagv = ld_agent(A.gen);
    precompute();
    STAMP(1, 1);
    WAITALL();
    STAMP(1, 5);
    __syncthreads();
    float na[8], nb[8];
    ln_bcast8(part, lane, na, nb);
    f32x2 o[8];
    {
      f32x2 h[8];
#pragma unroll
      for (int r = 0; r < 8; ++r) { f32x2 v = {bf_lo(xv[r]), bf_hi(xv[r])}; h[r] = v * na[r] + nb[r]; }
      const float cc = 0.70710678118654752f;
      f32x2 a0 = h[0] + h[4], a1 = h[1] + h[5], a2 = h[2] + h[6], a3 = h[3] + h[7];
      f32x2 b0 = h[0] - h[4], b1 = h[1] - h[5], b2 = h[2] - h[6], b3 = h[3] - h[7];
      f32x2 e = a0 + a2, f = a1 + a3, u = cc * (b1 - b3), v = cc * (b1 + b3);
      o[0] = e + f; o[1] = e - f; o[2] = b0 + u; o[3] = -b2 - v; o[4] = a0 - a2; o[5] = a3 - a1; o[6] = b0 - u; o[7] = b2 - v;
#pragma unroll
      for (int r = 0; r < 8; ++r) pin(o[r]);
    }
    const u32 tag = __builtin_amdgcn_readfirstlane(tagv) + 1u;
    if (tid == 0) st_agent(A.arr + bid, tag);
    f32x2 sa, gg;
    {
      const unsigned long long* ga_ = A.gran + 2 * tid;
      u32x4 va, vb, vc, vd;
      for (int spin = 0; spin < SPIN_LIMIT; ++spin) {
        asm volatile("global_load_dwordx4 %0, %4, off sc1\n\tglobal_load_dwordx4 %1, %5, off sc1\n\tglobal_load_dwordx4 %2, %6, off sc1\n\tglobal_load_dwordx4 %3, %7, off sc1\n\ts_waitcnt vmcnt(0)"
                     : "=&v"(va), "=&v"(vb), "=&v"(vc), "=&v"(vd) : "v"(ga_), "v"(ga_ + 1280), "v"(ga_ + 640), "v"(ga_ + 1920) : "memory");
        const bool ok = va[1] == tag && va[3] == tag && vb[1] == tag && vb[3] == tag && vc[1] == tag && vc[3] == tag && vd[1] == tag && vd[3] == tag;
        if (__all(ok)) break;
      }
      sa = f32x2{rbf(__uint_as_float(va[0]) + __uint_as_float(vb[0])), rbf(__uint_as_float(va[2]) + __uint_as_float(vb[2]))};
      gg = f32x2{rbf(1.0f + rbf(__uint_as_float(vc[0]) + __uint_as_float(vd[0]))), rbf(1.0f + rbf(__uint_as_float(vc[2]) + __uint_as_float(vd[2])))};
    }
    STAMP(1, 2);
    o[0] = o[0] * gg + 8.0f * sa;
    *(u32*)(hT + 4 * tid) = pk_bf(o[0][0], o[0][1]);
#pragma unroll
    for (int r = 1; r < 8; ++r) { const f32x2 v = o[r] * gg; *(u32*)(hT + r * HT_RS + 4 * tid) = pk_bf(v[0], v[1]); }
  } else {
    const int i = w - 5;
    u32x4 f[4];
    const u32 xo = (u32)((c & 7) * 256 * D_ + 128 * i + 8 * g) * 2u;
#pragma unroll
    for (int k = 0; k < 4; ++k) f[k] = ldg<u32x4>(xrow, xo + 64 * k);
    const s16x8 t0 = i < 2 ? ldg<s16x8>(g_f32a.v[i < 2 ? i : 0], lane * 16) : ldg<s16x8>(&g_f20a.v[0][0][0][0] + (i - 2) * 512, lane * 16);
    const s16x8 t1 = ldg<s16x8>(&g_f20a.v[0][0][0][0] + (i < 3 ? i + 3 : 5) * 512, lane * 16);
    precompute();
    if (c >= 8) {
      const u32 fill = c == 8 ? 0x3f803f80u : 0u;
#pragma unroll
      for (int k = 0; k < 4; ++k) f[k] = u32x4{fill, fill, fill, fill};
    }
    gram_part(f, part, i, lane);
    *(s16x8*)(tab + i * 1024 + lane * 16) = t0;
    if (i < 3) *(s16x8*)(tab + (i + 5) * 1024 + lane * 16) = t1;
    __syncthreads();
    const int task = (bid - K2_MAIN0) * 5 + i;
    if (task < 1080) prep_gu(task, A.wg, A.wu, A.Wgu, smem + 32768 + 4096 * i, lane);
    if (i == 4) {
      const int xc = bid & 7, li = (bid - K2_MAIN0) >> 3;
#pragma unroll
      for (int k = 0; k < 4; ++k) {
        const int e = (k & 1) * 64 + lane, id = li * 96 + e;
        const int ln = id % 6, row = 8 * ((id / 6) & 255) + (id < 1536 ? xc : ((8 - xc) & 7));
        const u32 off = min((u32)(row * 642 + ln * 128), (u32)(2048 * 642 - 4)) & ~3u;
        if (e < 96) l2_touch((k >> 1) ? A.wi : A.wr, off, lds_addr(smem) + sizeof(smem) - 256);
      }
    }
  }
  __syncthreads();
  STAMP(1, 3);
  {
    const LDSP(const char) base = (LDSP(const char))(size_t)ard;
    s16x8 b = cat8(__builtin_amdgcn_ds_read_tr16_b64_v4i16((LDSP(s16x4))base), __builtin_amdgcn_ds_read_tr16_b64_v4i16((LDSP(s16x4))(base + 160)));
    const s16x8 fa0 = *(const s16x8*)(tab + lane * 16), fa1 = *(const s16x8*)(tab + 1024 + lane * 16);
    const f32x4 z = {0.f, 0.f, 0.f, 0.f};
    f32x4 a0 = mfma16(fa0, b, z), a1 = mfma16(fa1, b, z);
    LDSP(u32) Ub = (LDSP(u32))(size_t)awr;
    const float x0 = g == 0 ? a0[1] : a0[0], x1 = g == 0 ? 0.f : a0[1];
    *(LDSP(u32))(size_t)aw0 = pk_bf(x0 * tw0[0] - x1 * tw0[1], x0 * tw0[1] + x1 * tw0[0]);
    if (g == 0) Ub[0] = pk_bf(a0[0] * z0r, a0[0] * z0i);
    Ub[20] = pk_bf(a0[2] * tw0[2] - a0[3] * tw0[3], a0[2] * tw0[3] + a0[3] * tw0[2]);
    Ub[8 * 20] = pk_bf(a1[0] * tw1[0] - a1[1] * tw1[1], a1[0] * tw1[1] + a1[1] * tw1[0]);
    Ub[9 * 20] = pk_bf(a1[2] * tw1[2] - a1[3] * tw1[3], a1[2] * tw1[3] + a1[3] * tw1[2]);
  }
  __syncthreads();
  STAMP(1, 4);
  const f32x4 z4 = {0.f, 0.f, 0.f, 0.f};
  u32* XTb = XT + (size_t)s0 * 16;
  const char* ft = tab + 2048 + lane * 16;
  const LDSP(const char) be = (LDSP(const char))(size_t)acr;
  const int P = w >> 1, mp = P, mm = P == 0 ? 4 : 8 - P;
  if (w < 8) {
    const s16x8 e0 = *(const LDSP(s16x8))(be + 16 * g), e1 = *(const LDSP(s16x8))(be + 64), o0 = *(const LDSP(s16x8))(be + 1360 + 16 * g), o1 = *(const LDSP(s16x8))(be + 1360 + 64);
    char* dp = (char*)(XTb + (size_t)(mp * 21) * 4096); char* dm = (char*)(XTb + (size_t)(mm * 21) * 4096);
    const u32 vo = (u32)(2 * g * 4096 + c) * 4u;
    if (!(w & 1)) {
#pragma unroll
      for (int mt = 0; mt < 2; ++mt) {
        const s16x8 f0 = *(const s16x8*)(ft + (2 * mt) * 1024), f1 = *(const s16x8*)(ft + (2 * mt + 1) * 1024);
        f32x4 ae = mfma16(f1, e1, mfma16(f0, e0, z4)), ao = mfma16(f1, o1, mfma16(f0, o0, z4));
        f32x4 tp = ae + ao, tm = ae - ao;
        if (P == 0) { tp = ae; tm = ao; }
        char* ep = dp + (size_t)(8 * mt) * 16384; char* em = dm + (size_t)(8 * mt) * 16384;
        const int k0 = 8 * mt + 2 * g;
        { const float d0_ = (c == 0 && k0 >= 1 && k0 <= 9) ? 2.f : 1.f, d1_ = (c == 0 && k0 + 1 <= 9) ? 2.f : 1.f;
          const f32x4 dd = {d0_, d0_, d1_, d1_}; tp = tp * dd; tm = tm * dd; }
        if (mt == 0 || c != 0 || k0 <= 10) { stg_sel<4>(ep, vo, pk_bf(tp[0], tp[1])); stg_sel<4>(em, vo, pk_bf(tm[0], tm[1])); }
        if (mt == 0 || c != 0 || k0 + 1 <= 10) { stg_sel<4>(ep + 16384, vo, pk_bf(tp[2], tp[3])); stg_sel<4>(em + 16384, vo, pk_bf(tm[2], tm[3])); }
      }
    } else {
      const s16x8 f0 = *(const s16x8*)(ft + 4 * 1024), f1 = *(const s16x8*)(ft + 5 * 1024);
      f32x4 ae = mfma16(f1, e1, mfma16(f0, e0, z4)), ao = mfma16(f1, o1, mfma16(f0, o0, z4));
      f32x4 tp = ae + ao, tm = ae - ao;
      if (P == 0) { tp = ae; tm = ao; }
      if (g < 2 && c != 0) {
        char* ep = dp + (size_t)16 * 16384; char* em = dm + (size_t)16 * 16384;
        stg_sel<4>(ep, vo, pk_bf(tp[0], tp[1])); stg_sel<4>(ep + 16384, vo, pk_bf(tp[2], tp[3]));
        stg_sel<4>(em, vo, pk_bf(tm[0], tm[1])); stg_sel<4>(em + 16384, vo, pk_bf(tm[2], tm[3]));
      }
    }
  } else {
    const int rh = c & 7, mt = w - 7;
    const s16x8 b0 = *(const LDSP(s16x8))(be + 16 * g), b1 = *(const LDSP(s16x8))(be + 64);
    const s16x8 f0 = *(const s16x8*)(ft + (2 * mt) * 1024), f1 = *(const s16x8*)(ft + (2 * mt + 1) * 1024);
    f32x4 a = mfma16(f1, b1, mfma16(f0, b0, z4));
    const float ca_ = (rh >= 2 && (rh & 1)) ? -1.f : 1.f, cb_ = rh >= 2 ? 1.f : 0.f;
    f32x4 t;
#pragma unroll
    for (int r = 0; r < 4; ++r) t[r] = ca_ * a[r] + cb_ * dpp_x1(a[r]);
    const int m1 = rh == 0 ? 0 : rh == 1 ? 4 : (rh & 1) ? 8 - (rh >> 1) : (rh >> 1);
    const int k0 = 8 * mt + 2 * g;
    const u32 o0 = (u32)((m1 * 21 + (k0 == 10 ? 20 : k0)) * 4096) * 4u, o1 = (u32)((m1 * 21 + k0 + 1) * 4096) * 4u;
    if (c < 8 && k0 >= 10 && k0 < 20) { stg_sel<4>(XTb, o0, pk_bf(t[0], t[1])); stg_sel<4>(XTb, o1, pk_bf(t[2], t[3])); }
    if (w == 9) asm volatile("s_waitcnt vmcnt(0)" ::: "memory");
  }
  STAMP(1, 7);
}

constexpr int EX_RS = 20;
__global__ __launch_bounds__(512) void k3_mid(const u32* __restrict__ XT, const u16* __restrict__ wr, const u16* __restrict__ wi, u32* __restrict__ ZT) {
  STAMP_DECL; STAMP(2, 0);
  __shared__ __attribute__((aligned(16))) u32 ex[2][256 * EX_RS];
  __shared__ __attribute__((aligned(16))) char tab[8 * 1024];
  const int tid = threadIdx.x, lane = tid & 63, w = __builtin_amdgcn_readfirstlane(tid >> 6), c = lane & 15, g = lane >> 4;
  const int m1 = blockIdx.x & 7, tile = blockIdx.x >> 3;
  const char* in = (const char*)(XT + (size_t)(m1 * 21 + tile) * 4096);
  dma1k(in + 2048 * w, (u32)lane * 16u, lds_addr(ex[1]) + 2048 * w);
  dma1k(in + 2048 * w + 1024, (u32)lane * 16u, lds_addr(ex[1]) + 2048 * w + 1024);
  const F8w* tsrc = w < 2 ? &g_a1[m1] : w < 4 ? &g_f16f : w < 6 ? &g_f16i : &g_a1i[m1];
  const s16x8 tfrag = ldg<s16x8>(tsrc->v[w & 1], lane * 16);
  u32 wra[2][4], wia[2][4];
  const int kb = tile == 20 ? 336 : (c == 0 && tile >= 11) ? 32 * tile + 16 : 32 * tile + c;
  const bool mir = kb > 320;
  {
    const int kk = mir ? 640 - kb : kb;
#pragma unroll
    for (int j = 0; j < 2; ++j)
#pragma unroll
      for (int r = 0; r < 4; ++r) {
        const int m = m1 + 8 * (2 * w + j) + 128 * (4 * g + r), mm = mir ? ((2048 - m) & 2047) : m;
        const u32 vo = (u32)(mm * DF + kk) * 2u;
        wra[j][r] = (u32)ldg<u16>(wr, vo) << 16; wia[j][r] = (u32)ldg<u16>(wi, vo) << 16;
      }
  }
  f32x4 fcs[2], fsn[2], ics[2], isn[2];
#pragma unroll
  for (int j = 0; j < 2; ++j)
#pragma unroll
    for (int r = 0; r < 4; ++r) {
      float s_, c_;
      sincos_rev((float)((2 * w + j) * (m1 + 8 * (4 * g + r))) * (1.0f / 2048), s_, c_); fcs[j][r] = c_; fsn[j][r] = s_;
      sincos_rev((float)((4 * g + r) * (m1 + 8 * (2 * w + j))) * (1.0f / 2048), s_, c_); ics[j][r] = c_; isn[j][r] = s_;
    }
  u32 awr = lds_addr(ex) + 4 * ((4 * g * 16 + c) * EX_RS + 2 * w);
  u32 ard = lds_addr(ex) + 4 * ((2 * w * 16 + c) * EX_RS + 4 * g);
#pragma unroll
  for (int j = 0; j < 2; ++j) { pin(fcs[j]); pin(fsn[j]); pin(ics[j]); pin(isn[j]); }
  pin(awr); pin(ard);
  *(s16x8*)(tab + w * 1024 + lane * 16) = tfrag;
  STAMP(2, 1);
  wait_vm<16>();
  __syncthreads();
  STAMP(2, 2);
  u32x4 bx[2];
#pragma unroll
  for (int j = 0; j < 2; ++j)
#pragma unroll
    for (int i = 0; i < 4; ++i) bx[j][i] = ex[1][(16 * (4 * g + i) + 2 * w + j) * 16 + c];
  const f32x4 z4 = {0.f, 0.f, 0.f, 0.f};
  const char* tb = tab + lane * 16;
  const LDSP(char) wr0 = (LDSP(char))(size_t)awr; const LDSP(const char) rd0 = (LDSP(const char))(size_t)ard;
  constexpr int RB = 16 * EX_RS * 4, XB = 256 * EX_RS * 4;
  {
    const s16x8 A10 = *(const s16x8*)tb, A11 = *(const s16x8*)(tb + 1024);
    u32 pk[2][4];
#pragma unroll
    for (int j = 0; j < 2; ++j) {
      f32x4 re = mfma16(A10, as8(bx[j]), z4), im = mfma16(A11, as8(bx[j]), z4);
      f32x4 tr = re * fcs[j] + im * fsn[j], ti = im * fcs[j] - re * fsn[j];
#pragma unroll
      for (int r = 0; r < 4; ++r) pk[j][r] = pk_bf(tr[r], ti[r]);
    }
#pragma unroll
    for (int r = 0; r < 4; ++r) *(LDSP(u32x2))(wr0 + r * RB) = u32x2{pk[0][r], pk[1][r]};
  }
  __syncthreads();
  STAMP(2, 3);
  {
    const s16x8 F0 = *(const s16x8*)(tb + 2048), F1 = *(const s16x8*)(tb + 3072), G0 = *(const s16x8*)(tb + 4096), G1 = *(const s16x8*)(tb + 5120);
    u32 pk[2][4];
#pragma unroll
    for (int j = 0; j < 2; ++j) {
      const u32x4 b = *(const LDSP(u32x4))(rd0 + j * RB);
      f32x4 xr = mfma16(F0, as8(b), z4), xi = mfma16(F1, as8(b), z4);
      const f32x4 a = {__uint_as_float(wra[j][0]), __uint_as_float(wra[j][1]), __uint_as_float(wra[j][2]), __uint_as_float(wra[j][3])};
      const u32 sg = mir ? 0x80000000u : 0u;
      const f32x4 bb = {__uint_as_float(wia[j][0] ^ sg), __uint_as_float(wia[j][1] ^ sg), __uint_as_float(wia[j][2] ^ sg), __uint_as_float(wia[j][3] ^ sg)};
      const f32x4 yr = xr * a - xi * bb, yi = xr * bb + xi * a;
      const u32x4 yb = {pk_bf(yr[0], yr[1]), pk_bf(yr[2], yr[3]), pk_bf(yi[0], yi[1]), pk_bf(yi[2], yi[3])};
      f32x4 re = mfma16(G0, as8(yb), z4), im = mfma16(G1, as8(yb), z4);
      f32x4 tr = re * ics[j] - im * isn[j], ti = im * ics[j] + re * isn[j];
#pragma unroll
      for (int r = 0; r < 4; ++r) pk[j][r] = pk_bf(tr[r], ti[r]);
    }
#pragma unroll
    for (int r = 0; r < 4; ++r) *(LDSP(u32x2))(wr0 + XB + r * RB) = u32x2{pk[0][r], pk[1][r]};
  }
  __syncthreads();
  STAMP(2, 4);
  char* out = (char*)(ZT + (size_t)(2 * w) * 8 * 21 * 16 + (m1 * 21 + tile) * 16);
  const u32 vout = (u32)(64 * g * 10752 + c * 4);
  const s16x8 I0 = *(const s16x8*)(tb + 6144), I1 = *(const s16x8*)(tb + 7168);
#pragma unroll
  for (int j = 0; j < 2; ++j) {
    const u32x4 b = *(const LDSP(u32x4))(rd0 + XB + j * RB);
    f32x4 zr = mfma16(I0, as8(b), z4), zi = mfma16(I1, as8(b), z4);
#pragma unroll
    for (int r = 0; r < 4; ++r) stg_sel<8>(out + (16 * r + j) * 10752, vout, pk_bf(zr[r], zi[r]));
  }
  STAMP(2, 7);
}

constexpr int Y_RS = 640 + 4;
__global__ __launch_bounds__(640) void k4_inv(const u32* __restrict__ ZT, const u16* __restrict__ x, const float* __restrict__ mod,
                                              u16* __restrict__ x1o, u16* __restrict__ h2o, const u16* __restrict__ Wgu) {
  STAMP_DECL; STAMP(3, 0);
  __shared__ __attribute__((aligned(16))) char smem[162 * 64 + 8 * Y_RS * 4 + 10 * 8 * 4 + 8 * 1024 + 10752 + 256];
  char* Vs = smem; float* Y = (float*)(Vs + 162 * 64); float* part = Y + 8 * Y_RS; char* tab = (char*)(part + 80);
  const int tid = threadIdx.x, lane = tid & 63, w = __builtin_amdgcn_readfirstlane(tid >> 6), c = lane & 15, g = lane >> 4;
  const int s0 = row_group(blockIdx.x);
  u32* Zs = (u32*)(tab + 8 * 1024);
  {
    const char* Zb = (const char*)(ZT + (size_t)s0 * 8 * 21 * 16);
    dma1k(Zb + 1024 * w, (u32)lane * 16u, lds_addr(Zs) + 1024 * w);
    if (w == 0 && lane < 32) dma1k(Zb + 10240, (u32)lane * 16u, lds_addr(Zs) + 10240);
  }
  const int rho = w < 8 ? w : (c & 7), P = rho >> 1, mA = rho == 1 ? 4 : P, mB = P == 0 ? 4 : 8 - P;
  const s16x8 tfrag = w < 6 ? ldg<s16x8>(&g_f20ai.v[0][0][0][0] + w * 512, lane * 16) : ldg<s16x8>(g_f32bi.v[(w - 6) & 1], lane * 16);
  const int hf = w >= 5, cp = tid - 320 * hf;
  u32 xv[4];
#pragma unroll
  for (int r = 0; r < 4; ++r) xv[r] = ldg<u32>(x + (size_t)(s0 + 256 * (4 * hf + r)) * D_, 4 * cp);
  const f32x2 ga = ldg<f32x2>(mod + 2 * 640, 8 * cp), sb = ldg<f32x2>(mod + 3 * 640, 8 * cp), cb = ldg<f32x2>(mod + 4 * 640, 8 * cp);
  f32x4 tw[3]; u32 aw[3];
  {
    const bool rot = rho >= 3 && (rho & 1);
    const float sc = w < 8 ? 1.0f / (2048.0f * 640.0f) : 2.0f / (2048.0f * 640.0f), fk = (float)(w < 8 ? c : 16) * (1.0f / 640.0f);
#pragma unroll
    for (int mt = 0; mt < 3; ++mt) {
#pragma unroll
      for (int rr = 0; rr < 2; ++rr) {
        float s_, c_; sincos_rev(fk * (float)(8 * mt + 2 * g + rr), s_, c_);
        tw[mt][2 * rr] = (rot ? -s_ : c_) * sc; tw[mt][2 * rr + 1] = (rot ? c_ : s_) * sc;
      }
      const int d0 = 8 * mt + 2 * g;
      const bool ok = d0 < 20 && (w < 8 || c < 8);
      aw[mt] = lds_addr(Vs) + (ok ? (rho * 20 + d0) * 64 : 160 * 64) + (w < 8 ? 4 * c : 2);
      pin(tw[mt]); pin(aw[mt]);
    }
  }
  float be = rho < 2 ? 0.f : (rho & 1) ? -1.f : 1.f;
  u32 msk[8], zoa[8];
#pragma unroll
  for (int i = 0; i < 8; ++i) {
    const int k0 = i < 4 ? 4 * g + i : 16 + (i - 4);
    const bool dead = w < 8 ? (c == 0 && k0 > 10) : (k0 < 10 || (i >= 4 && g != 0));
    msk[i] = dead ? 0u : 0xffffffffu;
    zoa[i] = w < 8 ? k0 * 16 + c : (k0 < 10 ? 11 : k0 == 10 ? 20 : k0) * 16;
    pin(msk[i]); pin(zoa[i]);
  }
  u32 zba = lds_addr(Zs) + mA * 21 * 64, zbb = lds_addr(Zs) + mB * 21 * 64;
  pin(be); pin(zba); pin(zbb);
  STAMP(3, 1);
  if (w < 8) *(s16x8*)(tab + w * 1024 + lane * 16) = tfrag;
  wait_vm<4>();
  __syncthreads();
  u32x4 b0, b1;
  if (w < 9) {
#pragma unroll
    for (int i = 0; i < 8; ++i) {
      const u32 zz = *(const LDSP(u32))(size_t)(zba + 4 * zoa[i]) & msk[i], zy = *(const LDSP(u32))(size_t)(zbb + 4 * zoa[i]) & msk[i];
      const f32x2 a = {bf_lo(zz), bf_hi(zz)}, b = {bf_lo(zy), bf_hi(zy)};
      const f32x2 v = a + be * b;
      if (i < 4) b0[i] = pk_bf(v[0], v[1]); else b1[i - 4] = pk_bf(v[0], v[1]);
    }
  }
  STAMP(3, 2);
  if (w < 9) {
    f32x4 a[3];
#pragma unroll
    for (int mt = 0; mt < 3; ++mt) {
      const f32x4 z = {0.f, 0.f, 0.f, 0.f};
      const s16x8 f0 = *(const s16x8*)(tab + (2 * mt) * 1024 + lane * 16), f1 = *(const s16x8*)(tab + (2 * mt + 1) * 1024 + lane * 16);
      a[mt] = mfma16(f1, as8(b1), mfma16(f0, as8(b0), z));
    }
    if (w < 8) {
      const u32 hi_skip = c == 0 ? 1u : 0u;
#pragma unroll
      for (int mt = 0; mt < 3; ++mt)
#pragma unroll
        for (int rr = 0; rr < 2; ++rr) {
          const u32 pv = pk_bf(a[mt][2 * rr] * tw[mt][2 * rr] - a[mt][2 * rr + 1] * tw[mt][2 * rr + 1], a[mt][2 * rr] * tw[mt][2 * rr + 1] + a[mt][2 * rr + 1] * tw[mt][2 * rr]);
          LDSP(char) dst = (LDSP(char))(size_t)aw[mt] + 64 * rr;
          if (hi_skip) *(LDSP(u16))dst = (u16)pv; else *(LDSP(u32))dst = pv;
        }
    } else {
#pragma unroll
      for (int mt = 0; mt < 3; ++mt)
#pragma unroll
        for (int rr = 0; rr < 2; ++rr)
          *(LDSP(u16))((LDSP(char))(size_t)aw[mt] + 64 * rr) = (u16)pk_bf(a[mt][2 * rr] * tw[mt][2 * rr] - a[mt][2 * rr + 1] * tw[mt][2 * rr + 1], 0.f);
    }
  }
  __syncthreads();
  STAMP(3, 3);
  {
    const s16x8 a = *(const s16x8*)(Vs + (16 * w + c) * 64 + 16 * g);
    const s16x8 fb0 = *(const s16x8*)(tab + 6144 + lane * 16), fb1 = *(const s16x8*)(tab + 7168 + lane * 16);
    const f32x4 z = {0.f, 0.f, 0.f, 0.f};
    const f32x4 y0 = mfma16(a, fb0, z), y1 = mfma16(a, fb1, z);
    const int R = 16 * w + 4 * g, rr = R / 20, d0 = R % 20;
    *(f32x4*)(Y + rr * Y_RS + 20 * c + d0) = y0;
    *(f32x4*)(Y + rr * Y_RS + 20 * (16 + c) + d0) = y1;
  }
  __syncthreads();
  STAMP(3, 4);
  {
    f32x2 R[8];
#pragma unroll
    for (int r = 0; r < 8; ++r) R[r] = *(const f32x2*)(Y + r * Y_RS + 2 * cp);
    const float cc = 0.70710678118654752f;
    const f32x2 G = R[0] + R[1], Fm = R[0] - R[1];
    const f32x2 u = cc * (R[2] - R[6]), v = cc * (R[3] + R[7]);
    const f32x2 c13 = R[2] + R[6], s13 = R[3] - R[7];
    const float sg = hf ? -1.f : 1.f;
    f32x2 y[4];
    y[0] = G + R[4] + sg * c13;
    y[2] = G - R[4] + sg * s13;
    y[1] = Fm + R[5] + sg * (u + v);
    y[3] = Fm - R[5] + sg * (v - u);
    u32 x1[4];
    float sv[8];
#pragma unroll
    for (int r = 0; r < 4; ++r) {
      const f32x2 xx = {bf_lo(xv[r]), bf_hi(xv[r])};
      const f32x2 t = xx + ga * y[r];
      x1[r] = pk_bf(t[0], t[1]);
      stg_sel<2>(x1o + (size_t)(8 * s0 + 4 * hf + r) * D_, (u32)cp * 4u, x1[r]);
      sv[r] = dot2bf(x1[r], 0x3f803f80u, 0.f); sv[4 + r] = dot2bf(x1[r], x1[r], 0.f);
    }
    const float tot = reduce8(sv, lane);
    if (lane < 8) part[w * 8 + lane] = tot;
    const f32x2 gg = {rbf(1.0f + cb[0]), rbf(1.0f + cb[1])};
    const int d = 2 * cp, wi = d & 31;
    char* stage = (char*)Y;
    const int so = (((d >> 5) * 4 + ((wi & 15) >> 2)) * 8 + 4 * hf) * 16 + (4 * (wi >> 4) + (wi & 3)) * 2;
    __syncthreads();
    STAMP(3, 5);
    const int r = lane & 3;
    float s = 0.f, qq = 0.f;
#pragma unroll
    for (int i = 0; i < 5; ++i) { s += part[(5 * hf + i) * 8 + r]; qq += part[(5 * hf + i) * 8 + 4 + r]; }
    float m_, i_; ln_finish(s, qq, m_, i_);
    const float nb_ = -m_ * i_;
#pragma unroll
    for (int i = 0; i < 4; ++i) {
      const float na = __int_as_float(__builtin_amdgcn_readlane(__float_as_int(i_), i)), nb = __int_as_float(__builtin_amdgcn_readlane(__float_as_int(nb_), i));
      const f32x2 v2 = {bf_lo(x1[i]), bf_hi(x1[i])};
      const f32x2 hv = (v2 * na + nb) * gg + sb;
      *(u32*)(stage + so + 16 * i) = pk_bf(hv[0], hv[1]);
    }
    __syncthreads();
    const u32x4 piece = *(const u32x4*)(stage + 16 * tid);
    stg_sel<16>(h2o + ((size_t)(s0 >> 1) * 20 * 64 + 8 * (s0 & 1)) * 8, (u32)((tid >> 3) * 16 + (tid & 7)) * 16u, piece);
  }
  STAMP(3, 7);
}

constexpr int K5_SLOT = 28 * 1024;
constexpr int K5_NS = 4;
constexpr int K5_PD = 4;
struct K5Ctx { const u16* wdp; int pfl; u32 dump; const u16* wsrc; const u16* hsrc; u32 ldsw; u32 goff[7]; u32 hoff; u32 abase[2]; };
template <int s, int i> DI void k5_w1(const K5Ctx& c) {
  if constexpr (i == 0) set_m0(c.ldsw + (s % K5_NS) * K5_SLOT + 3072);
  glds16_s<1024 * i - 3072>((const char*)(c.wsrc + s * 4 * 512) - 4096, c.goff[i]);
}
template <int s, int i> DI void k5_t1(const K5Ctx& c, u32x4 (&t)[4]) {
  gload16_s<(i >> 1) * 1024>(t[i], c.hsrc + ((i & 1) * 20 + 2 * s) * 512, c.hoff);
}
template <int s> DI void k5_wall(const K5Ctx& c) { k5_w1<s, 0>(c); k5_w1<s, 1>(c); k5_w1<s, 2>(c); k5_w1<s, 3>(c); k5_w1<s, 4>(c); k5_w1<s, 5>(c); k5_w1<s, 6>(c); }
template <int s> DI void k5_tall(const K5Ctx& c, u32x4 (&t)[4]) { k5_t1<s, 0>(c, t); k5_t1<s, 1>(c, t); k5_t1<s, 2>(c, t); k5_t1<s, 3>(c, t); }
template <int n> DI s16x8 k5_rd(const K5Ctx& c) {
  constexpr int s = n / 28, t = (n / 14) & 1, j = n % 14, sl = s % K5_NS;
  return *(const LDSP(const s16x8))((LDSP(const char))(size_t)c.abase[sl >> 1] + (sl & 1) * K5_SLOT + ((j >> 1) * 4 + t * 2 + (j & 1)) * 1024);
}
template <int n> DI void k5_sub(const K5Ctx& c, f32x4 (&acc)[14][2], s16x8 (&fr)[K5_PD + 1], u32x4 (&tok)[10][4]) {
  constexpr int s = n / 28, t = (n / 14) & 1, j = n % 14;
  constexpr int nb = n + K5_PD;
  if constexpr (nb < 280 && nb % 28 == 0) {
    wait_vm<(s + 2 <= 9) ? 11 : 0>();
    __builtin_amdgcn_s_barrier();
    SLAB(s + 1);
    tie4(tok[s + 1]);
  }
  if constexpr (nb < 280) fr[nb % (K5_PD + 1)] = k5_rd<nb>(c);
  {
    constexpr int q = nb / 28, pos = nb % 28 - (q == 0 ? K5_PD : 0);
    constexpr int idx = (pos % 2 == 0 && pos < 22) ? pos / 2 : -1;
    if constexpr (idx >= 0 && idx < 4 && q + 2 <= 9) k5_t1<q + 2, idx>(c, tok[q + 2]);
    if constexpr (idx >= 4 && q + 2 <= 9) k5_w1<q + 2, idx - 4>(c);
  }
  if constexpr (false) {
    constexpr int i = (n - 224) / 2;
    l2_touch(c.wdp, (u32)(c.pfl + i * 64) * 128u, c.dump);
  }
  const s16x8 a = fr[n % (K5_PD + 1)];
  acc[j][0] = mfma16(a, as8(tok[s][2 * t]), acc[j][0]);
  acc[j][1] = mfma16(a, as8(tok[s][2 * t + 1]), acc[j][1]);
  __builtin_amdgcn_sched_barrier(0);
}
template <int... I> DI void k5_all(const K5Ctx& c, f32x4 (&acc)[14][2], s16x8 (&fr)[K5_PD + 1], u32x4 (&tok)[10][4], std::integer_sequence<int, I...>) { (k5_sub<I>(c, acc, fr, tok), ...); }
__global__ __launch_bounds__(256, 2) void k5_glu(const u16* __restrict__ h2, const u16* __restrict__ Wgu, u16* __restrict__ hb, const u16* __restrict__ Wd) {
  STAMP_DECL; STAMP(4, 0);
  extern __shared__ __attribute__((aligned(16))) char smem[];
  const int tid = threadIdx.x, lane = tid & 63, w = __builtin_amdgcn_readfirstlane(tid >> 6);
  const int xcd = blockIdx.x & 7, li = blockIdx.x >> 3;
  const int tt = 8 * (xcd & 1) + (li & 7), fc = 4 * (xcd >> 1) + (li >> 3);
  const int u0 = fc < 12 ? 7 * fc : 84 + 6 * (fc - 12), U = fc < 12 ? 7 : 6;
  K5Ctx c;
  c.wsrc = Wgu + (size_t)u0 * 20 * 2 * 512;
  c.dump = lds_addr(smem) + 2 * K5_SLOT + w * 256;
  c.wdp = Wd + (size_t)(20 * (xcd >> 2)) * 54 * 512;
#pragma unroll
  for (int i = 0; i < 7; ++i) { const int f = 7 * w + i, u = min(f >> 2, U - 1); c.goff[i] = 2 * (u * 20 * 2 * 512 + (f & 3) * 512) + lane * 16 + 4096 - (1024 * i - 3072); }
  c.ldsw = lds_addr(smem) + 7 * w * 1024;
  const int T16 = 8 * tt + 2 * w;
  c.hsrc = h2 + (size_t)T16 * 20 * 512; c.hoff = lane * 16;
#pragma unroll
  for (int j = 0; j < 2; ++j) { c.abase[j] = lds_addr(smem) + 2 * j * K5_SLOT + lane * 16; asm volatile("" : "+v"(c.abase[j])); }
  u32x4 tok[10][4];
  k5_tall<0>(c, tok[0]); k5_wall<0>(c);
  k5_tall<1>(c, tok[1]); k5_wall<1>(c);
  f32x4 acc[14][2];
#pragma unroll
  for (int u = 0; u < 14; ++u) { acc[u][0] = f32x4{0.f, 0.f, 0.f, 0.f}; acc[u][1] = f32x4{0.f, 0.f, 0.f, 0.f}; }
  STAMP(4, 3);
  wait_vm<11>();
  __builtin_amdgcn_s_barrier();
  tie4(tok[0]);
  STAMP(4, 4); SLAB(0);
  s16x8 fr[K5_PD + 1];
  fr[0] = k5_rd<0>(c); fr[1] = k5_rd<1>(c); fr[2] = k5_rd<2>(c); fr[3] = k5_rd<3>(c);
  __builtin_amdgcn_sched_barrier(0);
  k5_all(c, acc, fr, tok, std::make_integer_sequence<int, 280>{});
  STAMP(4, 5); SLAB(10);
  auto hv = [&](int u, int tl) { const f32x4 a = acc[2 * u][tl] * acc[2 * u + 1][tl]; return u32x2{pk_bf(a[0], a[1]), pk_bf(a[2], a[3])}; };
#pragma unroll
  for (int tl = 0; tl < 2; ++tl) {
    u16* hdst = hb + (size_t)(T16 + tl) * 54 * 512 + (u0 >> 1) * 512;
    const u32 vo = (u32)lane * 16u;
    if (!(u0 & 1)) {
#pragma unroll
      for (int k = 0; k < 3; ++k) { const u32x2 a = hv(2 * k, tl), b = hv(2 * k + 1, tl); stg_sel<32>(hdst + k * 512, vo, u32x4{a[0], a[1], b[0], b[1]}); }
      if (U == 7) stg_sel<32>(hdst + 3 * 512, vo, hv(6, tl));
    } else {
      stg_sel<32>(hdst + 4, vo, hv(0, tl));
#pragma unroll
      for (int k = 0; k < 3; ++k) { const u32x2 a = hv(2 * k + 1, tl), b = hv(2 * k + 2, tl); stg_sel<32>(hdst + (k + 1) * 512, vo, u32x4{a[0], a[1], b[0], b[1]}); }
    }
  }
  STAMP(4, 7);
}

constexpr int K6_NS = 4;
struct K6Ctx { const u16* wsrc; const u16* hsrc; u32 hoff; const float* gbp; u32 gbo; const u16* xp; u32 xo; const u16* tbase; u32 toff; u32 dump; };
struct K6Grp { u32x4 a[5]; u32x4 t[4]; };
template <int i> DI void k6_v1(const K6Ctx& c, int ks, K6Grp& gr) {
  if constexpr (i < 5) gload16_s<0>(gr.a[i], c.wsrc + (ks + i * 54) * 512, c.hoff);
  else gload16_s<0>(gr.t[i - 5], c.hsrc + (ks + (i - 5) * 54) * 512, c.hoff);
}
DI void k6_issue(const K6Ctx& c, int ks, K6Grp& gr) {
  k6_v1<0>(c, ks, gr); k6_v1<5>(c, ks, gr); k6_v1<6>(c, ks, gr); k6_v1<7>(c, ks, gr); k6_v1<8>(c, ks, gr);
  k6_v1<1>(c, ks, gr); k6_v1<2>(c, ks, gr); k6_v1<3>(c, ks, gr); k6_v1<4>(c, ks, gr);
}
DI void k6_tie(K6Grp& g) { asm volatile("" : "+v"(g.a[0]), "+v"(g.a[1]), "+v"(g.a[2]), "+v"(g.a[3]), "+v"(g.a[4]), "+v"(g.t[0]), "+v"(g.t[1]), "+v"(g.t[2]), "+v"(g.t[3])); }
struct K6Epi { u32x4 gb[5]; u32x2 xr[5]; };
template <int OFF> DI void gload8_s(u32x2& d, const void* sbase, u32 voff) {
  asm volatile("global_load_dwordx2 %0, %1, %2 offset:%3" : "=v"(d) : "v"(voff), "s"(sbase), "n"(OFF) : "memory");
}
#define K6_TOUCH 0
#define K6_TDV 5
constexpr int K6_TD = K6_TDV, K6_TLAST = K6_TOUCH ? 12 : -1;
template <int j> DI void k6_touch(const K6Ctx& c, int lane) { if (lane < 13) l2_touch(c.tbase + j * 512, c.toff, c.dump); }
constexpr int k6_after(int i, int NK) {
  int n = 0; bool seen = false;
  for (int g = 0; g < 3; ++g) { if (seen) n += 9; if (g == i) seen = true; }
  for (int j = 3; j < K6_TD; ++j) if (seen && K6_TOUCH) n += 1;
  for (int st = 0; st < i; ++st) {
    if (st + K6_TD <= K6_TLAST && seen) n += 1;
    if (st == NK - 3 && seen) n += 10;
    if (st + 3 < NK) { if (seen) n += 9; if (st + 3 == i) seen = true; }
  }
  return n;
}
template <int i, int NK> DI void k6_step(const K6Ctx& c, int ks0, f32x4 (&acc)[5][4], K6Grp (&gr)[K6_NS], K6Epi& ep, f32x4* red, int w, int lane) {
  wait_vm<k6_after(i, NK)>();
  K6Grp& g = gr[i % K6_NS];
  k6_tie(g);
  K6Grp& nx = gr[(i + 3) % K6_NS];
  if constexpr (i + K6_TD <= K6_TLAST) k6_touch<i + K6_TD>(c, lane);
#pragma unroll
  for (int nt = 0; nt < 5; ++nt) {
    if constexpr (i + 3 < NK) {
      if (nt == 0) { k6_v1<0>(c, ks0 + i + 3, nx); k6_v1<5>(c, ks0 + i + 3, nx); }
      if (nt == 1) { k6_v1<6>(c, ks0 + i + 3, nx); k6_v1<7>(c, ks0 + i + 3, nx); }
      if (nt == 2) { k6_v1<8>(c, ks0 + i + 3, nx); k6_v1<1>(c, ks0 + i + 3, nx); }
      if (nt == 3) { k6_v1<2>(c, ks0 + i + 3, nx); k6_v1<3>(c, ks0 + i + 3, nx); }
      if (nt == 4) { k6_v1<4>(c, ks0 + i + 3, nx); }
    }
    if constexpr (i == NK - 3) {
      gload16_s<0>(ep.gb[nt], c.gbp + 16 * nt, c.gbo); gload8_s<0>(ep.xr[nt], c.xp + 16 * nt, c.xo);
    }
#pragma unroll
    for (int t = 0; t < 4; ++t) acc[nt][t] = mfma16(as8(g.a[nt]), as8(g.t[t]), acc[nt][t]);
    if constexpr (i == NK - 1) {
      if (nt >= 1) {
#pragma unroll
        for (int t = 0; t < 4; ++t) if (t != w) red[((w * 5 + nt - 1) * 4 + t) * 64 + lane] = acc[nt - 1][t];
      }
    }
    __builtin_amdgcn_sched_barrier(0);
  }
}
template <int i, int NK> struct K6Loop { static DI void run(const K6Ctx& c, int ks0, f32x4 (&acc)[5][4], K6Grp (&gr)[K6_NS], K6Epi& ep, f32x4* red, int w, int lane) {
  k6_step<i, NK>(c, ks0, acc, gr, ep, red, w, lane); K6Loop<i + 1, NK>::run(c, ks0, acc, gr, ep, red, w, lane); } };
template <int NK> struct K6Loop<NK, NK> { static DI void run(const K6Ctx&, int, f32x4 (&)[5][4], K6Grp (&)[K6_NS], K6Epi&, f32x4*, int, int) {} };
template <int NK> DI void k6_main(const K6Ctx& cx, int ks0, f32x4* red, int w, int lane, u16* __restrict__ out, u32 oo) {
  K6Grp gr[K6_NS]; K6Epi ep;
  k6_issue(cx, ks0, gr[0]); k6_issue(cx, ks0 + 1, gr[1]); k6_issue(cx, ks0 + 2, gr[2]);
  if constexpr (K6_TOUCH) { k6_touch<3>(cx, lane); k6_touch<4>(cx, lane); if constexpr (K6_TD > 5) k6_touch<5>(cx, lane); if constexpr (K6_TD > 6) k6_touch<6>(cx, lane); }
  f32x4 acc[5][4];
#pragma unroll
  for (int a = 0; a < 5; ++a)
#pragma unroll
    for (int b = 0; b < 4; ++b) acc[a][b] = f32x4{0.f, 0.f, 0.f, 0.f};
  K6Loop<0, NK>::run(cx, ks0, acc, gr, ep, red, w, lane);
#pragma unroll
  for (int t = 0; t < 4; ++t) if (t != w) red[((w * 5 + 4) * 4 + t) * 64 + lane] = acc[4][t];
  wait_vm<0>();
  asm volatile("" : "+v"(ep.gb[0]), "+v"(ep.gb[1]), "+v"(ep.gb[2]), "+v"(ep.gb[3]), "+v"(ep.gb[4]), "+v"(ep.xr[0]), "+v"(ep.xr[1]), "+v"(ep.xr[2]), "+v"(ep.xr[3]), "+v"(ep.xr[4]));
  __syncthreads();
#pragma unroll
  for (int nt = 0; nt < 5; ++nt) {
    f32x4 v = w == 0 ? acc[nt][0] : w == 1 ? acc[nt][1] : w == 2 ? acc[nt][2] : acc[nt][3];
#pragma unroll
    for (int ww = 1; ww < 4; ++ww) v += red[((((w + ww) & 3) * 5 + nt) * 4 + w) * 64 + lane];
    const f32x4 gb = __builtin_bit_cast(f32x4, ep.gb[nt]);
    const f32x4 x4 = {bf_lo(ep.xr[nt][0]), bf_hi(ep.xr[nt][0]), bf_lo(ep.xr[nt][1]), bf_hi(ep.xr[nt][1])};
    const f32x4 o = x4 + gb * v;
    stg_sel<64>(out + 16 * nt, oo, u32x2{pk_bf(o[0], o[1]), pk_bf(o[2], o[3])});
  }
}
__global__ __launch_bounds__(256, 2) void k6_down(const u16* __restrict__ hb, const u16* __restrict__ Wd, const u16* __restrict__ x1, const float* __restrict__ mod, u16* __restrict__ out) {
  STAMP_DECL; STAMP(5, 0);
  extern __shared__ __attribute__((aligned(16))) char smem[];
  const int tid = threadIdx.x, lane = tid & 63, w = __builtin_amdgcn_readfirstlane(tid >> 6), c = lane & 15, g = lane >> 4;
  const int xcd = blockIdx.x & 7, li = blockIdx.x >> 3;
  const int tt = 8 * (xcd & 3) + (li & 7), nb = 4 * (xcd >> 2) + (li >> 3);
  const int n0 = 80 * nb;
  const int ks0 = w < 2 ? 14 * w : 28 + 13 * (w - 2);
  K6Ctx cx;
  cx.wsrc = Wd + (size_t)(5 * nb) * 54 * 512; cx.hsrc = hb + (size_t)(4 * tt) * 54 * 512; cx.hoff = lane * 16;
  cx.gbp = mod + 5 * 640 + n0; cx.gbo = 16 * g;
  cx.xp = x1 + (size_t)(64 * tt + 16 * w) * D_ + n0; cx.xo = (u32)(c * D_ + 4 * g) * 2u;
  {
    const int L = min(13 * (4 * li + w) + lane, 1663), wp = L / 416, r = L % 416, k0p = wp < 2 ? 14 * wp : 28 + 13 * (wp - 2);
    const u32 wdelta = (u32)((const char*)Wd - (const char*)hb);
    const u32 oh = (u32)(((4 * (8 * (xcd & 3) + (r >> 5)) + ((r >> 3) & 3)) * 54 + k0p) * 1024 + (r & 7) * 128);
    const int r2 = r - 256;
    const u32 ow = wdelta + (u32)(((5 * (4 * (xcd >> 2) + r2 / 40) + (r2 % 40) / 8) * 54 + k0p) * 1024 + (r2 & 7) * 128);
    cx.tbase = hb; cx.toff = r < 256 ? oh : ow; cx.dump = lds_addr(smem) + 81920;
  }
  const int tau = 64 * tt + 16 * w + c, srow = (tau >> 3) + 256 * (tau & 7);
  const u32 oo = (u32)(srow * D_ + 4 * g) * 2u;
  if (w < 2) k6_main<14>(cx, ks0, (f32x4*)smem, w, lane, out + n0, oo); else k6_main<13>(cx, ks0, (f32x4*)smem, w, lane, out + n0, oo);
  STAMP(5, 7);
}

__global__ void dbg_print() {
  unsigned long long t0 = ~0ull;
  for (int k = 0; k < 6; ++k) for (int b = 0; b < 512; ++b) if (g_stamps[k][b][0]) t0 = min(t0, g_stamps[k][b][0]);
  for (int k = 0; k < 6; ++k) {
    unsigned long long s0 = ~0ull, s1 = 0, e0 = ~0ull, e1 = 0; double ph[8]; int n = 0; for (int i = 0; i < 8; ++i) ph[i] = 0;
    for (int b = 0; b < 512; ++b) if (g_stamps[k][b][0] && g_stamps[k][b][7]) { s0 = min(s0, g_stamps[k][b][0]); s1 = max(s1, g_stamps[k][b][0]); e0 = min(e0, g_stamps[k][b][7]); e1 = max(e1, g_stamps[k][b][7]); ++n;
      for (int i = 1; i < 8; ++i) if (g_cyc[k][b][i]) ph[i] += (double)(g_cyc[k][b][i] - g_cyc[k][b][0]); }
    if (n) printf("DBG k%d n=%d start %.2f..%.2f end %.2f..%.2f | cum cycles 1:%.0f 2:%.0f 3:%.0f 4:%.0f 5:%.0f 6:%.0f 7:%.0f\n", k + 1, n, (s0 - t0) / 100.0, (s1 - t0) / 100.0, (e0 - t0) / 100.0, (e1 - t0) / 100.0, ph[1] / n, ph[2] / n, ph[3] / n, ph[4] / n, ph[5] / n, ph[6] / n, ph[7] / n);
  }
  for (int k = 0; k < 6; ++k) for (int b = 0; b < 512; ++b) for (int i = 0; i < 8; ++i) { g_stamps[k][b][i] = 0; g_cyc[k][b][i] = 0; }
}
constexpr size_t WS_MOD = 0, WS_XT = 16384, WS_ZT = WS_XT + 8 * 21 * 256 * 16 * 4, WS_X1 = WS_ZT + 8 * 21 * 256 * 16 * 4,
                 WS_H2 = WS_X1 + 2048 * 640 * 2, WS_HB = WS_H2 + 2048 * 640 * 2, WS_WGU = WS_HB + 2048 * 1728 * 2, WS_WD = WS_WGU + 2 * 640 * 1728 * 2, WS_WP = WS_WD + 640 * 1728 * 2, WS_SYNC = WS_WP + 168 * 16384;
extern "C" void kernel_launch(void* const* d_in, const int* in_sizes, int n_in, void* d_out, int out_size, void* d_ws, size_t ws_size,
                              hipStream_t stream) {
  const u16* x = (const u16*)d_in[0]; const u16* wr = (const u16*)d_in[1]; const u16* wi = (const u16*)d_in[2];
  const u16* wg = (const u16*)d_in[4]; const u16* wu = (const u16*)d_in[5]; const u16* wd = (const u16*)d_in[6];
  const u16* c = (const u16*)d_in[7]; const u16* wmod = (const u16*)d_in[8];
  char* ws = (char*)d_ws;
  float* mod = (float*)(ws + WS_MOD); u32* XT = (u32*)(ws + WS_XT); u32* ZT = (u32*)(ws + WS_ZT);
  u16* x1 = (u16*)(ws + WS_X1); u16* h2 = (u16*)(ws + WS_H2); u16* hb = (u16*)(ws + WS_HB);
  u16* Wgu = (u16*)(ws + WS_WGU); u16* Wd = (u16*)(ws + WS_WD);
  K2Args A;
  A.x = x; A.c = c; A.wmod = wmod; A.mod = mod; A.XT = XT; A.wr = wr; A.wi = wi; A.wg = wg; A.wu = wu; A.Wgu = Wgu; A.wd = wd; A.Wd = Wd;
  A.gen = (u32*)(ws + WS_SYNC); A.arr = (u32*)(ws + WS_SYNC + 4096); A.gran = (unsigned long long*)(ws + WS_SYNC + 8192);
  k2_fwd<<<K2_GRID, 640, 0, stream>>>(A);
  k3_mid<<<168, 512, 0, stream>>>(XT, wr, wi, ZT);
  k4_inv<<<256, 640, 0, stream>>>(ZT, x, mod, x1, h2, Wgu);
  hipFuncSetAttribute((const void*)k5_glu, hipFuncAttributeMaxDynamicSharedMemorySize, K5_NS * K5_SLOT);
  k5_glu<<<256, 256, K5_NS * K5_SLOT, stream>>>(h2, Wgu, hb, Wd);
  hipFuncSetAttribute((const void*)k6_down, hipFuncAttributeMaxDynamicSharedMemorySize, 81920 + 256);
  k6_down<<<256, 256, 81920 + 256, stream>>>(hb, Wd, x1, mod, (u16*)d_out);
  dbg_print<<<1, 1, 0, stream>>>();
}
```

```cpp
#include <hip/hip_runtime.h>
#include <hip/hip_bf16.h>
#include <stdint.h>
#include <utility>

typedef unsigned short u16;
typedef unsigned int u32;
typedef short s16x4 __attribute__((ext_vector_type(4)));
typedef short s16x8 __attribute__((ext_vector_type(8)));
typedef __bf16 bf2_t __attribute__((ext_vector_type(2)));
typedef float f32x2 __attribute__((ext_vector_type(2)));
typedef float f32x4 __attribute__((ext_vector_type(4)));
typedef float f32x16 __attribute__((ext_vector_type(16)));
typedef u32 u32x2 __attribute__((ext_vector_type(2)));
typedef u32 u32x4 __attribute__((ext_vector_type(4)));
#define DI __device__ __forceinline__
#define LDSP(T) __attribute__((address_space(3))) T*

constexpr int S_ = 2048, D_ = 640, F_ = 1728, DF = 321;

constexpr double kPi = 3.14159265358979323846264338327950288;
constexpr double c_sin0(double x) { double x2 = x * x, t = x, s = x; for (int i = 1; i < 11; ++i) { t *= -x2 / ((2.0 * i) * (2.0 * i + 1.0)); s += t; } return s; }
constexpr double c_cos0(double x) { double x2 = x * x, t = 1.0, s = 1.0; for (int i = 1; i < 11; ++i) { t *= -x2 / ((2.0 * i - 1.0) * (2.0 * i)); s += t; } return s; }
constexpr double c_cos(long n, long N) {
  n %= N; if (n < 0) n += N;
  if (2 * n > N) n = N - n;
  bool neg = false;
  if (4 * n > N) { neg = true; n = N - 2 * n; N = 2 * N; n = n;   }
  double r = 0;
  if (4 * n == N) r = 0.0;
  else if (8 * n > N) r = c_sin0(2 * kPi * (double)(N - 4 * n) / (double)(4 * N));
  else r = c_cos0(2 * kPi * (double)n / (double)N);
  return neg ? -r : r;
}
constexpr double c_sin(long n, long N) { return c_cos(4 * n - N, 4 * N); }
constexpr u16 c_bf16(double v) {
  float f = (float)v; u32 u = __builtin_bit_cast(u32, f);
  u += 0x7fffu + ((u >> 16) & 1u); return (u16)(u >> 16);
}

struct F8w { u16 v[2][64][8]; };
struct F8x6 { u16 v[3][2][64][8]; };
struct TwA { float v[2][64][4]; };
struct TwC { float v[3][64][4]; };
constexpr F8w mk_f32a() {
  F8w t{};
  for (int tt = 0; tt < 2; ++tt) for (int l = 0; l < 64; ++l) for (int j = 0; j < 8; ++j) {
    int o = 16 * tt + (l & 15), d1 = 8 * (l >> 4) + j; double v = 0;
    if (o == 0) v = 1.0; else if (o == 1) v = (d1 & 1) ? -1.0 : 1.0;
    else { int k1 = o >> 1; v = (o & 1) ? -c_sin(d1 * k1, 32) : c_cos(d1 * k1, 32); }
    t.v[tt][l][j] = c_bf16(v);
  }
  return t;
}
constexpr TwA mk_twa(int j) {
  TwA t{};
  for (int tt = 0; tt < 2; ++tt) for (int l = 0; l < 64; ++l) for (int rr = 0; rr < 2; ++rr) {
    int c = l & 15, g = l >> 4, Q = 4 * j + (c >> 2), d0 = 4 * (Q % 5) + (c & 3), rho = Q / 5;
    int k1 = 8 * tt + 2 * g + rr; if (k1 == 0) k1 = 16;
    double co = c_cos(d0 * k1, 640), si = -c_sin(d0 * k1, 640);
    if (rho >= 3 && (rho & 1)) { double x = co; co = -si; si = x; }
    t.v[tt][l][2 * rr] = (float)co; t.v[tt][l][2 * rr + 1] = (float)si;
  }
  return t;
}
constexpr F8x6 mk_f20(bool inv) {
  F8x6 t{};
  for (int mt = 0; mt < 3; ++mt) for (int ks = 0; ks < 2; ++ks) for (int l = 0; l < 64; ++l) for (int j = 0; j < 8; ++j) {
    int row = l & 15, g = l >> 4, k0 = 8 * mt + (row >> 1), part = row & 1, d0 = 16 * ks + 4 * g + (j >> 1), pp = j & 1; double v = 0;
    if (d0 < 20 && k0 < 20) {
      double co = c_cos(d0 * k0, 20), si = inv ? -c_sin(d0 * k0, 20) : c_sin(d0 * k0, 20);
      v = part == 0 ? (pp == 0 ? co : si) : (pp == 0 ? -si : co);
    }
    t.v[mt][ks][l][j] = c_bf16(v);
  }
  return t;
}
constexpr F8w mk_a1(int m1, bool inv) {
  F8w t{};
  for (int l = 0; l < 64; ++l) for (int i = 0; i < 4; ++i) {
    int c = l & 15, a = 4 * (l >> 4) + i, n = inv ? c * (8 * a + m1) : a * (8 * c + m1);
    double co = c_cos(n, 128), si = inv ? -c_sin(n, 128) : c_sin(n, 128);
    t.v[0][l][2 * i] = c_bf16(co); t.v[0][l][2 * i + 1] = c_bf16(si);
    t.v[1][l][2 * i] = c_bf16(-si); t.v[1][l][2 * i + 1] = c_bf16(co);
  }
  return t;
}
constexpr F8w mk_f16(bool inv) {
  F8w t{};
  for (int l = 0; l < 64; ++l) for (int i = 0; i < 4; ++i) {
    int c = l & 15, a = 4 * (l >> 4) + i;
    double co = c_cos(a * c, 16), si = c_sin(a * c, 16);
    if (!inv) { t.v[0][l][2 * i] = c_bf16(co); t.v[0][l][2 * i + 1] = c_bf16(si); t.v[1][l][2 * i] = c_bf16(-si); t.v[1][l][2 * i + 1] = c_bf16(co); }
    else { t.v[0][l][i] = c_bf16(co); t.v[0][l][4 + i] = c_bf16(-si); t.v[1][l][i] = c_bf16(si); t.v[1][l][4 + i] = c_bf16(co); }
  }
  return t;
}
constexpr TwC mk_twc(int kind) {
  TwC t{};
  const double sc = 1.0 / (2048.0 * 640.0);
  for (int mt = 0; mt < 3; ++mt) for (int l = 0; l < 64; ++l) for (int rr = 0; rr < 2; ++rr) {
    int k1 = kind == 2 ? 16 : (l & 15), g = l >> 4, d0 = 8 * mt + 2 * g + rr, rho = l & 7;
    double co = c_cos(d0 * k1, 640) * sc * (kind == 2 ? 2 : 1), si = c_sin(d0 * k1, 640) * sc * (kind == 2 ? 2 : 1);
    if (kind == 1 || (kind == 2 && rho >= 3 && (rho & 1))) { double x = co; co = -si; si = x; }
    t.v[mt][l][2 * rr] = (float)co; t.v[mt][l][2 * rr + 1] = (float)si;
  }
  return t;
}
constexpr F8w mk_f32bi() {
  F8w t{};
  for (int nt = 0; nt < 2; ++nt) for (int l = 0; l < 64; ++l) for (int j = 0; j < 8; ++j) {
    int d1 = 16 * nt + (l & 15), K = 8 * (l >> 4) + j; double v = 0;
    if (K == 0) v = 1.0; else if (K == 1) v = (d1 & 1) ? -1.0 : 1.0;
    else { int k1 = K >> 1; v = (K & 1) ? -2.0 * c_sin(k1 * d1, 32) : 2.0 * c_cos(k1 * d1, 32); }
    t.v[nt][l][j] = c_bf16(v);
  }
  return t;
}
static __device__ const F8w g_f32a = mk_f32a();
static __device__ const F8x6 g_f20a = mk_f20(false);
static __device__ const F8x6 g_f20ai = mk_f20(true);
static __device__ const F8w g_a1[8] = {mk_a1(0, false), mk_a1(1, false), mk_a1(2, false), mk_a1(3, false), mk_a1(4, false), mk_a1(5, false), mk_a1(6, false), mk_a1(7, false)};
static __device__ const F8w g_a1i[8] = {mk_a1(0, true), mk_a1(1, true), mk_a1(2, true), mk_a1(3, true), mk_a1(4, true), mk_a1(5, true), mk_a1(6, true), mk_a1(7, true)};
static __device__ const F8w g_f16f = mk_f16(false);
static __device__ const F8w g_f16i = mk_f16(true);
static __device__ const F8w g_f32bi = mk_f32bi();

#define WAITALL() do {} while (0)
#define STAMP_DECL
#define STAMP(k, i) do {} while (0)
#define SLAB(i) do {} while (0)
#define WSTAMP(j) do {} while (0)
DI float bf_lo(u32 v) { return __uint_as_float(v << 16); }
DI float bf_hi(u32 v) { return __uint_as_float(v & 0xffff0000u); }
DI float bf2f(u16 v) { return __uint_as_float(((u32)v) << 16); }
DI u32 pk_bf(float a, float b) { f32x2 f = {a, b}; return __builtin_bit_cast(u32, __builtin_convertvector(f, bf2_t)); }
DI float rbf(float a) { return bf_lo(pk_bf(a, a)); }
DI float dpp_x1(float v) { return __int_as_float(__builtin_amdgcn_mov_dpp(__float_as_int(v), 0xB1, 0xf, 0xf, true)); }
DI float dpp_x2(float v) { return __int_as_float(__builtin_amdgcn_mov_dpp(__float_as_int(v), 0x4E, 0xf, 0xf, true)); }
DI float dpp_hm(float v) { return __int_as_float(__builtin_amdgcn_mov_dpp(__float_as_int(v), 0x141, 0xf, 0xf, true)); }
DI float dpp_rm(float v) { return __int_as_float(__builtin_amdgcn_mov_dpp(__float_as_int(v), 0x140, 0xf, 0xf, true)); }
DI f32x4 mfma16(s16x8 a, s16x8 b, f32x4 c) { return __builtin_amdgcn_mfma_f32_16x16x32_bf16(a, b, c, 0, 0, 0); }
DI f32x16 mfma32(s16x8 a, s16x8 b, f32x16 c) { return __builtin_amdgcn_mfma_f32_32x32x16_bf16(a, b, c, 0, 0, 0); }
DI s16x4 tr_read(const void* p) { return __builtin_amdgcn_ds_read_tr16_b64_v4i16((LDSP(s16x4))p); }
template <class T> DI void pin(T& v) { asm volatile("" : "+v"(v)); }
DI void l2_touch(const void* base, u32 voff, u32 lds) {
  asm volatile("s_mov_b32 m0, %0\n\ts_nop 0\n\tglobal_load_lds_dword %1, %2" :: "s"(lds), "v"(voff), "s"(base) : "memory");
}
DI void side_delay(int ticks10ns) {
  const unsigned long long t0 = __builtin_amdgcn_s_memrealtime();
  while (__builtin_amdgcn_s_memrealtime() - t0 < (unsigned long long)ticks10ns) __builtin_amdgcn_s_sleep(4);
}
#define GSP(T) __attribute__((address_space(1))) T*
template <class T> DI T ldg(const void* sbase, u32 voff) { GSP(const char) p = (GSP(const char))sbase; asm("" : "+s"(p)); return *(GSP(const T))(p + voff); }
template <class T> DI void stg(void* sbase, u32 voff, T v) { GSP(char) p = (GSP(char))sbase; asm("" : "+s"(p)); *(GSP(T))(p + voff) = v; }
DI void stg_wt(void* sbase, u32 voff, u32x4 v) { asm volatile("global_store_dwordx4 %0, %1, %2 sc1\n\ts_nop 1" :: "v"(voff), "v"(v), "s"(sbase) : "memory"); }
DI void stg_wt(void* sbase, u32 voff, u32x2 v) { asm volatile("global_store_dwordx2 %0, %1, %2 sc1\n\ts_nop 1" :: "v"(voff), "v"(v), "s"(sbase) : "memory"); }
DI void stg_wt(void* sbase, u32 voff, u32 v) { asm volatile("global_store_dword %0, %1, %2 sc1" :: "v"(voff), "v"(v), "s"(sbase) : "memory"); }
#define WT_MASK 127
template <int BIT, class T> DI void stg_sel(void* sbase, u32 voff, T v) { if constexpr ((WT_MASK & BIT) != 0) stg_wt(sbase, voff, v); else stg(sbase, voff, v); }
#define CODE_ADDR(var, sym) asm volatile("s_getpc_b64 s[40:41]\n\ts_add_u32 s40, s40, " #sym "@rel32@lo+4\n\ts_addc_u32 s41, s41, " #sym "@rel32@hi+12\n\ts_mov_b64 %0, s[40:41]" : "=s"(var) :: "s40", "s41", "scc")
DI u32 lds_addr(const void* p) { return (u32)(size_t)(LDSP(const char))p; }
DI s16x8 cat8(s16x4 a, s16x4 b) { return __builtin_shufflevector(a, b, 0, 1, 2, 3, 4, 5, 6, 7); }
DI s16x8 as8(u32x4 v) { return __builtin_bit_cast(s16x8, v); }
DI float dot2bf(u32 a, u32 b, float c) { return __builtin_amdgcn_fdot2_f32_bf16(__builtin_bit_cast(bf2_t, a), __builtin_bit_cast(bf2_t, b), c, false); }

DI float reduce16(const float (&v)[16], int lane) {
  float a[8], b[4], c[2], d;
  const bool s3 = lane & 8, s2 = lane & 4, s1 = lane & 2, s0 = lane & 1;
#pragma unroll
  for (int i = 0; i < 8; ++i) { float keep = s3 ? v[8 + i] : v[i], send = s3 ? v[i] : v[8 + i]; a[i] = keep + dpp_rm(send); }
#pragma unroll
  for (int i = 0; i < 4; ++i) { float keep = s2 ? a[4 + i] : a[i], send = s2 ? a[i] : a[4 + i]; b[i] = keep + dpp_hm(send); }
#pragma unroll
  for (int i = 0; i < 2; ++i) { float keep = s1 ? b[2 + i] : b[i], send = s1 ? b[i] : b[2 + i]; c[i] = keep + dpp_x2(send); }
  { float keep = s0 ? c[1] : c[0], send = s0 ? c[0] : c[1]; d = keep + dpp_x1(send); }
  auto r16 = __builtin_amdgcn_permlane16_swap(__float_as_uint(d), __float_as_uint(d), false, false);
  d = __uint_as_float(r16[0]) + __uint_as_float(r16[1]);
  auto r32 = __builtin_amdgcn_permlane32_swap(__float_as_uint(d), __float_as_uint(d), false, false);
  return __uint_as_float(r32[0]) + __uint_as_float(r32[1]);
}

DI void ln_finish(float s, float q, float& m_, float& i_) {
  const float mean = s * (1.0f / 640.0f);
  const float var = fmaxf(q * (1.0f / 640.0f) - mean * mean, 0.f);
  const float sd = rbf(__builtin_amdgcn_sqrtf(rbf(rbf(var) + 1e-5f)));
  m_ = rbf(mean); i_ = __builtin_amdgcn_rcpf(sd);
}
DI void ln_stats5(const u32 (&xv)[8], float* part  , int tid, float (&a_)[8], float (&b_)[8]) {
  const int lane = tid & 63, w = tid >> 6;
  float v[16];
#pragma unroll
  for (int r = 0; r < 8; ++r) { v[r] = dot2bf(xv[r], 0x3f803f80u, 0.f); v[8 + r] = dot2bf(xv[r], xv[r], 0.f); }
  float t = reduce16(v, lane);
  if (lane < 16) part[w * 16 + lane] = t;
  __syncthreads();
  const int r = lane & 7;
  float s = 0.f, q = 0.f;
#pragma unroll
  for (int i = 0; i < 5; ++i) { s += part[i * 16 + r]; q += part[i * 16 + 8 + r]; }
  float m_, i_; ln_finish(s, q, m_, i_);
  const float nb = -m_ * i_;
#pragma unroll
  for (int i = 0; i < 8; ++i) {
    a_[i] = __int_as_float(__builtin_amdgcn_readlane(__float_as_int(i_), i));
    b_[i] = __int_as_float(__builtin_amdgcn_readlane(__float_as_int(nb), i));
  }
}
DI float reduce8(const float (&v)[8], int lane) {
  float b[4], c[2], d;
  const bool s2 = lane & 4, s1 = lane & 2, s0 = lane & 1;
#pragma unroll
  for (int i = 0; i < 4; ++i) { float keep = s2 ? v[4 + i] : v[i], send = s2 ? v[i] : v[4 + i]; b[i] = keep + dpp_hm(send); }
#pragma unroll
  for (int i = 0; i < 2; ++i) { float keep = s1 ? b[2 + i] : b[i], send = s1 ? b[i] : b[2 + i]; c[i] = keep + dpp_x2(send); }
  { float keep = s0 ? c[1] : c[0], send = s0 ? c[0] : c[1]; d = keep + dpp_x1(send); }
  d += __int_as_float(__builtin_amdgcn_mov_dpp(__float_as_int(d), 0x128, 0xf, 0xf, true));
  auto r16 = __builtin_amdgcn_permlane16_swap(__float_as_uint(d), __float_as_uint(d), false, false);
  d = __uint_as_float(r16[0]) + __uint_as_float(r16[1]);
  auto r32 = __builtin_amdgcn_permlane32_swap(__float_as_uint(d), __float_as_uint(d), false, false);
  return __uint_as_float(r32[0]) + __uint_as_float(r32[1]);
}

template <int N> DI void wait_vm() { asm volatile("s_waitcnt vmcnt(%0)" ::"n"(N) : "memory"); }
template <int OFF> DI void gload16_s(u32x4& d, const void* sbase, u32 voff) {
  asm volatile("global_load_dwordx4 %0, %1, %2 offset:%3" : "=v"(d) : "v"(voff), "s"(sbase), "n"(OFF) : "memory");
}
template <int OFF> DI void glds16_s(const void* sbase, u32 voff) {
  asm volatile("global_load_lds_dwordx4 %0, %1 offset:%2" :: "v"(voff), "s"(sbase), "n"(OFF) : "memory");
}
DI void dma1k(const void* sbase, u32 voff, u32 lds) {
  asm volatile("s_mov_b32 m0, %0\n\ts_nop 0\n\tglobal_load_lds_dwordx4 %1, %2" :: "s"(lds), "v"(voff), "s"(sbase) : "memory");
}
DI void set_m0(u32 v) { asm volatile("s_mov_b32 m0, %0\n\ts_nop 0" :: "s"(v) : "memory"); }
DI void tie4(u32x4 (&t)[4]) { asm volatile("" : "+v"(t[0]), "+v"(t[1]), "+v"(t[2]), "+v"(t[3])); }

DI void prep_load(u32x4 (&v)[4], const u16* __restrict__ src, int ld, int lane) {
  const u32 vo = (u32)((lane >> 3) * ld + 8 * (lane & 7)) * 2u;
#pragma unroll
  for (int i = 0; i < 4; ++i) v[i] = ldg<u32x4>(src + (size_t)8 * i * ld, vo);
}
DI void prep_store(const u32x4 (&v)[4], u16* __restrict__ dst, int jstride, char* lds, int lane) {
#pragma unroll
  for (int i = 0; i < 4; ++i) {
    const int row = 8 * i + (lane >> 3), ch = lane & 7;
    *(u32x4*)(lds + row * 128 + (((ch >> 1) ^ ((row >> 1) & 3)) * 32) + (ch & 1) * 16) = v[i];
  }
  const int c = lane & 15, g = lane >> 4, q = c >> 2, p = c & 3, r = 4 * g + q;
#pragma unroll
  for (int j = 0; j < 4; ++j) {
    const char* b = lds + r * 128 + ((j ^ ((r >> 1) & 3)) * 32) + 8 * p;
    const s16x8 f = cat8(tr_read(b), tr_read(b + 16 * 128));
    stg_sel<1>(dst + (size_t)j * jstride, (u32)lane * 16u, __builtin_bit_cast(u32x4, f));
  }
}
DI void prep_wave(const u16* __restrict__ src, int ld, u16* __restrict__ dst, int jstride, char* lds, int lane) {
  u32x4 v[4]; prep_load(v, src, ld, lane); prep_store(v, dst, jstride, lds, lane);
}
DI void prep_gu(int task, const u16* wg, const u16* wu, u16* Wgu, char* lds, int lane) {
  const int mat = task >= 540, tk = task - 540 * mat, cg = tk / 20, ks = tk % 20;
  prep_wave((mat ? wu : wg) + (size_t)ks * 32 * F_ + 64 * cg, F_, Wgu + ((size_t)(4 * cg * 20 + ks) * 2 + mat) * 512, 20 * 2 * 512, lds, lane);
}
DI void prep_d(int task, const u16* wd, u16* Wd, char* lds, int lane) {
  const int cg = task / 54, ks = task % 54;
  prep_wave(wd + (size_t)ks * 32 * D_ + 64 * cg, D_, Wd + (size_t)(4 * cg * 54 + ks) * 512, 54 * 512, lds, lane);
}

typedef __attribute__((address_space(1))) u32 gu32;
typedef __attribute__((address_space(1))) unsigned long long gu64;
DI u32 ld_agent(const u32* p) { return __hip_atomic_load((const gu32*)p, __ATOMIC_RELAXED, __HIP_MEMORY_SCOPE_AGENT); }
DI void st_agent(u32* p, u32 v) { __hip_atomic_store((gu32*)p, v, __ATOMIC_RELAXED, __HIP_MEMORY_SCOPE_AGENT); }
DI void st_agent64(unsigned long long* p, unsigned long long v) { __hip_atomic_store((gu64*)p, v, __ATOMIC_RELAXED, __HIP_MEMORY_SCOPE_AGENT); }
#define K2_SIDE_DELAY 80
#define K4_SIDE_DELAY 60
constexpr int SPIN_LIMIT = 1 << 16;

template <int NR> DI void gemv_part(int col0, int row0, u32 tagv, bool publish, unsigned long long* gran, int gidx0, const u16* __restrict__ c, const u16* __restrict__ wmod,
                                    float* __restrict__ mod, char* lds) {
  float* sl = (float*)lds; float* part = sl + 640;
  const int t = threadIdx.x, lane = t & 63, w = t >> 6, cq = t & 7, rg = t >> 3;
  const u16 cv = ldg<u16>(c, 2 * t);
  u32x4 wv[NR];
  const u32 wo = (u32)((row0 + rg) * 3840 + col0 + 8 * cq) * 2u;
#pragma unroll
  for (int i = 0; i < NR; ++i) wv[i] = ldg<u32x4>(wmod + (size_t)80 * i * 3840, wo);
  { const float x = bf2f(cv); sl[t] = rbf(x * __builtin_amdgcn_rcpf(1.0f + __expf(-x))); }
  __syncthreads();
  float acc[8] = {};
#pragma unroll
  for (int i = 0; i < NR; ++i) {
    const float s = sl[row0 + rg + 80 * i];
#pragma unroll
    for (int k = 0; k < 4; ++k) { acc[2 * k] += s * bf_lo(wv[i][k]); acc[2 * k + 1] += s * bf_hi(wv[i][k]); }
  }
  float u[4];
  const bool b3 = lane & 8;
#pragma unroll
  for (int i = 0; i < 4; ++i) { const float keep = b3 ? acc[4 + i] : acc[i], send = b3 ? acc[i] : acc[4 + i]; u[i] = keep + __int_as_float(__builtin_amdgcn_mov_dpp(__float_as_int(send), 0x128, 0xf, 0xf, true)); }
  float r2[2];
#pragma unroll
  for (int i = 0; i < 2; ++i) { auto x = __builtin_amdgcn_permlane16_swap(__float_as_uint(u[i]), __float_as_uint(u[i + 2]), false, false); r2[i] = __uint_as_float(x[0]) + __uint_as_float(x[1]); }
  auto y = __builtin_amdgcn_permlane32_swap(__float_as_uint(r2[0]), __float_as_uint(r2[1]), false, false);
  part[w * 64 + 8 * cq + 4 * ((lane >> 3) & 1) + 2 * ((lane >> 4) & 1) + (lane >> 5)] = __uint_as_float(y[0]) + __uint_as_float(y[1]);
  __syncthreads();
  if (t < 64) {
    float s = 0.f;
#pragma unroll
    for (int i = 0; i < 10; ++i) s += part[i * 64 + t];
    if (publish) st_agent64(gran + gidx0 + t, ((unsigned long long)(tagv + 1u) << 32) | __float_as_uint(s));
    else mod[col0 + t] = rbf(s);
  }
}

DI int row_group(int b) { return 2 * (8 * (b >> 4) + (b & 7)) + ((b >> 3) & 1); }
DI void sincos_rev(float a, float& s, float& c) { s = __builtin_amdgcn_sinf(a); c = __builtin_amdgcn_cosf(a); }
DI void gram_part(const u32x4 (&f)[4], float* part  , int i, int lane) {
  f32x4 acc = {0.f, 0.f, 0.f, 0.f};
#pragma unroll
  for (int k = 0; k < 4; ++k) acc = mfma16(as8(f[k]), as8(f[k]), acc);
  const int c = lane & 15, g = lane >> 4;
  const float dg = (c & 2) ? ((c & 1) ? acc[3] : acc[2]) : ((c & 1) ? acc[1] : acc[0]);
  if (c < 8 && g == (c >> 2)) part[i * 16 + 8 + c] = dg;
  if (c < 8 && g == 2) part[i * 16 + c] = acc[0];
}
DI void ln_bcast8(const float* part, int lane, float (&a_)[8], float (&b_)[8]) {
  const int r = lane & 7;
  float s = 0.f, q = 0.f;
#pragma unroll
  for (int i = 0; i < 5; ++i) { s += part[i * 16 + r]; q += part[i * 16 + 8 + r]; }
  float m_, i_; ln_finish(s, q, m_, i_);
  const float nb = -m_ * i_;
#pragma unroll
  for (int i = 0; i < 8; ++i) {
    a_[i] = __int_as_float(__builtin_amdgcn_readlane(__float_as_int(i_), i));
    b_[i] = __int_as_float(__builtin_amdgcn_readlane(__float_as_int(nb), i));
  }
}
constexpr int HT_RS = 1296;
struct K2Args { const u16* x; const u16* c; const u16* wmod; float* mod; u32* XT; const u16* wr; const u16* wi; const u16* wg; const u16* wu; u16* Wgu; const u16* wd; u16* Wd; u32* gen; u32* arr; unsigned long long* gran; };
constexpr int K2_MAIN0 = 88, K2_SIDE0 = 344, K2_GRID = 344 + 54;
extern "C" __global__ __launch_bounds__(640) void k2_fwd(const K2Args A) {
  STAMP_DECL; STAMP(1, 0);
  __shared__ __attribute__((aligned(16))) char smem[32768 + 5 * 4096 + 256];
  const int bid = blockIdx.x;
  if (bid >= K2_SIDE0) {
    side_delay(K2_SIDE_DELAY);
    const int wv = __builtin_amdgcn_readfirstlane(threadIdx.x >> 6);
    prep_d((bid - K2_SIDE0) * 10 + wv, A.wd, A.Wd, smem + 4096 * wv, threadIdx.x & 63);
    STAMP(1, 6);
    return;
  }
  if (bid >= 40 && bid < 80) {
    side_delay(K2_SIDE_DELAY);
    gemv_part<8>(1280 + 64 * (bid - 40), 0, 0u, false, nullptr, 0, A.c, A.wmod, A.mod, smem); STAMP(0, 6); return;
  }
  if (bid >= 80 && bid < 88) {
    const void* c3; const void* c7;
    CODE_ADDR(c3, k3_mid); CODE_ADDR(c7, k7_end);
    const u32 len = (u32)((const char*)c7 - (const char*)c3);
    const u32 off = threadIdx.x * 128u;
    if (off < len) l2_touch(c3, off, lds_addr(smem) + __builtin_amdgcn_readfirstlane(threadIdx.x >> 6) * 256);
    asm volatile("s_waitcnt vmcnt(0)" ::: "memory");
    if (bid < 87) return;
  }
  if (bid < 40) {
    const u32 tagv = ld_agent(A.gen);
    gemv_part<4>(64 * (bid >> 1), 320 * (bid & 1), tagv, true, A.gran, 1280 * (bid & 1) + 64 * (bid >> 1), A.c, A.wmod, A.mod, smem);
    if (threadIdx.x == 0) st_agent(A.arr + bid, tagv + 1u);
    STAMP(0, 7); return;
  }
  if (bid == 87) {
    const u32 tag = ld_agent(A.gen) + 1u;
    const int t = threadIdx.x, who = t < 40 ? t : t + 48;
    for (int spin = 0; spin < SPIN_LIMIT; ++spin) {
      const bool ok = t >= 296 || ld_agent(A.arr + who) == tag;
      if (__syncthreads_and(ok)) break;
      __builtin_amdgcn_s_sleep(8);
    }
    if (t == 0) st_agent(A.gen, tag);
    return;
  }
  u32* __restrict__ XT = A.XT;
  char* hT = smem; char* U = smem + 8 * HT_RS; float* part = (float*)(smem + 8 * HT_RS + 8 * 17 * 80);
  char* tab = smem + 8 * HT_RS + 8 * 17 * 80 + 5 * 16 * 4;
  const int tid = threadIdx.x, lane = tid & 63, w = __builtin_amdgcn_readfirstlane(tid >> 6);
  const int s0 = row_group(bid - K2_MAIN0);
  const int c = lane & 15, g = lane >> 4, q = c >> 2, p = c & 3;
  const u16* xrow = A.x + (size_t)s0 * D_;
  f32x4 tw0, tw1; u32 ard, awr, aw0, acr; float z0r, z0i;
  auto precompute = [&]() {
    const int Qw = 4 * w + q, rho = Qw / 5, d0 = 4 * (Qw % 5) + p;
    const bool rot = rho >= 3 && (rho & 1);
    const float fd = (float)d0 * (1.0f / 640.0f);
    const int k10 = g == 0 ? 16 : 2 * g;
    float s_[4], c_[4];
    sincos_rev(fd * (float)k10, s_[0], c_[0]); sincos_rev(fd * (float)(2 * g + 1), s_[1], c_[1]);
    sincos_rev(fd * (float)(8 + 2 * g), s_[2], c_[2]); sincos_rev(fd * (float)(9 + 2 * g), s_[3], c_[3]);
    tw0 = f32x4{rot ? s_[0] : c_[0], rot ? c_[0] : -s_[0], rot ? s_[1] : c_[1], rot ? c_[1] : -s_[1]};
    tw1 = f32x4{rot ? s_[2] : c_[2], rot ? c_[2] : -s_[2], rot ? s_[3] : c_[3], rot ? c_[3] : -s_[3]};
    const int Qr = 4 * w + p;
    ard = lds_addr(hT) + (Qr / 5) * HT_RS + 2 * (20 * (8 * g + q) + 4 * (Qr % 5));
    awr = lds_addr(U) + 4 * (rho * 17 * 20 + d0 + 2 * g * 20);
    aw0 = g == 0 ? awr + 4 * 16 * 20 : awr;
    z0r = rot ? 0.f : 1.f; z0i = rot ? 1.f : 0.f;
    acr = lds_addr(U) + (w < 8 ? ((2 * (w >> 1)) * 17 + c) * 80 : ((c & 7) * 17 + 16) * 80);
    pin(tw0); pin(tw1); pin(ard); pin(awr); pin(aw0); pin(z0r); pin(z0i); pin(acr);
  };
  if (w < 5) {
    u32 xv[8];
#pragma unroll
    for (int r = 0; r < 8; ++r) xv[r] = ldg<u32>(xrow + (size_t)r * 256 * D_, 4 * tid);
    const u32 tagv = ld_agent(A.gen);
    precompute();
    STAMP(1, 1);
    WAITALL();
    STAMP(1, 5);
    __syncthreads();
    float na[8], nb[8];
    ln_bcast8(part, lane, na, nb);
    f32x2 o[8];
    {
      f32x2 h[8];
#pragma unroll
      for (int r = 0; r < 8; ++r) { f32x2 v = {bf_lo(xv[r]), bf_hi(xv[r])}; h[r] = v * na[r] + nb[r]; }
      const float cc = 0.70710678118654752f;
      f32x2 a0 = h[0] + h[4], a1 = h[1] + h[5], a2 = h[2] + h[6], a3 = h[3] + h[7];
      f32x2 b0 = h[0] - h[4], b1 = h[1] - h[5], b2 = h[2] - h[6], b3 = h[3] - h[7];
      f32x2 e = a0 + a2, f = a1 + a3, u = cc * (b1 - b3), v = cc * (b1 + b3);
      o[0] = e + f; o[1] = e - f; o[2] = b0 + u; o[3] = -b2 - v; o[4] = a0 - a2; o[5] = a3 - a1; o[6] = b0 - u; o[7] = b2 - v;
#pragma unroll
      for (int r = 0; r < 8; ++r) pin(o[r]);
    }
    const u32 tag = __builtin_amdgcn_readfirstlane(tagv) + 1u;
    if (tid == 0) st_agent(A.arr + bid, tag);
    f32x2 sa, gg;
    {
      const unsigned long long* ga_ = A.gran + 2 * tid;
      u32x4 va, vb, vc, vd;
      for (int spin = 0; spin < SPIN_LIMIT; ++spin) {
        asm volatile("global_load_dwordx4 %0, %4, off sc1\n\tglobal_load_dwordx4 %1, %5, off sc1\n\tglobal_load_dwordx4 %2, %6, off sc1\n\tglobal_load_dwordx4 %3, %7, off sc1\n\ts_waitcnt vmcnt(0)"
                     : "=&v"(va), "=&v"(vb), "=&v"(vc), "=&v"(vd) : "v"(ga_), "v"(ga_ + 1280), "v"(ga_ + 640), "v"(ga_ + 1920) : "memory");
        const bool ok = va[1] == tag && va[3] == tag && vb[1] == tag && vb[3] == tag && vc[1] == tag && vc[3] == tag && vd[1] == tag && vd[3] == tag;
        if (__all(ok)) break;
      }
      sa = f32x2{rbf(__uint_as_float(va[0]) + __uint_as_float(vb[0])), rbf(__uint_as_float(va[2]) + __uint_as_float(vb[2]))};
      gg = f32x2{rbf(1.0f + rbf(__uint_as_float(vc[0]) + __uint_as_float(vd[0]))), rbf(1.0f + rbf(__uint_as_float(vc[2]) + __uint_as_float(vd[2])))};
    }
    STAMP(1, 2);
    o[0] = o[0] * gg + 8.0f * sa;
    *(u32*)(hT + 4 * tid) = pk_bf(o[0][0], o[0][1]);
#pragma unroll
    for (int r = 1; r < 8; ++r) { const f32x2 v = o[r] * gg; *(u32*)(hT + r * HT_RS + 4 * tid) = pk_bf(v[0], v[1]); }
  } else {
    const int i = w - 5;
    u32x4 f[4];
    const u32 xo = (u32)((c & 7) * 256 * D_ + 128 * i + 8 * g) * 2u;
#pragma unroll
    for (int k = 0; k < 4; ++k) f[k] = ldg<u32x4>(xrow, xo + 64 * k);
    const s16x8 t0 = i < 2 ? ldg<s16x8>(g_f32a.v[i < 2 ? i : 0], lane * 16) : ldg<s16x8>(&g_f20a.v[0][0][0][0] + (i - 2) * 512, lane * 16);
    const s16x8 t1 = ldg<s16x8>(&g_f20a.v[0][0][0][0] + (i < 3 ? i + 3 : 5) * 512, lane * 16);
    precompute();
    if (c >= 8) {
      const u32 fill = c == 8 ? 0x3f803f80u : 0u;
#pragma unroll
      for (int k = 0; k < 4; ++k) f[k] = u32x4{fill, fill, fill, fill};
    }
    gram_part(f, part, i, lane);
    *(s16x8*)(tab + i * 1024 + lane * 16) = t0;
    if (i < 3) *(s16x8*)(tab + (i + 5) * 1024 + lane * 16) = t1;
    __syncthreads();
    const int task = (bid - K2_MAIN0) * 5 + i;
    if (task < 1080) prep_gu(task, A.wg, A.wu, A.Wgu, smem + 32768 + 4096 * i, lane);
    if (i == 4) {
      const int xc = bid & 7, li = (bid - K2_MAIN0) >> 3;
#pragma unroll
      for (int k = 0; k < 4; ++k) {
        const int e = (k & 1) * 64 + lane, id = li * 96 + e;
        const int ln = id % 6, row = 8 * ((id / 6) & 255) + (id < 1536 ? xc : ((8 - xc) & 7));
        const u32 off = min((u32)(row * 642 + ln * 128), (u32)(2048 * 642 - 4)) & ~3u;
        if (e < 96) l2_touch((k >> 1) ? A.wi : A.wr, off, lds_addr(smem) + sizeof(smem) - 256);
      }
    }
  }
  __syncthreads();
  STAMP(1, 3);
  {
    const LDSP(const char) base = (LDSP(const char))(size_t)ard;
    s16x8 b = cat8(__builtin_amdgcn_ds_read_tr16_b64_v4i16((LDSP(s16x4))base), __builtin_amdgcn_ds_read_tr16_b64_v4i16((LDSP(s16x4))(base + 160)));
    const s16x8 fa0 = *(const s16x8*)(tab + lane * 16), fa1 = *(const s16x8*)(tab + 1024 + lane * 16);
    const f32x4 z = {0.f, 0.f, 0.f, 0.f};
    f32x4 a0 = mfma16(fa0, b, z), a1 = mfma16(fa1, b, z);
    LDSP(u32) Ub = (LDSP(u32))(size_t)awr;
    const float x0 = g == 0 ? a0[1] : a0[0], x1 = g == 0 ? 0.f : a0[1];
    *(LDSP(u32))(size_t)aw0 = pk_bf(x0 * tw0[0] - x1 * tw0[1], x0 * tw0[1] + x1 * tw0[0]);
    if (g == 0) Ub[0] = pk_bf(a0[0] * z0r, a0[0] * z0i);
    Ub[20] = pk_bf(a0[2] * tw0[2] - a0[3] * tw0[3], a0[2] * tw0[3] + a0[3] * tw0[2]);
    Ub[8 * 20] = pk_bf(a1[0] * tw1[0] - a1[1] * tw1[1], a1[0] * tw1[1] + a1[1] * tw1[0]);
    Ub[9 * 20] = pk_bf(a1[2] * tw1[2] - a1[3] * tw1[3], a1[2] * tw1[3] + a1[3] * tw1[2]);
  }
  __syncthreads();
  STAMP(1, 4);
  const f32x4 z4 = {0.f, 0.f, 0.f, 0.f};
  u32* XTb = XT + (size_t)s0 * 16;
  const char* ft = tab + 2048 + lane * 16;
  const LDSP(const char) be = (LDSP(const char))(size_t)acr;
  const int P = w >> 1, mp = P, mm = P == 0 ? 4 : 8 - P;
  if (w < 8) {
    const s16x8 e0 = *(const LDSP(s16x8))(be + 16 * g), e1 = *(const LDSP(s16x8))(be + 64), o0 = *(const LDSP(s16x8))(be + 1360 + 16 * g), o1 = *(const LDSP(s16x8))(be + 1360 + 64);
    char* dp = (char*)(XTb + (size_t)(mp * 21) * 4096); char* dm = (char*)(XTb + (size_t)(mm * 21) * 4096);
    const u32 vo = (u32)(2 * g * 4096 + c) * 4u;
    if (!(w & 1)) {
#pragma unroll
      for (int mt = 0; mt < 2; ++mt) {
        const s16x8 f0 = *(const s16x8*)(ft + (2 * mt) * 1024), f1 = *(const s16x8*)(ft + (2 * mt + 1) * 1024);
        f32x4 ae = mfma16(f1, e1, mfma16(f0, e0, z4)), ao = mfma16(f1, o1, mfma16(f0, o0, z4));
        f32x4 tp = ae + ao, tm = ae - ao;
        if (P == 0) { tp = ae; tm = ao; }
        char* ep = dp + (size_t)(8 * mt) * 16384; char* em = dm + (size_t)(8 * mt) * 16384;
        const int k0 = 8 * mt + 2 * g;
        { const float d0_ = (c == 0 && k0 >= 1 && k0 <= 9) ? 2.f : 1.f, d1_ = (c == 0 && k0 + 1 <= 9) ? 2.f : 1.f;
          const f32x4 dd = {d0_, d0_, d1_, d1_}; tp = tp * dd; tm = tm * dd; }
        if (mt == 0 || c != 0 || k0 <= 10) { stg_sel<4>(ep, vo, pk_bf(tp[0], tp[1])); stg_sel<4>(em, vo, pk_bf(tm[0], tm[1])); }
        if (mt == 0 || c != 0 || k0 + 1 <= 10) { stg_sel<4>(ep + 16384, vo, pk_bf(tp[2], tp[3])); stg_sel<4>(em + 16384, vo, pk_bf(tm[2], tm[3])); }
      }
    } else {
      const s16x8 f0 = *(const s16x8*)(ft + 4 * 1024), f1 = *(const s16x8*)(ft + 5 * 1024);
      f32x4 ae = mfma16(f1, e1, mfma16(f0, e0, z4)), ao = mfma16(f1, o1, mfma16(f0, o0, z4));
      f32x4 tp = ae + ao, tm = ae - ao;
      if (P == 0) { tp = ae; tm = ao; }
      if (g < 2 && c != 0) {
        char* ep = dp + (size_t)16 * 16384; char* em = dm + (size_t)16 * 16384;
        stg_sel<4>(ep, vo, pk_bf(tp[0], tp[1])); stg_sel<4>(ep + 16384, vo, pk_bf(tp[2], tp[3]));
        stg_sel<4>(em, vo, pk_bf(tm[0], tm[1])); stg_sel<4>(em + 16384, vo, pk_bf(tm[2], tm[3]));
      }
    }
  } else {
    const int rh = c & 7, mt = w - 7;
    const s16x8 b0 = *(const LDSP(s16x8))(be + 16 * g), b1 = *(const LDSP(s16x8))(be + 64);
    const s16x8 f0 = *(const s16x8*)(ft + (2 * mt) * 1024), f1 = *(const s16x8*)(ft + (2 * mt + 1) * 1024);
    f32x4 a = mfma16(f1, b1, mfma16(f0, b0, z4));
    const float ca_ = (rh >= 2 && (rh & 1)) ? -1.f : 1.f, cb_ = rh >= 2 ? 1.f : 0.f;
    f32x4 t;
#pragma unroll
    for (int r = 0; r < 4; ++r) t[r] = ca_ * a[r] + cb_ * dpp_x1(a[r]);
    const int m1 = rh == 0 ? 0 : rh == 1 ? 4 : (rh & 1) ? 8 - (rh >> 1) : (rh >> 1);
    const int k0 = 8 * mt + 2 * g;
    const u32 o0 = (u32)((m1 * 21 + (k0 == 10 ? 20 : k0)) * 4096) * 4u, o1 = (u32)((m1 * 21 + k0 + 1) * 4096) * 4u;
    if (c < 8 && k0 >= 10 && k0 < 20) { stg_sel<4>(XTb, o0, pk_bf(t[0], t[1])); stg_sel<4>(XTb, o1, pk_bf(t[2], t[3])); }
    if (w == 9) asm volatile("s_waitcnt vmcnt(0)" ::: "memory");
  }
  STAMP(1, 7);
}

constexpr int EX_RS = 20;
extern "C" __global__ __launch_bounds__(512) void k3_mid(const u32* __restrict__ XT, const u16* __restrict__ wr, const u16* __restrict__ wi, u32* __restrict__ ZT) {
  STAMP_DECL; STAMP(2, 0);
  __shared__ __attribute__((aligned(16))) u32 ex[2][256 * EX_RS];
  __shared__ __attribute__((aligned(16))) char tab[8 * 1024];
  const int tid = threadIdx.x, lane = tid & 63, w = __builtin_amdgcn_readfirstlane(tid >> 6), c = lane & 15, g = lane >> 4;
  const int m1 = blockIdx.x & 7, tile = blockIdx.x >> 3;
  const char* in = (const char*)(XT + (size_t)(m1 * 21 + tile) * 4096);
  dma1k(in + 2048 * w, (u32)lane * 16u, lds_addr(ex[1]) + 2048 * w);
  dma1k(in + 2048 * w + 1024, (u32)lane * 16u, lds_addr(ex[1]) + 2048 * w + 1024);
  const F8w* tsrc = w < 2 ? &g_a1[m1] : w < 4 ? &g_f16f : w < 6 ? &g_f16i : &g_a1i[m1];
  const s16x8 tfrag = ldg<s16x8>(tsrc->v[w & 1], lane * 16);
  u32 wra[2][4], wia[2][4];
  const int kb = tile == 20 ? 336 : (c == 0 && tile >= 11) ? 32 * tile + 16 : 32 * tile + c;
  const bool mir = kb > 320;
  {
    const int kk = mir ? 640 - kb : kb;
#pragma unroll
    for (int j = 0; j < 2; ++j)
#pragma unroll
      for (int r = 0; r < 4; ++r) {
        const int m = m1 + 8 * (2 * w + j) + 128 * (4 * g + r), mm = mir ? ((2048 - m) & 2047) : m;
        const u32 vo = (u32)(mm * DF + kk) * 2u;
        wra[j][r] = (u32)ldg<u16>(wr, vo) << 16; wia[j][r] = (u32)ldg<u16>(wi, vo) << 16;
      }
  }
  f32x4 fcs[2], fsn[2], ics[2], isn[2];
#pragma unroll
  for (int j = 0; j < 2; ++j)
#pragma unroll
    for (int r = 0; r < 4; ++r) {
      float s_, c_;
      sincos_rev((float)((2 * w + j) * (m1 + 8 * (4 * g + r))) * (1.0f / 2048), s_, c_); fcs[j][r] = c_; fsn[j][r] = s_;
      sincos_rev((float)((4 * g + r) * (m1 + 8 * (2 * w + j))) * (1.0f / 2048), s_, c_); ics[j][r] = c_; isn[j][r] = s_;
    }
  u32 awr = lds_addr(ex) + 4 * ((4 * g * 16 + c) * EX_RS + 2 * w);
  u32 ard = lds_addr(ex) + 4 * ((2 * w * 16 + c) * EX_RS + 4 * g);
#pragma unroll
  for (int j = 0; j < 2; ++j) { pin(fcs[j]); pin(fsn[j]); pin(ics[j]); pin(isn[j]); }
  pin(awr); pin(ard);
  *(s16x8*)(tab + w * 1024 + lane * 16) = tfrag;
  STAMP(2, 1);
  wait_vm<16>();
  __syncthreads();
  STAMP(2, 2);
  u32x4 bx[2];
#pragma unroll
  for (int j = 0; j < 2; ++j)
#pragma unroll
    for (int i = 0; i < 4; ++i) bx[j][i] = ex[1][(16 * (4 * g + i) + 2 * w + j) * 16 + c];
  const f32x4 z4 = {0.f, 0.f, 0.f, 0.f};
  const char* tb = tab + lane * 16;
  const LDSP(char) wr0 = (LDSP(char))(size_t)awr; const LDSP(const char) rd0 = (LDSP(const char))(size_t)ard;
  constexpr int RB = 16 * EX_RS * 4, XB = 256 * EX_RS * 4;
  {
    const s16x8 A10 = *(const s16x8*)tb, A11 = *(const s16x8*)(tb + 1024);
    u32 pk[2][4];
#pragma unroll
    for (int j = 0; j < 2; ++j) {
      f32x4 re = mfma16(A10, as8(bx[j]), z4), im = mfma16(A11, as8(bx[j]), z4);
      f32x4 tr = re * fcs[j] + im * fsn[j], ti = im * fcs[j] - re * fsn[j];
#pragma unroll
      for (int r = 0; r < 4; ++r) pk[j][r] = pk_bf(tr[r], ti[r]);
    }
#pragma unroll
    for (int r = 0; r < 4; ++r) *(LDSP(u32x2))(wr0 + r * RB) = u32x2{pk[0][r], pk[1][r]};
  }
  __syncthreads();
  STAMP(2, 3);
  {
    const s16x8 F0 = *(const s16x8*)(tb + 2048), F1 = *(const s16x8*)(tb + 3072), G0 = *(const s16x8*)(tb + 4096), G1 = *(const s16x8*)(tb + 5120);
    u32 pk[2][4];
#pragma unroll
    for (int j = 0; j < 2; ++j) {
      const u32x4 b = *(const LDSP(u32x4))(rd0 + j * RB);
      f32x4 xr = mfma16(F0, as8(b), z4), xi = mfma16(F1, as8(b), z4);
      const f32x4 a = {__uint_as_float(wra[j][0]), __uint_as_float(wra[j][1]), __uint_as_float(wra[j][2]), __uint_as_float(wra[j][3])};
      const u32 sg = mir ? 0x80000000u : 0u;
      const f32x4 bb = {__uint_as_float(wia[j][0] ^ sg), __uint_as_float(wia[j][1] ^ sg), __uint_as_float(wia[j][2] ^ sg), __uint_as_float(wia[j][3] ^ sg)};
      const f32x4 yr = xr * a - xi * bb, yi = xr * bb + xi * a;
      const u32x4 yb = {pk_bf(yr[0], yr[1]), pk_bf(yr[2], yr[3]), pk_bf(yi[0], yi[1]), pk_bf(yi[2], yi[3])};
      f32x4 re = mfma16(G0, as8(yb), z4), im = mfma16(G1, as8(yb), z4);
      f32x4 tr = re * ics[j] - im * isn[j], ti = im * ics[j] + re * isn[j];
#pragma unroll
      for (int r = 0; r < 4; ++r) pk[j][r] = pk_bf(tr[r], ti[r]);
    }
#pragma unroll
    for (int r = 0; r < 4; ++r) *(LDSP(u32x2))(wr0 + XB + r * RB) = u32x2{pk[0][r], pk[1][r]};
  }
  __syncthreads();
  STAMP(2, 4);
  char* out = (char*)(ZT + (size_t)(2 * w) * 8 * 21 * 16 + (m1 * 21 + tile) * 16);
  const u32 vout = (u32)(64 * g * 10752 + c * 4);
  const s16x8 I0 = *(const s16x8*)(tb + 6144), I1 = *(const s16x8*)(tb + 7168);
#pragma unroll
  for (int j = 0; j < 2; ++j) {
    const u32x4 b = *(const LDSP(u32x4))(rd0 + XB + j * RB);
    f32x4 zr = mfma16(I0, as8(b), z4), zi = mfma16(I1, as8(b), z4);
#pragma unroll
    for (int r = 0; r < 4; ++r) stg_sel<8>(out + (16 * r + j) * 10752, vout, pk_bf(zr[r], zi[r]));
  }
  STAMP(2, 7);
}

constexpr int Y_RS = 640 + 4;
extern "C" __global__ __launch_bounds__(640) void k4_inv(const u32* __restrict__ ZT, const u16* __restrict__ x, const float* __restrict__ mod,
                                              u16* __restrict__ x1o, u16* __restrict__ h2o, const u16* __restrict__ Wgu) {
  STAMP_DECL; STAMP(3, 0);
  __shared__ __attribute__((aligned(16))) char smem[162 * 64 + 8 * Y_RS * 4 + 10 * 8 * 4 + 8 * 1024 + 10752 + 256];
  char* Vs = smem; float* Y = (float*)(Vs + 162 * 64); float* part = Y + 8 * Y_RS; char* tab = (char*)(part + 80);
  const int tid = threadIdx.x, lane = tid & 63, w = __builtin_amdgcn_readfirstlane(tid >> 6), c = lane & 15, g = lane >> 4;
  const int s0 = row_group(blockIdx.x);
  u32* Zs = (u32*)(tab + 8 * 1024);
  {
    const char* Zb = (const char*)(ZT + (size_t)s0 * 8 * 21 * 16);
    dma1k(Zb + 1024 * w, (u32)lane * 16u, lds_addr(Zs) + 1024 * w);
    if (w == 0 && lane < 32) dma1k(Zb + 10240, (u32)lane * 16u, lds_addr(Zs) + 10240);
  }
  const int rho = w < 8 ? w : (c & 7), P = rho >> 1, mA = rho == 1 ? 4 : P, mB = P == 0 ? 4 : 8 - P;
  const s16x8 tfrag = w < 6 ? ldg<s16x8>(&g_f20ai.v[0][0][0][0] + w * 512, lane * 16) : ldg<s16x8>(g_f32bi.v[(w - 6) & 1], lane * 16);
  const int hf = w >= 5, cp = tid - 320 * hf;
  u32 xv[4];
#pragma unroll
  for (int r = 0; r < 4; ++r) xv[r] = ldg<u32>(x + (size_t)(s0 + 256 * (4 * hf + r)) * D_, 4 * cp);
  const f32x2 ga = ldg<f32x2>(mod + 2 * 640, 8 * cp), sb = ldg<f32x2>(mod + 3 * 640, 8 * cp), cb = ldg<f32x2>(mod + 4 * 640, 8 * cp);
  f32x4 tw[3]; u32 aw[3];
  {
    const bool rot = rho >= 3 && (rho & 1);
    const float sc = w < 8 ? 1.0f / (2048.0f * 640.0f) : 2.0f / (2048.0f * 640.0f), fk = (float)(w < 8 ? c : 16) * (1.0f / 640.0f);
#pragma unroll
    for (int mt = 0; mt < 3; ++mt) {
#pragma unroll
      for (int rr = 0; rr < 2; ++rr) {
        float s_, c_; sincos_rev(fk * (float)(8 * mt + 2 * g + rr), s_, c_);
        tw[mt][2 * rr] = (rot ? -s_ : c_) * sc; tw[mt][2 * rr + 1] = (rot ? c_ : s_) * sc;
      }
      const int d0 = 8 * mt + 2 * g;
      const bool ok = d0 < 20 && (w < 8 || c < 8);
      aw[mt] = lds_addr(Vs) + (ok ? (rho * 20 + d0) * 64 : 160 * 64) + (w < 8 ? 4 * c : 2);
      pin(tw[mt]); pin(aw[mt]);
    }
  }
  float be = rho < 2 ? 0.f : (rho & 1) ? -1.f : 1.f;
  u32 msk[8], zoa[8];
#pragma unroll
  for (int i = 0; i < 8; ++i) {
    const int k0 = i < 4 ? 4 * g + i : 16 + (i - 4);
    const bool dead = w < 8 ? (c == 0 && k0 > 10) : (k0 < 10 || (i >= 4 && g != 0));
    msk[i] = dead ? 0u : 0xffffffffu;
    zoa[i] = w < 8 ? k0 * 16 + c : (k0 < 10 ? 11 : k0 == 10 ? 20 : k0) * 16;
    pin(msk[i]); pin(zoa[i]);
  }
  u32 zba = lds_addr(Zs) + mA * 21 * 64, zbb = lds_addr(Zs) + mB * 21 * 64;
  pin(be); pin(zba); pin(zbb);
  if (w == 9 && blockIdx.x < 8) {
    const void* c5; const void* c6; CODE_ADDR(c5, k5_glu); CODE_ADDR(c6, k6_down);
    const u32 len = (u32)((const char*)c6 - (const char*)c5);
    if (lane * 128u < len) l2_touch(c5, (u32)lane * 128u, lds_addr(smem) + sizeof(smem) - 256);
    if (lane * 128u + 8192u < len) l2_touch(c5, (u32)lane * 128u + 8192u, lds_addr(smem) + sizeof(smem) - 256);
  }
  STAMP(3, 1);
  if (w < 8) *(s16x8*)(tab + w * 1024 + lane * 16) = tfrag;
  wait_vm<4>();
  __syncthreads();
  u32x4 b0, b1;
  if (w < 9) {
#pragma unroll
    for (int i = 0; i < 8; ++i) {
      const u32 zz = *(const LDSP(u32))(size_t)(zba + 4 * zoa[i]) & msk[i], zy = *(const LDSP(u32))(size_t)(zbb + 4 * zoa[i]) & msk[i];
      const f32x2 a = {bf_lo(zz), bf_hi(zz)}, b = {bf_lo(zy), bf_hi(zy)};
      const f32x2 v = a + be * b;
      if (i < 4) b0[i] = pk_bf(v[0], v[1]); else b1[i - 4] = pk_bf(v[0], v[1]);
    }
  }
  STAMP(3, 2);
  if (w < 9) {
    f32x4 a[3];
#pragma unroll
    for (int mt = 0; mt < 3; ++mt) {
      const f32x4 z = {0.f, 0.f, 0.f, 0.f};
      const s16x8 f0 = *(const s16x8*)(tab + (2 * mt) * 1024 + lane * 16), f1 = *(const s16x8*)(tab + (2 * mt + 1) * 1024 + lane * 16);
      a[mt] = mfma16(f1, as8(b1), mfma16(f0, as8(b0), z));
    }
    if (w < 8) {
      const u32 hi_skip = c == 0 ? 1u : 0u;
#pragma unroll
      for (int mt = 0; mt < 3; ++mt)
#pragma unroll
        for (int rr = 0; rr < 2; ++rr) {
          const u32 pv = pk_bf(a[mt][2 * rr] * tw[mt][2 * rr] - a[mt][2 * rr + 1] * tw[mt][2 * rr + 1], a[mt][2 * rr] * tw[mt][2 * rr + 1] + a[mt][2 * rr + 1] * tw[mt][2 * rr]);
          LDSP(char) dst = (LDSP(char))(size_t)aw[mt] + 64 * rr;
          if (hi_skip) *(LDSP(u16))dst = (u16)pv; else *(LDSP(u32))dst = pv;
        }
    } else {
#pragma unroll
      for (int mt = 0; mt < 3; ++mt)
#pragma unroll
        for (int rr = 0; rr < 2; ++rr)
          *(LDSP(u16))((LDSP(char))(size_t)aw[mt] + 64 * rr) = (u16)pk_bf(a[mt][2 * rr] * tw[mt][2 * rr] - a[mt][2 * rr + 1] * tw[mt][2 * rr + 1], 0.f);
    }
  }
  __syncthreads();
  STAMP(3, 3);
  {
    const s16x8 a = *(const s16x8*)(Vs + (16 * w + c) * 64 + 16 * g);
    const s16x8 fb0 = *(const s16x8*)(tab + 6144 + lane * 16), fb1 = *(const s16x8*)(tab + 7168 + lane * 16);
    const f32x4 z = {0.f, 0.f, 0.f, 0.f};
    const f32x4 y0 = mfma16(a, fb0, z), y1 = mfma16(a, fb1, z);
    const int R = 16 * w + 4 * g, rr = R / 20, d0 = R % 20;
    *(f32x4*)(Y + rr * Y_RS + 20 * c + d0) = y0;
    *(f32x4*)(Y + rr * Y_RS + 20 * (16 + c) + d0) = y1;
  }
  __syncthreads();
  STAMP(3, 4);
  {
    f32x2 R[8];
#pragma unroll
    for (int r = 0; r < 8; ++r) R[r] = *(const f32x2*)(Y + r * Y_RS + 2 * cp);
    const float cc = 0.70710678118654752f;
    const f32x2 G = R[0] + R[1], Fm = R[0] - R[1];
    const f32x2 u = cc * (R[2] - R[6]), v = cc * (R[3] + R[7]);
    const f32x2 c13 = R[2] + R[6], s13 = R[3] - R[7];
    const float sg = hf ? -1.f : 1.f;
    f32x2 y[4];
    y[0] = G + R[4] + sg * c13;
    y[2] = G - R[4] + sg * s13;
    y[1] = Fm + R[5] + sg * (u + v);
    y[3] = Fm - R[5] + sg * (v - u);
    u32 x1[4];
    float sv[8];
#pragma unroll
    for (int r = 0; r < 4; ++r) {
      const f32x2 xx = {bf_lo(xv[r]), bf_hi(xv[r])};
      const f32x2 t = xx + ga * y[r];
      x1[r] = pk_bf(t[0], t[1]);
      stg_sel<2>(x1o + (size_t)(8 * s0 + 4 * hf + r) * D_, (u32)cp * 4u, x1[r]);
      sv[r] = dot2bf(x1[r], 0x3f803f80u, 0.f); sv[4 + r] = dot2bf(x1[r], x1[r], 0.f);
    }
    const float tot = reduce8(sv, lane);
    if (lane < 8) part[w * 8 + lane] = tot;
    const f32x2 gg = {rbf(1.0f + cb[0]), rbf(1.0f + cb[1])};
    const int d = 2 * cp, wi = d & 31;
    char* stage = (char*)Y;
    const int so = (((d >> 5) * 4 + ((wi & 15) >> 2)) * 8 + 4 * hf) * 16 + (4 * (wi >> 4) + (wi & 3)) * 2;
    __syncthreads();
    STAMP(3, 5);
    const int r = lane & 3;
    float s = 0.f, qq = 0.f;
#pragma unroll
    for (int i = 0; i < 5; ++i) { s += part[(5 * hf + i) * 8 + r]; qq += part[(5 * hf + i) * 8 + 4 + r]; }
    float m_, i_; ln_finish(s, qq, m_, i_);
    const float nb_ = -m_ * i_;
#pragma unroll
    for (int i = 0; i < 4; ++i) {
      const float na = __int_as_float(__builtin_amdgcn_readlane(__float_as_int(i_), i)), nb = __int_as_float(__builtin_amdgcn_readlane(__float_as_int(nb_), i));
      const f32x2 v2 = {bf_lo(x1[i]), bf_hi(x1[i])};
      const f32x2 hv = (v2 * na + nb) * gg + sb;
      *(u32*)(stage + so + 16 * i) = pk_bf(hv[0], hv[1]);
    }
    __syncthreads();
    const u32x4 piece = *(const u32x4*)(stage + 16 * tid);
    stg_sel<16>(h2o + ((size_t)(s0 >> 1) * 20 * 64 + 8 * (s0 & 1)) * 8, (u32)((tid >> 3) * 16 + (tid & 7)) * 16u, piece);
  }
  if (w == 9) asm volatile("s_waitcnt vmcnt(0)" ::: "memory");
  STAMP(3, 7);
}

constexpr int K5_SLOT = 28 * 1024;
constexpr int K5_NS = 4;
constexpr int K5_PD = 4;
struct K5Ctx { const u16* wdp; int pfl; u32 dump; const u16* wsrc; const u16* hsrc; u32 ldsw; u32 goff[7]; u32 hoff; u32 abase[2]; };
template <int s, int i> DI void k5_w1(const K5Ctx& c) {
  if constexpr (i == 0) set_m0(c.ldsw + (s % K5_NS) * K5_SLOT + 3072);
  glds16_s<1024 * i - 3072>((const char*)(c.wsrc + s * 4 * 512) - 4096, c.goff[i]);
}
template <int s, int i> DI void k5_t1(const K5Ctx& c, u32x4 (&t)[4]) {
  gload16_s<(i >> 1) * 1024>(t[i], c.hsrc + ((i & 1) * 20 + 2 * s) * 512, c.hoff);
}
template <int s> DI void k5_wall(const K5Ctx& c) { k5_w1<s, 0>(c); k5_w1<s, 1>(c); k5_w1<s, 2>(c); k5_w1<s, 3>(c); k5_w1<s, 4>(c); k5_w1<s, 5>(c); k5_w1<s, 6>(c); }
template <int s> DI void k5_tall(const K5Ctx& c, u32x4 (&t)[4]) { k5_t1<s, 0>(c, t); k5_t1<s, 1>(c, t); k5_t1<s, 2>(c, t); k5_t1<s, 3>(c, t); }
template <int n> DI s16x8 k5_rd(const K5Ctx& c) {
  constexpr int s = n / 28, t = (n / 14) & 1, j = n % 14, sl = s % K5_NS;
  return *(const LDSP(const s16x8))((LDSP(const char))(size_t)c.abase[sl >> 1] + (sl & 1) * K5_SLOT + ((j >> 1) * 4 + t * 2 + (j & 1)) * 1024);
}
template <int n> DI void k5_sub(const K5Ctx& c, f32x4 (&acc)[14][2], s16x8 (&fr)[K5_PD + 1], u32x4 (&tok)[10][4]) {
  constexpr int s = n / 28, t = (n / 14) & 1, j = n % 14;
  constexpr int nb = n + K5_PD;
  if constexpr (nb < 280 && nb % 28 == 0) {
    wait_vm<(s + 2 <= 9) ? 11 : 0>();
    __builtin_amdgcn_s_barrier();
    SLAB(s + 1);
    tie4(tok[s + 1]);
  }
  if constexpr (nb < 280) fr[nb % (K5_PD + 1)] = k5_rd<nb>(c);
  {
    constexpr int q = nb / 28, pos = nb % 28 - (q == 0 ? K5_PD : 0);
    constexpr int idx = (pos % 2 == 0 && pos < 22) ? pos / 2 : -1;
    if constexpr (idx >= 0 && idx < 4 && q + 2 <= 9) k5_t1<q + 2, idx>(c, tok[q + 2]);
    if constexpr (idx >= 4 && q + 2 <= 9) k5_w1<q + 2, idx - 4>(c);
  }
  if constexpr (false) {
    constexpr int i = (n - 224) / 2;
    l2_touch(c.wdp, (u32)(c.pfl + i * 64) * 128u, c.dump);
  }
  const s16x8 a = fr[n % (K5_PD + 1)];
  acc[j][0] = mfma16(a, as8(tok[s][2 * t]), acc[j][0]);
  acc[j][1] = mfma16(a, as8(tok[s][2 * t + 1]), acc[j][1]);
  __builtin_amdgcn_sched_barrier(0);
}
template <int... I> DI void k5_all(const K5Ctx& c, f32x4 (&acc)[14][2], s16x8 (&fr)[K5_PD + 1], u32x4 (&tok)[10][4], std::integer_sequence<int, I...>) { (k5_sub<I>(c, acc, fr, tok), ...); }
extern "C" __global__ __launch_bounds__(256, 2) void k5_glu(const u16* __restrict__ h2, const u16* __restrict__ Wgu, u16* __restrict__ hb, const u16* __restrict__ Wd) {
  STAMP_DECL; STAMP(4, 0);
  extern __shared__ __attribute__((aligned(16))) char smem[];
  const int tid = threadIdx.x, lane = tid & 63, w = __builtin_amdgcn_readfirstlane(tid >> 6);
  const int xcd = blockIdx.x & 7, li = blockIdx.x >> 3;
  const int tt = 8 * (xcd & 1) + (li & 7), fc = 4 * (xcd >> 1) + (li >> 3);
  const int u0 = fc < 12 ? 7 * fc : 84 + 6 * (fc - 12), U = fc < 12 ? 7 : 6;
  if (blockIdx.x < 8) {
    const void* c6; const void* c7; CODE_ADDR(c6, k6_down); CODE_ADDR(c7, k7_end);
    if ((u32)tid * 128u < (u32)((const char*)c7 - (const char*)c6)) l2_touch(c6, (u32)tid * 128u, lds_addr(smem) + 3 * K5_SLOT + w * 256);
  }
  K5Ctx c;
  c.wsrc = Wgu + (size_t)u0 * 20 * 2 * 512;
  c.dump = lds_addr(smem) + 2 * K5_SLOT + w * 256;
  c.wdp = Wd + (size_t)(20 * (xcd >> 2)) * 54 * 512;
#pragma unroll
  for (int i = 0; i < 7; ++i) { const int f = 7 * w + i, u = min(f >> 2, U - 1); c.goff[i] = 2 * (u * 20 * 2 * 512 + (f & 3) * 512) + lane * 16 + 4096 - (1024 * i - 3072); }
  c.ldsw = lds_addr(smem) + 7 * w * 1024;
  const int T16 = 8 * tt + 2 * w;
  c.hsrc = h2 + (size_t)T16 * 20 * 512; c.hoff = lane * 16;
#pragma unroll
  for (int j = 0; j < 2; ++j) { c.abase[j] = lds_addr(smem) + 2 * j * K5_SLOT + lane * 16; asm volatile("" : "+v"(c.abase[j])); }
  u32x4 tok[10][4];
  k5_tall<0>(c, tok[0]); k5_wall<0>(c);
  k5_tall<1>(c, tok[1]); k5_wall<1>(c);
  f32x4 acc[14][2];
#pragma unroll
  for (int u = 0; u < 14; ++u) { acc[u][0] = f32x4{0.f, 0.f, 0.f, 0.f}; acc[u][1] = f32x4{0.f, 0.f, 0.f, 0.f}; }
  STAMP(4, 3);
  wait_vm<11>();
  __builtin_amdgcn_s_barrier();
  tie4(tok[0]);
  STAMP(4, 4); SLAB(0);
  s16x8 fr[K5_PD + 1];
  fr[0] = k5_rd<0>(c); fr[1] = k5_rd<1>(c); fr[2] = k5_rd<2>(c); fr[3] = k5_rd<3>(c);
  __builtin_amdgcn_sched_barrier(0);
  k5_all(c, acc, fr, tok, std::make_integer_sequence<int, 280>{});
  STAMP(4, 5); SLAB(10);
  auto hv = [&](int u, int tl) { const f32x4 a = acc[2 * u][tl] * acc[2 * u + 1][tl]; return u32x2{pk_bf(a[0], a[1]), pk_bf(a[2], a[3])}; };
#pragma unroll
  for (int tl = 0; tl < 2; ++tl) {
    u16* hdst = hb + (size_t)(T16 + tl) * 54 * 512 + (u0 >> 1) * 512;
    const u32 vo = (u32)lane * 16u;
    if (!(u0 & 1)) {
#pragma unroll
      for (int k = 0; k < 3; ++k) { const u32x2 a = hv(2 * k, tl), b = hv(2 * k + 1, tl); stg_sel<32>(hdst + k * 512, vo, u32x4{a[0], a[1], b[0], b[1]}); }
      if (U == 7) stg_sel<32>(hdst + 3 * 512, vo, hv(6, tl));
    } else {
      stg_sel<32>(hdst + 4, vo, hv(0, tl));
#pragma unroll
      for (int k = 0; k < 3; ++k) { const u32x2 a = hv(2 * k + 1, tl), b = hv(2 * k + 2, tl); stg_sel<32>(hdst + (k + 1) * 512, vo, u32x4{a[0], a[1], b[0], b[1]}); }
    }
  }
  STAMP(4, 7);
}

constexpr int K6_NS = 4;
struct K6Ctx { const u16* wsrc; const u16* hsrc; u32 hoff; const float* gbp; u32 gbo; const u16* xp; u32 xo; const u16* tbase; u32 toff; u32 dump; };
struct K6Grp { u32x4 a[5]; u32x4 t[4]; };
template <int i> DI void k6_v1(const K6Ctx& c, int ks, K6Grp& gr) {
  if constexpr (i < 5) gload16_s<0>(gr.a[i], c.wsrc + (ks + i * 54) * 512, c.hoff);
  else gload16_s<0>(gr.t[i - 5], c.hsrc + (ks + (i - 5) * 54) * 512, c.hoff);
}
DI void k6_issue(const K6Ctx& c, int ks, K6Grp& gr) {
  k6_v1<0>(c, ks, gr); k6_v1<5>(c, ks, gr); k6_v1<6>(c, ks, gr); k6_v1<7>(c, ks, gr); k6_v1<8>(c, ks, gr);
  k6_v1<1>(c, ks, gr); k6_v1<2>(c, ks, gr); k6_v1<3>(c, ks, gr); k6_v1<4>(c, ks, gr);
}
DI void k6_tie(K6Grp& g) { asm volatile("" : "+v"(g.a[0]), "+v"(g.a[1]), "+v"(g.a[2]), "+v"(g.a[3]), "+v"(g.a[4]), "+v"(g.t[0]), "+v"(g.t[1]), "+v"(g.t[2]), "+v"(g.t[3])); }
struct K6Epi { u32x4 gb[5]; u32x2 xr[5]; };
template <int OFF> DI void gload8_s(u32x2& d, const void* sbase, u32 voff) {
  asm volatile("global_load_dwordx2 %0, %1, %2 offset:%3" : "=v"(d) : "v"(voff), "s"(sbase), "n"(OFF) : "memory");
}
#define K6_TOUCH 0
#define K6_TDV 5
constexpr int K6_TD = K6_TDV, K6_TLAST = K6_TOUCH ? 12 : -1;
template <int j> DI void k6_touch(const K6Ctx& c, int lane) { if (lane < 13) l2_touch(c.tbase + j * 512, c.toff, c.dump); }
constexpr int k6_after(int i, int NK) {
  int n = 0; bool seen = false;
  for (int g = 0; g < 3; ++g) { if (seen) n += 9; if (g == i) seen = true; }
  for (int j = 3; j < K6_TD; ++j) if (seen && K6_TOUCH) n += 1;
  for (int st = 0; st < i; ++st) {
    if (st + K6_TD <= K6_TLAST && seen) n += 1;
    if (st == NK - 3 && seen) n += 10;
    if (st + 3 < NK) { if (seen) n += 9; if (st + 3 == i) seen = true; }
  }
  return n;
}
template <int i, int NK> DI void k6_step(const K6Ctx& c, int ks0, f32x4 (&acc)[5][4], K6Grp (&gr)[K6_NS], K6Epi& ep, f32x4* red, int w, int lane) {
  wait_vm<k6_after(i, NK)>();
  K6Grp& g = gr[i % K6_NS];
  k6_tie(g);
  K6Grp& nx = gr[(i + 3) % K6_NS];
  if constexpr (i + K6_TD <= K6_TLAST) k6_touch<i + K6_TD>(c, lane);
#pragma unroll
  for (int nt = 0; nt < 5; ++nt) {
    if constexpr (i + 3 < NK) {
      if (nt == 0) { k6_v1<0>(c, ks0 + i + 3, nx); k6_v1<5>(c, ks0 + i + 3, nx); }
      if (nt == 1) { k6_v1<6>(c, ks0 + i + 3, nx); k6_v1<7>(c, ks0 + i + 3, nx); }
      if (nt == 2) { k6_v1<8>(c, ks0 + i + 3, nx); k6_v1<1>(c, ks0 + i + 3, nx); }
      if (nt == 3) { k6_v1<2>(c, ks0 + i + 3, nx); k6_v1<3>(c, ks0 + i + 3, nx); }
      if (nt == 4) { k6_v1<4>(c, ks0 + i + 3, nx); }
    }
    if constexpr (i == NK - 3) {
      gload16_s<0>(ep.gb[nt], c.gbp + 16 * nt, c.gbo); gload8_s<0>(ep.xr[nt], c.xp + 16 * nt, c.xo);
    }
#pragma unroll
    for (int t = 0; t < 4; ++t) acc[nt][t] = mfma16(as8(g.a[nt]), as8(g.t[t]), acc[nt][t]);
    if constexpr (i == NK - 1) {
      if (nt >= 1) {
#pragma unroll
        for (int t = 0; t < 4; ++t) if (t != w) red[((w * 5 + nt - 1) * 4 + t) * 64 + lane] = acc[nt - 1][t];
      }
    }
    __builtin_amdgcn_sched_barrier(0);
  }
}
template <int i, int NK> struct K6Loop { static DI void run(const K6Ctx& c, int ks0, f32x4 (&acc)[5][4], K6Grp (&gr)[K6_NS], K6Epi& ep, f32x4* red, int w, int lane) {
  k6_step<i, NK>(c, ks0, acc, gr, ep, red, w, lane); K6Loop<i + 1, NK>::run(c, ks0, acc, gr, ep, red, w, lane); } };
template <int NK> struct K6Loop<NK, NK> { static DI void run(const K6Ctx&, int, f32x4 (&)[5][4], K6Grp (&)[K6_NS], K6Epi&, f32x4*, int, int) {} };
template <int NK> DI void k6_main(const K6Ctx& cx, int ks0, f32x4* red, int w, int lane, u16* __restrict__ out, u32 oo) {
  K6Grp gr[K6_NS]; K6Epi ep;
  k6_issue(cx, ks0, gr[0]); k6_issue(cx, ks0 + 1, gr[1]); k6_issue(cx, ks0 + 2, gr[2]);
  if constexpr (K6_TOUCH) { k6_touch<3>(cx, lane); k6_touch<4>(cx, lane); if constexpr (K6_TD > 5) k6_touch<5>(cx, lane); if constexpr (K6_TD > 6) k6_touch<6>(cx, lane); }
  f32x4 acc[5][4];
#pragma unroll
  for (int a = 0; a < 5; ++a)
#pragma unroll
    for (int b = 0; b < 4; ++b) acc[a][b] = f32x4{0.f, 0.f, 0.f, 0.f};
  K6Loop<0, NK>::run(cx, ks0, acc, gr, ep, red, w, lane);
#pragma unroll
  for (int t = 0; t < 4; ++t) if (t != w) red[((w * 5 + 4) * 4 + t) * 64 + lane] = acc[4][t];
  wait_vm<0>();
  asm volatile("" : "+v"(ep.gb[0]), "+v"(ep.gb[1]), "+v"(ep.gb[2]), "+v"(ep.gb[3]), "+v"(ep.gb[4]), "+v"(ep.xr[0]), "+v"(ep.xr[1]), "+v"(ep.xr[2]), "+v"(ep.xr[3]), "+v"(ep.xr[4]));
  __syncthreads();
#pragma unroll
  for (int nt = 0; nt < 5; ++nt) {
    f32x4 v = w == 0 ? acc[nt][0] : w == 1 ? acc[nt][1] : w == 2 ? acc[nt][2] : acc[nt][3];
#pragma unroll
    for (int ww = 1; ww < 4; ++ww) v += red[((((w + ww) & 3) * 5 + nt) * 4 + w) * 64 + lane];
    const f32x4 gb = __builtin_bit_cast(f32x4, ep.gb[nt]);
    const f32x4 x4 = {bf_lo(ep.xr[nt][0]), bf_hi(ep.xr[nt][0]), bf_lo(ep.xr[nt][1]), bf_hi(ep.xr[nt][1])};
    const f32x4 o = x4 + gb * v;
    stg_sel<64>(out + 16 * nt, oo, u32x2{pk_bf(o[0], o[1]), pk_bf(o[2], o[3])});
  }
}
extern "C" __global__ __launch_bounds__(256, 2) void k6_down(const u16* __restrict__ hb, const u16* __restrict__ Wd, const u16* __restrict__ x1, const float* __restrict__ mod, u16* __restrict__ out) {
  STAMP_DECL; STAMP(5, 0);
  extern __shared__ __attribute__((aligned(16))) char smem[];
  const int tid = threadIdx.x, lane = tid & 63, w = __builtin_amdgcn_readfirstlane(tid >> 6), c = lane & 15, g = lane >> 4;
  const int xcd = blockIdx.x & 7, li = blockIdx.x >> 3;
  const int tt = 8 * (xcd & 3) + (li & 7), nb = 4 * (xcd >> 2) + (li >> 3);
  const int n0 = 80 * nb;
  const int ks0 = w < 2 ? 14 * w : 28 + 13 * (w - 2);
  K6Ctx cx;
  cx.wsrc = Wd + (size_t)(5 * nb) * 54 * 512; cx.hsrc = hb + (size_t)(4 * tt) * 54 * 512; cx.hoff = lane * 16;
  cx.gbp = mod + 5 * 640 + n0; cx.gbo = 16 * g;
  cx.xp = x1 + (size_t)(64 * tt + 16 * w) * D_ + n0; cx.xo = (u32)(c * D_ + 4 * g) * 2u;
  {
    const int L = min(13 * (4 * li + w) + lane, 1663), wp = L / 416, r = L % 416, k0p = wp < 2 ? 14 * wp : 28 + 13 * (wp - 2);
    const u32 wdelta = (u32)((const char*)Wd - (const char*)hb);
    const u32 oh = (u32)(((4 * (8 * (xcd & 3) + (r >> 5)) + ((r >> 3) & 3)) * 54 + k0p) * 1024 + (r & 7) * 128);
    const int r2 = r - 256;
    const u32 ow = wdelta + (u32)(((5 * (4 * (xcd >> 2) + r2 / 40) + (r2 % 40) / 8) * 54 + k0p) * 1024 + (r2 & 7) * 128);
    cx.tbase = hb; cx.toff = r < 256 ? oh : ow; cx.dump = lds_addr(smem) + 81920;
  }
  const int tau = 64 * tt + 16 * w + c, srow = (tau >> 3) + 256 * (tau & 7);
  const u32 oo = (u32)(srow * D_ + 4 * g) * 2u;
  if (w < 2) k6_main<14>(cx, ks0, (f32x4*)smem, w, lane, out + n0, oo); else k6_main<13>(cx, ks0, (f32x4*)smem, w, lane, out + n0, oo);
  STAMP(5, 7);
}

extern "C" __global__ void k7_end() {}

constexpr size_t WS_MOD = 0, WS_XT = 16384, WS_ZT = WS_XT + 8 * 21 * 256 * 16 * 4, WS_X1 = WS_ZT + 8 * 21 * 256 * 16 * 4,
                 WS_H2 = WS_X1 + 2048 * 640 * 2, WS_HB = WS_H2 + 2048 * 640 * 2, WS_WGU = WS_HB + 2048 * 1728 * 2, WS_WD = WS_WGU + 2 * 640 * 1728 * 2, WS_WP = WS_WD + 640 * 1728 * 2, WS_SYNC = WS_WP + 168 * 16384;
extern "C" void kernel_launch(void* const* d_in, const int* in_sizes, int n_in, void* d_out, int out_size, void* d_ws, size_t ws_size,
                              hipStream_t stream) {
  const u16* x = (const u16*)d_in[0]; const u16* wr = (const u16*)d_in[1]; const u16* wi = (const u16*)d_in[2];
  const u16* wg = (const u16*)d_in[4]; const u16* wu = (const u16*)d_in[5]; const u16* wd = (const u16*)d_in[6];
  const u16* c = (const u16*)d_in[7]; const u16* wmod = (const u16*)d_in[8];
  char* ws = (char*)d_ws;
  float* mod = (float*)(ws + WS_MOD); u32* XT = (u32*)(ws + WS_XT); u32* ZT = (u32*)(ws + WS_ZT);
  u16* x1 = (u16*)(ws + WS_X1); u16* h2 = (u16*)(ws + WS_H2); u16* hb = (u16*)(ws + WS_HB);
  u16* Wgu = (u16*)(ws + WS_WGU); u16* Wd = (u16*)(ws + WS_WD);
  K2Args A;
  A.x = x; A.c = c; A.wmod = wmod; A.mod = mod; A.XT = XT; A.wr = wr; A.wi = wi; A.wg = wg; A.wu = wu; A.Wgu = Wgu; A.wd = wd; A.Wd = Wd;
  A.gen = (u32*)(ws + WS_SYNC); A.arr = (u32*)(ws + WS_SYNC + 4096); A.gran = (unsigned long long*)(ws + WS_SYNC + 8192);
  k2_fwd<<<K2_GRID, 640, 0, stream>>>(A);
  k3_mid<<<168, 512, 0, stream>>>(XT, wr, wi, ZT);
  k4_inv<<<256, 640, 0, stream>>>(ZT, x, mod, x1, h2, Wgu);
  hipFuncSetAttribute((const void*)k5_glu, hipFuncAttributeMaxDynamicSharedMemorySize, K5_NS * K5_SLOT);
  k5_glu<<<256, 256, K5_NS * K5_SLOT, stream>>>(h2, Wgu, hb, Wd);
  hipFuncSetAttribute((const void*)k6_down, hipFuncAttributeMaxDynamicSharedMemorySize, 81920 + 256);
  k6_down<<<256, 256, 81920 + 256, stream>>>(hb, Wd, x1, mod, (u16*)d_out);
}
```

```cpp
#include <hip/hip_runtime.h>
#include <hip/hip_bf16.h>
#include <stdint.h>
#include <utility>

typedef unsigned short u16;
typedef unsigned int u32;
typedef short s16x4 __attribute__((ext_vector_type(4)));
typedef short s16x8 __attribute__((ext_vector_type(8)));
typedef __bf16 bf2_t __attribute__((ext_vector_type(2)));
typedef float f32x2 __attribute__((ext_vector_type(2)));
typedef float f32x4 __attribute__((ext_vector_type(4)));
typedef float f32x16 __attribute__((ext_vector_type(16)));
typedef u32 u32x2 __attribute__((ext_vector_type(2)));
typedef u32 u32x4 __attribute__((ext_vector_type(4)));
#define DI __device__ __forceinline__
#define LDSP(T) __attribute__((address_space(3))) T*

constexpr int S_ = 2048, D_ = 640, F_ = 1728, DF = 321;

constexpr double kPi = 3.14159265358979323846264338327950288;
constexpr double c_sin0(double x) { double x2 = x * x, t = x, s = x; for (int i = 1; i < 11; ++i) { t *= -x2 / ((2.0 * i) * (2.0 * i + 1.0)); s += t; } return s; }
constexpr double c_cos0(double x) { double x2 = x * x, t = 1.0, s = 1.0; for (int i = 1; i < 11; ++i) { t *= -x2 / ((2.0 * i - 1.0) * (2.0 * i)); s += t; } return s; }
constexpr double c_cos(long n, long N) {
  n %= N; if (n < 0) n += N;
  if (2 * n > N) n = N - n;
  bool neg = false;
  if (4 * n > N) { neg = true; n = N - 2 * n; N = 2 * N; n = n;   }
  double r = 0;
  if (4 * n == N) r = 0.0;
  else if (8 * n > N) r = c_sin0(2 * kPi * (double)(N - 4 * n) / (double)(4 * N));
  else r = c_cos0(2 * kPi * (double)n / (double)N);
  return neg ? -r : r;
}
constexpr double c_sin(long n, long N) { return c_cos(4 * n - N, 4 * N); }
constexpr u16 c_bf16(double v) {
  float f = (float)v; u32 u = __builtin_bit_cast(u32, f);
  u += 0x7fffu + ((u >> 16) & 1u); return (u16)(u >> 16);
}

struct F8w { u16 v[2][64][8]; };
struct F8x6 { u16 v[3][2][64][8]; };
struct TwA { float v[2][64][4]; };
struct TwC { float v[3][64][4]; };
constexpr F8w mk_f32a() {
  F8w t{};
  for (int tt = 0; tt < 2; ++tt) for (int l = 0; l < 64; ++l) for (int j = 0; j < 8; ++j) {
    int o = 16 * tt + (l & 15), d1 = 8 * (l >> 4) + j; double v = 0;
    if (o == 0) v = 1.0; else if (o == 1) v = (d1 & 1) ? -1.0 : 1.0;
    else { int k1 = o >> 1; v = (o & 1) ? -c_sin(d1 * k1, 32) : c_cos(d1 * k1, 32); }
    t.v[tt][l][j] = c_bf16(v);
  }
  return t;
}
constexpr TwA mk_twa(int j) {
  TwA t{};
  for (int tt = 0; tt < 2; ++tt) for (int l = 0; l < 64; ++l) for (int rr = 0; rr < 2; ++rr) {
    int c = l & 15, g = l >> 4, Q = 4 * j + (c >> 2), d0 = 4 * (Q % 5) + (c & 3), rho = Q / 5;
    int k1 = 8 * tt + 2 * g + rr; if (k1 == 0) k1 = 16;
    double co = c_cos(d0 * k1, 640), si = -c_sin(d0 * k1, 640);
    if (rho >= 3 && (rho & 1)) { double x = co; co = -si; si = x; }
    t.v[tt][l][2 * rr] = (float)co; t.v[tt][l][2 * rr + 1] = (float)si;
  }
  return t;
}
constexpr F8x6 mk_f20(bool inv) {
  F8x6 t{};
  for (int mt = 0; mt < 3; ++mt) for (int ks = 0; ks < 2; ++ks) for (int l = 0; l < 64; ++l) for (int j = 0; j < 8; ++j) {
    int row = l & 15, g = l >> 4, k0 = 8 * mt + (row >> 1), part = row & 1, d0 = 16 * ks + 4 * g + (j >> 1), pp = j & 1; double v = 0;
    if (d0 < 20 && k0 < 20) {
      double co = c_cos(d0 * k0, 20), si = inv ? -c_sin(d0 * k0, 20) : c_sin(d0 * k0, 20);
      v = part == 0 ? (pp == 0 ? co : si) : (pp == 0 ? -si : co);
    }
    t.v[mt][ks][l][j] = c_bf16(v);
  }
  return t;
}
constexpr F8w mk_a1(int m1, bool inv) {
  F8w t{};
  for (int l = 0; l < 64; ++l) for (int i = 0; i < 4; ++i) {
    int c = l & 15, a = 4 * (l >> 4) + i, n = inv ? c * (8 * a + m1) : a * (8 * c + m1);
    double co = c_cos(n, 128), si = inv ? -c_sin(n, 128) : c_sin(n, 128);
    t.v[0][l][2 * i] = c_bf16(co); t.v[0][l][2 * i + 1] = c_bf16(si);
    t.v[1][l][2 * i] = c_bf16(-si); t.v[1][l][2 * i + 1] = c_bf16(co);
  }
  return t;
}
constexpr F8w mk_f16(bool inv) {
  F8w t{};
  for (int l = 0; l < 64; ++l) for (int i = 0; i < 4; ++i) {
    int c = l & 15, a = 4 * (l >> 4) + i;
    double co = c_cos(a * c, 16), si = c_sin(a * c, 16);
    if (!inv) { t.v[0][l][2 * i] = c_bf16(co); t.v[0][l][2 * i + 1] = c_bf16(si); t.v[1][l][2 * i] = c_bf16(-si); t.v[1][l][2 * i + 1] = c_bf16(co); }
    else { t.v[0][l][i] = c_bf16(co); t.v[0][l][4 + i] = c_bf16(-si); t.v[1][l][i] = c_bf16(si); t.v[1][l][4 + i] = c_bf16(co); }
  }
  return t;
}
constexpr TwC mk_twc(int kind) {
  TwC t{};
  const double sc = 1.0 / (2048.0 * 640.0);
  for (int mt = 0; mt < 3; ++mt) for (int l = 0; l < 64; ++l) for (int rr = 0; rr < 2; ++rr) {
    int k1 = kind == 2 ? 16 : (l & 15), g = l >> 4, d0 = 8 * mt + 2 * g + rr, rho = l & 7;
    double co = c_cos(d0 * k1, 640) * sc * (kind == 2 ? 2 : 1), si = c_sin(d0 * k1, 640) * sc * (kind == 2 ? 2 : 1);
    if (kind == 1 || (kind == 2 && rho >= 3 && (rho & 1))) { double x = co; co = -si; si = x; }
    t.v[mt][l][2 * rr] = (float)co; t.v[mt][l][2 * rr + 1] = (float)si;
  }
  return t;
}
constexpr F8w mk_f32bi() {
  F8w t{};
  for (int nt = 0; nt < 2; ++nt) for (int l = 0; l < 64; ++l) for (int j = 0; j < 8; ++j) {
    int d1 = 16 * nt + (l & 15), K = 8 * (l >> 4) + j; double v = 0;
    if (K == 0) v = 1.0; else if (K == 1) v = (d1 & 1) ? -1.0 : 1.0;
    else { int k1 = K >> 1; v = (K & 1) ? -2.0 * c_sin(k1 * d1, 32) : 2.0 * c_cos(k1 * d1, 32); }
    t.v[nt][l][j] = c_bf16(v);
  }
  return t;
}
static __device__ const F8w g_f32a = mk_f32a();
static __device__ const F8x6 g_f20a = mk_f20(false);
static __device__ const F8x6 g_f20ai = mk_f20(true);
static __device__ const F8w g_a1[8] = {mk_a1(0, false), mk_a1(1, false), mk_a1(2, false), mk_a1(3, false), mk_a1(4, false), mk_a1(5, false), mk_a1(6, false), mk_a1(7, false)};
static __device__ const F8w g_a1i[8] = {mk_a1(0, true), mk_a1(1, true), mk_a1(2, true), mk_a1(3, true), mk_a1(4, true), mk_a1(5, true), mk_a1(6, true), mk_a1(7, true)};
static __device__ const F8w g_f16f = mk_f16(false);
static __device__ const F8w g_f16i = mk_f16(true);
static __device__ const F8w g_f32bi = mk_f32bi();

#define GST_ARG
#define GST_PASS
#define WAITALL() do {} while (0)
#define STAMP_DECL
#define STAMP(k, i) do {} while (0)
#define SLAB(i) do {} while (0)
#define WSTAMP(j) do {} while (0)
DI float bf_lo(u32 v) { return __uint_as_float(v << 16); }
DI float bf_hi(u32 v) { return __uint_as_float(v & 0xffff0000u); }
DI float bf2f(u16 v) { return __uint_as_float(((u32)v) << 16); }
DI u32 pk_bf(float a, float b) { f32x2 f = {a, b}; return __builtin_bit_cast(u32, __builtin_convertvector(f, bf2_t)); }
DI float rbf(float a) { return bf_lo(pk_bf(a, a)); }
DI float dpp_x1(float v) { return __int_as_float(__builtin_amdgcn_mov_dpp(__float_as_int(v), 0xB1, 0xf, 0xf, true)); }
DI float dpp_x2(float v) { return __int_as_float(__builtin_amdgcn_mov_dpp(__float_as_int(v), 0x4E, 0xf, 0xf, true)); }
DI float dpp_hm(float v) { return __int_as_float(__builtin_amdgcn_mov_dpp(__float_as_int(v), 0x141, 0xf, 0xf, true)); }
DI float dpp_rm(float v) { return __int_as_float(__builtin_amdgcn_mov_dpp(__float_as_int(v), 0x140, 0xf, 0xf, true)); }
DI f32x4 mfma16(s16x8 a, s16x8 b, f32x4 c) { return __builtin_amdgcn_mfma_f32_16x16x32_bf16(a, b, c, 0, 0, 0); }
DI f32x16 mfma32(s16x8 a, s16x8 b, f32x16 c) { return __builtin_amdgcn_mfma_f32_32x32x16_bf16(a, b, c, 0, 0, 0); }
DI s16x4 tr_read(const void* p) { return __builtin_amdgcn_ds_read_tr16_b64_v4i16((LDSP(s16x4))p); }
template <class T> DI void pin(T& v) { asm volatile("" : "+v"(v)); }
DI void l2_touch(const void* base, u32 voff, u32 lds) {
  asm volatile("s_mov_b32 m0, %0\n\ts_nop 0\n\tglobal_load_lds_dword %1, %2" :: "s"(lds), "v"(voff), "s"(base) : "memory");
}
DI void side_delay(int ticks10ns) {
  const unsigned long long t0 = __builtin_amdgcn_s_memrealtime();
  while (__builtin_amdgcn_s_memrealtime() - t0 < (unsigned long long)ticks10ns) __builtin_amdgcn_s_sleep(4);
}
#define GSP(T) __attribute__((address_space(1))) T*
template <class T> DI T ldg(const void* sbase, u32 voff) { GSP(const char) p = (GSP(const char))sbase; asm("" : "+s"(p)); return *(GSP(const T))(p + voff); }
template <class T> DI void stg(void* sbase, u32 voff, T v) { GSP(char) p = (GSP(char))sbase; asm("" : "+s"(p)); *(GSP(T))(p + voff) = v; }
DI void stg_wt(void* sbase, u32 voff, u32x4 v) { asm volatile("global_store_dwordx4 %0, %1, %2 sc1\n\ts_nop 1" :: "v"(voff), "v"(v), "s"(sbase) : "memory"); }
DI void stg_wt(void* sbase, u32 voff, u32x2 v) { asm volatile("global_store_dwordx2 %0, %1, %2 sc1\n\ts_nop 1" :: "v"(voff), "v"(v), "s"(sbase) : "memory"); }
DI void stg_wt(void* sbase, u32 voff, u32 v) { asm volatile("global_store_dword %0, %1, %2 sc1" :: "v"(voff), "v"(v), "s"(sbase) : "memory"); }
#define WT_MASK 127
template <int BIT, class T> DI void stg_sel(void* sbase, u32 voff, T v) { if constexpr ((WT_MASK & BIT) != 0) stg_wt(sbase, voff, v); else stg(sbase, voff, v); }
#define CODE_ADDR(var, sym) asm volatile("s_getpc_b64 s[40:41]\n\ts_add_u32 s40, s40, " #sym "@rel32@lo+4\n\ts_addc_u32 s41, s41, " #sym "@rel32@hi+12\n\ts_mov_b64 %0, s[40:41]" : "=s"(var) :: "s40", "s41", "scc")
DI u32 lds_addr(const void* p) { return (u32)(size_t)(LDSP(const char))p; }
DI s16x8 cat8(s16x4 a, s16x4 b) { return __builtin_shufflevector(a, b, 0, 1, 2, 3, 4, 5, 6, 7); }
DI s16x8 as8(u32x4 v) { return __builtin_bit_cast(s16x8, v); }
DI float dot2bf(u32 a, u32 b, float c) { return __builtin_amdgcn_fdot2_f32_bf16(__builtin_bit_cast(bf2_t, a), __builtin_bit_cast(bf2_t, b), c, false); }

DI float reduce16(const float (&v)[16], int lane) {
  float a[8], b[4], c[2], d;
  const bool s3 = lane & 8, s2 = lane & 4, s1 = lane & 2, s0 = lane & 1;
#pragma unroll
  for (int i = 0; i < 8; ++i) { float keep = s3 ? v[8 + i] : v[i], send = s3 ? v[i] : v[8 + i]; a[i] = keep + dpp_rm(send); }
#pragma unroll
  for (int i = 0; i < 4; ++i) { float keep = s2 ? a[4 + i] : a[i], send = s2 ? a[i] : a[4 + i]; b[i] = keep + dpp_hm(send); }
#pragma unroll
  for (int i = 0; i < 2; ++i) { float keep = s1 ? b[2 + i] : b[i], send = s1 ? b[i] : b[2 + i]; c[i] = keep + dpp_x2(send); }
  { float keep = s0 ? c[1] : c[0], send = s0 ? c[0] : c[1]; d = keep + dpp_x1(send); }
  auto r16 = __builtin_amdgcn_permlane16_swap(__float_as_uint(d), __float_as_uint(d), false, false);
  d = __uint_as_float(r16[0]) + __uint_as_float(r16[1]);
  auto r32 = __builtin_amdgcn_permlane32_swap(__float_as_uint(d), __float_as_uint(d), false, false);
  return __uint_as_float(r32[0]) + __uint_as_float(r32[1]);
}

DI void ln_finish(float s, float q, float& m_, float& i_) {
  const float mean = s * (1.0f / 640.0f);
  const float var = fmaxf(q * (1.0f / 640.0f) - mean * mean, 0.f);
  const float sd = rbf(__builtin_amdgcn_sqrtf(rbf(rbf(var) + 1e-5f)));
  m_ = rbf(mean); i_ = __builtin_amdgcn_rcpf(sd);
}
DI void ln_stats5(const u32 (&xv)[8], float* part  , int tid, float (&a_)[8], float (&b_)[8]) {
  const int lane = tid & 63, w = tid >> 6;
  float v[16];
#pragma unroll
  for (int r = 0; r < 8; ++r) { v[r] = dot2bf(xv[r], 0x3f803f80u, 0.f); v[8 + r] = dot2bf(xv[r], xv[r], 0.f); }
  float t = reduce16(v, lane);
  if (lane < 16) part[w * 16 + lane] = t;
  __syncthreads();
  const int r = lane & 7;
  float s = 0.f, q = 0.f;
#pragma unroll
  for (int i = 0; i < 5; ++i) { s += part[i * 16 + r]; q += part[i * 16 + 8 + r]; }
  float m_, i_; ln_finish(s, q, m_, i_);
  const float nb = -m_ * i_;
#pragma unroll
  for (int i = 0; i < 8; ++i) {
    a_[i] = __int_as_float(__builtin_amdgcn_readlane(__float_as_int(i_), i));
    b_[i] = __int_as_float(__builtin_amdgcn_readlane(__float_as_int(nb), i));
  }
}
DI float reduce8(const float (&v)[8], int lane) {
  float b[4], c[2], d;
  const bool s2 = lane & 4, s1 = lane & 2, s0 = lane & 1;
#pragma unroll
  for (int i = 0; i < 4; ++i) { float keep = s2 ? v[4 + i] : v[i], send = s2 ? v[i] : v[4 + i]; b[i] = keep + dpp_hm(send); }
#pragma unroll
  for (int i = 0; i < 2; ++i) { float keep = s1 ? b[2 + i] : b[i], send = s1 ? b[i] : b[2 + i]; c[i] = keep + dpp_x2(send); }
  { float keep = s0 ? c[1] : c[0], send = s0 ? c[0] : c[1]; d = keep + dpp_x1(send); }
  d += __int_as_float(__builtin_amdgcn_mov_dpp(__float_as_int(d), 0x128, 0xf, 0xf, true));
  auto r16 = __builtin_amdgcn_permlane16_swap(__float_as_uint(d), __float_as_uint(d), false, false);
  d = __uint_as_float(r16[0]) + __uint_as_float(r16[1]);
  auto r32 = __builtin_amdgcn_permlane32_swap(__float_as_uint(d), __float_as_uint(d), false, false);
  return __uint_as_float(r32[0]) + __uint_as_float(r32[1]);
}

template <int N> DI void wait_vm() { asm volatile("s_waitcnt vmcnt(%0)" ::"n"(N) : "memory"); }
template <int OFF> DI void gload16_s(u32x4& d, const void* sbase, u32 voff) {
  asm volatile("global_load_dwordx4 %0, %1, %2 offset:%3" : "=v"(d) : "v"(voff), "s"(sbase), "n"(OFF) : "memory");
}
template <int OFF> DI void glds16_s(const void* sbase, u32 voff) {
  asm volatile("global_load_lds_dwordx4 %0, %1 offset:%2" :: "v"(voff), "s"(sbase), "n"(OFF) : "memory");
}
DI void dma1k(const void* sbase, u32 voff, u32 lds) {
  asm volatile("s_mov_b32 m0, %0\n\ts_nop 0\n\tglobal_load_lds_dwordx4 %1, %2" :: "s"(lds), "v"(voff), "s"(sbase) : "memory");
}
DI void set_m0(u32 v) { asm volatile("s_mov_b32 m0, %0\n\ts_nop 0" :: "s"(v) : "memory"); }
DI void tie4(u32x4 (&t)[4]) { asm volatile("" : "+v"(t[0]), "+v"(t[1]), "+v"(t[2]), "+v"(t[3])); }

DI void prep_load(u32x4 (&v)[4], const u16* __restrict__ src, int ld, int lane) {
  const u32 vo = (u32)((lane >> 3) * ld + 8 * (lane & 7)) * 2u;
#pragma unroll
  for (int i = 0; i < 4; ++i) v[i] = ldg<u32x4>(src + (size_t)8 * i * ld, vo);
}
DI void prep_store(const u32x4 (&v)[4], u16* __restrict__ dst, int jstride, char* lds, int lane) {
#pragma unroll
  for (int i = 0; i < 4; ++i) {
    const int row = 8 * i + (lane >> 3), ch = lane & 7;
    *(u32x4*)(lds + row * 128 + (((ch >> 1) ^ ((row >> 1) & 3)) * 32) + (ch & 1) * 16) = v[i];
  }
  const int c = lane & 15, g = lane >> 4, q = c >> 2, p = c & 3, r = 4 * g + q;
#pragma unroll
  for (int j = 0; j < 4; ++j) {
    const char* b = lds + r * 128 + ((j ^ ((r >> 1) & 3)) * 32) + 8 * p;
    const s16x8 f = cat8(tr_read(b), tr_read(b + 16 * 128));
    stg_sel<1>(dst + (size_t)j * jstride, (u32)lane * 16u, __builtin_bit_cast(u32x4, f));
  }
}
DI void prep_wave(const u16* __restrict__ src, int ld, u16* __restrict__ dst, int jstride, char* lds, int lane) {
  u32x4 v[4]; prep_load(v, src, ld, lane); prep_store(v, dst, jstride, lds, lane);
}
DI void prep_gu(int task, const u16* wg, const u16* wu, u16* Wgu, char* lds, int lane) {
  const int mat = task >= 540, tk = task - 540 * mat, cg = tk / 20, ks = tk % 20;
  prep_wave((mat ? wu : wg) + (size_t)ks * 32 * F_ + 64 * cg, F_, Wgu + ((size_t)(4 * cg * 20 + ks) * 2 + mat) * 512, 20 * 2 * 512, lds, lane);
}
DI void prep_d(int task, const u16* wd, u16* Wd, char* lds, int lane) {
  const int cg = task / 54, ks = task % 54;
  prep_wave(wd + (size_t)ks * 32 * D_ + 64 * cg, D_, Wd + (size_t)(4 * cg * 54 + ks) * 512, 54 * 512, lds, lane);
}

typedef __attribute__((address_space(1))) u32 gu32;
typedef __attribute__((address_space(1))) unsigned long long gu64;
DI u32 ld_agent(const u32* p) { return __hip_atomic_load((const gu32*)p, __ATOMIC_RELAXED, __HIP_MEMORY_SCOPE_AGENT); }
DI void st_agent(u32* p, u32 v) { __hip_atomic_store((gu32*)p, v, __ATOMIC_RELAXED, __HIP_MEMORY_SCOPE_AGENT); }
DI void st_agent64(unsigned long long* p, unsigned long long v) { __hip_atomic_store((gu64*)p, v, __ATOMIC_RELAXED, __HIP_MEMORY_SCOPE_AGENT); }
#define K2_SIDE_DELAY 80
#define K4_SIDE_DELAY 60
constexpr int SPIN_LIMIT = 1 << 16;

template <int NR> DI void gemv_part(int col0, int row0, u32 tagv, bool publish, unsigned long long* gran, int gidx0, const u16* __restrict__ c, const u16* __restrict__ wmod,
                                    float* __restrict__ mod, char* lds) {
  float* sl = (float*)lds; float* part = sl + 640;
  const int t = threadIdx.x, lane = t & 63, w = t >> 6, cq = t & 7, rg = t >> 3;
  const u16 cv = ldg<u16>(c, 2 * t);
  u32x4 wv[NR];
  const u32 wo = (u32)((row0 + rg) * 3840 + col0 + 8 * cq) * 2u;
#pragma unroll
  for (int i = 0; i < NR; ++i) wv[i] = ldg<u32x4>(wmod + (size_t)80 * i * 3840, wo);
  { const float x = bf2f(cv); sl[t] = rbf(x * __builtin_amdgcn_rcpf(1.0f + __expf(-x))); }
  __syncthreads();
  float acc[8] = {};
#pragma unroll
  for (int i = 0; i < NR; ++i) {
    const float s = sl[row0 + rg + 80 * i];
#pragma unroll
    for (int k = 0; k < 4; ++k) { acc[2 * k] += s * bf_lo(wv[i][k]); acc[2 * k + 1] += s * bf_hi(wv[i][k]); }
  }
  float u[4];
  const bool b3 = lane & 8;
#pragma unroll
  for (int i = 0; i < 4; ++i) { const float keep = b3 ? acc[4 + i] : acc[i], send = b3 ? acc[i] : acc[4 + i]; u[i] = keep + __int_as_float(__builtin_amdgcn_mov_dpp(__float_as_int(send), 0x128, 0xf, 0xf, true)); }
  float r2[2];
#pragma unroll
  for (int i = 0; i < 2; ++i) { auto x = __builtin_amdgcn_permlane16_swap(__float_as_uint(u[i]), __float_as_uint(u[i + 2]), false, false); r2[i] = __uint_as_float(x[0]) + __uint_as_float(x[1]); }
  auto y = __builtin_amdgcn_permlane32_swap(__float_as_uint(r2[0]), __float_as_uint(r2[1]), false, false);
  part[w * 64 + 8 * cq + 4 * ((lane >> 3) & 1) + 2 * ((lane >> 4) & 1) + (lane >> 5)] = __uint_as_float(y[0]) + __uint_as_float(y[1]);
  __syncthreads();
  if (t < 64) {
    float s = 0.f;
#pragma unroll
    for (int i = 0; i < 10; ++i) s += part[i * 64 + t];
    if (publish) st_agent64(gran + gidx0 + t, ((unsigned long long)(tagv + 1u) << 32) | __float_as_uint(s));
    else mod[col0 + t] = rbf(s);
  }
}

DI int row_group(int b) { return 2 * (8 * (b >> 4) + (b & 7)) + ((b >> 3) & 1); }
DI void sincos_rev(float a, float& s, float& c) { s = __builtin_amdgcn_sinf(a); c = __builtin_amdgcn_cosf(a); }
DI void gram_part(const u32x4 (&f)[4], float* part  , int i, int lane) {
  f32x4 acc = {0.f, 0.f, 0.f, 0.f};
#pragma unroll
  for (int k = 0; k < 4; ++k) acc = mfma16(as8(f[k]), as8(f[k]), acc);
  const int c = lane & 15, g = lane >> 4;
  const float dg = (c & 2) ? ((c & 1) ? acc[3] : acc[2]) : ((c & 1) ? acc[1] : acc[0]);
  if (c < 8 && g == (c >> 2)) part[i * 16 + 8 + c] = dg;
  if (c < 8 && g == 2) part[i * 16 + c] = acc[0];
}
DI void ln_bcast8(const float* part, int lane, float (&a_)[8], float (&b_)[8]) {
  const int r = lane & 7;
  float s = 0.f, q = 0.f;
#pragma unroll
  for (int i = 0; i < 5; ++i) { s += part[i * 16 + r]; q += part[i * 16 + 8 + r]; }
  float m_, i_; ln_finish(s, q, m_, i_);
  const float nb = -m_ * i_;
#pragma unroll
  for (int i = 0; i < 8; ++i) {
    a_[i] = __int_as_float(__builtin_amdgcn_readlane(__float_as_int(i_), i));
    b_[i] = __int_as_float(__builtin_amdgcn_readlane(__float_as_int(nb), i));
  }
}
constexpr int HT_RS = 1296;
struct K2Args { const u16* x; const u16* c; const u16* wmod; float* mod; u32* XT; const u16* wr; const u16* wi; const u16* wg; const u16* wu; u16* Wgu; const u16* wd; u16* Wd; u32* gen; u32* arr; unsigned long long* gran; };
constexpr int K2_MAIN0 = 88, K2_SIDE0 = 344, K2_GRID = 344 + 54;
extern "C" __global__ __launch_bounds__(640) void k2_fwd(const K2Args A) {
  STAMP_DECL; STAMP(1, 0);
  __shared__ __attribute__((aligned(16))) char smem[32768 + 5 * 4096 + 256];
  const int bid = blockIdx.x;
  if (bid >= K2_SIDE0) {
    side_delay(K2_SIDE_DELAY);
    const int wv = __builtin_amdgcn_readfirstlane(threadIdx.x >> 6);
    prep_d((bid - K2_SIDE0) * 10 + wv, A.wd, A.Wd, smem + 4096 * wv, threadIdx.x & 63);
    STAMP(1, 6);
    return;
  }
  if (bid >= 40 && bid < 80) {
    side_delay(K2_SIDE_DELAY);
    gemv_part<8>(1280 + 64 * (bid - 40), 0, 0u, false, nullptr, 0, A.c, A.wmod, A.mod, smem); STAMP(0, 6); return;
  }
  if (bid >= 80 && bid < 88) {
    const void* c3; const void* c7;
    CODE_ADDR(c3, k3_mid); CODE_ADDR(c7, k7_end);
    const u32 len = (u32)((const char*)c7 - (const char*)c3);
    const u32 off = threadIdx.x * 128u;
    if (off < len) l2_touch(c3, off, lds_addr(smem) + __builtin_amdgcn_readfirstlane(threadIdx.x >> 6) * 256);
    asm volatile("s_waitcnt vmcnt(0)" ::: "memory");
    if (bid < 87) return;
  }
  if (bid < 40) {
    const u32 tagv = ld_agent(A.gen);
    gemv_part<4>(64 * (bid >> 1), 320 * (bid & 1), tagv, true, A.gran, 1280 * (bid & 1) + 64 * (bid >> 1), A.c, A.wmod, A.mod, smem);
    if (threadIdx.x == 0) st_agent(A.arr + bid, tagv + 1u);
    STAMP(0, 7); return;
  }
  if (bid == 87) {
    const u32 tag = ld_agent(A.gen) + 1u;
    const int t = threadIdx.x, who = t < 40 ? t : t + 48;
    for (int spin = 0; spin < SPIN_LIMIT; ++spin) {
      const bool ok = t >= 296 || ld_agent(A.arr + who) == tag;
      if (__syncthreads_and(ok)) break;
      __builtin_amdgcn_s_sleep(8);
    }
    if (t == 0) st_agent(A.gen, tag);
    return;
  }
  u32* __restrict__ XT = A.XT;
  char* hT = smem; char* U = smem + 8 * HT_RS; float* part = (float*)(smem + 8 * HT_RS + 8 * 17 * 80);
  char* tab = smem + 8 * HT_RS + 8 * 17 * 80 + 5 * 16 * 4;
  const int tid = threadIdx.x, lane = tid & 63, w = __builtin_amdgcn_readfirstlane(tid >> 6);
  const int s0 = row_group(bid - K2_MAIN0);
  const int c = lane & 15, g = lane >> 4, q = c >> 2, p = c & 3;
  const u16* xrow = A.x + (size_t)s0 * D_;
  f32x4 tw0, tw1; u32 ard, awr, aw0, acr; float z0r, z0i;
  auto precompute = [&]() {
    const int Qw = 4 * w + q, rho = Qw / 5, d0 = 4 * (Qw % 5) + p;
    const bool rot = rho >= 3 && (rho & 1);
    const float fd = (float)d0 * (1.0f / 640.0f);
    const int k10 = g == 0 ? 16 : 2 * g;
    float s_[4], c_[4];
    sincos_rev(fd * (float)k10, s_[0], c_[0]); sincos_rev(fd * (float)(2 * g + 1), s_[1], c_[1]);
    sincos_rev(fd * (float)(8 + 2 * g), s_[2], c_[2]); sincos_rev(fd * (float)(9 + 2 * g), s_[3], c_[3]);
    tw0 = f32x4{rot ? s_[0] : c_[0], rot ? c_[0] : -s_[0], rot ? s_[1] : c_[1], rot ? c_[1] : -s_[1]};
    tw1 = f32x4{rot ? s_[2] : c_[2], rot ? c_[2] : -s_[2], rot ? s_[3] : c_[3], rot ? c_[3] : -s_[3]};
    const int Qr = 4 * w + p;
    ard = lds_addr(hT) + (Qr / 5) * HT_RS + 2 * (20 * (8 * g + q) + 4 * (Qr % 5));
    awr = lds_addr(U) + 4 * (rho * 17 * 20 + d0 + 2 * g * 20);
    aw0 = g == 0 ? awr + 4 * 16 * 20 : awr;
    z0r = rot ? 0.f : 1.f; z0i = rot ? 1.f : 0.f;
    acr = lds_addr(U) + (w < 8 ? ((2 * (w >> 1)) * 17 + c) * 80 : ((c & 7) * 17 + 16) * 80);
    pin(tw0); pin(tw1); pin(ard); pin(awr); pin(aw0); pin(z0r); pin(z0i); pin(acr);
  };
  if (w < 5) {
    u32 xv[8];
#pragma unroll
    for (int r = 0; r < 8; ++r) xv[r] = ldg<u32>(xrow + (size_t)r * 256 * D_, 4 * tid);
    const u32 tagv = ld_agent(A.gen);
    precompute();
    STAMP(1, 1);
    WAITALL();
    STAMP(1, 5);
    __syncthreads();
    float na[8], nb[8];
    ln_bcast8(part, lane, na, nb);
    f32x2 o[8];
    {
      f32x2 h[8];
#pragma unroll
      for (int r = 0; r < 8; ++r) { f32x2 v = {bf_lo(xv[r]), bf_hi(xv[r])}; h[r] = v * na[r] + nb[r]; }
      const float cc = 0.70710678118654752f;
      f32x2 a0 = h[0] + h[4], a1 = h[1] + h[5], a2 = h[2] + h[6], a3 = h[3] + h[7];
      f32x2 b0 = h[0] - h[4], b1 = h[1] - h[5], b2 = h[2] - h[6], b3 = h[3] - h[7];
      f32x2 e = a0 + a2, f = a1 + a3, u = cc * (b1 - b3), v = cc * (b1 + b3);
      o[0] = e + f; o[1] = e - f; o[2] = b0 + u; o[3] = -b2 - v; o[4] = a0 - a2; o[5] = a3 - a1; o[6] = b0 - u; o[7] = b2 - v;
#pragma unroll
      for (int r = 0; r < 8; ++r) pin(o[r]);
    }
    const u32 tag = __builtin_amdgcn_readfirstlane(tagv) + 1u;
    if (tid == 0) st_agent(A.arr + bid, tag);
    f32x2 sa, gg;
    {
      const unsigned long long* ga_ = A.gran + 2 * tid;
      u32x4 va, vb, vc, vd;
      for (int spin = 0; spin < SPIN_LIMIT; ++spin) {
        asm volatile("global_load_dwordx4 %0, %4, off sc1\n\tglobal_load_dwordx4 %1, %5, off sc1\n\tglobal_load_dwordx4 %2, %6, off sc1\n\tglobal_load_dwordx4 %3, %7, off sc1\n\ts_waitcnt vmcnt(0)"
                     : "=&v"(va), "=&v"(vb), "=&v"(vc), "=&v"(vd) : "v"(ga_), "v"(ga_ + 1280), "v"(ga_ + 640), "v"(ga_ + 1920) : "memory");
        const bool ok = va[1] == tag && va[3] == tag && vb[1] == tag && vb[3] == tag && vc[1] == tag && vc[3] == tag && vd[1] == tag && vd[3] == tag;
        if (__all(ok)) break;
      }
      sa = f32x2{rbf(__uint_as_float(va[0]) + __uint_as_float(vb[0])), rbf(__uint_as_float(va[2]) + __uint_as_float(vb[2]))};
      gg = f32x2{rbf(1.0f + rbf(__uint_as_float(vc[0]) + __uint_as_float(vd[0]))), rbf(1.0f + rbf(__uint_as_float(vc[2]) + __uint_as_float(vd[2])))};
    }
    STAMP(1, 2);
    o[0] = o[0] * gg + 8.0f * sa;
    *(u32*)(hT + 4 * tid) = pk_bf(o[0][0], o[0][1]);
#pragma unroll
    for (int r = 1; r < 8; ++r) { const f32x2 v = o[r] * gg; *(u32*)(hT + r * HT_RS + 4 * tid) = pk_bf(v[0], v[1]); }
  } else {
    const int i = w - 5;
    u32x4 f[4];
    const u32 xo = (u32)((c & 7) * 256 * D_ + 128 * i + 8 * g) * 2u;
#pragma unroll
    for (int k = 0; k < 4; ++k) f[k] = ldg<u32x4>(xrow, xo + 64 * k);
    const s16x8 t0 = i < 2 ? ldg<s16x8>(g_f32a.v[i < 2 ? i : 0], lane * 16) : ldg<s16x8>(&g_f20a.v[0][0][0][0] + (i - 2) * 512, lane * 16);
    const s16x8 t1 = ldg<s16x8>(&g_f20a.v[0][0][0][0] + (i < 3 ? i + 3 : 5) * 512, lane * 16);
    precompute();
    if (c >= 8) {
      const u32 fill = c == 8 ? 0x3f803f80u : 0u;
#pragma unroll
      for (int k = 0; k < 4; ++k) f[k] = u32x4{fill, fill, fill, fill};
    }
    gram_part(f, part, i, lane);
    *(s16x8*)(tab + i * 1024 + lane * 16) = t0;
    if (i < 3) *(s16x8*)(tab + (i + 5) * 1024 + lane * 16) = t1;
    __syncthreads();
    const int task = (bid - K2_MAIN0) * 5 + i;
    if (task < 1080) prep_gu(task, A.wg, A.wu, A.Wgu, smem + 32768 + 4096 * i, lane);
    if (i == 4) {
      const int xc = bid & 7, li = (bid - K2_MAIN0) >> 3;
#pragma unroll
      for (int k = 0; k < 4; ++k) {
        const int e = (k & 1) * 64 + lane, id = li * 96 + e;
        const int ln = id % 6, row = 8 * ((id / 6) & 255) + (id < 1536 ? xc : ((8 - xc) & 7));
        const u32 off = min((u32)(row * 642 + ln * 128), (u32)(2048 * 642 - 4)) & ~3u;
        if (e < 96) l2_touch((k >> 1) ? A.wi : A.wr, off, lds_addr(smem) + sizeof(smem) - 256);
      }
    }
  }
  __syncthreads();
  STAMP(1, 3);
  {
    const LDSP(const char) base = (LDSP(const char))(size_t)ard;
    s16x8 b = cat8(__builtin_amdgcn_ds_read_tr16_b64_v4i16((LDSP(s16x4))base), __builtin_amdgcn_ds_read_tr16_b64_v4i16((LDSP(s16x4))(base + 160)));
    const s16x8 fa0 = *(const s16x8*)(tab + lane * 16), fa1 = *(const s16x8*)(tab + 1024 + lane * 16);
    const f32x4 z = {0.f, 0.f, 0.f, 0.f};
    f32x4 a0 = mfma16(fa0, b, z), a1 = mfma16(fa1, b, z);
    LDSP(u32) Ub = (LDSP(u32))(size_t)awr;
    const float x0 = g == 0 ? a0[1] : a0[0], x1 = g == 0 ? 0.f : a0[1];
    *(LDSP(u32))(size_t)aw0 = pk_bf(x0 * tw0[0] - x1 * tw0[1], x0 * tw0[1] + x1 * tw0[0]);
    if (g == 0) Ub[0] = pk_bf(a0[0] * z0r, a0[0] * z0i);
    Ub[20] = pk_bf(a0[2] * tw0[2] - a0[3] * tw0[3], a0[2] * tw0[3] + a0[3] * tw0[2]);
    Ub[8 * 20] = pk_bf(a1[0] * tw1[0] - a1[1] * tw1[1], a1[0] * tw1[1] + a1[1] * tw1[0]);
    Ub[9 * 20] = pk_bf(a1[2] * tw1[2] - a1[3] * tw1[3], a1[2] * tw1[3] + a1[3] * tw1[2]);
  }
  __syncthreads();
  STAMP(1, 4);
  const f32x4 z4 = {0.f, 0.f, 0.f, 0.f};
  u32* XTb = XT + (size_t)s0 * 16;
  const char* ft = tab + 2048 + lane * 16;
  const LDSP(const char) be = (LDSP(const char))(size_t)acr;
  const int P = w >> 1, mp = P, mm = P == 0 ? 4 : 8 - P;
  if (w < 8) {
    const s16x8 e0 = *(const LDSP(s16x8))(be + 16 * g), e1 = *(const LDSP(s16x8))(be + 64), o0 = *(const LDSP(s16x8))(be + 1360 + 16 * g), o1 = *(const LDSP(s16x8))(be + 1360 + 64);
    char* dp = (char*)(XTb + (size_t)(mp * 21) * 4096); char* dm = (char*)(XTb + (size_t)(mm * 21) * 4096);
    const u32 vo = (u32)(2 * g * 4096 + c) * 4u;
    if (!(w & 1)) {
#pragma unroll
      for (int mt = 0; mt < 2; ++mt) {
        const s16x8 f0 = *(const s16x8*)(ft + (2 * mt) * 1024), f1 = *(const s16x8*)(ft + (2 * mt + 1) * 1024);
        f32x4 ae = mfma16(f1, e1, mfma16(f0, e0, z4)), ao = mfma16(f1, o1, mfma16(f0, o0, z4));
        f32x4 tp = ae + ao, tm = ae - ao;
        if (P == 0) { tp = ae; tm = ao; }
        char* ep = dp + (size_t)(8 * mt) * 16384; char* em = dm + (size_t)(8 * mt) * 16384;
        const int k0 = 8 * mt + 2 * g;
        { const float d0_ = (c == 0 && k0 >= 1 && k0 <= 9) ? 2.f : 1.f, d1_ = (c == 0 && k0 + 1 <= 9) ? 2.f : 1.f;
          const f32x4 dd = {d0_, d0_, d1_, d1_}; tp = tp * dd; tm = tm * dd; }
        if (mt == 0 || c != 0 || k0 <= 10) { stg_sel<4>(ep, vo, pk_bf(tp[0], tp[1])); stg_sel<4>(em, vo, pk_bf(tm[0], tm[1])); }
        if (mt == 0 || c != 0 || k0 + 1 <= 10) { stg_sel<4>(ep + 16384, vo, pk_bf(tp[2], tp[3])); stg_sel<4>(em + 16384, vo, pk_bf(tm[2], tm[3])); }
      }
    } else {
      const s16x8 f0 = *(const s16x8*)(ft + 4 * 1024), f1 = *(const s16x8*)(ft + 5 * 1024);
      f32x4 ae = mfma16(f1, e1, mfma16(f0, e0, z4)), ao = mfma16(f1, o1, mfma16(f0, o0, z4));
      f32x4 tp = ae + ao, tm = ae - ao;
      if (P == 0) { tp = ae; tm = ao; }
      if (g < 2 && c != 0) {
        char* ep = dp + (size_t)16 * 16384; char* em = dm + (size_t)16 * 16384;
        stg_sel<4>(ep, vo, pk_bf(tp[0], tp[1])); stg_sel<4>(ep + 16384, vo, pk_bf(tp[2], tp[3]));
        stg_sel<4>(em, vo, pk_bf(tm[0], tm[1])); stg_sel<4>(em + 16384, vo, pk_bf(tm[2], tm[3]));
      }
    }
  } else {
    const int rh = c & 7, mt = w - 7;
    const s16x8 b0 = *(const LDSP(s16x8))(be + 16 * g), b1 = *(const LDSP(s16x8))(be + 64);
    const s16x8 f0 = *(const s16x8*)(ft + (2 * mt) * 1024), f1 = *(const s16x8*)(ft + (2 * mt + 1) * 1024);
    f32x4 a = mfma16(f1, b1, mfma16(f0, b0, z4));
    const float ca_ = (rh >= 2 && (rh & 1)) ? -1.f : 1.f, cb_ = rh >= 2 ? 1.f : 0.f;
    f32x4 t;
#pragma unroll
    for (int r = 0; r < 4; ++r) t[r] = ca_ * a[r] + cb_ * dpp_x1(a[r]);
    const int m1 = rh == 0 ? 0 : rh == 1 ? 4 : (rh & 1) ? 8 - (rh >> 1) : (rh >> 1);
    const int k0 = 8 * mt + 2 * g;
    const u32 o0 = (u32)((m1 * 21 + (k0 == 10 ? 20 : k0)) * 4096) * 4u, o1 = (u32)((m1 * 21 + k0 + 1) * 4096) * 4u;
    if (c < 8 && k0 >= 10 && k0 < 20) { stg_sel<4>(XTb, o0, pk_bf(t[0], t[1])); stg_sel<4>(XTb, o1, pk_bf(t[2], t[3])); }
    if (w == 9) asm volatile("s_waitcnt vmcnt(0)" ::: "memory");
  }
  STAMP(1, 7);
}

constexpr int EX_RS = 20;
extern "C" __global__ __launch_bounds__(512) void k3_mid(const u32* __restrict__ XT, const u16* __restrict__ wr, const u16* __restrict__ wi, u32* __restrict__ ZT) {
  STAMP_DECL; STAMP(2, 0);
  __shared__ __attribute__((aligned(16))) u32 ex[2][256 * EX_RS];
  __shared__ __attribute__((aligned(16))) char tab[8 * 1024];
  const int tid = threadIdx.x, lane = tid & 63, w = __builtin_amdgcn_readfirstlane(tid >> 6), c = lane & 15, g = lane >> 4;
  const int m1 = blockIdx.x & 7, tile = blockIdx.x >> 3;
  const char* in = (const char*)(XT + (size_t)(m1 * 21 + tile) * 4096);
  dma1k(in + 2048 * w, (u32)lane * 16u, lds_addr(ex[1]) + 2048 * w);
  dma1k(in + 2048 * w + 1024, (u32)lane * 16u, lds_addr(ex[1]) + 2048 * w + 1024);
  const F8w* tsrc = w < 2 ? &g_a1[m1] : w < 4 ? &g_f16f : w < 6 ? &g_f16i : &g_a1i[m1];
  const s16x8 tfrag = ldg<s16x8>(tsrc->v[w & 1], lane * 16);
  u32 wra[2][4], wia[2][4];
  const int kb = tile == 20 ? 336 : (c == 0 && tile >= 11) ? 32 * tile + 16 : 32 * tile + c;
  const bool mir = kb > 320;
  {
    const int kk = mir ? 640 - kb : kb;
#pragma unroll
    for (int j = 0; j < 2; ++j)
#pragma unroll
      for (int r = 0; r < 4; ++r) {
        const int m = m1 + 8 * (2 * w + j) + 128 * (4 * g + r), mm = mir ? ((2048 - m) & 2047) : m;
        const u32 vo = (u32)(mm * DF + kk) * 2u;
        wra[j][r] = (u32)ldg<u16>(wr, vo) << 16; wia[j][r] = (u32)ldg<u16>(wi, vo) << 16;
      }
  }
  f32x4 fcs[2], fsn[2], ics[2], isn[2];
#pragma unroll
  for (int j = 0; j < 2; ++j)
#pragma unroll
    for (int r = 0; r < 4; ++r) {
      float s_, c_;
      sincos_rev((float)((2 * w + j) * (m1 + 8 * (4 * g + r))) * (1.0f / 2048), s_, c_); fcs[j][r] = c_; fsn[j][r] = s_;
      sincos_rev((float)((4 * g + r) * (m1 + 8 * (2 * w + j))) * (1.0f / 2048), s_, c_); ics[j][r] = c_; isn[j][r] = s_;
    }
  u32 awr = lds_addr(ex) + 4 * ((4 * g * 16 + c) * EX_RS + 2 * w);
  u32 ard = lds_addr(ex) + 4 * ((2 * w * 16 + c) * EX_RS + 4 * g);
#pragma unroll
  for (int j = 0; j < 2; ++j) { pin(fcs[j]); pin(fsn[j]); pin(ics[j]); pin(isn[j]); }
  pin(awr); pin(ard);
  *(s16x8*)(tab + w * 1024 + lane * 16) = tfrag;
  STAMP(2, 1);
  wait_vm<16>();
  __syncthreads();
  STAMP(2, 2);
  u32x4 bx[2];
#pragma unroll
  for (int j = 0; j < 2; ++j)
#pragma unroll
    for (int i = 0; i < 4; ++i) bx[j][i] = ex[1][(16 * (4 * g + i) + 2 * w + j) * 16 + c];
  const f32x4 z4 = {0.f, 0.f, 0.f, 0.f};
  const char* tb = tab + lane * 16;
  const LDSP(char) wr0 = (LDSP(char))(size_t)awr; const LDSP(const char) rd0 = (LDSP(const char))(size_t)ard;
  constexpr int RB = 16 * EX_RS * 4, XB = 256 * EX_RS * 4;
  {
    const s16x8 A10 = *(const s16x8*)tb, A11 = *(const s16x8*)(tb + 1024);
    u32 pk[2][4];
#pragma unroll
    for (int j = 0; j < 2; ++j) {
      f32x4 re = mfma16(A10, as8(bx[j]), z4), im = mfma16(A11, as8(bx[j]), z4);
      f32x4 tr = re * fcs[j] + im * fsn[j], ti = im * fcs[j] - re * fsn[j];
#pragma unroll
      for (int r = 0; r < 4; ++r) pk[j][r] = pk_bf(tr[r], ti[r]);
    }
#pragma unroll
    for (int r = 0; r < 4; ++r) *(LDSP(u32x2))(wr0 + r * RB) = u32x2{pk[0][r], pk[1][r]};
  }
  __syncthreads();
  STAMP(2, 3);
  {
    const s16x8 F0 = *(const s16x8*)(tb + 2048), F1 = *(const s16x8*)(tb + 3072), G0 = *(const s16x8*)(tb + 4096), G1 = *(const s16x8*)(tb + 5120);
    u32 pk[2][4];
#pragma unroll
    for (int j = 0; j < 2; ++j) {
      const u32x4 b = *(const LDSP(u32x4))(rd0 + j * RB);
      f32x4 xr = mfma16(F0, as8(b), z4), xi = mfma16(F1, as8(b), z4);
      const f32x4 a = {__uint_as_float(wra[j][0]), __uint_as_float(wra[j][1]), __uint_as_float(wra[j][2]), __uint_as_float(wra[j][3])};
      const u32 sg = mir ? 0x80000000u : 0u;
      const f32x4 bb = {__uint_as_float(wia[j][0] ^ sg), __uint_as_float(wia[j][1] ^ sg), __uint_as_float(wia[j][2] ^ sg), __uint_as_float(wia[j][3] ^ sg)};
      const f32x4 yr = xr * a - xi * bb, yi = xr * bb + xi * a;
      const u32x4 yb = {pk_bf(yr[0], yr[1]), pk_bf(yr[2], yr[3]), pk_bf(yi[0], yi[1]), pk_bf(yi[2], yi[3])};
      f32x4 re = mfma16(G0, as8(yb), z4), im = mfma16(G1, as8(yb), z4);
      f32x4 tr = re * ics[j] - im * isn[j], ti = im * ics[j] + re * isn[j];
#pragma unroll
      for (int r = 0; r < 4; ++r) pk[j][r] = pk_bf(tr[r], ti[r]);
    }
#pragma unroll
    for (int r = 0; r < 4; ++r) *(LDSP(u32x2))(wr0 + XB + r * RB) = u32x2{pk[0][r], pk[1][r]};
  }
  __syncthreads();
  STAMP(2, 4);
  char* out = (char*)(ZT + (size_t)(2 * w) * 8 * 21 * 16 + (m1 * 21 + tile) * 16);
  const u32 vout = (u32)(64 * g * 10752 + c * 4);
  const s16x8 I0 = *(const s16x8*)(tb + 6144), I1 = *(const s16x8*)(tb + 7168);
#pragma unroll
  for (int j = 0; j < 2; ++j) {
    const u32x4 b = *(const LDSP(u32x4))(rd0 + XB + j * RB);
    f32x4 zr = mfma16(I0, as8(b), z4), zi = mfma16(I1, as8(b), z4);
#pragma unroll
    for (int r = 0; r < 4; ++r) stg_sel<8>(out + (16 * r + j) * 10752, vout, pk_bf(zr[r], zi[r]));
  }
  STAMP(2, 7);
}

constexpr int Y_RS = 640 + 4;
extern "C" __global__ __launch_bounds__(640) void k4_inv(const u32* __restrict__ ZT, const u16* __restrict__ x, const float* __restrict__ mod,
                                              u16* __restrict__ x1o, u16* __restrict__ h2o, const u16* __restrict__ Wgu, u32* __restrict__ flags) {
  STAMP_DECL; STAMP(3, 0);
  if (threadIdx.x == 0) flags[blockIdx.x] = 0u;
  __shared__ __attribute__((aligned(16))) char smem[162 * 64 + 8 * Y_RS * 4 + 10 * 8 * 4 + 8 * 1024 + 10752 + 256];
  char* Vs = smem; float* Y = (float*)(Vs + 162 * 64); float* part = Y + 8 * Y_RS; char* tab = (char*)(part + 80);
  const int tid = threadIdx.x, lane = tid & 63, w = __builtin_amdgcn_readfirstlane(tid >> 6), c = lane & 15, g = lane >> 4;
  const int s0 = row_group(blockIdx.x);
  u32* Zs = (u32*)(tab + 8 * 1024);
  {
    const char* Zb = (const char*)(ZT + (size_t)s0 * 8 * 21 * 16);
    dma1k(Zb + 1024 * w, (u32)lane * 16u, lds_addr(Zs) + 1024 * w);
    if (w == 0 && lane < 32) dma1k(Zb + 10240, (u32)lane * 16u, lds_addr(Zs) + 10240);
  }
  const int rho = w < 8 ? w : (c & 7), P = rho >> 1, mA = rho == 1 ? 4 : P, mB = P == 0 ? 4 : 8 - P;
  const s16x8 tfrag = w < 6 ? ldg<s16x8>(&g_f20ai.v[0][0][0][0] + w * 512, lane * 16) : ldg<s16x8>(g_f32bi.v[(w - 6) & 1], lane * 16);
  const int hf = w >= 5, cp = tid - 320 * hf;
  u32 xv[4];
#pragma unroll
  for (int r = 0; r < 4; ++r) xv[r] = ldg<u32>(x + (size_t)(s0 + 256 * (4 * hf + r)) * D_, 4 * cp);
  const f32x2 ga = ldg<f32x2>(mod + 2 * 640, 8 * cp), sb = ldg<f32x2>(mod + 3 * 640, 8 * cp), cb = ldg<f32x2>(mod + 4 * 640, 8 * cp);
  f32x4 tw[3]; u32 aw[3];
  {
    const bool rot = rho >= 3 && (rho & 1);
    const float sc = w < 8 ? 1.0f / (2048.0f * 640.0f) : 2.0f / (2048.0f * 640.0f), fk = (float)(w < 8 ? c : 16) * (1.0f / 640.0f);
#pragma unroll
    for (int mt = 0; mt < 3; ++mt) {
#pragma unroll
      for (int rr = 0; rr < 2; ++rr) {
        float s_, c_; sincos_rev(fk * (float)(8 * mt + 2 * g + rr), s_, c_);
        tw[mt][2 * rr] = (rot ? -s_ : c_) * sc; tw[mt][2 * rr + 1] = (rot ? c_ : s_) * sc;
      }
      const int d0 = 8 * mt + 2 * g;
      const bool ok = d0 < 20 && (w < 8 || c < 8);
      aw[mt] = lds_addr(Vs) + (ok ? (rho * 20 + d0) * 64 : 160 * 64) + (w < 8 ? 4 * c : 2);
      pin(tw[mt]); pin(aw[mt]);
    }
  }
  float be = rho < 2 ? 0.f : (rho & 1) ? -1.f : 1.f;
  u32 msk[8], zoa[8];
#pragma unroll
  for (int i = 0; i < 8; ++i) {
    const int k0 = i < 4 ? 4 * g + i : 16 + (i - 4);
    const bool dead = w < 8 ? (c == 0 && k0 > 10) : (k0 < 10 || (i >= 4 && g != 0));
    msk[i] = dead ? 0u : 0xffffffffu;
    zoa[i] = w < 8 ? k0 * 16 + c : (k0 < 10 ? 11 : k0 == 10 ? 20 : k0) * 16;
    pin(msk[i]); pin(zoa[i]);
  }
  u32 zba = lds_addr(Zs) + mA * 21 * 64, zbb = lds_addr(Zs) + mB * 21 * 64;
  pin(be); pin(zba); pin(zbb);
  if (w == 9 && blockIdx.x < 8) {
    const void* c5; const void* c7; CODE_ADDR(c5, k5_glu); CODE_ADDR(c7, k7_end);
    const u32 len = (u32)((const char*)c7 - (const char*)c5);
#pragma unroll
    for (int i = 0; i < 4; ++i) if (lane * 128u + 8192u * i < len) l2_touch(c5, (u32)lane * 128u + 8192u * i, lds_addr(smem) + sizeof(smem) - 256);
  }
  STAMP(3, 1);
  if (w < 8) *(s16x8*)(tab + w * 1024 + lane * 16) = tfrag;
  wait_vm<4>();
  __syncthreads();
  u32x4 b0, b1;
  if (w < 9) {
#pragma unroll
    for (int i = 0; i < 8; ++i) {
      const u32 zz = *(const LDSP(u32))(size_t)(zba + 4 * zoa[i]) & msk[i], zy = *(const LDSP(u32))(size_t)(zbb + 4 * zoa[i]) & msk[i];
      const f32x2 a = {bf_lo(zz), bf_hi(zz)}, b = {bf_lo(zy), bf_hi(zy)};
      const f32x2 v = a + be * b;
      if (i < 4) b0[i] = pk_bf(v[0], v[1]); else b1[i - 4] = pk_bf(v[0], v[1]);
    }
  }
  STAMP(3, 2);
  if (w < 9) {
    f32x4 a[3];
#pragma unroll
    for (int mt = 0; mt < 3; ++mt) {
      const f32x4 z = {0.f, 0.f, 0.f, 0.f};
      const s16x8 f0 = *(const s16x8*)(tab + (2 * mt) * 1024 + lane * 16), f1 = *(const s16x8*)(tab + (2 * mt + 1) * 1024 + lane * 16);
      a[mt] = mfma16(f1, as8(b1), mfma16(f0, as8(b0), z));
    }
    if (w < 8) {
      const u32 hi_skip = c == 0 ? 1u : 0u;
#pragma unroll
      for (int mt = 0; mt < 3; ++mt)
#pragma unroll
        for (int rr = 0; rr < 2; ++rr) {
          const u32 pv = pk_bf(a[mt][2 * rr] * tw[mt][2 * rr] - a[mt][2 * rr + 1] * tw[mt][2 * rr + 1], a[mt][2 * rr] * tw[mt][2 * rr + 1] + a[mt][2 * rr + 1] * tw[mt][2 * rr]);
          LDSP(char) dst = (LDSP(char))(size_t)aw[mt] + 64 * rr;
          if (hi_skip) *(LDSP(u16))dst = (u16)pv; else *(LDSP(u32))dst = pv;
        }
    } else {
#pragma unroll
      for (int mt = 0; mt < 3; ++mt)
#pragma unroll
        for (int rr = 0; rr < 2; ++rr)
          *(LDSP(u16))((LDSP(char))(size_t)aw[mt] + 64 * rr) = (u16)pk_bf(a[mt][2 * rr] * tw[mt][2 * rr] - a[mt][2 * rr + 1] * tw[mt][2 * rr + 1], 0.f);
    }
  }
  __syncthreads();
  STAMP(3, 3);
  {
    const s16x8 a = *(const s16x8*)(Vs + (16 * w + c) * 64 + 16 * g);
    const s16x8 fb0 = *(const s16x8*)(tab + 6144 + lane * 16), fb1 = *(const s16x8*)(tab + 7168 + lane * 16);
    const f32x4 z = {0.f, 0.f, 0.f, 0.f};
    const f32x4 y0 = mfma16(a, fb0, z), y1 = mfma16(a, fb1, z);
    const int R = 16 * w + 4 * g, rr = R / 20, d0 = R % 20;
    *(f32x4*)(Y + rr * Y_RS + 20 * c + d0) = y0;
    *(f32x4*)(Y + rr * Y_RS + 20 * (16 + c) + d0) = y1;
  }
  __syncthreads();
  STAMP(3, 4);
  {
    f32x2 R[8];
#pragma unroll
    for (int r = 0; r < 8; ++r) R[r] = *(const f32x2*)(Y + r * Y_RS + 2 * cp);
    const float cc = 0.70710678118654752f;
    const f32x2 G = R[0] + R[1], Fm = R[0] - R[1];
    const f32x2 u = cc * (R[2] - R[6]), v = cc * (R[3] + R[7]);
    const f32x2 c13 = R[2] + R[6], s13 = R[3] - R[7];
    const float sg = hf ? -1.f : 1.f;
    f32x2 y[4];
    y[0] = G + R[4] + sg * c13;
    y[2] = G - R[4] + sg * s13;
    y[1] = Fm + R[5] + sg * (u + v);
    y[3] = Fm - R[5] + sg * (v - u);
    u32 x1[4];
    float sv[8];
#pragma unroll
    for (int r = 0; r < 4; ++r) {
      const f32x2 xx = {bf_lo(xv[r]), bf_hi(xv[r])};
      const f32x2 t = xx + ga * y[r];
      x1[r] = pk_bf(t[0], t[1]);
      stg_sel<2>(x1o + (size_t)(8 * s0 + 4 * hf + r) * D_, (u32)cp * 4u, x1[r]);
      sv[r] = dot2bf(x1[r], 0x3f803f80u, 0.f); sv[4 + r] = dot2bf(x1[r], x1[r], 0.f);
    }
    const float tot = reduce8(sv, lane);
    if (lane < 8) part[w * 8 + lane] = tot;
    const f32x2 gg = {rbf(1.0f + cb[0]), rbf(1.0f + cb[1])};
    const int d = 2 * cp, wi = d & 31;
    char* stage = (char*)Y;
    const int so = (((d >> 5) * 4 + ((wi & 15) >> 2)) * 8 + 4 * hf) * 16 + (4 * (wi >> 4) + (wi & 3)) * 2;
    __syncthreads();
    STAMP(3, 5);
    const int r = lane & 3;
    float s = 0.f, qq = 0.f;
#pragma unroll
    for (int i = 0; i < 5; ++i) { s += part[(5 * hf + i) * 8 + r]; qq += part[(5 * hf + i) * 8 + 4 + r]; }
    float m_, i_; ln_finish(s, qq, m_, i_);
    const float nb_ = -m_ * i_;
#pragma unroll
    for (int i = 0; i < 4; ++i) {
      const float na = __int_as_float(__builtin_amdgcn_readlane(__float_as_int(i_), i)), nb = __int_as_float(__builtin_amdgcn_readlane(__float_as_int(nb_), i));
      const f32x2 v2 = {bf_lo(x1[i]), bf_hi(x1[i])};
      const f32x2 hv = (v2 * na + nb) * gg + sb;
      *(u32*)(stage + so + 16 * i) = pk_bf(hv[0], hv[1]);
    }
    __syncthreads();
    const u32x4 piece = *(const u32x4*)(stage + 16 * tid);
    stg_sel<16>(h2o + ((size_t)(s0 >> 1) * 20 * 64 + 8 * (s0 & 1)) * 8, (u32)((tid >> 3) * 16 + (tid & 7)) * 16u, piece);
  }
  if (w == 9) asm volatile("s_waitcnt vmcnt(0)" ::: "memory");
  STAMP(3, 7);
}

constexpr int K5_SLOT = 28 * 1024;
constexpr int K5_NS = 4;
constexpr int K5_PD = 4;
struct K5Ctx { const u16* wsrc; const u16* hsrc; u32 ldsw; u32 goff[7]; u32 hoff; u32 abase[2]; };
template <int s, int i> DI void k5_w1(const K5Ctx& c) {
  if constexpr (i == 0) set_m0(c.ldsw + (s % K5_NS) * K5_SLOT + 3072);
  glds16_s<1024 * i - 3072>((const char*)(c.wsrc + s * 4 * 512) - 4096, c.goff[i]);
}
template <int s, int i> DI void k5_t1(const K5Ctx& c, u32x4 (&t)[4]) {
  gload16_s<(i >> 1) * 1024>(t[i], c.hsrc + ((i & 1) * 20 + 2 * s) * 512, c.hoff);
}
template <int s> DI void k5_wall(const K5Ctx& c) { k5_w1<s, 0>(c); k5_w1<s, 1>(c); k5_w1<s, 2>(c); k5_w1<s, 3>(c); k5_w1<s, 4>(c); k5_w1<s, 5>(c); k5_w1<s, 6>(c); }
template <int s> DI void k5_tall(const K5Ctx& c, u32x4 (&t)[4]) { k5_t1<s, 0>(c, t); k5_t1<s, 1>(c, t); k5_t1<s, 2>(c, t); k5_t1<s, 3>(c, t); }
template <int n> DI s16x8 k5_rd(const K5Ctx& c) {
  constexpr int s = n / 28, t = (n / 14) & 1, j = n % 14, sl = s % K5_NS;
  return *(const LDSP(const s16x8))((LDSP(const char))(size_t)c.abase[sl >> 1] + (sl & 1) * K5_SLOT + ((j >> 1) * 4 + t * 2 + (j & 1)) * 1024);
}
template <int n> DI void k5_sub(const K5Ctx& c, f32x4 (&acc)[14][2], s16x8 (&fr)[K5_PD + 1], u32x4 (&tok)[10][4]) {
  constexpr int s = n / 28, t = (n / 14) & 1, j = n % 14;
  constexpr int nb = n + K5_PD;
  if constexpr (nb < 280 && nb % 28 == 0) {
    wait_vm<(s + 2 <= 9) ? 11 : 0>();
    __builtin_amdgcn_s_barrier();
    SLAB(s + 1);
    tie4(tok[s + 1]);
  }
  if constexpr (nb < 280) fr[nb % (K5_PD + 1)] = k5_rd<nb>(c);
  {
    constexpr int q = nb / 28, pos = nb % 28 - (q == 0 ? K5_PD : 0);
    constexpr int idx = (pos % 2 == 0 && pos < 22) ? pos / 2 : -1;
    if constexpr (idx >= 0 && idx < 4 && q + 2 <= 9) k5_t1<q + 2, idx>(c, tok[q + 2]);
    if constexpr (idx >= 4 && q + 2 <= 9) k5_w1<q + 2, idx - 4>(c);
  }
  const s16x8 a = fr[n % (K5_PD + 1)];
  acc[j][0] = mfma16(a, as8(tok[s][2 * t]), acc[j][0]);
  acc[j][1] = mfma16(a, as8(tok[s][2 * t + 1]), acc[j][1]);
  __builtin_amdgcn_sched_barrier(0);
}
template <int... I> DI void k5_all(const K5Ctx& c, f32x4 (&acc)[14][2], s16x8 (&fr)[K5_PD + 1], u32x4 (&tok)[10][4], std::integer_sequence<int, I...>) { (k5_sub<I>(c, acc, fr, tok), ...); }
constexpr int K6_NS = 4;
struct K6Ctx { const u16* wsrc; const u16* hsrc; u32 hoff; const float* gbp; u32 gbo; const u16* xp; u32 xo; u32* flags; int myflag; int waitbase; };
struct K6Grp { u32x4 a[5]; u32x4 t[4]; };
template <int OFF> DI void gload16_sc1(u32x4& d, const void* sbase, u32 voff) {
  asm volatile("global_load_dwordx4 %0, %1, %2 offset:%3 sc1" : "=v"(d) : "v"(voff), "s"(sbase), "n"(OFF) : "memory");
}
template <int i> DI void k6_v1(const K6Ctx& c, int ks, K6Grp& gr) {
  if constexpr (i < 5) gload16_s<0>(gr.a[i], c.wsrc + (ks + i * 54) * 512, c.hoff);
  else gload16_sc1<0>(gr.t[i - 5], c.hsrc + (ks + (i - 5) * 54) * 512, c.hoff);
}
DI void k6_issue_w(const K6Ctx& c, int ks, K6Grp& gr) { k6_v1<0>(c, ks, gr); k6_v1<1>(c, ks, gr); k6_v1<2>(c, ks, gr); k6_v1<3>(c, ks, gr); k6_v1<4>(c, ks, gr); }
DI void k6_issue_t(const K6Ctx& c, int ks, K6Grp& gr) { k6_v1<5>(c, ks, gr); k6_v1<6>(c, ks, gr); k6_v1<7>(c, ks, gr); k6_v1<8>(c, ks, gr); }
DI void k6_tie(K6Grp& g) { asm volatile("" : "+v"(g.a[0]), "+v"(g.a[1]), "+v"(g.a[2]), "+v"(g.a[3]), "+v"(g.a[4]), "+v"(g.t[0]), "+v"(g.t[1]), "+v"(g.t[2]), "+v"(g.t[3])); }
struct K6Epi { u32x4 gb[5]; u32x2 xr[5]; };
template <int OFF> DI void gload8_s(u32x2& d, const void* sbase, u32 voff) {
  asm volatile("global_load_dwordx2 %0, %1, %2 offset:%3" : "=v"(d) : "v"(voff), "s"(sbase), "n"(OFF) : "memory");
}
constexpr int k6_after(int i, int NK) {
  int n = 0; bool seen = false;
  for (int g = 0; g < 3; ++g) { if (seen) n += 4; if (g == i) seen = true; }
  for (int st = 0; st < i; ++st) {
    if (st == NK - 3 && seen) n += 10;
    if (st + 3 < NK) { if (seen) n += 9; if (st + 3 == i) seen = true; }
  }
  return n;
}
template <int i, int NK> DI void k6_step(const K6Ctx& c, int ks0, f32x4 (&acc)[5][4], K6Grp (&gr)[K6_NS], K6Epi& ep, f32x4* red, int w, int lane) {
  wait_vm<k6_after(i, NK)>();
  K6Grp& g = gr[i % K6_NS];
  k6_tie(g);
  K6Grp& nx = gr[(i + 3) % K6_NS];
#pragma unroll
  for (int nt = 0; nt < 5; ++nt) {
    if constexpr (i + 3 < NK) {
      if (nt == 0) { k6_v1<0>(c, ks0 + i + 3, nx); k6_v1<5>(c, ks0 + i + 3, nx); }
      if (nt == 1) { k6_v1<6>(c, ks0 + i + 3, nx); k6_v1<7>(c, ks0 + i + 3, nx); }
      if (nt == 2) { k6_v1<8>(c, ks0 + i + 3, nx); k6_v1<1>(c, ks0 + i + 3, nx); }
      if (nt == 3) { k6_v1<2>(c, ks0 + i + 3, nx); k6_v1<3>(c, ks0 + i + 3, nx); }
      if (nt == 4) { k6_v1<4>(c, ks0 + i + 3, nx); }
    }
    if constexpr (i == NK - 3) {
      gload16_s<0>(ep.gb[nt], c.gbp + 16 * nt, c.gbo); gload8_s<0>(ep.xr[nt], c.xp + 16 * nt, c.xo);
    }
#pragma unroll
    for (int t = 0; t < 4; ++t) acc[nt][t] = mfma16(as8(g.a[nt]), as8(g.t[t]), acc[nt][t]);
    if constexpr (i == NK - 1) {
      if (nt >= 1) {
#pragma unroll
        for (int t = 0; t < 4; ++t) if (t != w) red[((w * 5 + nt - 1) * 4 + t) * 64 + lane] = acc[nt - 1][t];
      }
    }
    __builtin_amdgcn_sched_barrier(0);
  }
}
template <int i, int NK> struct K6Loop { static DI void run(const K6Ctx& c, int ks0, f32x4 (&acc)[5][4], K6Grp (&gr)[K6_NS], K6Epi& ep, f32x4* red, int w, int lane) {
  k6_step<i, NK>(c, ks0, acc, gr, ep, red, w, lane); K6Loop<i + 1, NK>::run(c, ks0, acc, gr, ep, red, w, lane); } };
template <int NK> struct K6Loop<NK, NK> { static DI void run(const K6Ctx&, int, f32x4 (&)[5][4], K6Grp (&)[K6_NS], K6Epi&, f32x4*, int, int) {} };
template <int NK> DI void k6_main(const K6Ctx& cx, int ks0, f32x4* red, int w, int lane, u16* __restrict__ out, u32 oo GST_ARG) {
  K6Grp gr[K6_NS]; K6Epi ep;
  k6_issue_w(cx, ks0, gr[0]); k6_issue_w(cx, ks0 + 1, gr[1]); k6_issue_w(cx, ks0 + 2, gr[2]);
  wait_vm<15>();
  __builtin_amdgcn_s_barrier();
  STAMP(4, 1);
  if (w == 0) {
    if (lane == 0) st_agent(cx.flags + cx.myflag, 1u);
    for (int spin = 0; spin < SPIN_LIMIT; ++spin) {
      const u32 v = lane < 16 ? ld_agent(cx.flags + cx.waitbase + lane) : 1u;
      if (__all(v == 1u)) break;
      __builtin_amdgcn_s_sleep(1);
    }
  }
  __builtin_amdgcn_s_barrier();
  STAMP(4, 2);
  k6_issue_t(cx, ks0, gr[0]); k6_issue_t(cx, ks0 + 1, gr[1]); k6_issue_t(cx, ks0 + 2, gr[2]);
  f32x4 acc[5][4];
#pragma unroll
  for (int a = 0; a < 5; ++a)
#pragma unroll
    for (int b = 0; b < 4; ++b) acc[a][b] = f32x4{0.f, 0.f, 0.f, 0.f};
  K6Loop<0, NK>::run(cx, ks0, acc, gr, ep, red, w, lane);
#pragma unroll
  for (int t = 0; t < 4; ++t) if (t != w) red[((w * 5 + 4) * 4 + t) * 64 + lane] = acc[4][t];
  wait_vm<0>();
  asm volatile("" : "+v"(ep.gb[0]), "+v"(ep.gb[1]), "+v"(ep.gb[2]), "+v"(ep.gb[3]), "+v"(ep.gb[4]), "+v"(ep.xr[0]), "+v"(ep.xr[1]), "+v"(ep.xr[2]), "+v"(ep.xr[3]), "+v"(ep.xr[4]));
  __syncthreads();
#pragma unroll
  for (int nt = 0; nt < 5; ++nt) {
    f32x4 v = w == 0 ? acc[nt][0] : w == 1 ? acc[nt][1] : w == 2 ? acc[nt][2] : acc[nt][3];
#pragma unroll
    for (int ww = 1; ww < 4; ++ww) v += red[((((w + ww) & 3) * 5 + nt) * 4 + w) * 64 + lane];
    const f32x4 gb = __builtin_bit_cast(f32x4, ep.gb[nt]);
    const f32x4 x4 = {bf_lo(ep.xr[nt][0]), bf_hi(ep.xr[nt][0]), bf_lo(ep.xr[nt][1]), bf_hi(ep.xr[nt][1])};
    const f32x4 o = x4 + gb * v;
    stg_sel<64>(out + 16 * nt, oo, u32x2{pk_bf(o[0], o[1]), pk_bf(o[2], o[3])});
  }
}

extern "C" __global__ __launch_bounds__(256, 2) void k5_glu(const u16* __restrict__ h2, const u16* __restrict__ Wgu, u16* __restrict__ hb, const u16* __restrict__ Wd,
                                                      const u16* __restrict__ x1, const float* __restrict__ mod, u16* __restrict__ out, u32* __restrict__ flags) {
  STAMP_DECL; STAMP(4, 0);
  extern __shared__ __attribute__((aligned(16))) char smem[];
  const int tid = threadIdx.x, lane = tid & 63, w = __builtin_amdgcn_readfirstlane(tid >> 6);
  const int xcd = blockIdx.x & 7, li = blockIdx.x >> 3;
  const int tt = 4 * (xcd & 3) + (li & 3), fc = 8 * (xcd >> 2) + (li >> 2);
  const int u0 = fc < 12 ? 7 * fc : 84 + 6 * (fc - 12), U = fc < 12 ? 7 : 6;
  K5Ctx c;
  c.wsrc = Wgu + (size_t)u0 * 20 * 2 * 512;
#pragma unroll
  for (int i = 0; i < 7; ++i) { const int f = 7 * w + i, u = min(f >> 2, U - 1); c.goff[i] = 2 * (u * 20 * 2 * 512 + (f & 3) * 512) + lane * 16 + 4096 - (1024 * i - 3072); }
  c.ldsw = lds_addr(smem) + 7 * w * 1024;
  const int T16 = 8 * tt + 2 * w;
  c.hsrc = h2 + (size_t)T16 * 20 * 512; c.hoff = lane * 16;
#pragma unroll
  for (int j = 0; j < 2; ++j) { c.abase[j] = lds_addr(smem) + 2 * j * K5_SLOT + lane * 16; asm volatile("" : "+v"(c.abase[j])); }
  u32x4 tok[10][4];
  k5_tall<0>(c, tok[0]); k5_wall<0>(c);
  k5_tall<1>(c, tok[1]); k5_wall<1>(c);
  f32x4 acc[14][2];
#pragma unroll
  for (int u = 0; u < 14; ++u) { acc[u][0] = f32x4{0.f, 0.f, 0.f, 0.f}; acc[u][1] = f32x4{0.f, 0.f, 0.f, 0.f}; }
  STAMP(4, 3);
  wait_vm<11>();
  __builtin_amdgcn_s_barrier();
  tie4(tok[0]);
  STAMP(4, 4); SLAB(0);
  s16x8 fr[K5_PD + 1];
  fr[0] = k5_rd<0>(c); fr[1] = k5_rd<1>(c); fr[2] = k5_rd<2>(c); fr[3] = k5_rd<3>(c);
  __builtin_amdgcn_sched_barrier(0);
  k5_all(c, acc, fr, tok, std::make_integer_sequence<int, 280>{});
  STAMP(4, 5); SLAB(10);
  auto hv = [&](int u, int tl) { const f32x4 a = acc[2 * u][tl] * acc[2 * u + 1][tl]; return u32x2{pk_bf(a[0], a[1]), pk_bf(a[2], a[3])}; };
#pragma unroll
  for (int tl = 0; tl < 2; ++tl) {
    u16* hdst = hb + (size_t)(T16 + tl) * 54 * 512 + (u0 >> 1) * 512;
    const u32 vo = (u32)lane * 16u;
    if (!(u0 & 1)) {
#pragma unroll
      for (int k = 0; k < 3; ++k) { const u32x2 a = hv(2 * k, tl), b = hv(2 * k + 1, tl); stg_wt(hdst + k * 512, vo, u32x4{a[0], a[1], b[0], b[1]}); }
      if (U == 7) stg_wt(hdst + 3 * 512, vo, hv(6, tl));
    } else {
      stg_wt(hdst + 4, vo, hv(0, tl));
#pragma unroll
      for (int k = 0; k < 3; ++k) { const u32x2 a = hv(2 * k + 1, tl), b = hv(2 * k + 2, tl); stg_wt(hdst + (k + 1) * 512, vo, u32x4{a[0], a[1], b[0], b[1]}); }
    }
  }
  STAMP(4, 6);
  {
    const int c = lane & 15, g = lane >> 4;
    const int t6 = 2 * tt + (fc & 1), nb = 4 * (xcd >> 2) + ((fc & 7) >> 1), n0 = 80 * nb;
    const int ks0 = w < 2 ? 14 * w : 28 + 13 * (w - 2);
    K6Ctx cx;
    cx.wsrc = Wd + (size_t)(5 * nb) * 54 * 512; cx.hsrc = hb + (size_t)(4 * t6) * 54 * 512; cx.hoff = lane * 16;
    cx.gbp = mod + 5 * 640 + n0; cx.gbo = 16 * g;
    cx.xp = x1 + (size_t)(64 * t6 + 16 * w) * D_ + n0; cx.xo = (u32)(c * D_ + 4 * g) * 2u;
    cx.flags = flags; cx.myflag = tt * 16 + fc; cx.waitbase = tt * 16;
    const int tau = 64 * t6 + 16 * w + c, srow = (tau >> 3) + 256 * (tau & 7);
    const u32 oo = (u32)(srow * D_ + 4 * g) * 2u;
    if (w < 2) k6_main<14>(cx, ks0, (f32x4*)smem, w, lane, out + n0, oo GST_PASS); else k6_main<13>(cx, ks0, (f32x4*)smem, w, lane, out + n0, oo GST_PASS);
  }
  STAMP(4, 7);
}

extern "C" __global__ void k7_end() {}

constexpr size_t WS_MOD = 0, WS_XT = 16384, WS_ZT = WS_XT + 8 * 21 * 256 * 16 * 4, WS_X1 = WS_ZT + 8 * 21 * 256 * 16 * 4,
                 WS_H2 = WS_X1 + 2048 * 640 * 2, WS_HB = WS_H2 + 2048 * 640 * 2, WS_WGU = WS_HB + 2048 * 1728 * 2, WS_WD = WS_WGU + 2 * 640 * 1728 * 2, WS_WP = WS_WD + 640 * 1728 * 2, WS_SYNC = WS_WP + 168 * 16384;
extern "C" void kernel_launch(void* const* d_in, const int* in_sizes, int n_in, void* d_out, int out_size, void* d_ws, size_t ws_size,
                              hipStream_t stream) {
  const u16* x = (const u16*)d_in[0]; const u16* wr = (const u16*)d_in[1]; const u16* wi = (const u16*)d_in[2];
  const u16* wg = (const u16*)d_in[4]; const u16* wu = (const u16*)d_in[5]; const u16* wd = (const u16*)d_in[6];
  const u16* c = (const u16*)d_in[7]; const u16* wmod = (const u16*)d_in[8];
  char* ws = (char*)d_ws;
  float* mod = (float*)(ws + WS_MOD); u32* XT = (u32*)(ws + WS_XT); u32* ZT = (u32*)(ws + WS_ZT);
  u16* x1 = (u16*)(ws + WS_X1); u16* h2 = (u16*)(ws + WS_H2); u16* hb = (u16*)(ws + WS_HB);
  u16* Wgu = (u16*)(ws + WS_WGU); u16* Wd = (u16*)(ws + WS_WD);
  K2Args A;
  A.x = x; A.c = c; A.wmod = wmod; A.mod = mod; A.XT = XT; A.wr = wr; A.wi = wi; A.wg = wg; A.wu = wu; A.Wgu = Wgu; A.wd = wd; A.Wd = Wd;
  A.gen = (u32*)(ws + WS_SYNC); A.arr = (u32*)(ws + WS_SYNC + 4096); A.gran = (unsigned long long*)(ws + WS_SYNC + 8192);
  k2_fwd<<<K2_GRID, 640, 0, stream>>>(A);
  k3_mid<<<168, 512, 0, stream>>>(XT, wr, wi, ZT);
  u32* flags = (u32*)(ws + WS_SYNC + 65536);
  k4_inv<<<256, 640, 0, stream>>>(ZT, x, mod, x1, h2, Wgu, flags);
  hipFuncSetAttribute((const void*)k5_glu, hipFuncAttributeMaxDynamicSharedMemorySize, K5_NS * K5_SLOT);
  k5_glu<<<256, 256, K5_NS * K5_SLOT, stream>>>(h2, Wgu, hb, Wd, x1, mod, (u16*)d_out, flags);
}
```

```cpp
#include <hip/hip_runtime.h>
#include <hip/hip_bf16.h>
#include <stdint.h>
#include <utility>

typedef unsigned short u16;
typedef unsigned int u32;
typedef short s16x4 __attribute__((ext_vector_type(4)));
typedef short s16x8 __attribute__((ext_vector_type(8)));
typedef __bf16 bf2_t __attribute__((ext_vector_type(2)));
typedef float f32x2 __attribute__((ext_vector_type(2)));
typedef float f32x4 __attribute__((ext_vector_type(4)));
typedef float f32x16 __attribute__((ext_vector_type(16)));
typedef u32 u32x2 __attribute__((ext_vector_type(2)));
typedef u32 u32x4 __attribute__((ext_vector_type(4)));
#define DI __device__ __forceinline__
#define LDSP(T) __attribute__((address_space(3))) T*

constexpr int S_ = 2048, D_ = 640, F_ = 1728, DF = 321;

constexpr double kPi = 3.14159265358979323846264338327950288;
constexpr double c_sin0(double x) { double x2 = x * x, t = x, s = x; for (int i = 1; i < 11; ++i) { t *= -x2 / ((2.0 * i) * (2.0 * i + 1.0)); s += t; } return s; }
constexpr double c_cos0(double x) { double x2 = x * x, t = 1.0, s = 1.0; for (int i = 1; i < 11; ++i) { t *= -x2 / ((2.0 * i - 1.0) * (2.0 * i)); s += t; } return s; }
constexpr double c_cos(long n, long N) {
  n %= N; if (n < 0) n += N;
  if (2 * n > N) n = N - n;
  bool neg = false;
  if (4 * n > N) { neg = true; n = N - 2 * n; N = 2 * N; n = n;   }
  double r = 0;
  if (4 * n == N) r = 0.0;
  else if (8 * n > N) r = c_sin0(2 * kPi * (double)(N - 4 * n) / (double)(4 * N));
  else r = c_cos0(2 * kPi * (double)n / (double)N);
  return neg ? -r : r;
}
constexpr double c_sin(long n, long N) { return c_cos(4 * n - N, 4 * N); }
constexpr u16 c_bf16(double v) {
  float f = (float)v; u32 u = __builtin_bit_cast(u32, f);
  u += 0x7fffu + ((u >> 16) & 1u); return (u16)(u >> 16);
}

struct F8w { u16 v[2][64][8]; };
struct F8x6 { u16 v[3][2][64][8]; };
struct TwA { float v[2][64][4]; };
struct TwC { float v[3][64][4]; };
constexpr F8w mk_f32a() {
  F8w t{};
  for (int tt = 0; tt < 2; ++tt) for (int l = 0; l < 64; ++l) for (int j = 0; j < 8; ++j) {
    int o = 16 * tt + (l & 15), d1 = 8 * (l >> 4) + j; double v = 0;
    if (o == 0) v = 1.0; else if (o == 1) v = (d1 & 1) ? -1.0 : 1.0;
    else { int k1 = o >> 1; v = (o & 1) ? -c_sin(d1 * k1, 32) : c_cos(d1 * k1, 32); }
    t.v[tt][l][j] = c_bf16(v);
  }
  return t;
}
constexpr TwA mk_twa(int j) {
  TwA t{};
  for (int tt = 0; tt < 2; ++tt) for (int l = 0; l < 64; ++l) for (int rr = 0; rr < 2; ++rr) {
    int c = l & 15, g = l >> 4, Q = 4 * j + (c >> 2), d0 = 4 * (Q % 5) + (c & 3), rho = Q / 5;
    int k1 = 8 * tt + 2 * g + rr; if (k1 == 0) k1 = 16;
    double co = c_cos(d0 * k1, 640), si = -c_sin(d0 * k1, 640);
    if (rho >= 3 && (rho & 1)) { double x = co; co = -si; si = x; }
    t.v[tt][l][2 * rr] = (float)co; t.v[tt][l][2 * rr + 1] = (float)si;
  }
  return t;
}
constexpr F8x6 mk_f20(bool inv) {
  F8x6 t{};
  for (int mt = 0; mt < 3; ++mt) for (int ks = 0; ks < 2; ++ks) for (int l = 0; l < 64; ++l) for (int j = 0; j < 8; ++j) {
    int row = l & 15, g = l >> 4, k0 = 8 * mt + (row >> 1), part = row & 1, d0 = 16 * ks + 4 * g + (j >> 1), pp = j & 1; double v = 0;
    if (d0 < 20 && k0 < 20) {
      double co = c_cos(d0 * k0, 20), si = inv ? -c_sin(d0 * k0, 20) : c_sin(d0 * k0, 20);
      v = part == 0 ? (pp == 0 ? co : si) : (pp == 0 ? -si : co);
    }
    t.v[mt][ks][l][j] = c_bf16(v);
  }
  return t;
}
constexpr F8w mk_a1(int m1, bool inv) {
  F8w t{};
  for (int l = 0; l < 64; ++l) for (int i = 0; i < 4; ++i) {
    int c = l & 15, a = 4 * (l >> 4) + i, n = inv ? c * (8 * a + m1) : a * (8 * c + m1);
    double co = c_cos(n, 128), si = inv ? -c_sin(n, 128) : c_sin(n, 128);
    t.v[0][l][2 * i] = c_bf16(co); t.v[0][l][2 * i + 1] = c_bf16(si);
    t.v[1][l][2 * i] = c_bf16(-si); t.v[1][l][2 * i + 1] = c_bf16(co);
  }
  return t;
}
constexpr F8w mk_f16(bool inv) {
  F8w t{};
  for (int l = 0; l < 64; ++l) for (int i = 0; i < 4; ++i) {
    int c = l & 15, a = 4 * (l >> 4) + i;
    double co = c_cos(a * c, 16), si = c_sin(a * c, 16);
    if (!inv) { t.v[0][l][2 * i] = c_bf16(co); t.v[0][l][2 * i + 1] = c_bf16(si); t.v[1][l][2 * i] = c_bf16(-si); t.v[1][l][2 * i + 1] = c_bf16(co); }
    else { t.v[0][l][i] = c_bf16(co); t.v[0][l][4 + i] = c_bf16(-si); t.v[1][l][i] = c_bf16(si); t.v[1][l][4 + i] = c_bf16(co); }
  }
  return t;
}
constexpr TwC mk_twc(int kind) {
  TwC t{};
  const double sc = 1.0 / (2048.0 * 640.0);
  for (int mt = 0; mt < 3; ++mt) for (int l = 0; l < 64; ++l) for (int rr = 0; rr < 2; ++rr) {
    int k1 = kind == 2 ? 16 : (l & 15), g = l >> 4, d0 = 8 * mt + 2 * g + rr, rho = l & 7;
    double co = c_cos(d0 * k1, 640) * sc * (kind == 2 ? 2 : 1), si = c_sin(d0 * k1, 640) * sc * (kind == 2 ? 2 : 1);
    if (kind == 1 || (kind == 2 && rho >= 3 && (rho & 1))) { double x = co; co = -si; si = x; }
    t.v[mt][l][2 * rr] = (float)co; t.v[mt][l][2 * rr + 1] = (float)si;
  }
  return t;
}
constexpr F8w mk_f32bi() {
  F8w t{};
  for (int nt = 0; nt < 2; ++nt) for (int l = 0; l < 64; ++l) for (int j = 0; j < 8; ++j) {
    int d1 = 16 * nt + (l & 15), K = 8 * (l >> 4) + j; double v = 0;
    if (K == 0) v = 1.0; else if (K == 1) v = (d1 & 1) ? -1.0 : 1.0;
    else { int k1 = K >> 1; v = (K & 1) ? -2.0 * c_sin(k1 * d1, 32) : 2.0 * c_cos(k1 * d1, 32); }
    t.v[nt][l][j] = c_bf16(v);
  }
  return t;
}
static __device__ const F8w g_f32a = mk_f32a();
static __device__ const F8x6 g_f20a = mk_f20(false);
static __device__ const F8x6 g_f20ai = mk_f20(true);
static __device__ const F8w g_a1[8] = {mk_a1(0, false), mk_a1(1, false), mk_a1(2, false), mk_a1(3, false), mk_a1(4, false), mk_a1(5, false), mk_a1(6, false), mk_a1(7, false)};
static __device__ const F8w g_a1i[8] = {mk_a1(0, true), mk_a1(1, true), mk_a1(2, true), mk_a1(3, true), mk_a1(4, true), mk_a1(5, true), mk_a1(6, true), mk_a1(7, true)};
static __device__ const F8w g_f16f = mk_f16(false);
static __device__ const F8w g_f16i = mk_f16(true);
static __device__ const F8w g_f32bi = mk_f32bi();

#define WAITALL() do {} while (0)
#define STAMP_DECL
#define STAMP(k, i) do {} while (0)
#define SLAB(i) do {} while (0)
#define WSTAMP(j) do {} while (0)
DI float bf_lo(u32 v) { return __uint_as_float(v << 16); }
DI float bf_hi(u32 v) { return __uint_as_float(v & 0xffff0000u); }
DI float bf2f(u16 v) { return __uint_as_float(((u32)v) << 16); }
DI u32 pk_bf(float a, float b) { f32x2 f = {a, b}; return __builtin_bit_cast(u32, __builtin_convertvector(f, bf2_t)); }
DI float rbf(float a) { return bf_lo(pk_bf(a, a)); }
DI float dpp_x1(float v) { return __int_as_float(__builtin_amdgcn_mov_dpp(__float_as_int(v), 0xB1, 0xf, 0xf, true)); }
DI float dpp_x2(float v) { return __int_as_float(__builtin_amdgcn_mov_dpp(__float_as_int(v), 0x4E, 0xf, 0xf, true)); }
DI float dpp_hm(float v) { return __int_as_float(__builtin_amdgcn_mov_dpp(__float_as_int(v), 0x141, 0xf, 0xf, true)); }
DI float dpp_rm(float v) { return __int_as_float(__builtin_amdgcn_mov_dpp(__float_as_int(v), 0x140, 0xf, 0xf, true)); }
DI f32x4 mfma16(s16x8 a, s16x8 b, f32x4 c) { return __builtin_amdgcn_mfma_f32_16x16x32_bf16(a, b, c, 0, 0, 0); }
DI f32x16 mfma32(s16x8 a, s16x8 b, f32x16 c) { return __builtin_amdgcn_mfma_f32_32x32x16_bf16(a, b, c, 0, 0, 0); }
DI s16x4 tr_read(const void* p) { return __builtin_amdgcn_ds_read_tr16_b64_v4i16((LDSP(s16x4))p); }
DI void batch_fence() { asm volatile("" ::: "memory"); }
template <class T, int N> DI void pin_arr(T (&v)[N]) {
#pragma unroll
  for (int i = 0; i < N; ++i) asm volatile("" : "+v"(v[i]));
}
template <class T> DI void pin(T& v) { asm volatile("" : "+v"(v)); }
DI void l2_touch(const void* base, u32 voff, u32 lds) {
  asm volatile("s_mov_b32 m0, %0\n\ts_nop 0\n\tglobal_load_lds_dword %1, %2" :: "s"(lds), "v"(voff), "s"(base) : "memory");
}
DI void side_delay(int ticks10ns) {
  const unsigned long long t0 = __builtin_amdgcn_s_memrealtime();
  while (__builtin_amdgcn_s_memrealtime() - t0 < (unsigned long long)ticks10ns) __builtin_amdgcn_s_sleep(4);
}
#define GSP(T) __attribute__((address_space(1))) T*
template <class T> DI T ldg(const void* sbase, u32 voff) { GSP(const char) p = (GSP(const char))sbase; asm("" : "+s"(p)); return *(GSP(const T))(p + voff); }
template <class T> DI void stg(void* sbase, u32 voff, T v) { GSP(char) p = (GSP(char))sbase; asm("" : "+s"(p)); *(GSP(T))(p + voff) = v; }
DI void stg_wt(void* sbase, u32 voff, u32x4 v) { asm volatile("global_store_dwordx4 %0, %1, %2 sc1\n\ts_nop 1" :: "v"(voff), "v"(v), "s"(sbase) : "memory"); }
DI void stg_wt(void* sbase, u32 voff, u32x2 v) { asm volatile("global_store_dwordx2 %0, %1, %2 sc1\n\ts_nop 1" :: "v"(voff), "v"(v), "s"(sbase) : "memory"); }
DI void stg_wt(void* sbase, u32 voff, u32 v) { asm volatile("global_store_dword %0, %1, %2 sc1" :: "v"(voff), "v"(v), "s"(sbase) : "memory"); }
#define WT_MASK 127
template <int BIT, class T> DI void stg_sel(void* sbase, u32 voff, T v) { if constexpr ((WT_MASK & BIT) != 0) stg_wt(sbase, voff, v); else stg(sbase, voff, v); }
#define CODE_ADDR(var, sym) asm volatile("s_getpc_b64 s[40:41]\n\ts_add_u32 s40, s40, " #sym "@rel32@lo+4\n\ts_addc_u32 s41, s41, " #sym "@rel32@hi+12\n\ts_mov_b64 %0, s[40:41]" : "=s"(var) :: "s40", "s41", "scc")
DI u32 lds_addr(const void* p) { return (u32)(size_t)(LDSP(const char))p; }
DI s16x8 cat8(s16x4 a, s16x4 b) { return __builtin_shufflevector(a, b, 0, 1, 2, 3, 4, 5, 6, 7); }
DI s16x8 as8(u32x4 v) { return __builtin_bit_cast(s16x8, v); }
DI float dot2bf(u32 a, u32 b, float c) { return __builtin_amdgcn_fdot2_f32_bf16(__builtin_bit_cast(bf2_t, a), __builtin_bit_cast(bf2_t, b), c, false); }

DI float reduce16(const float (&v)[16], int lane) {
  float a[8], b[4], c[2], d;
  const bool s3 = lane & 8, s2 = lane & 4, s1 = lane & 2, s0 = lane & 1;
#pragma unroll
  for (int i = 0; i < 8; ++i) { float keep = s3 ? v[8 + i] : v[i], send = s3 ? v[i] : v[8 + i]; a[i] = keep + dpp_rm(send); }
#pragma unroll
  for (int i = 0; i < 4; ++i) { float keep = s2 ? a[4 + i] : a[i], send = s2 ? a[i] : a[4 + i]; b[i] = keep + dpp_hm(send); }
#pragma unroll
  for (int i = 0; i < 2; ++i) { float keep = s1 ? b[2 + i] : b[i], send = s1 ? b[i] : b[2 + i]; c[i] = keep + dpp_x2(send); }
  { float keep = s0 ? c[1] : c[0], send = s0 ? c[0] : c[1]; d = keep + dpp_x1(send); }
  auto r16 = __builtin_amdgcn_permlane16_swap(__float_as_uint(d), __float_as_uint(d), false, false);
  d = __uint_as_float(r16[0]) + __uint_as_float(r16[1]);
  auto r32 = __builtin_amdgcn_permlane32_swap(__float_as_uint(d), __float_as_uint(d), false, false);
  return __uint_as_float(r32[0]) + __uint_as_float(r32[1]);
}

DI void ln_finish(float s, float q, float& m_, float& i_) {
  const float mean = s * (1.0f / 640.0f);
  const float var = fmaxf(q * (1.0f / 640.0f) - mean * mean, 0.f);
  const float sd = rbf(__builtin_amdgcn_sqrtf(rbf(rbf(var) + 1e-5f)));
  m_ = rbf(mean); i_ = __builtin_amdgcn_rcpf(sd);
}
DI void ln_stats5(const u32 (&xv)[8], float* part  , int tid, float (&a_)[8], float (&b_)[8]) {
  const int lane = tid & 63, w = tid >> 6;
  float v[16];
#pragma unroll
  for (int r = 0; r < 8; ++r) { v[r] = dot2bf(xv[r], 0x3f803f80u, 0.f); v[8 + r] = dot2bf(xv[r], xv[r], 0.f); }
  float t = reduce16(v, lane);
  if (lane < 16) part[w * 16 + lane] = t;
  __syncthreads();
  const int r = lane & 7;
  float s = 0.f, q = 0.f;
#pragma unroll
  for (int i = 0; i < 5; ++i) { s += part[i * 16 + r]; q += part[i * 16 + 8 + r]; }
  float m_, i_; ln_finish(s, q, m_, i_);
  const float nb = -m_ * i_;
#pragma unroll
  for (int i = 0; i < 8; ++i) {
    a_[i] = __int_as_float(__builtin_amdgcn_readlane(__float_as_int(i_), i));
    b_[i] = __int_as_float(__builtin_amdgcn_readlane(__float_as_int(nb), i));
  }
}
DI float reduce8(const float (&v)[8], int lane) {
  float b[4], c[2], d;
  const bool s2 = lane & 4, s1 = lane & 2, s0 = lane & 1;
#pragma unroll
  for (int i = 0; i < 4; ++i) { float keep = s2 ? v[4 + i] : v[i], send = s2 ? v[i] : v[4 + i]; b[i] = keep + dpp_hm(send); }
#pragma unroll
  for (int i = 0; i < 2; ++i) { float keep = s1 ? b[2 + i] : b[i], send = s1 ? b[i] : b[2 + i]; c[i] = keep + dpp_x2(send); }
  { float keep = s0 ? c[1] : c[0], send = s0 ? c[0] : c[1]; d = keep + dpp_x1(send); }
  d += __int_as_float(__builtin_amdgcn_mov_dpp(__float_as_int(d), 0x128, 0xf, 0xf, true));
  auto r16 = __builtin_amdgcn_permlane16_swap(__float_as_uint(d), __float_as_uint(d), false, false);
  d = __uint_as_float(r16[0]) + __uint_as_float(r16[1]);
  auto r32 = __builtin_amdgcn_permlane32_swap(__float_as_uint(d), __float_as_uint(d), false, false);
  return __uint_as_float(r32[0]) + __uint_as_float(r32[1]);
}

template <int N> DI void wait_vm() { asm volatile("s_waitcnt vmcnt(%0)" ::"n"(N) : "memory"); }
template <int OFF> DI void gload16_s(u32x4& d, const void* sbase, u32 voff) {
  asm volatile("global_load_dwordx4 %0, %1, %2 offset:%3" : "=v"(d) : "v"(voff), "s"(sbase), "n"(OFF) : "memory");
}
template <int OFF> DI void glds16_s(const void* sbase, u32 voff) {
  asm volatile("global_load_lds_dwordx4 %0, %1 offset:%2" :: "v"(voff), "s"(sbase), "n"(OFF) : "memory");
}
DI void dma1k(const void* sbase, u32 voff, u32 lds) {
  asm volatile("s_mov_b32 m0, %0\n\ts_nop 0\n\tglobal_load_lds_dwordx4 %1, %2" :: "s"(lds), "v"(voff), "s"(sbase) : "memory");
}
DI void set_m0(u32 v) { asm volatile("s_mov_b32 m0, %0\n\ts_nop 0" :: "s"(v) : "memory"); }
DI void tie4(u32x4 (&t)[4]) { asm volatile("" : "+v"(t[0]), "+v"(t[1]), "+v"(t[2]), "+v"(t[3])); }

DI void prep_load(u32x4 (&v)[4], const u16* __restrict__ src, int ld, int lane) {
  const u32 vo = (u32)((lane >> 3) * ld + 8 * (lane & 7)) * 2u;
#pragma unroll
  for (int i = 0; i < 4; ++i) v[i] = ldg<u32x4>(src + (size_t)8 * i * ld, vo);
}
DI void prep_store(const u32x4 (&v)[4], u16* __restrict__ dst, int jstride, char* lds, int lane) {
#pragma unroll
  for (int i = 0; i < 4; ++i) {
    const int row = 8 * i + (lane >> 3), ch = lane & 7;
    *(u32x4*)(lds + row * 128 + (((ch >> 1) ^ ((row >> 1) & 3)) * 32) + (ch & 1) * 16) = v[i];
  }
  const int c = lane & 15, g = lane >> 4, q = c >> 2, p = c & 3, r = 4 * g + q;
#pragma unroll
  for (int j = 0; j < 4; ++j) {
    const char* b = lds + r * 128 + ((j ^ ((r >> 1) & 3)) * 32) + 8 * p;
    const s16x8 f = cat8(tr_read(b), tr_read(b + 16 * 128));
    stg_sel<1>(dst + (size_t)j * jstride, (u32)lane * 16u, __builtin_bit_cast(u32x4, f));
  }
}
DI void prep_wave(const u16* __restrict__ src, int ld, u16* __restrict__ dst, int jstride, char* lds, int lane) {
  u32x4 v[4]; prep_load(v, src, ld, lane); prep_store(v, dst, jstride, lds, lane);
}
DI void prep_gu(int task, const u16* wg, const u16* wu, u16* Wgu, char* lds, int lane) {
  const int mat = task >= 540, tk = task - 540 * mat, cg = tk / 20, ks = tk % 20;
  prep_wave((mat ? wu : wg) + (size_t)ks * 32 * F_ + 64 * cg, F_, Wgu + ((size_t)(4 * cg * 20 + ks) * 2 + mat) * 512, 20 * 2 * 512, lds, lane);
}
DI void prep_d(int task, const u16* wd, u16* Wd, char* lds, int lane) {
  const int cg = task / 54, ks = task % 54;
  prep_wave(wd + (size_t)ks * 32 * D_ + 64 * cg, D_, Wd + (size_t)(4 * cg * 54 + ks) * 512, 54 * 512, lds, lane);
}

typedef __attribute__((address_space(1))) u32 gu32;
typedef __attribute__((address_space(1))) unsigned long long gu64;
DI u32 ld_agent(const u32* p) { return __hip_atomic_load((const gu32*)p, __ATOMIC_RELAXED, __HIP_MEMORY_SCOPE_AGENT); }
DI void st_agent(u32* p, u32 v) { __hip_atomic_store((gu32*)p, v, __ATOMIC_RELAXED, __HIP_MEMORY_SCOPE_AGENT); }
DI void st_agent64(unsigned long long* p, unsigned long long v) { __hip_atomic_store((gu64*)p, v, __ATOMIC_RELAXED, __HIP_MEMORY_SCOPE_AGENT); }
#define K2_SIDE_DELAY 80
#define K4_SIDE_DELAY 60
constexpr int SPIN_LIMIT = 1 << 16;

template <int NR> DI void gemv_part(int col0, int row0, u32 tagv, bool publish, unsigned long long* gran, int gidx0, const u16* __restrict__ c, const u16* __restrict__ wmod,
                                    float* __restrict__ mod, char* lds) {
  float* sl = (float*)lds; float* part = sl + 640;
  const int t = threadIdx.x, lane = t & 63, w = t >> 6, cq = t & 7, rg = t >> 3;
  const u16 cv = ldg<u16>(c, 2 * t);
  u32x4 wv[NR];
  const u32 wo = (u32)((row0 + rg) * 3840 + col0 + 8 * cq) * 2u;
#pragma unroll
  for (int i = 0; i < NR; ++i) wv[i] = ldg<u32x4>(wmod + (size_t)80 * i * 3840, wo);
  { const float x = bf2f(cv); sl[t] = rbf(x * __builtin_amdgcn_rcpf(1.0f + __expf(-x))); }
  __syncthreads();
  float acc[8] = {};
#pragma unroll
  for (int i = 0; i < NR; ++i) {
    const float s = sl[row0 + rg + 80 * i];
#pragma unroll
    for (int k = 0; k < 4; ++k) { acc[2 * k] += s * bf_lo(wv[i][k]); acc[2 * k + 1] += s * bf_hi(wv[i][k]); }
  }
  float u[4];
  const bool b3 = lane & 8;
#pragma unroll
  for (int i = 0; i < 4; ++i) { const float keep = b3 ? acc[4 + i] : acc[i], send = b3 ? acc[i] : acc[4 + i]; u[i] = keep + __int_as_float(__builtin_amdgcn_mov_dpp(__float_as_int(send), 0x128, 0xf, 0xf, true)); }
  float r2[2];
#pragma unroll
  for (int i = 0; i < 2; ++i) { auto x = __builtin_amdgcn_permlane16_swap(__float_as_uint(u[i]), __float_as_uint(u[i + 2]), false, false); r2[i] = __uint_as_float(x[0]) + __uint_as_float(x[1]); }
  auto y = __builtin_amdgcn_permlane32_swap(__float_as_uint(r2[0]), __float_as_uint(r2[1]), false, false);
  part[w * 64 + 8 * cq + 4 * ((lane >> 3) & 1) + 2 * ((lane >> 4) & 1) + (lane >> 5)] = __uint_as_float(y[0]) + __uint_as_float(y[1]);
  __syncthreads();
  if (t < 64) {
    float s = 0.f;
#pragma unroll
    for (int i = 0; i < 10; ++i) s += part[i * 64 + t];
    if (publish) st_agent64(gran + gidx0 + t, ((unsigned long long)(tagv + 1u) << 32) | __float_as_uint(s));
    else mod[col0 + t] = rbf(s);
  }
}

DI int row_group(int b) { return 2 * (8 * (b >> 4) + (b & 7)) + ((b >> 3) & 1); }
DI void sincos_rev(float a, float& s, float& c) { s = __builtin_amdgcn_sinf(a); c = __builtin_amdgcn_cosf(a); }
DI void gram_part(const u32x4 (&f)[4], float* part  , int i, int lane) {
  f32x4 acc = {0.f, 0.f, 0.f, 0.f};
#pragma unroll
  for (int k = 0; k < 4; ++k) acc = mfma16(as8(f[k]), as8(f[k]), acc);
  const int c = lane & 15, g = lane >> 4;
  const float dg = (c & 2) ? ((c & 1) ? acc[3] : acc[2]) : ((c & 1) ? acc[1] : acc[0]);
  if (c < 8 && g == (c >> 2)) part[i * 16 + 8 + c] = dg;
  if (c < 8 && g == 2) part[i * 16 + c] = acc[0];
}
DI void ln_bcast8(const float* part, int lane, float (&a_)[8], float (&b_)[8]) {
  const int r = lane & 7;
  float ps[5], pq[5];
#pragma unroll
  for (int i = 0; i < 5; ++i) { ps[i] = part[i * 16 + r]; pq[i] = part[i * 16 + 8 + r]; }
  batch_fence(); pin_arr(ps); pin_arr(pq);
  const float s = ((ps[0] + ps[1]) + (ps[2] + ps[3])) + ps[4], q = ((pq[0] + pq[1]) + (pq[2] + pq[3])) + pq[4];
  float m_, i_; ln_finish(s, q, m_, i_);
  const float nb = -m_ * i_;
#pragma unroll
  for (int i = 0; i < 8; ++i) {
    a_[i] = __int_as_float(__builtin_amdgcn_readlane(__float_as_int(i_), i));
    b_[i] = __int_as_float(__builtin_amdgcn_readlane(__float_as_int(nb), i));
  }
}
constexpr int HT_RS = 1296;
struct K2Args { const u16* x; const u16* c; const u16* wmod; float* mod; u32* XT; const u16* wr; const u16* wi; const u16* wg; const u16* wu; u16* Wgu; const u16* wd; u16* Wd; u32* gen; u32* arr; unsigned long long* gran; };
constexpr int K2_MAIN0 = 88, K2_SIDE0 = 344, K2_GRID = 344 + 54;
extern "C" __global__ __launch_bounds__(640) void k2_fwd(const K2Args A) {
  STAMP_DECL; STAMP(1, 0);
  __shared__ __attribute__((aligned(16))) char smem[32768 + 5 * 4096 + 256];
  const int bid = blockIdx.x;
  if (bid >= K2_SIDE0) {
    side_delay(K2_SIDE_DELAY);
    const int wv = __builtin_amdgcn_readfirstlane(threadIdx.x >> 6);
    prep_d((bid - K2_SIDE0) * 10 + wv, A.wd, A.Wd, smem + 4096 * wv, threadIdx.x & 63);
    STAMP(1, 6);
    return;
  }
  if (bid >= 40 && bid < 80) {
    side_delay(K2_SIDE_DELAY);
    gemv_part<8>(1280 + 64 * (bid - 40), 0, 0u, false, nullptr, 0, A.c, A.wmod, A.mod, smem); STAMP(0, 6); return;
  }
  if (bid >= 80 && bid < 88) {
    const void* c3; const void* c7;
    CODE_ADDR(c3, k3_mid); CODE_ADDR(c7, k7_end);
    const u32 len = (u32)((const char*)c7 - (const char*)c3);
    const u32 off = threadIdx.x * 128u;
    if (off < len) l2_touch(c3, off, lds_addr(smem) + __builtin_amdgcn_readfirstlane(threadIdx.x >> 6) * 256);
    asm volatile("s_waitcnt vmcnt(0)" ::: "memory");
    if (bid < 87) return;
  }
  if (bid < 40) {
    const u32 tagv = ld_agent(A.gen);
    gemv_part<4>(64 * (bid >> 1), 320 * (bid & 1), tagv, true, A.gran, 1280 * (bid & 1) + 64 * (bid >> 1), A.c, A.wmod, A.mod, smem);
    if (threadIdx.x == 0) st_agent(A.arr + bid, tagv + 1u);
    STAMP(0, 7); return;
  }
  if (bid == 87) {
    const u32 tag = ld_agent(A.gen) + 1u;
    const int t = threadIdx.x, who = t < 40 ? t : t + 48;
    for (int spin = 0; spin < SPIN_LIMIT; ++spin) {
      const bool ok = t >= 296 || ld_agent(A.arr + who) == tag;
      if (__syncthreads_and(ok)) break;
      __builtin_amdgcn_s_sleep(8);
    }
    if (t == 0) st_agent(A.gen, tag);
    return;
  }
  u32* __restrict__ XT = A.XT;
  char* hT = smem; char* U = smem + 8 * HT_RS; float* part = (float*)(smem + 8 * HT_RS + 8 * 17 * 80);
  char* tab = smem + 8 * HT_RS + 8 * 17 * 80 + 5 * 16 * 4;
  const int tid = threadIdx.x, lane = tid & 63, w = __builtin_amdgcn_readfirstlane(tid >> 6);
  const int s0 = row_group(bid - K2_MAIN0);
  const int c = lane & 15, g = lane >> 4, q = c >> 2, p = c & 3;
  const u16* xrow = A.x + (size_t)s0 * D_;
  f32x4 tw0, tw1; u32 ard, awr, aw0, acr; float z0r, z0i;
  auto precompute = [&]() {
    const int Qw = 4 * w + q, rho = Qw / 5, d0 = 4 * (Qw % 5) + p;
    const bool rot = rho >= 3 && (rho & 1);
    const float fd = (float)d0 * (1.0f / 640.0f);
    const int k10 = g == 0 ? 16 : 2 * g;
    float s_[4], c_[4];
    sincos_rev(fd * (float)k10, s_[0], c_[0]); sincos_rev(fd * (float)(2 * g + 1), s_[1], c_[1]);
    sincos_rev(fd * (float)(8 + 2 * g), s_[2], c_[2]); sincos_rev(fd * (float)(9 + 2 * g), s_[3], c_[3]);
    tw0 = f32x4{rot ? s_[0] : c_[0], rot ? c_[0] : -s_[0], rot ? s_[1] : c_[1], rot ? c_[1] : -s_[1]};
    tw1 = f32x4{rot ? s_[2] : c_[2], rot ? c_[2] : -s_[2], rot ? s_[3] : c_[3], rot ? c_[3] : -s_[3]};
    const int Qr = 4 * w + p;
    ard = lds_addr(hT) + (Qr / 5) * HT_RS + 2 * (20 * (8 * g + q) + 4 * (Qr % 5));
    awr = lds_addr(U) + 4 * (rho * 17 * 20 + d0 + 2 * g * 20);
    aw0 = g == 0 ? awr + 4 * 16 * 20 : awr;
    z0r = rot ? 0.f : 1.f; z0i = rot ? 1.f : 0.f;
    acr = lds_addr(U) + (w < 8 ? ((2 * (w >> 1)) * 17 + c) * 80 : ((c & 7) * 17 + 16) * 80);
    pin(tw0); pin(tw1); pin(ard); pin(awr); pin(aw0); pin(z0r); pin(z0i); pin(acr);
  };
  if (w < 5) {
    u32 xv[8];
#pragma unroll
    for (int r = 0; r < 8; ++r) xv[r] = ldg<u32>(xrow + (size_t)r * 256 * D_, 4 * tid);
    const u32 tagv = ld_agent(A.gen);
    precompute();
    STAMP(1, 1);
    WAITALL();
    STAMP(1, 5);
    __syncthreads();
    float na[8], nb[8];
    ln_bcast8(part, lane, na, nb);
    f32x2 o[8];
    {
      f32x2 h[8];
#pragma unroll
      for (int r = 0; r < 8; ++r) { f32x2 v = {bf_lo(xv[r]), bf_hi(xv[r])}; h[r] = v * na[r] + nb[r]; }
      const float cc = 0.70710678118654752f;
      f32x2 a0 = h[0] + h[4], a1 = h[1] + h[5], a2 = h[2] + h[6], a3 = h[3] + h[7];
      f32x2 b0 = h[0] - h[4], b1 = h[1] - h[5], b2 = h[2] - h[6], b3 = h[3] - h[7];
      f32x2 e = a0 + a2, f = a1 + a3, u = cc * (b1 - b3), v = cc * (b1 + b3);
      o[0] = e + f; o[1] = e - f; o[2] = b0 + u; o[3] = -b2 - v; o[4] = a0 - a2; o[5] = a3 - a1; o[6] = b0 - u; o[7] = b2 - v;
#pragma unroll
      for (int r = 0; r < 8; ++r) pin(o[r]);
    }
    const u32 tag = __builtin_amdgcn_readfirstlane(tagv) + 1u;
    if (tid == 0) st_agent(A.arr + bid, tag);
    f32x2 sa, gg;
    {
      const unsigned long long* ga_ = A.gran + 2 * tid;
      u32x4 va, vb, vc, vd;
      for (int spin = 0; spin < SPIN_LIMIT; ++spin) {
        asm volatile("global_load_dwordx4 %0, %4, off sc1\n\tglobal_load_dwordx4 %1, %5, off sc1\n\tglobal_load_dwordx4 %2, %6, off sc1\n\tglobal_load_dwordx4 %3, %7, off sc1\n\ts_waitcnt vmcnt(0)"
                     : "=&v"(va), "=&v"(vb), "=&v"(vc), "=&v"(vd) : "v"(ga_), "v"(ga_ + 1280), "v"(ga_ + 640), "v"(ga_ + 1920) : "memory");
        const bool ok = va[1] == tag && va[3] == tag && vb[1] == tag && vb[3] == tag && vc[1] == tag && vc[3] == tag && vd[1] == tag && vd[3] == tag;
        if (__all(ok)) break;
      }
      sa = f32x2{rbf(__uint_as_float(va[0]) + __uint_as_float(vb[0])), rbf(__uint_as_float(va[2]) + __uint_as_float(vb[2]))};
      gg = f32x2{rbf(1.0f + rbf(__uint_as_float(vc[0]) + __uint_as_float(vd[0]))), rbf(1.0f + rbf(__uint_as_float(vc[2]) + __uint_as_float(vd[2])))};
    }
    STAMP(1, 2);
    o[0] = o[0] * gg + 8.0f * sa;
    *(u32*)(hT + 4 * tid) = pk_bf(o[0][0], o[0][1]);
#pragma unroll
    for (int r = 1; r < 8; ++r) { const f32x2 v = o[r] * gg; *(u32*)(hT + r * HT_RS + 4 * tid) = pk_bf(v[0], v[1]); }
  } else {
    const int i = w - 5;
    u32x4 f[4];
    const u32 xo = (u32)((c & 7) * 256 * D_ + 128 * i + 8 * g) * 2u;
#pragma unroll
    for (int k = 0; k < 4; ++k) f[k] = ldg<u32x4>(xrow, xo + 64 * k);
    const s16x8 t0 = i < 2 ? ldg<s16x8>(g_f32a.v[i < 2 ? i : 0], lane * 16) : ldg<s16x8>(&g_f20a.v[0][0][0][0] + (i - 2) * 512, lane * 16);
    const s16x8 t1 = ldg<s16x8>(&g_f20a.v[0][0][0][0] + (i < 3 ? i + 3 : 5) * 512, lane * 16);
    precompute();
    if (c >= 8) {
      const u32 fill = c == 8 ? 0x3f803f80u : 0u;
#pragma unroll
      for (int k = 0; k < 4; ++k) f[k] = u32x4{fill, fill, fill, fill};
    }
    gram_part(f, part, i, lane);
    *(s16x8*)(tab + i * 1024 + lane * 16) = t0;
    if (i < 3) *(s16x8*)(tab + (i + 5) * 1024 + lane * 16) = t1;
    __syncthreads();
    const int task = (bid - K2_MAIN0) * 5 + i;
    if (task < 1080) prep_gu(task, A.wg, A.wu, A.Wgu, smem + 32768 + 4096 * i, lane);
    if (i == 4) {
      const int xc = bid & 7, li = (bid - K2_MAIN0) >> 3;
#pragma unroll
      for (int k = 0; k < 4; ++k) {
        const int e = (k & 1) * 64 + lane, id = li * 96 + e;
        const int ln = id % 6, row = 8 * ((id / 6) & 255) + (id < 1536 ? xc : ((8 - xc) & 7));
        const u32 off = min((u32)(row * 642 + ln * 128), (u32)(2048 * 642 - 4)) & ~3u;
        if (e < 96) l2_touch((k >> 1) ? A.wi : A.wr, off, lds_addr(smem) + sizeof(smem) - 256);
      }
    }
  }
  __syncthreads();
  STAMP(1, 3);
  {
    const LDSP(const char) base = (LDSP(const char))(size_t)ard;
    s16x8 b = cat8(__builtin_amdgcn_ds_read_tr16_b64_v4i16((LDSP(s16x4))base), __builtin_amdgcn_ds_read_tr16_b64_v4i16((LDSP(s16x4))(base + 160)));
    s16x8 fa0 = *(const s16x8*)(tab + lane * 16), fa1 = *(const s16x8*)(tab + 1024 + lane * 16);
    batch_fence(); pin(b); pin(fa0); pin(fa1);
    const f32x4 z = {0.f, 0.f, 0.f, 0.f};
    f32x4 a0 = mfma16(fa0, b, z), a1 = mfma16(fa1, b, z);
    LDSP(u32) Ub = (LDSP(u32))(size_t)awr;
    const float x0 = g == 0 ? a0[1] : a0[0], x1 = g == 0 ? 0.f : a0[1];
    *(LDSP(u32))(size_t)aw0 = pk_bf(x0 * tw0[0] - x1 * tw0[1], x0 * tw0[1] + x1 * tw0[0]);
    if (g == 0) Ub[0] = pk_bf(a0[0] * z0r, a0[0] * z0i);
    Ub[20] = pk_bf(a0[2] * tw0[2] - a0[3] * tw0[3], a0[2] * tw0[3] + a0[3] * tw0[2]);
    Ub[8 * 20] = pk_bf(a1[0] * tw1[0] - a1[1] * tw1[1], a1[0] * tw1[1] + a1[1] * tw1[0]);
    Ub[9 * 20] = pk_bf(a1[2] * tw1[2] - a1[3] * tw1[3], a1[2] * tw1[3] + a1[3] * tw1[2]);
  }
  __syncthreads();
  STAMP(1, 4);
  const f32x4 z4 = {0.f, 0.f, 0.f, 0.f};
  u32* XTb = XT + (size_t)s0 * 16;
  const char* ft = tab + 2048 + lane * 16;
  const LDSP(const char) be = (LDSP(const char))(size_t)acr;
  const int P = w >> 1, mp = P, mm = P == 0 ? 4 : 8 - P;
  if (w < 8) {
    s16x8 e0 = *(const LDSP(s16x8))(be + 16 * g), e1 = *(const LDSP(s16x8))(be + 64), o0 = *(const LDSP(s16x8))(be + 1360 + 16 * g), o1 = *(const LDSP(s16x8))(be + 1360 + 64);
    char* dp = (char*)(XTb + (size_t)(mp * 21) * 4096); char* dm = (char*)(XTb + (size_t)(mm * 21) * 4096);
    const u32 vo = (u32)(2 * g * 4096 + c) * 4u;
    if (!(w & 1)) {
      s16x8 fm[4];
#pragma unroll
      for (int k = 0; k < 4; ++k) fm[k] = *(const s16x8*)(ft + k * 1024);
      batch_fence(); pin(e0); pin(e1); pin(o0); pin(o1); pin_arr(fm);
#pragma unroll
      for (int mt = 0; mt < 2; ++mt) {
        const s16x8 f0 = fm[2 * mt], f1 = fm[2 * mt + 1];
        f32x4 ae = mfma16(f1, e1, mfma16(f0, e0, z4)), ao = mfma16(f1, o1, mfma16(f0, o0, z4));
        f32x4 tp = ae + ao, tm = ae - ao;
        if (P == 0) { tp = ae; tm = ao; }
        char* ep = dp + (size_t)(8 * mt) * 16384; char* em = dm + (size_t)(8 * mt) * 16384;
        const int k0 = 8 * mt + 2 * g;
        { const float d0_ = (c == 0 && k0 >= 1 && k0 <= 9) ? 2.f : 1.f, d1_ = (c == 0 && k0 + 1 <= 9) ? 2.f : 1.f;
          const f32x4 dd = {d0_, d0_, d1_, d1_}; tp = tp * dd; tm = tm * dd; }
        if (mt == 0 || c != 0 || k0 <= 10) { stg_sel<4>(ep, vo, pk_bf(tp[0], tp[1])); stg_sel<4>(em, vo, pk_bf(tm[0], tm[1])); }
        if (mt == 0 || c != 0 || k0 + 1 <= 10) { stg_sel<4>(ep + 16384, vo, pk_bf(tp[2], tp[3])); stg_sel<4>(em + 16384, vo, pk_bf(tm[2], tm[3])); }
      }
    } else {
      s16x8 f0 = *(const s16x8*)(ft + 4 * 1024), f1 = *(const s16x8*)(ft + 5 * 1024);
      batch_fence(); pin(e0); pin(e1); pin(o0); pin(o1); pin(f0); pin(f1);
      f32x4 ae = mfma16(f1, e1, mfma16(f0, e0, z4)), ao = mfma16(f1, o1, mfma16(f0, o0, z4));
      f32x4 tp = ae + ao, tm = ae - ao;
      if (P == 0) { tp = ae; tm = ao; }
      if (g < 2 && c != 0) {
        char* ep = dp + (size_t)16 * 16384; char* em = dm + (size_t)16 * 16384;
        stg_sel<4>(ep, vo, pk_bf(tp[0], tp[1])); stg_sel<4>(ep + 16384, vo, pk_bf(tp[2], tp[3]));
        stg_sel<4>(em, vo, pk_bf(tm[0], tm[1])); stg_sel<4>(em + 16384, vo, pk_bf(tm[2], tm[3]));
      }
    }
  } else {
    const int rh = c & 7, mt = w - 7;
    s16x8 b0 = *(const LDSP(s16x8))(be + 16 * g), b1 = *(const LDSP(s16x8))(be + 64);
    s16x8 f0 = *(const s16x8*)(ft + (2 * mt) * 1024), f1 = *(const s16x8*)(ft + (2 * mt + 1) * 1024);
    batch_fence(); pin(b0); pin(b1); pin(f0); pin(f1);
    f32x4 a = mfma16(f1, b1, mfma16(f0, b0, z4));
    const float ca_ = (rh >= 2 && (rh & 1)) ? -1.f : 1.f, cb_ = rh >= 2 ? 1.f : 0.f;
    f32x4 t;
#pragma unroll
    for (int r = 0; r < 4; ++r) t[r] = ca_ * a[r] + cb_ * dpp_x1(a[r]);
    const int m1 = rh == 0 ? 0 : rh == 1 ? 4 : (rh & 1) ? 8 - (rh >> 1) : (rh >> 1);
    const int k0 = 8 * mt + 2 * g;
    const u32 o0 = (u32)((m1 * 21 + (k0 == 10 ? 20 : k0)) * 4096) * 4u, o1 = (u32)((m1 * 21 + k0 + 1) * 4096) * 4u;
    if (c < 8 && k0 >= 10 && k0 < 20) { stg_sel<4>(XTb, o0, pk_bf(t[0], t[1])); stg_sel<4>(XTb, o1, pk_bf(t[2], t[3])); }
    if (w == 9) asm volatile("s_waitcnt vmcnt(0)" ::: "memory");
  }
  STAMP(1, 7);
}

constexpr int EX_RS = 20;
extern "C" __global__ __launch_bounds__(512) void k3_mid(const u32* __restrict__ XT, const u16* __restrict__ wr, const u16* __restrict__ wi, u32* __restrict__ ZT) {
  STAMP_DECL; STAMP(2, 0);
  __shared__ __attribute__((aligned(16))) u32 ex[2][256 * EX_RS];
  __shared__ __attribute__((aligned(16))) char tab[8 * 1024];
  const int tid = threadIdx.x, lane = tid & 63, w = __builtin_amdgcn_readfirstlane(tid >> 6), c = lane & 15, g = lane >> 4;
  const int m1 = blockIdx.x & 7, tile = blockIdx.x >> 3;
  const char* in = (const char*)(XT + (size_t)(m1 * 21 + tile) * 4096);
  dma1k(in + 2048 * w, (u32)lane * 16u, lds_addr(ex[1]) + 2048 * w);
  dma1k(in + 2048 * w + 1024, (u32)lane * 16u, lds_addr(ex[1]) + 2048 * w + 1024);
  const F8w* tsrc = w < 2 ? &g_a1[m1] : w < 4 ? &g_f16f : w < 6 ? &g_f16i : &g_a1i[m1];
  const s16x8 tfrag = ldg<s16x8>(tsrc->v[w & 1], lane * 16);
  u32 wra[2][4], wia[2][4];
  const int kb = tile == 20 ? 336 : (c == 0 && tile >= 11) ? 32 * tile + 16 : 32 * tile + c;
  const bool mir = kb > 320;
  {
    const int kk = mir ? 640 - kb : kb;
#pragma unroll
    for (int j = 0; j < 2; ++j)
#pragma unroll
      for (int r = 0; r < 4; ++r) {
        const int m = m1 + 8 * (2 * w + j) + 128 * (4 * g + r), mm = mir ? ((2048 - m) & 2047) : m;
        const u32 vo = (u32)(mm * DF + kk) * 2u;
        wra[j][r] = (u32)ldg<u16>(wr, vo) << 16; wia[j][r] = (u32)ldg<u16>(wi, vo) << 16;
      }
  }
  f32x4 fcs[2], fsn[2], ics[2], isn[2];
  {
#pragma unroll
    for (int j = 0; j < 2; ++j) {
      float s0_, c0_, s1_, c1_;
      sincos_rev((float)((2 * w + j) * (m1 + 32 * g)) * (1.0f / 2048), s0_, c0_); sincos_rev((float)(2 * w + j) * (1.0f / 256), s1_, c1_);
#pragma unroll
      for (int r = 0; r < 4; ++r) { fcs[j][r] = c0_; fsn[j][r] = s0_; const float nc = c0_ * c1_ - s0_ * s1_, ns = s0_ * c1_ + c0_ * s1_; c0_ = nc; s0_ = ns; }
      const int M = m1 + 16 * w + 8 * j;
      sincos_rev((float)(4 * g * M) * (1.0f / 2048), s0_, c0_); sincos_rev((float)M * (1.0f / 2048), s1_, c1_);
#pragma unroll
      for (int r = 0; r < 4; ++r) { ics[j][r] = c0_; isn[j][r] = s0_; const float nc = c0_ * c1_ - s0_ * s1_, ns = s0_ * c1_ + c0_ * s1_; c0_ = nc; s0_ = ns; }
    }
  }
  u32 awr = lds_addr(ex) + 4 * ((4 * g * 16 + c) * EX_RS + 2 * w);
  u32 ard = lds_addr(ex) + 4 * ((2 * w * 16 + c) * EX_RS + 4 * g);
#pragma unroll
  for (int j = 0; j < 2; ++j) { pin(fcs[j]); pin(fsn[j]); pin(ics[j]); pin(isn[j]); }
  pin(awr); pin(ard);
  *(s16x8*)(tab + w * 1024 + lane * 16) = tfrag;
  STAMP(2, 1);
  wait_vm<16>();
  __syncthreads();
  STAMP(2, 2);
  u32x4 bx[2];
#pragma unroll
  for (int j = 0; j < 2; ++j)
#pragma unroll
    for (int i = 0; i < 4; ++i) bx[j][i] = ex[1][(16 * (4 * g + i) + 2 * w + j) * 16 + c];
  s16x8 A10 = *(const s16x8*)(tab + lane * 16), A11 = *(const s16x8*)(tab + lane * 16 + 1024);
  batch_fence(); pin_arr(bx); pin(A10); pin(A11);
  const f32x4 z4 = {0.f, 0.f, 0.f, 0.f};
  const char* tb = tab + lane * 16;
  const LDSP(char) wr0 = (LDSP(char))(size_t)awr; const LDSP(const char) rd0 = (LDSP(const char))(size_t)ard;
  constexpr int RB = 16 * EX_RS * 4, XB = 256 * EX_RS * 4;
  {
    u32 pk[2][4];
#pragma unroll
    for (int j = 0; j < 2; ++j) {
      f32x4 re = mfma16(A10, as8(bx[j]), z4), im = mfma16(A11, as8(bx[j]), z4);
      f32x4 tr = re * fcs[j] + im * fsn[j], ti = im * fcs[j] - re * fsn[j];
#pragma unroll
      for (int r = 0; r < 4; ++r) pk[j][r] = pk_bf(tr[r], ti[r]);
    }
#pragma unroll
    for (int r = 0; r < 4; ++r) *(LDSP(u32x2))(wr0 + r * RB) = u32x2{pk[0][r], pk[1][r]};
  }
  __syncthreads();
  STAMP(2, 3);
  {
    u32x4 bj[2] = {*(const LDSP(u32x4))(rd0), *(const LDSP(u32x4))(rd0 + RB)};
    s16x8 F0 = *(const s16x8*)(tb + 2048), F1 = *(const s16x8*)(tb + 3072), G0 = *(const s16x8*)(tb + 4096), G1 = *(const s16x8*)(tb + 5120);
    batch_fence(); pin_arr(bj); pin(F0); pin(F1); pin(G0); pin(G1);
    u32 pk[2][4];
#pragma unroll
    for (int j = 0; j < 2; ++j) {
      const u32x4 b = bj[j];
      f32x4 xr = mfma16(F0, as8(b), z4), xi = mfma16(F1, as8(b), z4);
      const f32x4 a = {__uint_as_float(wra[j][0]), __uint_as_float(wra[j][1]), __uint_as_float(wra[j][2]), __uint_as_float(wra[j][3])};
      const u32 sg = mir ? 0x80000000u : 0u;
      const f32x4 bb = {__uint_as_float(wia[j][0] ^ sg), __uint_as_float(wia[j][1] ^ sg), __uint_as_float(wia[j][2] ^ sg), __uint_as_float(wia[j][3] ^ sg)};
      const f32x4 yr = xr * a - xi * bb, yi = xr * bb + xi * a;
      const u32x4 yb = {pk_bf(yr[0], yr[1]), pk_bf(yr[2], yr[3]), pk_bf(yi[0], yi[1]), pk_bf(yi[2], yi[3])};
      f32x4 re = mfma16(G0, as8(yb), z4), im = mfma16(G1, as8(yb), z4);
      f32x4 tr = re * ics[j] - im * isn[j], ti = im * ics[j] + re * isn[j];
#pragma unroll
      for (int r = 0; r < 4; ++r) pk[j][r] = pk_bf(tr[r], ti[r]);
    }
#pragma unroll
    for (int r = 0; r < 4; ++r) *(LDSP(u32x2))(wr0 + XB + r * RB) = u32x2{pk[0][r], pk[1][r]};
  }
  __syncthreads();
  STAMP(2, 4);
  char* out = (char*)(ZT + (size_t)(2 * w) * 8 * 21 * 16 + (m1 * 21 + tile) * 16);
  const u32 vout = (u32)(64 * g * 10752 + c * 4);
  u32x4 bz[2] = {*(const LDSP(u32x4))(rd0 + XB), *(const LDSP(u32x4))(rd0 + XB + RB)};
  s16x8 I0 = *(const s16x8*)(tb + 6144), I1 = *(const s16x8*)(tb + 7168);
  batch_fence(); pin_arr(bz); pin(I0); pin(I1);
#pragma unroll
  for (int j = 0; j < 2; ++j) {
    const u32x4 b = bz[j];
    f32x4 zr = mfma16(I0, as8(b), z4), zi = mfma16(I1, as8(b), z4);
#pragma unroll
    for (int r = 0; r < 4; ++r) stg_sel<8>(out + (16 * r + j) * 10752, vout, pk_bf(zr[r], zi[r]));
  }
  STAMP(2, 7);
}

constexpr int Y_RS = 640 + 4;
extern "C" __global__ __launch_bounds__(640) void k4_inv(const u32* __restrict__ ZT, const u16* __restrict__ x, const float* __restrict__ mod,
                                              u16* __restrict__ x1o, u16* __restrict__ h2o, const u16* __restrict__ Wgu) {
  STAMP_DECL; STAMP(3, 0);
  __shared__ __attribute__((aligned(16))) char smem[162 * 64 + 8 * Y_RS * 4 + 10 * 8 * 4 + 8 * 1024 + 10752 + 256];
  char* Vs = smem; float* Y = (float*)(Vs + 162 * 64); float* part = Y + 8 * Y_RS; char* tab = (char*)(part + 80);
  const int tid = threadIdx.x, lane = tid & 63, w = __builtin_amdgcn_readfirstlane(tid >> 6), c = lane & 15, g = lane >> 4;
  const int s0 = row_group(blockIdx.x);
  u32* Zs = (u32*)(tab + 8 * 1024);
  {
    const char* Zb = (const char*)(ZT + (size_t)s0 * 8 * 21 * 16);
    dma1k(Zb + 1024 * w, (u32)lane * 16u, lds_addr(Zs) + 1024 * w);
    if (w == 0 && lane < 32) dma1k(Zb + 10240, (u32)lane * 16u, lds_addr(Zs) + 10240);
  }
  const int rho = w < 8 ? w : (c & 7), P = rho >> 1, mA = rho == 1 ? 4 : P, mB = P == 0 ? 4 : 8 - P;
  const s16x8 tfrag = w < 6 ? ldg<s16x8>(&g_f20ai.v[0][0][0][0] + w * 512, lane * 16) : ldg<s16x8>(g_f32bi.v[(w - 6) & 1], lane * 16);
  const int hf = w >= 5, cp = tid - 320 * hf;
  u32 xv[4];
#pragma unroll
  for (int r = 0; r < 4; ++r) xv[r] = ldg<u32>(x + (size_t)(s0 + 256 * (4 * hf + r)) * D_, 4 * cp);
  const f32x2 ga = ldg<f32x2>(mod + 2 * 640, 8 * cp), sb = ldg<f32x2>(mod + 3 * 640, 8 * cp), cb = ldg<f32x2>(mod + 4 * 640, 8 * cp);
  f32x4 tw[3]; u32 aw[3];
  {
    const bool rot = rho >= 3 && (rho & 1);
    const float sc = w < 8 ? 1.0f / (2048.0f * 640.0f) : 2.0f / (2048.0f * 640.0f), fk = (float)(w < 8 ? c : 16) * (1.0f / 640.0f);
    float s0_, c0_, s1_, c1_, s8_, c8_;
    sincos_rev(fk * (float)(2 * g), s0_, c0_); sincos_rev(fk, s1_, c1_); sincos_rev(fk * 8.0f, s8_, c8_);
    float br = (rot ? -s0_ : c0_) * sc, bi = (rot ? c0_ : s0_) * sc;
#pragma unroll
    for (int mt = 0; mt < 3; ++mt) {
      tw[mt] = f32x4{br, bi, br * c1_ - bi * s1_, br * s1_ + bi * c1_};
      const float nr = br * c8_ - bi * s8_, ni = br * s8_ + bi * c8_; br = nr; bi = ni;
      const int d0 = 8 * mt + 2 * g;
      const bool ok = d0 < 20 && (w < 8 || c < 8);
      aw[mt] = lds_addr(Vs) + (ok ? (rho * 20 + d0) * 64 : 160 * 64) + (w < 8 ? 4 * c : 2);
      pin(tw[mt]); pin(aw[mt]);
    }
  }
  float be = rho < 2 ? 0.f : (rho & 1) ? -1.f : 1.f;
  u32 za0, za1, dbits, zdel = (u32)(mB - mA) * 21u * 64u;
  {
    const u32 zb = lds_addr(Zs) + mA * 21 * 64;
    if (w < 8) { za0 = zb + (64 * g + c) * 4; za1 = zb + 16 * 64 + c * 4; dbits = c == 0 ? (0xF0u | (g == 2 ? 8u : g == 3 ? 15u : 0u)) : 0u; }
    else {
      za0 = zb + (g == 3 ? 12 : 11 - 3) * 64; za1 = zb + 16 * 64; dbits = (g < 2 ? 0xFu : g == 2 ? 3u : 0u) | (g == 0 ? 0u : 0xF0u);
    }
  }
  const u32 z10 = lds_addr(Zs) + (mA * 21 + 20) * 64;
  pin(be); pin(za0); pin(za1); pin(dbits); pin(zdel);
  if (w == 9 && blockIdx.x < 8) {
    const void* c5; const void* c6; CODE_ADDR(c5, k5_glu); CODE_ADDR(c6, k6_down);
    const u32 len = (u32)((const char*)c6 - (const char*)c5);
    if (lane * 128u < len) l2_touch(c5, (u32)lane * 128u, lds_addr(smem) + sizeof(smem) - 256);
    if (lane * 128u + 8192u < len) l2_touch(c5, (u32)lane * 128u + 8192u, lds_addr(smem) + sizeof(smem) - 256);
  }
  STAMP(3, 1);
  if (w < 8) *(s16x8*)(tab + w * 1024 + lane * 16) = tfrag;
  wait_vm<4>();
  __syncthreads();
  if (w < 9) {
    u32 zz[8], zy[8]; s16x8 ft[6];
#pragma unroll
    for (int i = 0; i < 8; ++i) {
      u32 ad = (i < 4 ? za0 : za1) + 64 * (i & 3);
      if (i == 2) ad = (w == 8 && g == 2) ? z10 : ad;
      zz[i] = *(const LDSP(u32))(size_t)ad; zy[i] = *(const LDSP(u32))(size_t)(ad + zdel);
    }
#pragma unroll
    for (int k = 0; k < 6; ++k) ft[k] = *(const s16x8*)(tab + k * 1024 + lane * 16);
    batch_fence(); pin_arr(zz); pin_arr(zy); pin_arr(ft);
    u32x4 b0, b1;
#pragma unroll
    for (int i = 0; i < 8; ++i) {
      const u32 keep = ~(u32)__builtin_amdgcn_sbfe((int)dbits, i, 1);
      const u32 p_ = zz[i] & keep, q_ = zy[i] & keep;
      const f32x2 a = {bf_lo(p_), bf_hi(p_)}, b = {bf_lo(q_), bf_hi(q_)};
      const f32x2 v = a + be * b;
      if (i < 4) b0[i] = pk_bf(v[0], v[1]); else b1[i - 4] = pk_bf(v[0], v[1]);
    }
    STAMP(3, 2);
    f32x4 a[3];
#pragma unroll
    for (int mt = 0; mt < 3; ++mt) {
      const f32x4 z = {0.f, 0.f, 0.f, 0.f};
      a[mt] = mfma16(ft[2 * mt + 1], as8(b1), mfma16(ft[2 * mt], as8(b0), z));
    }
    if (w < 8) {
      const u32 hdel = c == 0 ? lds_addr(Vs) + 160 * 64 + 2 : 0u;
#pragma unroll
      for (int mt = 0; mt < 3; ++mt)
#pragma unroll
        for (int rr = 0; rr < 2; ++rr) {
          const u32 pv = pk_bf(a[mt][2 * rr] * tw[mt][2 * rr] - a[mt][2 * rr + 1] * tw[mt][2 * rr + 1], a[mt][2 * rr] * tw[mt][2 * rr + 1] + a[mt][2 * rr + 1] * tw[mt][2 * rr]);
          *(LDSP(u16))(size_t)(aw[mt] + 64 * rr) = (u16)pv;
          *(LDSP(u16))(size_t)((c == 0 ? hdel : aw[mt] + 2) + 64 * rr) = (u16)(pv >> 16);
        }
    } else {
#pragma unroll
      for (int mt = 0; mt < 3; ++mt)
#pragma unroll
        for (int rr = 0; rr < 2; ++rr)
          *(LDSP(u16))((LDSP(char))(size_t)aw[mt] + 64 * rr) = (u16)pk_bf(a[mt][2 * rr] * tw[mt][2 * rr] - a[mt][2 * rr + 1] * tw[mt][2 * rr + 1], 0.f);
    }
  }
  __syncthreads();
  STAMP(3, 3);
  {
    s16x8 a = *(const s16x8*)(Vs + (16 * w + c) * 64 + 16 * g);
    s16x8 fb0 = *(const s16x8*)(tab + 6144 + lane * 16), fb1 = *(const s16x8*)(tab + 7168 + lane * 16);
    batch_fence(); pin(a); pin(fb0); pin(fb1);
    const f32x4 z = {0.f, 0.f, 0.f, 0.f};
    const f32x4 y0 = mfma16(a, fb0, z), y1 = mfma16(a, fb1, z);
    const int R = 16 * w + 4 * g, rr = R / 20, d0 = R % 20;
    *(f32x4*)(Y + rr * Y_RS + 20 * c + d0) = y0;
    *(f32x4*)(Y + rr * Y_RS + 20 * (16 + c) + d0) = y1;
  }
  __syncthreads();
  STAMP(3, 4);
  {
    f32x2 R[8];
#pragma unroll
    for (int r = 0; r < 8; ++r) R[r] = *(const f32x2*)(Y + r * Y_RS + 2 * cp);
    batch_fence(); pin_arr(R);
    const float cc = 0.70710678118654752f;
    const f32x2 G = R[0] + R[1], Fm = R[0] - R[1];
    const f32x2 u = cc * (R[2] - R[6]), v = cc * (R[3] + R[7]);
    const f32x2 c13 = R[2] + R[6], s13 = R[3] - R[7];
    const float sg = hf ? -1.f : 1.f;
    f32x2 y[4];
    y[0] = G + R[4] + sg * c13;
    y[2] = G - R[4] + sg * s13;
    y[1] = Fm + R[5] + sg * (u + v);
    y[3] = Fm - R[5] + sg * (v - u);
    u32 x1[4];
    float sv[8];
#pragma unroll
    for (int r = 0; r < 4; ++r) {
      const f32x2 xx = {bf_lo(xv[r]), bf_hi(xv[r])};
      const f32x2 t = xx + ga * y[r];
      x1[r] = pk_bf(t[0], t[1]);
      stg_sel<2>(x1o + (size_t)(8 * s0 + 4 * hf + r) * D_, (u32)cp * 4u, x1[r]);
      sv[r] = dot2bf(x1[r], 0x3f803f80u, 0.f); sv[4 + r] = dot2bf(x1[r], x1[r], 0.f);
    }
    const float tot = reduce8(sv, lane);
    if (lane < 8) part[w * 8 + lane] = tot;
    const f32x2 gg = {rbf(1.0f + cb[0]), rbf(1.0f + cb[1])};
    const int d = 2 * cp, wi = d & 31;
    char* stage = (char*)Y;
    const int so = (((d >> 5) * 4 + ((wi & 15) >> 2)) * 8 + 4 * hf) * 16 + (4 * (wi >> 4) + (wi & 3)) * 2;
    __syncthreads();
    STAMP(3, 5);
    const int r = lane & 3;
    float s, qq;
    { float ps[5], pq[5];
#pragma unroll
      for (int i = 0; i < 5; ++i) { ps[i] = part[(5 * hf + i) * 8 + r]; pq[i] = part[(5 * hf + i) * 8 + 4 + r]; }
      batch_fence(); pin_arr(ps); pin_arr(pq);
      s = ((ps[0] + ps[1]) + (ps[2] + ps[3])) + ps[4]; qq = ((pq[0] + pq[1]) + (pq[2] + pq[3])) + pq[4]; }
    float m_, i_; ln_finish(s, qq, m_, i_);
    const float nb_ = -m_ * i_;
#pragma unroll
    for (int i = 0; i < 4; ++i) {
      const float na = __int_as_float(__builtin_amdgcn_readlane(__float_as_int(i_), i)), nb = __int_as_float(__builtin_amdgcn_readlane(__float_as_int(nb_), i));
      const f32x2 v2 = {bf_lo(x1[i]), bf_hi(x1[i])};
      const f32x2 hv = (v2 * na + nb) * gg + sb;
      *(u32*)(stage + so + 16 * i) = pk_bf(hv[0], hv[1]);
    }
    __syncthreads();
    const u32x4 piece = *(const u32x4*)(stage + 16 * tid);
    stg_sel<16>(h2o + ((size_t)(s0 >> 1) * 20 * 64 + 8 * (s0 & 1)) * 8, (u32)((tid >> 3) * 16 + (tid & 7)) * 16u, piece);
  }
  if (w == 9) asm volatile("s_waitcnt vmcnt(0)" ::: "memory");
  STAMP(3, 7);
}

constexpr int K5_SLOT = 28 * 1024;
constexpr int K5_NS = 4;
constexpr int K5_PD = 4;
struct K5Ctx { const u16* wdp; int pfl; u32 dump; const u16* wsrc; const u16* hsrc; u32 ldsw; u32 goff[7]; u32 hoff; u32 abase[2]; };
template <int s, int i> DI void k5_w1(const K5Ctx& c) {
  if constexpr (i == 0) set_m0(c.ldsw + (s % K5_NS) * K5_SLOT + 3072);
  glds16_s<1024 * i - 3072>((const char*)(c.wsrc + s * 4 * 512) - 4096, c.goff[i]);
}
template <int s, int i> DI void k5_t1(const K5Ctx& c, u32x4 (&t)[4]) {
  gload16_s<(i >> 1) * 1024>(t[i], c.hsrc + ((i & 1) * 20 + 2 * s) * 512, c.hoff);
}
template <int s> DI void k5_wall(const K5Ctx& c) { k5_w1<s, 0>(c); k5_w1<s, 1>(c); k5_w1<s, 2>(c); k5_w1<s, 3>(c); k5_w1<s, 4>(c); k5_w1<s, 5>(c); k5_w1<s, 6>(c); }
template <int s> DI void k5_tall(const K5Ctx& c, u32x4 (&t)[4]) { k5_t1<s, 0>(c, t); k5_t1<s, 1>(c, t); k5_t1<s, 2>(c, t); k5_t1<s, 3>(c, t); }
template <int n> DI s16x8 k5_rd(const K5Ctx& c) {
  constexpr int s = n / 28, t = (n / 14) & 1, j = n % 14, sl = s % K5_NS;
  return *(const LDSP(const s16x8))((LDSP(const char))(size_t)c.abase[sl >> 1] + (sl & 1) * K5_SLOT + ((j >> 1) * 4 + t * 2 + (j & 1)) * 1024);
}
template <int n> DI void k5_sub(const K5Ctx& c, f32x4 (&acc)[14][2], s16x8 (&fr)[K5_PD + 1], u32x4 (&tok)[10][4]) {
  constexpr int s = n / 28, t = (n / 14) & 1, j = n % 14;
  constexpr int nb = n + K5_PD;
  if constexpr (nb < 280 && nb % 28 == 0) {
    wait_vm<(s + 2 <= 9) ? 11 : 0>();
    __builtin_amdgcn_s_barrier();
    SLAB(s + 1);
    tie4(tok[s + 1]);
  }
  if constexpr (nb < 280) fr[nb % (K5_PD + 1)] = k5_rd<nb>(c);
  {
    constexpr int q = nb / 28, pos = nb % 28 - (q == 0 ? K5_PD : 0);
    constexpr int idx = (pos % 2 == 0 && pos < 22) ? pos / 2 : -1;
    if constexpr (idx >= 0 && idx < 4 && q + 2 <= 9) k5_t1<q + 2, idx>(c, tok[q + 2]);
    if constexpr (idx >= 4 && q + 2 <= 9) k5_w1<q + 2, idx - 4>(c);
  }
  if constexpr (false) {
    constexpr int i = (n - 224) / 2;
    l2_touch(c.wdp, (u32)(c.pfl + i * 64) * 128u, c.dump);
  }
  const s16x8 a = fr[n % (K5_PD + 1)];
  acc[j][0] = mfma16(a, as8(tok[s][2 * t]), acc[j][0]);
  acc[j][1] = mfma16(a, as8(tok[s][2 * t + 1]), acc[j][1]);
  __builtin_amdgcn_sched_barrier(0);
}
template <int... I> DI void k5_all(const K5Ctx& c, f32x4 (&acc)[14][2], s16x8 (&fr)[K5_PD + 1], u32x4 (&tok)[10][4], std::integer_sequence<int, I...>) { (k5_sub<I>(c, acc, fr, tok), ...); }
extern "C" __global__ __launch_bounds__(256, 2) void k5_glu(const u16* __restrict__ h2, const u16* __restrict__ Wgu, u16* __restrict__ hb, const u16* __restrict__ Wd) {
  STAMP_DECL; STAMP(4, 0);
  extern __shared__ __attribute__((aligned(16))) char smem[];
  const int tid = threadIdx.x, lane = tid & 63, w = __builtin_amdgcn_readfirstlane(tid >> 6);
  const int xcd = blockIdx.x & 7, li = blockIdx.x >> 3;
  const int tt = 8 * (xcd & 1) + (li & 7), fc = 4 * (xcd >> 1) + (li >> 3);
  const int u0 = fc < 12 ? 7 * fc : 84 + 6 * (fc - 12), U = fc < 12 ? 7 : 6;
  if (blockIdx.x < 8) {
    const void* c6; const void* c7; CODE_ADDR(c6, k6_down); CODE_ADDR(c7, k7_end);
    if ((u32)tid * 128u < (u32)((const char*)c7 - (const char*)c6)) l2_touch(c6, (u32)tid * 128u, lds_addr(smem) + 3 * K5_SLOT + w * 256);
  }
  K5Ctx c;
  c.wsrc = Wgu + (size_t)u0 * 20 * 2 * 512;
  c.dump = lds_addr(smem) + 2 * K5_SLOT + w * 256;
  c.wdp = Wd + (size_t)(20 * (xcd >> 2)) * 54 * 512;
#pragma unroll
  for (int i = 0; i < 7; ++i) { const int f = 7 * w + i, u = min(f >> 2, U - 1); c.goff[i] = 2 * (u * 20 * 2 * 512 + (f & 3) * 512) + lane * 16 + 4096 - (1024 * i - 3072); }
  c.ldsw = lds_addr(smem) + 7 * w * 1024;
  const int T16 = 8 * tt + 2 * w;
  c.hsrc = h2 + (size_t)T16 * 20 * 512; c.hoff = lane * 16;
#pragma unroll
  for (int j = 0; j < 2; ++j) { c.abase[j] = lds_addr(smem) + 2 * j * K5_SLOT + lane * 16; asm volatile("" : "+v"(c.abase[j])); }
  u32x4 tok[10][4];
  k5_tall<0>(c, tok[0]); k5_wall<0>(c);
  k5_tall<1>(c, tok[1]); k5_wall<1>(c);
  f32x4 acc[14][2];
#pragma unroll
  for (int u = 0; u < 14; ++u) { acc[u][0] = f32x4{0.f, 0.f, 0.f, 0.f}; acc[u][1] = f32x4{0.f, 0.f, 0.f, 0.f}; }
  STAMP(4, 3);
  wait_vm<11>();
  __builtin_amdgcn_s_barrier();
  tie4(tok[0]);
  STAMP(4, 4); SLAB(0);
  s16x8 fr[K5_PD + 1];
  fr[0] = k5_rd<0>(c); fr[1] = k5_rd<1>(c); fr[2] = k5_rd<2>(c); fr[3] = k5_rd<3>(c);
  __builtin_amdgcn_sched_barrier(0);
  k5_all(c, acc, fr, tok, std::make_integer_sequence<int, 280>{});
  STAMP(4, 5); SLAB(10);
  auto hv = [&](int u, int tl) { const f32x4 a = acc[2 * u][tl] * acc[2 * u + 1][tl]; return u32x2{pk_bf(a[0], a[1]), pk_bf(a[2], a[3])}; };
#pragma unroll
  for (int tl = 0; tl < 2; ++tl) {
    u16* hdst = hb + (size_t)(T16 + tl) * 54 * 512 + (u0 >> 1) * 512;
    const u32 vo = (u32)lane * 16u;
    if (!(u0 & 1)) {
#pragma unroll
      for (int k = 0; k < 3; ++k) { const u32x2 a = hv(2 * k, tl), b = hv(2 * k + 1, tl); stg_sel<32>(hdst + k * 512, vo, u32x4{a[0], a[1], b[0], b[1]}); }
      if (U == 7) stg_sel<32>(hdst + 3 * 512, vo, hv(6, tl));
    } else {
      stg_sel<32>(hdst + 4, vo, hv(0, tl));
#pragma unroll
      for (int k = 0; k < 3; ++k) { const u32x2 a = hv(2 * k + 1, tl), b = hv(2 * k + 2, tl); stg_sel<32>(hdst + (k + 1) * 512, vo, u32x4{a[0], a[1], b[0], b[1]}); }
    }
  }
  STAMP(4, 7);
}

constexpr int K6_NS = 4;
struct K6Ctx { const u16* wsrc; const u16* hsrc; u32 hoff; const float* gbp; u32 gbo; const u16* xp; u32 xo; const u16* tbase; u32 toff; u32 dump; };
struct K6Grp { u32x4 a[5]; u32x4 t[4]; };
template <int i> DI void k6_v1(const K6Ctx& c, int ks, K6Grp& gr) {
  if constexpr (i < 5) gload16_s<0>(gr.a[i], c.wsrc + (ks + i * 54) * 512, c.hoff);
  else gload16_s<0>(gr.t[i - 5], c.hsrc + (ks + (i - 5) * 54) * 512, c.hoff);
}
DI void k6_issue(const K6Ctx& c, int ks, K6Grp& gr) {
  k6_v1<0>(c, ks, gr); k6_v1<5>(c, ks, gr); k6_v1<6>(c, ks, gr); k6_v1<7>(c, ks, gr); k6_v1<8>(c, ks, gr);
  k6_v1<1>(c, ks, gr); k6_v1<2>(c, ks, gr); k6_v1<3>(c, ks, gr); k6_v1<4>(c, ks, gr);
}
DI void k6_tie(K6Grp& g) { asm volatile("" : "+v"(g.a[0]), "+v"(g.a[1]), "+v"(g.a[2]), "+v"(g.a[3]), "+v"(g.a[4]), "+v"(g.t[0]), "+v"(g.t[1]), "+v"(g.t[2]), "+v"(g.t[3])); }
struct K6Epi { u32x4 gb[5]; u32x2 xr[5]; };
template <int OFF> DI void gload8_s(u32x2& d, const void* sbase, u32 voff) {
  asm volatile("global_load_dwordx2 %0, %1, %2 offset:%3" : "=v"(d) : "v"(voff), "s"(sbase), "n"(OFF) : "memory");
}
#define K6_TOUCH 0
#define K6_TDV 5
constexpr int K6_TD = K6_TDV, K6_TLAST = K6_TOUCH ? 12 : -1;
template <int j> DI void k6_touch(const K6Ctx& c, int lane) { if (lane < 13) l2_touch(c.tbase + j * 512, c.toff, c.dump); }
constexpr int k6_after(int i, int NK) {
  int n = 0; bool seen = false;
  for (int g = 0; g < 3; ++g) { if (seen) n += 9; if (g == i) seen = true; }
  for (int j = 3; j < K6_TD; ++j) if (seen && K6_TOUCH) n += 1;
  for (int st = 0; st < i; ++st) {
    if (st + K6_TD <= K6_TLAST && seen) n += 1;
    if (st == NK - 3 && seen) n += 10;
    if (st + 3 < NK) { if (seen) n += 9; if (st + 3 == i) seen = true; }
  }
  return n;
}
template <int i, int NK> DI void k6_step(const K6Ctx& c, int ks0, f32x4 (&acc)[5][4], K6Grp (&gr)[K6_NS], K6Epi& ep, f32x4* red, int w, int lane) {
  wait_vm<k6_after(i, NK)>();
  K6Grp& g = gr[i % K6_NS];
  k6_tie(g);
  K6Grp& nx = gr[(i + 3) % K6_NS];
  if constexpr (i + K6_TD <= K6_TLAST) k6_touch<i + K6_TD>(c, lane);
#pragma unroll
  for (int nt = 0; nt < 5; ++nt) {
    if constexpr (i + 3 < NK) {
      if (nt == 0) { k6_v1<0>(c, ks0 + i + 3, nx); k6_v1<5>(c, ks0 + i + 3, nx); }
      if (nt == 1) { k6_v1<6>(c, ks0 + i + 3, nx); k6_v1<7>(c, ks0 + i + 3, nx); }
      if (nt == 2) { k6_v1<8>(c, ks0 + i + 3, nx); k6_v1<1>(c, ks0 + i + 3, nx); }
      if (nt == 3) { k6_v1<2>(c, ks0 + i + 3, nx); k6_v1<3>(c, ks0 + i + 3, nx); }
      if (nt == 4) { k6_v1<4>(c, ks0 + i + 3, nx); }
    }
    if constexpr (i == NK - 3) {
      gload16_s<0>(ep.gb[nt], c.gbp + 16 * nt, c.gbo); gload8_s<0>(ep.xr[nt], c.xp + 16 * nt, c.xo);
    }
#pragma unroll
    for (int t = 0; t < 4; ++t) acc[nt][t] = mfma16(as8(g.a[nt]), as8(g.t[t]), acc[nt][t]);
    if constexpr (i == NK - 1) {
      if (nt >= 1) {
#pragma unroll
        for (int t = 0; t < 4; ++t) if (t != w) red[((w * 5 + nt - 1) * 4 + t) * 64 + lane] = acc[nt - 1][t];
      }
    }
    __builtin_amdgcn_sched_barrier(0);
  }
}
template <int i, int NK> struct K6Loop { static DI void run(const K6Ctx& c, int ks0, f32x4 (&acc)[5][4], K6Grp (&gr)[K6_NS], K6Epi& ep, f32x4* red, int w, int lane) {
  k6_step<i, NK>(c, ks0, acc, gr, ep, red, w, lane); K6Loop<i + 1, NK>::run(c, ks0, acc, gr, ep, red, w, lane); } };
template <int NK> struct K6Loop<NK, NK> { static DI void run(const K6Ctx&, int, f32x4 (&)[5][4], K6Grp (&)[K6_NS], K6Epi&, f32x4*, int, int) {} };
template <int NK> DI void k6_main(const K6Ctx& cx, int ks0, f32x4* red, int w, int lane, u16* __restrict__ out, u32 oo) {
  K6Grp gr[K6_NS]; K6Epi ep;
  k6_issue(cx, ks0, gr[0]); k6_issue(cx, ks0 + 1, gr[1]); k6_issue(cx, ks0 + 2, gr[2]);
  if constexpr (K6_TOUCH) { k6_touch<3>(cx, lane); k6_touch<4>(cx, lane); if constexpr (K6_TD > 5) k6_touch<5>(cx, lane); if constexpr (K6_TD > 6) k6_touch<6>(cx, lane); }
  f32x4 acc[5][4];
#pragma unroll
  for (int a = 0; a < 5; ++a)
#pragma unroll
    for (int b = 0; b < 4; ++b) acc[a][b] = f32x4{0.f, 0.f, 0.f, 0.f};
  K6Loop<0, NK>::run(cx, ks0, acc, gr, ep, red, w, lane);
#pragma unroll
  for (int t = 0; t < 4; ++t) if (t != w) red[((w * 5 + 4) * 4 + t) * 64 + lane] = acc[4][t];
  wait_vm<0>();
  asm volatile("" : "+v"(ep.gb[0]), "+v"(ep.gb[1]), "+v"(ep.gb[2]), "+v"(ep.gb[3]), "+v"(ep.gb[4]), "+v"(ep.xr[0]), "+v"(ep.xr[1]), "+v"(ep.xr[2]), "+v"(ep.xr[3]), "+v"(ep.xr[4]));
  __syncthreads();
#pragma unroll
  for (int nt = 0; nt < 5; ++nt) {
    f32x4 v = w == 0 ? acc[nt][0] : w == 1 ? acc[nt][1] : w == 2 ? acc[nt][2] : acc[nt][3];
#pragma unroll
    for (int ww = 1; ww < 4; ++ww) v += red[((((w + ww) & 3) * 5 + nt) * 4 + w) * 64 + lane];
    const f32x4 gb = __builtin_bit_cast(f32x4, ep.gb[nt]);
    const f32x4 x4 = {bf_lo(ep.xr[nt][0]), bf_hi(ep.xr[nt][0]), bf_lo(ep.xr[nt][1]), bf_hi(ep.xr[nt][1])};
    const f32x4 o = x4 + gb * v;
    stg_sel<64>(out + 16 * nt, oo, u32x2{pk_bf(o[0], o[1]), pk_bf(o[2], o[3])});
  }
}
extern "C" __global__ __launch_bounds__(256, 2) void k6_down(const u16* __restrict__ hb, const u16* __restrict__ Wd, const u16* __restrict__ x1, const float* __restrict__ mod, u16* __restrict__ out) {
  STAMP_DECL; STAMP(5, 0);
  extern __shared__ __attribute__((aligned(16))) char smem[];
  const int tid = threadIdx.x, lane = tid & 63, w = __builtin_amdgcn_readfirstlane(tid >> 6), c = lane & 15, g = lane >> 4;
  const int xcd = blockIdx.x & 7, li = blockIdx.x >> 3;
  const int tt = 8 * (xcd & 3) + (li & 7), nb = 4 * (xcd >> 2) + (li >> 3);
  const int n0 = 80 * nb;
  const int ks0 = w < 2 ? 14 * w : 28 + 13 * (w - 2);
  K6Ctx cx;
  cx.wsrc = Wd + (size_t)(5 * nb) * 54 * 512; cx.hsrc = hb + (size_t)(4 * tt) * 54 * 512; cx.hoff = lane * 16;
  cx.gbp = mod + 5 * 640 + n0; cx.gbo = 16 * g;
  cx.xp = x1 + (size_t)(64 * tt + 16 * w) * D_ + n0; cx.xo = (u32)(c * D_ + 4 * g) * 2u;
  {
    const int L = min(13 * (4 * li + w) + lane, 1663), wp = L / 416, r = L % 416, k0p = wp < 2 ? 14 * wp : 28 + 13 * (wp - 2);
    const u32 wdelta = (u32)((const char*)Wd - (const char*)hb);
    const u32 oh = (u32)(((4 * (8 * (xcd & 3) + (r >> 5)) + ((r >> 3) & 3)) * 54 + k0p) * 1024 + (r & 7) * 128);
    const int r2 = r - 256;
    const u32 ow = wdelta + (u32)(((5 * (4 * (xcd >> 2) + r2 / 40) + (r2 % 40) / 8) * 54 + k0p) * 1024 + (r2 & 7) * 128);
    cx.tbase = hb; cx.toff = r < 256 ? oh : ow; cx.dump = lds_addr(smem) + 81920;
  }
  const int tau = 64 * tt + 16 * w + c, srow = (tau >> 3) + 256 * (tau & 7);
  const u32 oo = (u32)(srow * D_ + 4 * g) * 2u;
  if (w < 2) k6_main<14>(cx, ks0, (f32x4*)smem, w, lane, out + n0, oo); else k6_main<13>(cx, ks0, (f32x4*)smem, w, lane, out + n0, oo);
  STAMP(5, 7);
}

extern "C" __global__ void k7_end() {}

constexpr size_t WS_MOD = 0, WS_XT = 16384, WS_ZT = WS_XT + 8 * 21 * 256 * 16 * 4, WS_X1 = WS_ZT + 8 * 21 * 256 * 16 * 4,
                 WS_H2 = WS_X1 + 2048 * 640 * 2, WS_HB = WS_H2 + 2048 * 640 * 2, WS_WGU = WS_HB + 2048 * 1728 * 2, WS_WD = WS_WGU + 2 * 640 * 1728 * 2, WS_WP = WS_WD + 640 * 1728 * 2, WS_SYNC = WS_WP + 168 * 16384;
extern "C" void kernel_launch(void* const* d_in, const int* in_sizes, int n_in, void* d_out, int out_size, void* d_ws, size_t ws_size,
                              hipStream_t stream) {
  const u16* x = (const u16*)d_in[0]; const u16* wr = (const u16*)d_in[1]; const u16* wi = (const u16*)d_in[2];
  const u16* wg = (const u16*)d_in[4]; const u16* wu = (const u16*)d_in[5]; const u16* wd = (const u16*)d_in[6];
  const u16* c = (const u16*)d_in[7]; const u16* wmod = (const u16*)d_in[8];
  char* ws = (char*)d_ws;
  float* mod = (float*)(ws + WS_MOD); u32* XT = (u32*)(ws + WS_XT); u32* ZT = (u32*)(ws + WS_ZT);
  u16* x1 = (u16*)(ws + WS_X1); u16* h2 = (u16*)(ws + WS_H2); u16* hb = (u16*)(ws + WS_HB);
  u16* Wgu = (u16*)(ws + WS_WGU); u16* Wd = (u16*)(ws + WS_WD);
  K2Args A;
  A.x = x; A.c = c; A.wmod = wmod; A.mod = mod; A.XT = XT; A.wr = wr; A.wi = wi; A.wg = wg; A.wu = wu; A.Wgu = Wgu; A.wd = wd; A.Wd = Wd;
  A.gen = (u32*)(ws + WS_SYNC); A.arr = (u32*)(ws + WS_SYNC + 4096); A.gran = (unsigned long long*)(ws + WS_SYNC + 8192);
  k2_fwd<<<K2_GRID, 640, 0, stream>>>(A);
  k3_mid<<<168, 512, 0, stream>>>(XT, wr, wi, ZT);
  k4_inv<<<256, 640, 0, stream>>>(ZT, x, mod, x1, h2, Wgu);
  hipFuncSetAttribute((const void*)k5_glu, hipFuncAttributeMaxDynamicSharedMemorySize, K5_NS * K5_SLOT);
  k5_glu<<<256, 256, K5_NS * K5_SLOT, stream>>>(h2, Wgu, hb, Wd);
  hipFuncSetAttribute((const void*)k6_down, hipFuncAttributeMaxDynamicSharedMemorySize, 81920 + 256);
  k6_down<<<256, 256, 81920 + 256, stream>>>(hb, Wd, x1, mod, (u16*)d_out);
}
```

```cpp
#include <hip/hip_runtime.h>
#include <hip/hip_bf16.h>
#include <stdint.h>
#include <utility>

typedef unsigned short u16;
typedef unsigned int u32;
typedef short s16x4 __attribute__((ext_vector_type(4)));
typedef short s16x8 __attribute__((ext_vector_type(8)));
typedef __bf16 bf2_t __attribute__((ext_vector_type(2)));
typedef float f32x2 __attribute__((ext_vector_type(2)));
typedef float f32x4 __attribute__((ext_vector_type(4)));
typedef u32 u32x2 __attribute__((ext_vector_type(2)));
typedef u32 u32x4 __attribute__((ext_vector_type(4)));
#define DI __device__ __forceinline__
#define LDSP(T) __attribute__((address_space(3))) T*

constexpr int S_ = 2048, D_ = 640, F_ = 1728, DF = 321;

constexpr double kPi = 3.14159265358979323846264338327950288;
constexpr double c_sin0(double x) { double x2 = x * x, t = x, s = x; for (int i = 1; i < 11; ++i) { t *= -x2 / ((2.0 * i) * (2.0 * i + 1.0)); s += t; } return s; }
constexpr double c_cos0(double x) { double x2 = x * x, t = 1.0, s = 1.0; for (int i = 1; i < 11; ++i) { t *= -x2 / ((2.0 * i - 1.0) * (2.0 * i)); s += t; } return s; }
constexpr double c_cos(long n, long N) {
  n %= N; if (n < 0) n += N;
  if (2 * n > N) n = N - n;
  bool neg = false;
  if (4 * n > N) { neg = true; n = N - 2 * n; N = 2 * N; }
  double r = 0;
  if (4 * n == N) r = 0.0;
  else if (8 * n > N) r = c_sin0(2 * kPi * (double)(N - 4 * n) / (double)(4 * N));
  else r = c_cos0(2 * kPi * (double)n / (double)N);
  return neg ? -r : r;
}
constexpr double c_sin(long n, long N) { return c_cos(4 * n - N, 4 * N); }
constexpr u16 c_bf16(double v) {
  float f = (float)v; u32 u = __builtin_bit_cast(u32, f);
  u += 0x7fffu + ((u >> 16) & 1u); return (u16)(u >> 16);
}

struct F8w { u16 v[2][64][8]; };
struct F8x6 { u16 v[3][2][64][8]; };
constexpr F8w mk_f32a() {
  F8w t{};
  for (int tt = 0; tt < 2; ++tt) for (int l = 0; l < 64; ++l) for (int j = 0; j < 8; ++j) {
    int o = 16 * tt + (l & 15), d1 = 8 * (l >> 4) + j; double v = 0;
    if (o == 0) v = 1.0; else if (o == 1) v = (d1 & 1) ? -1.0 : 1.0;
    else { int k1 = o >> 1; v = (o & 1) ? -c_sin(d1 * k1, 32) : c_cos(d1 * k1, 32); }
    t.v[tt][l][j] = c_bf16(v);
  }
  return t;
}
constexpr F8x6 mk_f20(bool inv) {
  F8x6 t{};
  for (int mt = 0; mt < 3; ++mt) for (int ks = 0; ks < 2; ++ks) for (int l = 0; l < 64; ++l) for (int j = 0; j < 8; ++j) {
    int row = l & 15, g = l >> 4, k0 = 8 * mt + (row >> 1), part = row & 1, d0 = 16 * ks + 4 * g + (j >> 1), pp = j & 1; double v = 0;
    if (d0 < 20 && k0 < 20) {
      double co = c_cos(d0 * k0, 20), si = inv ? -c_sin(d0 * k0, 20) : c_sin(d0 * k0, 20);
      v = part == 0 ? (pp == 0 ? co : si) : (pp == 0 ? -si : co);
    }
    t.v[mt][ks][l][j] = c_bf16(v);
  }
  return t;
}
constexpr F8w mk_a1(int m1, bool inv) {
  F8w t{};
  for (int l = 0; l < 64; ++l) for (int i = 0; i < 4; ++i) {
    int c = l & 15, a = 4 * (l >> 4) + i, n = inv ? c * (8 * a + m1) : a * (8 * c + m1);
    double co = c_cos(n, 128), si = inv ? -c_sin(n, 128) : c_sin(n, 128);
    t.v[0][l][2 * i] = c_bf16(co); t.v[0][l][2 * i + 1] = c_bf16(si);
    t.v[1][l][2 * i] = c_bf16(-si); t.v[1][l][2 * i + 1] = c_bf16(co);
  }
  return t;
}
constexpr F8w mk_f16(bool inv) {
  F8w t{};
  for (int l = 0; l < 64; ++l) for (int i = 0; i < 4; ++i) {
    int c = l & 15, a = 4 * (l >> 4) + i;
    double co = c_cos(a * c, 16), si = c_sin(a * c, 16);
    if (!inv) { t.v[0][l][2 * i] = c_bf16(co); t.v[0][l][2 * i + 1] = c_bf16(si); t.v[1][l][2 * i] = c_bf16(-si); t.v[1][l][2 * i + 1] = c_bf16(co); }
    else { t.v[0][l][i] = c_bf16(co); t.v[0][l][4 + i] = c_bf16(-si); t.v[1][l][i] = c_bf16(si); t.v[1][l][4 + i] = c_bf16(co); }
  }
  return t;
}
constexpr F8w mk_f32bi() {
  F8w t{};
  for (int nt = 0; nt < 2; ++nt) for (int l = 0; l < 64; ++l) for (int j = 0; j < 8; ++j) {
    int d1 = 16 * nt + (l & 15), K = 8 * (l >> 4) + j; double v = 0;
    if (K == 0) v = 1.0; else if (K == 1) v = (d1 & 1) ? -1.0 : 1.0;
    else { int k1 = K >> 1; v = (K & 1) ? -2.0 * c_sin(k1 * d1, 32) : 2.0 * c_cos(k1 * d1, 32); }
    t.v[nt][l][j] = c_bf16(v);
  }
  return t;
}
static __device__ const F8w g_f32a = mk_f32a();
static __device__ const F8x6 g_f20a = mk_f20(false);
static __device__ const F8x6 g_f20ai = mk_f20(true);
static __device__ const F8w g_a1[8] = {mk_a1(0, false), mk_a1(1, false), mk_a1(2, false), mk_a1(3, false), mk_a1(4, false), mk_a1(5, false), mk_a1(6, false), mk_a1(7, false)};
static __device__ const F8w g_a1i[8] = {mk_a1(0, true), mk_a1(1, true), mk_a1(2, true), mk_a1(3, true), mk_a1(4, true), mk_a1(5, true), mk_a1(6, true), mk_a1(7, true)};
static __device__ const F8w g_f16f = mk_f16(false);
static __device__ const F8w g_f16i = mk_f16(true);
static __device__ const F8w g_f32bi = mk_f32bi();

DI float bf_lo(u32 v) { return __uint_as_float(v << 16); }
DI float bf_hi(u32 v) { return __uint_as_float(v & 0xffff0000u); }
DI float bf2f(u16 v) { return __uint_as_float(((u32)v) << 16); }
DI u32 pk_bf(float a, float b) { f32x2 f = {a, b}; return __builtin_bit_cast(u32, __builtin_convertvector(f, bf2_t)); }
DI float rbf(float a) { return bf_lo(pk_bf(a, a)); }
DI float dpp_x1(float v) { return __int_as_float(__builtin_amdgcn_mov_dpp(__float_as_int(v), 0xB1, 0xf, 0xf, true)); }
DI float dpp_x2(float v) { return __int_as_float(__builtin_amdgcn_mov_dpp(__float_as_int(v), 0x4E, 0xf, 0xf, true)); }
DI float dpp_hm(float v) { return __int_as_float(__builtin_amdgcn_mov_dpp(__float_as_int(v), 0x141, 0xf, 0xf, true)); }
DI f32x4 mfma16(s16x8 a, s16x8 b, f32x4 c) { return __builtin_amdgcn_mfma_f32_16x16x32_bf16(a, b, c, 0, 0, 0); }
DI s16x4 tr_read(const void* p) { return __builtin_amdgcn_ds_read_tr16_b64_v4i16((LDSP(s16x4))p); }
DI void batch_fence() { asm volatile("" ::: "memory"); }
template <class T, int N> DI void pin_arr(T (&v)[N]) {
#pragma unroll
  for (int i = 0; i < N; ++i) asm volatile("" : "+v"(v[i]));
}
template <class T> DI void pin(T& v) { asm volatile("" : "+v"(v)); }
DI void l2_touch(const void* base, u32 voff, u32 lds) {
  asm volatile("s_mov_b32 m0, %0\n\ts_nop 0\n\tglobal_load_lds_dword %1, %2" :: "s"(lds), "v"(voff), "s"(base) : "memory");
}
DI void side_delay(int ticks10ns) {
  const unsigned long long t0 = __builtin_amdgcn_s_memrealtime();
  while (__builtin_amdgcn_s_memrealtime() - t0 < (unsigned long long)ticks10ns) __builtin_amdgcn_s_sleep(4);
}
#define GSP(T) __attribute__((address_space(1))) T*
template <class T> DI T ldg(const void* sbase, u32 voff) { GSP(const char) p = (GSP(const char))sbase; asm("" : "+s"(p)); return *(GSP(const T))(p + voff); }
DI void stg_wt(void* sbase, u32 voff, u32x4 v) { asm volatile("global_store_dwordx4 %0, %1, %2 sc1\n\ts_nop 1" :: "v"(voff), "v"(v), "s"(sbase) : "memory"); }
DI void stg_wt(void* sbase, u32 voff, u32x2 v) { asm volatile("global_store_dwordx2 %0, %1, %2 sc1\n\ts_nop 1" :: "v"(voff), "v"(v), "s"(sbase) : "memory"); }
DI void stg_wt(void* sbase, u32 voff, u32 v) { asm volatile("global_store_dword %0, %1, %2 sc1" :: "v"(voff), "v"(v), "s"(sbase) : "memory"); }
#define CODE_ADDR(var, sym) asm volatile("s_getpc_b64 s[40:41]\n\ts_add_u32 s40, s40, " #sym "@rel32@lo+4\n\ts_addc_u32 s41, s41, " #sym "@rel32@hi+12\n\ts_mov_b64 %0, s[40:41]" : "=s"(var) :: "s40", "s41", "scc")
DI u32 lds_addr(const void* p) { return (u32)(size_t)(LDSP(const char))p; }
DI s16x8 cat8(s16x4 a, s16x4 b) { return __builtin_shufflevector(a, b, 0, 1, 2, 3, 4, 5, 6, 7); }
DI s16x8 as8(u32x4 v) { return __builtin_bit_cast(s16x8, v); }
DI float dot2bf(u32 a, u32 b, float c) { return __builtin_amdgcn_fdot2_f32_bf16(__builtin_bit_cast(bf2_t, a), __builtin_bit_cast(bf2_t, b), c, false); }

DI void ln_finish(float s, float q, float& m_, float& i_) {
  const float mean = s * (1.0f / 640.0f);
  const float var = fmaxf(q * (1.0f / 640.0f) - mean * mean, 0.f);
  const float sd = rbf(__builtin_amdgcn_sqrtf(rbf(rbf(var) + 1e-5f)));
  m_ = rbf(mean); i_ = __builtin_amdgcn_rcpf(sd);
}
DI float reduce8(const float (&v)[8], int lane) {
  float b[4], c[2], d;
  const bool s2 = lane & 4, s1 = lane & 2, s0 = lane & 1;
#pragma unroll
  for (int i = 0; i < 4; ++i) { float keep = s2 ? v[4 + i] : v[i], send = s2 ? v[i] : v[4 + i]; b[i] = keep + dpp_hm(send); }
#pragma unroll
  for (int i = 0; i < 2; ++i) { float keep = s1 ? b[2 + i] : b[i], send = s1 ? b[i] : b[2 + i]; c[i] = keep + dpp_x2(send); }
  { float keep = s0 ? c[1] : c[0], send = s0 ? c[0] : c[1]; d = keep + dpp_x1(send); }
  d += __int_as_float(__builtin_amdgcn_mov_dpp(__float_as_int(d), 0x128, 0xf, 0xf, true));
  auto r16 = __builtin_amdgcn_permlane16_swap(__float_as_uint(d), __float_as_uint(d), false, false);
  d = __uint_as_float(r16[0]) + __uint_as_float(r16[1]);
  auto r32 = __builtin_amdgcn_permlane32_swap(__float_as_uint(d), __float_as_uint(d), false, false);
  return __uint_as_float(r32[0]) + __uint_as_float(r32[1]);
}

template <int N> DI void wait_vm() { asm volatile("s_waitcnt vmcnt(%0)" ::"n"(N) : "memory"); }
template <int OFF> DI void gload16_s(u32x4& d, const void* sbase, u32 voff) {
  asm volatile("global_load_dwordx4 %0, %1, %2 offset:%3" : "=v"(d) : "v"(voff), "s"(sbase), "n"(OFF) : "memory");
}
template <int OFF> DI void glds16_s(const void* sbase, u32 voff) {
  asm volatile("global_load_lds_dwordx4 %0, %1 offset:%2" :: "v"(voff), "s"(sbase), "n"(OFF) : "memory");
}
DI void dma1k(const void* sbase, u32 voff, u32 lds) {
  asm volatile("s_mov_b32 m0, %0\n\ts_nop 0\n\tglobal_load_lds_dwordx4 %1, %2" :: "s"(lds), "v"(voff), "s"(sbase) : "memory");
}
DI void set_m0(u32 v) { asm volatile("s_mov_b32 m0, %0\n\ts_nop 0" :: "s"(v) : "memory"); }
DI void tie4(u32x4 (&t)[4]) { asm volatile("" : "+v"(t[0]), "+v"(t[1]), "+v"(t[2]), "+v"(t[3])); }

DI void prep_load(u32x4 (&v)[4], const u16* __restrict__ src, int ld, int lane) {
  const u32 vo = (u32)((lane >> 3) * ld + 8 * (lane & 7)) * 2u;
#pragma unroll
  for (int i = 0; i < 4; ++i) v[i] = ldg<u32x4>(src + (size_t)8 * i * ld, vo);
}
DI void prep_store(const u32x4 (&v)[4], u16* __restrict__ dst, int jstride, char* lds, int lane) {
#pragma unroll
  for (int i = 0; i < 4; ++i) {
    const int row = 8 * i + (lane >> 3), ch = lane & 7;
    *(u32x4*)(lds + row * 128 + (((ch >> 1) ^ ((row >> 1) & 3)) * 32) + (ch & 1) * 16) = v[i];
  }
  const int c = lane & 15, g = lane >> 4, q = c >> 2, p = c & 3, r = 4 * g + q;
#pragma unroll
  for (int j = 0; j < 4; ++j) {
    const char* b = lds + r * 128 + ((j ^ ((r >> 1) & 3)) * 32) + 8 * p;
    const s16x8 f = cat8(tr_read(b), tr_read(b + 16 * 128));
    stg_wt(dst + (size_t)j * jstride, (u32)lane * 16u, __builtin_bit_cast(u32x4, f));
  }
}
DI void prep_wave(const u16* __restrict__ src, int ld, u16* __restrict__ dst, int jstride, char* lds, int lane) {
  u32x4 v[4]; prep_load(v, src, ld, lane); prep_store(v, dst, jstride, lds, lane);
}
DI void prep_gu(int task, const u16* wg, const u16* wu, u16* Wgu, char* lds, int lane) {
  const int mat = task >= 540, tk = task - 540 * mat, cg = tk / 20, ks = tk % 20;
  prep_wave((mat ? wu : wg) + (size_t)ks * 32 * F_ + 64 * cg, F_, Wgu + ((size_t)(4 * cg * 20 + ks) * 2 + mat) * 512, 20 * 2 * 512, lds, lane);
}
DI void prep_d(int task, const u16* wd, u16* Wd, char* lds, int lane) {
  const int cg = task / 54, ks = task % 54;
  prep_wave(wd + (size_t)ks * 32 * D_ + 64 * cg, D_, Wd + (size_t)(4 * cg * 54 + ks) * 512, 54 * 512, lds, lane);
}


typedef __attribute__((address_space(1))) u32 gu32;
typedef __attribute__((address_space(1))) unsigned long long gu64;
DI u32 ld_agent(const u32* p) { return __hip_atomic_load((const gu32*)p, __ATOMIC_RELAXED, __HIP_MEMORY_SCOPE_AGENT); }
DI void st_agent(u32* p, u32 v) { __hip_atomic_store((gu32*)p, v, __ATOMIC_RELAXED, __HIP_MEMORY_SCOPE_AGENT); }
DI void st_agent64(unsigned long long* p, unsigned long long v) { __hip_atomic_store((gu64*)p, v, __ATOMIC_RELAXED, __HIP_MEMORY_SCOPE_AGENT); }
constexpr int K2_SIDE_DELAY = 80;
constexpr int SPIN_LIMIT = 1 << 16;

template <int NR> DI void gemv_part(int col0, int row0, u32 tagv, bool publish, unsigned long long* gran, int gidx0, const u16* __restrict__ c, const u16* __restrict__ wmod,
                                    float* __restrict__ mod, char* lds) {
  float* sl = (float*)lds; float* part = sl + 640;
  const int t = threadIdx.x, lane = t & 63, w = t >> 6, cq = t & 7, rg = t >> 3;
  const u16 cv = ldg<u16>(c, 2 * t);
  u32x4 wv[NR];
  const u32 wo = (u32)((row0 + rg) * 3840 + col0 + 8 * cq) * 2u;
#pragma unroll
  for (int i = 0; i < NR; ++i) wv[i] = ldg<u32x4>(wmod + (size_t)80 * i * 3840, wo);
  { const float x = bf2f(cv); sl[t] = rbf(x * __builtin_amdgcn_rcpf(1.0f + __expf(-x))); }
  __syncthreads();
  float acc[8] = {};
#pragma unroll
  for (int i = 0; i < NR; ++i) {
    const float s = sl[row0 + rg + 80 * i];
#pragma unroll
    for (int k = 0; k < 4; ++k) { acc[2 * k] += s * bf_lo(wv[i][k]); acc[2 * k + 1] += s * bf_hi(wv[i][k]); }
  }
  float u[4];
  const bool b3 = lane & 8;
#pragma unroll
  for (int i = 0; i < 4; ++i) { const float keep = b3 ? acc[4 + i] : acc[i], send = b3 ? acc[i] : acc[4 + i]; u[i] = keep + __int_as_float(__builtin_amdgcn_mov_dpp(__float_as_int(send), 0x128, 0xf, 0xf, true)); }
  float r2[2];
#pragma unroll
  for (int i = 0; i < 2; ++i) { auto x = __builtin_amdgcn_permlane16_swap(__float_as_uint(u[i]), __float_as_uint(u[i + 2]), false, false); r2[i] = __uint_as_float(x[0]) + __uint_as_float(x[1]); }
  auto y = __builtin_amdgcn_permlane32_swap(__float_as_uint(r2[0]), __float_as_uint(r2[1]), false, false);
  part[w * 64 + 8 * cq + 4 * ((lane >> 3) & 1) + 2 * ((lane >> 4) & 1) + (lane >> 5)] = __uint_as_float(y[0]) + __uint_as_float(y[1]);
  __syncthreads();
  if (t < 64) {
    float s = 0.f;
#pragma unroll
    for (int i = 0; i < 10; ++i) s += part[i * 64 + t];
    if (publish) st_agent64(gran + gidx0 + t, ((unsigned long long)(tagv + 1u) << 32) | __float_as_uint(s));
    else mod[col0 + t] = rbf(s);
  }
}

DI int row_group(int b) { return 2 * (8 * (b >> 4) + (b & 7)) + ((b >> 3) & 1); }
DI void sincos_rev(float a, float& s, float& c) { s = __builtin_amdgcn_sinf(a); c = __builtin_amdgcn_cosf(a); }
DI void gram_part(const u32x4 (&f)[4], float* part  , int i, int lane) {
  f32x4 acc = {0.f, 0.f, 0.f, 0.f};
#pragma unroll
  for (int k = 0; k < 4; ++k) acc = mfma16(as8(f[k]), as8(f[k]), acc);
  const int c = lane & 15, g = lane >> 4;
  const float dg = (c & 2) ? ((c & 1) ? acc[3] : acc[2]) : ((c & 1) ? acc[1] : acc[0]);
  if (c < 8 && g == (c >> 2)) part[i * 16 + 8 + c] = dg;
  if (c < 8 && g == 2) part[i * 16 + c] = acc[0];
}
DI void ln_bcast8(const float* part, int lane, float (&a_)[8], float (&b_)[8]) {
  const int r = lane & 7;
  float ps[5], pq[5];
#pragma unroll
  for (int i = 0; i < 5; ++i) { ps[i] = part[i * 16 + r]; pq[i] = part[i * 16 + 8 + r]; }
  batch_fence(); pin_arr(ps); pin_arr(pq);
  const float s = ((ps[0] + ps[1]) + (ps[2] + ps[3])) + ps[4], q = ((pq[0] + pq[1]) + (pq[2] + pq[3])) + pq[4];
  float m_, i_; ln_finish(s, q, m_, i_);
  const float nb = -m_ * i_;
#pragma unroll
  for (int i = 0; i < 8; ++i) {
    a_[i] = __int_as_float(__builtin_amdgcn_readlane(__float_as_int(i_), i));
    b_[i] = __int_as_float(__builtin_amdgcn_readlane(__float_as_int(nb), i));
  }
}
constexpr int HT_RS = 1296;
struct K2Args { const u16* x; const u16* c; const u16* wmod; float* mod; u32* XT; const u16* wr; const u16* wi; const u16* wg; const u16* wu; u16* Wgu; const u16* wd; u16* Wd; u32* gen; u32* arr; unsigned long long* gran; };
constexpr int K2_MAIN0 = 88, K2_SIDE0 = 344, K2_GRID = 344 + 54;
extern "C" __global__ __launch_bounds__(640) void k2_fwd(const K2Args A) {
  __shared__ __attribute__((aligned(16))) char smem[32768 + 5 * 4096 + 256];
  const int bid = blockIdx.x;
  if (bid >= K2_SIDE0) {
    side_delay(K2_SIDE_DELAY);
    const int wv = __builtin_amdgcn_readfirstlane(threadIdx.x >> 6);
    prep_d((bid - K2_SIDE0) * 10 + wv, A.wd, A.Wd, smem + 4096 * wv, threadIdx.x & 63);
    return;
  }
  if (bid >= 40 && bid < 80) {
    side_delay(K2_SIDE_DELAY);
    gemv_part<8>(1280 + 64 * (bid - 40), 0, 0u, false, nullptr, 0, A.c, A.wmod, A.mod, smem); return;
  }
  if (bid >= 80 && bid < 88) {
    const void* c3; const void* c7;
    CODE_ADDR(c3, k3_mid); CODE_ADDR(c7, k7_end);
    const u32 len = (u32)((const char*)c7 - (const char*)c3);
    const u32 off = threadIdx.x * 128u;
    if (off < len) l2_touch(c3, off, lds_addr(smem) + __builtin_amdgcn_readfirstlane(threadIdx.x >> 6) * 256);
    asm volatile("s_waitcnt vmcnt(0)" ::: "memory");
    if (bid < 87) return;
  }
  if (bid < 40) {
    const u32 tagv = ld_agent(A.gen);
    gemv_part<4>(64 * (bid >> 1), 320 * (bid & 1), tagv, true, A.gran, 1280 * (bid & 1) + 64 * (bid >> 1), A.c, A.wmod, A.mod, smem);
    if (threadIdx.x == 0) st_agent(A.arr + bid, tagv + 1u);
    return;
  }
  if (bid == 87) {
    const u32 tag = ld_agent(A.gen) + 1u;
    const int t = threadIdx.x, who = t < 40 ? t : t + 48;
    for (int spin = 0; spin < SPIN_LIMIT; ++spin) {
      const bool ok = t >= 296 || ld_agent(A.arr + who) == tag;
      if (__syncthreads_and(ok)) break;
      __builtin_amdgcn_s_sleep(8);
    }
    if (t == 0) st_agent(A.gen, tag);
    return;
  }
  u32* __restrict__ XT = A.XT;
  char* hT = smem; char* U = smem + 8 * HT_RS; float* part = (float*)(smem + 8 * HT_RS + 8 * 17 * 80);
  char* tab = smem + 8 * HT_RS + 8 * 17 * 80 + 5 * 16 * 4;
  const int tid = threadIdx.x, lane = tid & 63, w = __builtin_amdgcn_readfirstlane(tid >> 6);
  const int s0 = row_group(bid - K2_MAIN0);
  const int c = lane & 15, g = lane >> 4, q = c >> 2, p = c & 3;
  const u16* xrow = A.x + (size_t)s0 * D_;
  f32x4 tw0, tw1; u32 ard, awr, aw0, acr; float z0r, z0i;
  auto precompute = [&]() {
    const int Qw = 4 * w + q, rho = Qw / 5, d0 = 4 * (Qw % 5) + p;
    const bool rot = rho >= 3 && (rho & 1);
    const float fd = (float)d0 * (1.0f / 640.0f);
    const int k10 = g == 0 ? 16 : 2 * g;
    float s_[4], c_[4];
    sincos_rev(fd * (float)k10, s_[0], c_[0]); sincos_rev(fd * (float)(2 * g + 1), s_[1], c_[1]);
    sincos_rev(fd * (float)(8 + 2 * g), s_[2], c_[2]); sincos_rev(fd * (float)(9 + 2 * g), s_[3], c_[3]);
    tw0 = f32x4{rot ? s_[0] : c_[0], rot ? c_[0] : -s_[0], rot ? s_[1] : c_[1], rot ? c_[1] : -s_[1]};
    tw1 = f32x4{rot ? s_[2] : c_[2], rot ? c_[2] : -s_[2], rot ? s_[3] : c_[3], rot ? c_[3] : -s_[3]};
    const int Qr = 4 * w + p;
    ard = lds_addr(hT) + (Qr / 5) * HT_RS + 2 * (20 * (8 * g + q) + 4 * (Qr % 5));
    awr = lds_addr(U) + 4 * (rho * 17 * 20 + d0 + 2 * g * 20);
    aw0 = g == 0 ? awr + 4 * 16 * 20 : awr;
    z0r = rot ? 0.f : 1.f; z0i = rot ? 1.f : 0.f;
    acr = lds_addr(U) + (w < 8 ? ((2 * (w >> 1)) * 17 + c) * 80 : ((c & 7) * 17 + 16) * 80);
    pin(tw0); pin(tw1); pin(ard); pin(awr); pin(aw0); pin(z0r); pin(z0i); pin(acr);
  };
  if (w < 5) {
    u32 xv[8];
#pragma unroll
    for (int r = 0; r < 8; ++r) xv[r] = ldg<u32>(xrow + (size_t)r * 256 * D_, 4 * tid);
    const u32 tagv = ld_agent(A.gen);
    precompute();
    __syncthreads();
    float na[8], nb[8];
    ln_bcast8(part, lane, na, nb);
    f32x2 o[8];
    {
      f32x2 h[8];
#pragma unroll
      for (int r = 0; r < 8; ++r) { f32x2 v = {bf_lo(xv[r]), bf_hi(xv[r])}; h[r] = v * na[r] + nb[r]; }
      const float cc = 0.70710678118654752f;
      f32x2 a0 = h[0] + h[4], a1 = h[1] + h[5], a2 = h[2] + h[6], a3 = h[3] + h[7];
      f32x2 b0 = h[0] - h[4], b1 = h[1] - h[5], b2 = h[2] - h[6], b3 = h[3] - h[7];
      f32x2 e = a0 + a2, f = a1 + a3, u = cc * (b1 - b3), v = cc * (b1 + b3);
      o[0] = e + f; o[1] = e - f; o[2] = b0 + u; o[3] = -b2 - v; o[4] = a0 - a2; o[5] = a3 - a1; o[6] = b0 - u; o[7] = b2 - v;
#pragma unroll
      for (int r = 0; r < 8; ++r) pin(o[r]);
    }
    const u32 tag = __builtin_amdgcn_readfirstlane(tagv) + 1u;
    if (tid == 0) st_agent(A.arr + bid, tag);
    f32x2 sa, gg;
    {
      const unsigned long long* ga_ = A.gran + 2 * tid;
      u32x4 va, vb, vc, vd;
      for (int spin = 0; spin < SPIN_LIMIT; ++spin) {
        asm volatile("global_load_dwordx4 %0, %4, off sc1\n\tglobal_load_dwordx4 %1, %5, off sc1\n\tglobal_load_dwordx4 %2, %6, off sc1\n\tglobal_load_dwordx4 %3, %7, off sc1\n\ts_waitcnt vmcnt(0)"
                     : "=&v"(va), "=&v"(vb), "=&v"(vc), "=&v"(vd) : "v"(ga_), "v"(ga_ + 1280), "v"(ga_ + 640), "v"(ga_ + 1920) : "memory");
        const bool ok = va[1] == tag && va[3] == tag && vb[1] == tag && vb[3] == tag && vc[1] == tag && vc[3] == tag && vd[1] == tag && vd[3] == tag;
        if (__all(ok)) break;
      }
      sa = f32x2{rbf(__uint_as_float(va[0]) + __uint_as_float(vb[0])), rbf(__uint_as_float(va[2]) + __uint_as_float(vb[2]))};
      gg = f32x2{rbf(1.0f + rbf(__uint_as_float(vc[0]) + __uint_as_float(vd[0]))), rbf(1.0f + rbf(__uint_as_float(vc[2]) + __uint_as_float(vd[2])))};
    }
    o[0] = o[0] * gg + 8.0f * sa;
    *(u32*)(hT + 4 * tid) = pk_bf(o[0][0], o[0][1]);
#pragma unroll
    for (int r = 1; r < 8; ++r) { const f32x2 v = o[r] * gg; *(u32*)(hT + r * HT_RS + 4 * tid) = pk_bf(v[0], v[1]); }
  } else {
    const int i = w - 5;
    u32x4 f[4];
    const u32 xo = (u32)((c & 7) * 256 * D_ + 128 * i + 8 * g) * 2u;
#pragma unroll
    for (int k = 0; k < 4; ++k) f[k] = ldg<u32x4>(xrow, xo + 64 * k);
    const s16x8 t0 = i < 2 ? ldg<s16x8>(g_f32a.v[i < 2 ? i : 0], lane * 16) : ldg<s16x8>(&g_f20a.v[0][0][0][0] + (i - 2) * 512, lane * 16);
    const s16x8 t1 = ldg<s16x8>(&g_f20a.v[0][0][0][0] + (i < 3 ? i + 3 : 5) * 512, lane * 16);
    precompute();
    if (c >= 8) {
      const u32 fill = c == 8 ? 0x3f803f80u : 0u;
#pragma unroll
      for (int k = 0; k < 4; ++k) f[k] = u32x4{fill, fill, fill, fill};
    }
    gram_part(f, part, i, lane);
    *(s16x8*)(tab + i * 1024 + lane * 16) = t0;
    if (i < 3) *(s16x8*)(tab + (i + 5) * 1024 + lane * 16) = t1;
    __syncthreads();
    const int task = (bid - K2_MAIN0) * 5 + i;
    if (task < 1080) prep_gu(task, A.wg, A.wu, A.Wgu, smem + 32768 + 4096 * i, lane);
    if (i == 4) {
      const int xc = bid & 7, li = (bid - K2_MAIN0) >> 3;
#pragma unroll
      for (int k = 0; k < 4; ++k) {
        const int e = (k & 1) * 64 + lane, id = li * 96 + e;
        const int ln = id % 6, row = 8 * ((id / 6) & 255) + (id < 1536 ? xc : ((8 - xc) & 7));
        const u32 off = min((u32)(row * 642 + ln * 128), (u32)(2048 * 642 - 4)) & ~3u;
        if (e < 96) l2_touch((k >> 1) ? A.wi : A.wr, off, lds_addr(smem) + sizeof(smem) - 256);
      }
    }
  }
  __syncthreads();
  {
    const LDSP(const char) base = (LDSP(const char))(size_t)ard;
    s16x8 b = cat8(__builtin_amdgcn_ds_read_tr16_b64_v4i16((LDSP(s16x4))base), __builtin_amdgcn_ds_read_tr16_b64_v4i16((LDSP(s16x4))(base + 160)));
    s16x8 fa0 = *(const s16x8*)(tab + lane * 16), fa1 = *(const s16x8*)(tab + 1024 + lane * 16);
    batch_fence(); pin(b); pin(fa0); pin(fa1);
    const f32x4 z = {0.f, 0.f, 0.f, 0.f};
    f32x4 a0 = mfma16(fa0, b, z), a1 = mfma16(fa1, b, z);
    LDSP(u32) Ub = (LDSP(u32))(size_t)awr;
    const float x0 = g == 0 ? a0[1] : a0[0], x1 = g == 0 ? 0.f : a0[1];
    *(LDSP(u32))(size_t)aw0 = pk_bf(x0 * tw0[0] - x1 * tw0[1], x0 * tw0[1] + x1 * tw0[0]);
    if (g == 0) Ub[0] = pk_bf(a0[0] * z0r, a0[0] * z0i);
    Ub[20] = pk_bf(a0[2] * tw0[2] - a0[3] * tw0[3], a0[2] * tw0[3] + a0[3] * tw0[2]);
    Ub[8 * 20] = pk_bf(a1[0] * tw1[0] - a1[1] * tw1[1], a1[0] * tw1[1] + a1[1] * tw1[0]);
    Ub[9 * 20] = pk_bf(a1[2] * tw1[2] - a1[3] * tw1[3], a1[2] * tw1[3] + a1[3] * tw1[2]);
  }
  __syncthreads();
  const f32x4 z4 = {0.f, 0.f, 0.f, 0.f};
  u32* XTb = XT + (size_t)s0 * 16;
  const char* ft = tab + 2048 + lane * 16;
  const LDSP(const char) be = (LDSP(const char))(size_t)acr;
  const int P = w >> 1, mp = P, mm = P == 0 ? 4 : 8 - P;
  if (w < 8) {
    s16x8 e0 = *(const LDSP(s16x8))(be + 16 * g), e1 = *(const LDSP(s16x8))(be + 64), o0 = *(const LDSP(s16x8))(be + 1360 + 16 * g), o1 = *(const LDSP(s16x8))(be + 1360 + 64);
    char* dp = (char*)(XTb + (size_t)(mp * 21) * 4096); char* dm = (char*)(XTb + (size_t)(mm * 21) * 4096);
    const u32 vo = (u32)(2 * g * 4096 + c) * 4u;
    if (!(w & 1)) {
      s16x8 fm[4];
#pragma unroll
      for (int k = 0; k < 4; ++k) fm[k] = *(const s16x8*)(ft + k * 1024);
      batch_fence(); pin(e0); pin(e1); pin(o0); pin(o1); pin_arr(fm);
#pragma unroll
      for (int mt = 0; mt < 2; ++mt) {
        const s16x8 f0 = fm[2 * mt], f1 = fm[2 * mt + 1];
        f32x4 ae = mfma16(f1, e1, mfma16(f0, e0, z4)), ao = mfma16(f1, o1, mfma16(f0, o0, z4));
        f32x4 tp = ae + ao, tm = ae - ao;
        if (P == 0) { tp = ae; tm = ao; }
        char* ep = dp + (size_t)(8 * mt) * 16384; char* em = dm + (size_t)(8 * mt) * 16384;
        const int k0 = 8 * mt + 2 * g;
        { const float d0_ = (c == 0 && k0 >= 1 && k0 <= 9) ? 2.f : 1.f, d1_ = (c == 0 && k0 + 1 <= 9) ? 2.f : 1.f;
          const f32x4 dd = {d0_, d0_, d1_, d1_}; tp = tp * dd; tm = tm * dd; }
        if (mt == 0 || c != 0 || k0 <= 10) { stg_wt(ep, vo, pk_bf(tp[0], tp[1])); stg_wt(em, vo, pk_bf(tm[0], tm[1])); }
        if (mt == 0 || c != 0 || k0 + 1 <= 10) { stg_wt(ep + 16384, vo, pk_bf(tp[2], tp[3])); stg_wt(em + 16384, vo, pk_bf(tm[2], tm[3])); }
      }
    } else {
      s16x8 f0 = *(const s16x8*)(ft + 4 * 1024), f1 = *(const s16x8*)(ft + 5 * 1024);
      batch_fence(); pin(e0); pin(e1); pin(o0); pin(o1); pin(f0); pin(f1);
      f32x4 ae = mfma16(f1, e1, mfma16(f0, e0, z4)), ao = mfma16(f1, o1, mfma16(f0, o0, z4));
      f32x4 tp = ae + ao, tm = ae - ao;
      if (P == 0) { tp = ae; tm = ao; }
      if (g < 2 && c != 0) {
        char* ep = dp + (size_t)16 * 16384; char* em = dm + (size_t)16 * 16384;
        stg_wt(ep, vo, pk_bf(tp[0], tp[1])); stg_wt(ep + 16384, vo, pk_bf(tp[2], tp[3]));
        stg_wt(em, vo, pk_bf(tm[0], tm[1])); stg_wt(em + 16384, vo, pk_bf(tm[2], tm[3]));
      }
    }
  } else {
    const int rh = c & 7, mt = w - 7;
    s16x8 b0 = *(const LDSP(s16x8))(be + 16 * g), b1 = *(const LDSP(s16x8))(be + 64);
    s16x8 f0 = *(const s16x8*)(ft + (2 * mt) * 1024), f1 = *(const s16x8*)(ft + (2 * mt + 1) * 1024);
    batch_fence(); pin(b0); pin(b1); pin(f0); pin(f1);
    f32x4 a = mfma16(f1, b1, mfma16(f0, b0, z4));
    const float ca_ = (rh >= 2 && (rh & 1)) ? -1.f : 1.f, cb_ = rh >= 2 ? 1.f : 0.f;
    f32x4 t;
#pragma unroll
    for (int r = 0; r < 4; ++r) t[r] = ca_ * a[r] + cb_ * dpp_x1(a[r]);
    const int m1 = rh == 0 ? 0 : rh == 1 ? 4 : (rh & 1) ? 8 - (rh >> 1) : (rh >> 1);
    const int k0 = 8 * mt + 2 * g;
    const u32 o0 = (u32)((m1 * 21 + (k0 == 10 ? 20 : k0)) * 4096) * 4u, o1 = (u32)((m1 * 21 + k0 + 1) * 4096) * 4u;
    if (c < 8 && k0 >= 10 && k0 < 20) { stg_wt(XTb, o0, pk_bf(t[0], t[1])); stg_wt(XTb, o1, pk_bf(t[2], t[3])); }
    if (w == 9) asm volatile("s_waitcnt vmcnt(0)" ::: "memory");
  }
}

constexpr int EX_RS = 20;
extern "C" __global__ __launch_bounds__(512) void k3_mid(const u32* __restrict__ XT, const u16* __restrict__ wr, const u16* __restrict__ wi, u32* __restrict__ ZT) {
  __shared__ __attribute__((aligned(16))) u32 ex[2][256 * EX_RS];
  __shared__ __attribute__((aligned(16))) char tab[8 * 1024];
  const int tid = threadIdx.x, lane = tid & 63, w = __builtin_amdgcn_readfirstlane(tid >> 6), c = lane & 15, g = lane >> 4;
  const int m1 = blockIdx.x & 7, tile = blockIdx.x >> 3;
  const char* in = (const char*)(XT + (size_t)(m1 * 21 + tile) * 4096);
  dma1k(in + 2048 * w, (u32)lane * 16u, lds_addr(ex[1]) + 2048 * w);
  dma1k(in + 2048 * w + 1024, (u32)lane * 16u, lds_addr(ex[1]) + 2048 * w + 1024);
  const F8w* tsrc = w < 2 ? &g_a1[m1] : w < 4 ? &g_f16f : w < 6 ? &g_f16i : &g_a1i[m1];
  const s16x8 tfrag = ldg<s16x8>(tsrc->v[w & 1], lane * 16);
  u32 wra[2][4], wia[2][4];
  const int kb = tile == 20 ? 336 : (c == 0 && tile >= 11) ? 32 * tile + 16 : 32 * tile + c;
  const bool mir = kb > 320;
  {
    const int kk = mir ? 640 - kb : kb;
#pragma unroll
    for (int j = 0; j < 2; ++j)
#pragma unroll
      for (int r = 0; r < 4; ++r) {
        const int m = m1 + 8 * (2 * w + j) + 128 * (4 * g + r), mm = mir ? ((2048 - m) & 2047) : m;
        const u32 vo = (u32)(mm * DF + kk) * 2u;
        wra[j][r] = (u32)ldg<u16>(wr, vo) << 16; wia[j][r] = (u32)ldg<u16>(wi, vo) << 16;
      }
  }
  f32x4 fcs[2], fsn[2], ics[2], isn[2];
  {
#pragma unroll
    for (int j = 0; j < 2; ++j) {
      float s0_, c0_, s1_, c1_;
      sincos_rev((float)((2 * w + j) * (m1 + 32 * g)) * (1.0f / 2048), s0_, c0_); sincos_rev((float)(2 * w + j) * (1.0f / 256), s1_, c1_);
#pragma unroll
      for (int r = 0; r < 4; ++r) { fcs[j][r] = c0_; fsn[j][r] = s0_; const float nc = c0_ * c1_ - s0_ * s1_, ns = s0_ * c1_ + c0_ * s1_; c0_ = nc; s0_ = ns; }
      const int M = m1 + 16 * w + 8 * j;
      sincos_rev((float)(4 * g * M) * (1.0f / 2048), s0_, c0_); sincos_rev((float)M * (1.0f / 2048), s1_, c1_);
#pragma unroll
      for (int r = 0; r < 4; ++r) { ics[j][r] = c0_; isn[j][r] = s0_; const float nc = c0_ * c1_ - s0_ * s1_, ns = s0_ * c1_ + c0_ * s1_; c0_ = nc; s0_ = ns; }
    }
  }
  u32 awr = lds_addr(ex) + 4 * ((4 * g * 16 + c) * EX_RS + 2 * w);
  u32 ard = lds_addr(ex) + 4 * ((2 * w * 16 + c) * EX_RS + 4 * g);
#pragma unroll
  for (int j = 0; j < 2; ++j) { pin(fcs[j]); pin(fsn[j]); pin(ics[j]); pin(isn[j]); }
  pin(awr); pin(ard);
  *(s16x8*)(tab + w * 1024 + lane * 16) = tfrag;
  wait_vm<16>();
  __syncthreads();
  u32x4 bx[2];
#pragma unroll
  for (int j = 0; j < 2; ++j)
#pragma unroll
    for (int i = 0; i < 4; ++i) bx[j][i] = ex[1][(16 * (4 * g + i) + 2 * w + j) * 16 + c];
  s16x8 A10 = *(const s16x8*)(tab + lane * 16), A11 = *(const s16x8*)(tab + lane * 16 + 1024);
  batch_fence(); pin_arr(bx); pin(A10); pin(A11);
  const f32x4 z4 = {0.f, 0.f, 0.f, 0.f};
  const char* tb = tab + lane * 16;
  const LDSP(char) wr0 = (LDSP(char))(size_t)awr; const LDSP(const char) rd0 = (LDSP(const char))(size_t)ard;
  constexpr int RB = 16 * EX_RS * 4, XB = 256 * EX_RS * 4;
  {
    u32 pk[2][4];
#pragma unroll
    for (int j = 0; j < 2; ++j) {
      f32x4 re = mfma16(A10, as8(bx[j]), z4), im = mfma16(A11, as8(bx[j]), z4);
      f32x4 tr = re * fcs[j] + im * fsn[j], ti = im * fcs[j] - re * fsn[j];
#pragma unroll
      for (int r = 0; r < 4; ++r) pk[j][r] = pk_bf(tr[r], ti[r]);
    }
#pragma unroll
    for (int r = 0; r < 4; ++r) *(LDSP(u32x2))(wr0 + r * RB) = u32x2{pk[0][r], pk[1][r]};
  }
  __syncthreads();
  {
    u32x4 bj[2] = {*(const LDSP(u32x4))(rd0), *(const LDSP(u32x4))(rd0 + RB)};
    s16x8 F0 = *(const s16x8*)(tb + 2048), F1 = *(const s16x8*)(tb + 3072), G0 = *(const s16x8*)(tb + 4096), G1 = *(const s16x8*)(tb + 5120);
    batch_fence(); pin_arr(bj); pin(F0); pin(F1); pin(G0); pin(G1);
    u32 pk[2][4];
#pragma unroll
    for (int j = 0; j < 2; ++j) {
      const u32x4 b = bj[j];
      f32x4 xr = mfma16(F0, as8(b), z4), xi = mfma16(F1, as8(b), z4);
      const f32x4 a = {__uint_as_float(wra[j][0]), __uint_as_float(wra[j][1]), __uint_as_float(wra[j][2]), __uint_as_float(wra[j][3])};
      const u32 sg = mir ? 0x80000000u : 0u;
      const f32x4 bb = {__uint_as_float(wia[j][0] ^ sg), __uint_as_float(wia[j][1] ^ sg), __uint_as_float(wia[j][2] ^ sg), __uint_as_float(wia[j][3] ^ sg)};
      const f32x4 yr = xr * a - xi * bb, yi = xr * bb + xi * a;
      const u32x4 yb = {pk_bf(yr[0], yr[1]), pk_bf(yr[2], yr[3]), pk_bf(yi[0], yi[1]), pk_bf(yi[2], yi[3])};
      f32x4 re = mfma16(G0, as8(yb), z4), im = mfma16(G1, as8(yb), z4);
      f32x4 tr = re * ics[j] - im * isn[j], ti = im * ics[j] + re * isn[j];
#pragma unroll
      for (int r = 0; r < 4; ++r) pk[j][r] = pk_bf(tr[r], ti[r]);
    }
#pragma unroll
    for (int r = 0; r < 4; ++r) *(LDSP(u32x2))(wr0 + XB + r * RB) = u32x2{pk[0][r], pk[1][r]};
  }
  __syncthreads();
  char* out = (char*)(ZT + (size_t)(2 * w) * 8 * 21 * 16 + (m1 * 21 + tile) * 16);
  const u32 vout = (u32)(64 * g * 10752 + c * 4);
  u32x4 bz[2] = {*(const LDSP(u32x4))(rd0 + XB), *(const LDSP(u32x4))(rd0 + XB + RB)};
  s16x8 I0 = *(const s16x8*)(tb + 6144), I1 = *(const s16x8*)(tb + 7168);
  batch_fence(); pin_arr(bz); pin(I0); pin(I1);
#pragma unroll
  for (int j = 0; j < 2; ++j) {
    const u32x4 b = bz[j];
    f32x4 zr = mfma16(I0, as8(b), z4), zi = mfma16(I1, as8(b), z4);
#pragma unroll
    for (int r = 0; r < 4; ++r) stg_wt(out + (16 * r + j) * 10752, vout, pk_bf(zr[r], zi[r]));
  }
}

constexpr int Y_RS = 640 + 4;
extern "C" __global__ __launch_bounds__(640) void k4_inv(const u32* __restrict__ ZT, const u16* __restrict__ x, const float* __restrict__ mod,
                                              u16* __restrict__ x1o, u16* __restrict__ h2o) {
  __shared__ __attribute__((aligned(16))) char smem[162 * 64 + 8 * Y_RS * 4 + 10 * 8 * 4 + 8 * 1024 + 10752 + 256];
  char* Vs = smem; float* Y = (float*)(Vs + 162 * 64); float* part = Y + 8 * Y_RS; char* tab = (char*)(part + 80);
  const int tid = threadIdx.x, lane = tid & 63, w = __builtin_amdgcn_readfirstlane(tid >> 6), c = lane & 15, g = lane >> 4;
  const int s0 = row_group(blockIdx.x);
  u32* Zs = (u32*)(tab + 8 * 1024);
  {
    const char* Zb = (const char*)(ZT + (size_t)s0 * 8 * 21 * 16);
    dma1k(Zb + 1024 * w, (u32)lane * 16u, lds_addr(Zs) + 1024 * w);
    if (w == 0 && lane < 32) dma1k(Zb + 10240, (u32)lane * 16u, lds_addr(Zs) + 10240);
  }
  const int rho = w < 8 ? w : (c & 7), P = rho >> 1, mA = rho == 1 ? 4 : P, mB = P == 0 ? 4 : 8 - P;
  const s16x8 tfrag = w < 6 ? ldg<s16x8>(&g_f20ai.v[0][0][0][0] + w * 512, lane * 16) : ldg<s16x8>(g_f32bi.v[(w - 6) & 1], lane * 16);
  const int hf = w >= 5, cp = tid - 320 * hf;
  u32 xv[4];
#pragma unroll
  for (int r = 0; r < 4; ++r) xv[r] = ldg<u32>(x + (size_t)(s0 + 256 * (4 * hf + r)) * D_, 4 * cp);
  const f32x2 ga = ldg<f32x2>(mod + 2 * 640, 8 * cp), sb = ldg<f32x2>(mod + 3 * 640, 8 * cp), cb = ldg<f32x2>(mod + 4 * 640, 8 * cp);
  f32x4 tw[3]; u32 aw[3];
  {
    const bool rot = rho >= 3 && (rho & 1);
    const float sc = w < 8 ? 1.0f / (2048.0f * 640.0f) : 2.0f / (2048.0f * 640.0f), fk = (float)(w < 8 ? c : 16) * (1.0f / 640.0f);
    float s0_, c0_, s1_, c1_, s8_, c8_;
    sincos_rev(fk * (float)(2 * g), s0_, c0_); sincos_rev(fk, s1_, c1_); sincos_rev(fk * 8.0f, s8_, c8_);
    float br = (rot ? -s0_ : c0_) * sc, bi = (rot ? c0_ : s0_) * sc;
#pragma unroll
    for (int mt = 0; mt < 3; ++mt) {
      tw[mt] = f32x4{br, bi, br * c1_ - bi * s1_, br * s1_ + bi * c1_};
      const float nr = br * c8_ - bi * s8_, ni = br * s8_ + bi * c8_; br = nr; bi = ni;
      const int d0 = 8 * mt + 2 * g;
      const bool ok = d0 < 20 && (w < 8 || c < 8);
      aw[mt] = lds_addr(Vs) + (ok ? (rho * 20 + d0) * 64 : 160 * 64) + (w < 8 ? 4 * c : 2);
      pin(tw[mt]); pin(aw[mt]);
    }
  }
  float be = rho < 2 ? 0.f : (rho & 1) ? -1.f : 1.f;
  u32 za0, za1, dbits, zdel = (u32)(mB - mA) * 21u * 64u;
  {
    const u32 zb = lds_addr(Zs) + mA * 21 * 64;
    if (w < 8) { za0 = zb + (64 * g + c) * 4; za1 = zb + 16 * 64 + c * 4; dbits = c == 0 ? (0xF0u | (g == 2 ? 8u : g == 3 ? 15u : 0u)) : 0u; }
    else {
      za0 = zb + (g == 3 ? 12 : 11 - 3) * 64; za1 = zb + 16 * 64; dbits = (g < 2 ? 0xFu : g == 2 ? 3u : 0u) | (g == 0 ? 0u : 0xF0u);
    }
  }
  const u32 z10 = lds_addr(Zs) + (mA * 21 + 20) * 64;
  pin(be); pin(za0); pin(za1); pin(dbits); pin(zdel);
  if (w == 9 && blockIdx.x < 8) {
    const void* c5; const void* c6; CODE_ADDR(c5, k5_glu); CODE_ADDR(c6, k6_down);
    const u32 len = (u32)((const char*)c6 - (const char*)c5);
    if (lane * 128u < len) l2_touch(c5, (u32)lane * 128u, lds_addr(smem) + sizeof(smem) - 256);
    if (lane * 128u + 8192u < len) l2_touch(c5, (u32)lane * 128u + 8192u, lds_addr(smem) + sizeof(smem) - 256);
  }
  if (w < 8) *(s16x8*)(tab + w * 1024 + lane * 16) = tfrag;
  wait_vm<4>();
  __syncthreads();
  if (w < 9) {
    u32 zz[8], zy[8]; s16x8 ft[6];
#pragma unroll
    for (int i = 0; i < 8; ++i) {
      u32 ad = (i < 4 ? za0 : za1) + 64 * (i & 3);
      if (i == 2) ad = (w == 8 && g == 2) ? z10 : ad;
      zz[i] = *(const LDSP(u32))(size_t)ad; zy[i] = *(const LDSP(u32))(size_t)(ad + zdel);
    }
#pragma unroll
    for (int k = 0; k < 6; ++k) ft[k] = *(const s16x8*)(tab + k * 1024 + lane * 16);
    batch_fence(); pin_arr(zz); pin_arr(zy); pin_arr(ft);
    u32x4 b0, b1;
#pragma unroll
    for (int i = 0; i < 8; ++i) {
      const u32 keep = ~(u32)__builtin_amdgcn_sbfe((int)dbits, i, 1);
      const u32 p_ = zz[i] & keep, q_ = zy[i] & keep;
      const f32x2 a = {bf_lo(p_), bf_hi(p_)}, b = {bf_lo(q_), bf_hi(q_)};
      const f32x2 v = a + be * b;
      if (i < 4) b0[i] = pk_bf(v[0], v[1]); else b1[i - 4] = pk_bf(v[0], v[1]);
    }
    f32x4 a[3];
#pragma unroll
    for (int mt = 0; mt < 3; ++mt) {
      const f32x4 z = {0.f, 0.f, 0.f, 0.f};
      a[mt] = mfma16(ft[2 * mt + 1], as8(b1), mfma16(ft[2 * mt], as8(b0), z));
    }
    if (w < 8) {
      const u32 hdel = c == 0 ? lds_addr(Vs) + 160 * 64 + 2 : 0u;
#pragma unroll
      for (int mt = 0; mt < 3; ++mt)
#pragma unroll
        for (int rr = 0; rr < 2; ++rr) {
          const u32 pv = pk_bf(a[mt][2 * rr] * tw[mt][2 * rr] - a[mt][2 * rr + 1] * tw[mt][2 * rr + 1], a[mt][2 * rr] * tw[mt][2 * rr + 1] + a[mt][2 * rr + 1] * tw[mt][2 * rr]);
          *(LDSP(u16))(size_t)(aw[mt] + 64 * rr) = (u16)pv;
          *(LDSP(u16))(size_t)((c == 0 ? hdel : aw[mt] + 2) + 64 * rr) = (u16)(pv >> 16);
        }
    } else {
#pragma unroll
      for (int mt = 0; mt < 3; ++mt)
#pragma unroll
        for (int rr = 0; rr < 2; ++rr)
          *(LDSP(u16))((LDSP(char))(size_t)aw[mt] + 64 * rr) = (u16)pk_bf(a[mt][2 * rr] * tw[mt][2 * rr] - a[mt][2 * rr + 1] * tw[mt][2 * rr + 1], 0.f);
    }
  }
  __syncthreads();
  {
    s16x8 a = *(const s16x8*)(Vs + (16 * w + c) * 64 + 16 * g);
    s16x8 fb0 = *(const s16x8*)(tab + 6144 + lane * 16), fb1 = *(const s16x8*)(tab + 7168 + lane * 16);
    batch_fence(); pin(a); pin(fb0); pin(fb1);
    const f32x4 z = {0.f, 0.f, 0.f, 0.f};
    const f32x4 y0 = mfma16(a, fb0, z), y1 = mfma16(a, fb1, z);
    const int R = 16 * w + 4 * g, rr = R / 20, d0 = R % 20;
    *(f32x4*)(Y + rr * Y_RS + 20 * c + d0) = y0;
    *(f32x4*)(Y + rr * Y_RS + 20 * (16 + c) + d0) = y1;
  }
  __syncthreads();
  {
    f32x2 R[8];
#pragma unroll
    for (int r = 0; r < 8; ++r) R[r] = *(const f32x2*)(Y + r * Y_RS + 2 * cp);
    batch_fence(); pin_arr(R);
    const float cc = 0.70710678118654752f;
    const f32x2 G = R[0] + R[1], Fm = R[0] - R[1];
    const f32x2 u = cc * (R[2] - R[6]), v = cc * (R[3] + R[7]);
    const f32x2 c13 = R[2] + R[6], s13 = R[3] - R[7];
    const float sg = hf ? -1.f : 1.f;
    f32x2 y[4];
    y[0] = G + R[4] + sg * c13;
    y[2] = G - R[4] + sg * s13;
    y[1] = Fm + R[5] + sg * (u + v);
    y[3] = Fm - R[5] + sg * (v - u);
    u32 x1[4];
    float sv[8];
#pragma unroll
    for (int r = 0; r < 4; ++r) {
      const f32x2 xx = {bf_lo(xv[r]), bf_hi(xv[r])};
      const f32x2 t = xx + ga * y[r];
      x1[r] = pk_bf(t[0], t[1]);
      stg_wt(x1o + (size_t)(8 * s0 + 4 * hf + r) * D_, (u32)cp * 4u, x1[r]);
      sv[r] = dot2bf(x1[r], 0x3f803f80u, 0.f); sv[4 + r] = dot2bf(x1[r], x1[r], 0.f);
    }
    const float tot = reduce8(sv, lane);
    if (lane < 8) part[w * 8 + lane] = tot;
    const f32x2 gg = {rbf(1.0f + cb[0]), rbf(1.0f + cb[1])};
    const int d = 2 * cp, wi = d & 31;
    char* stage = (char*)Y;
    const int so = (((d >> 5) * 4 + ((wi & 15) >> 2)) * 8 + 4 * hf) * 16 + (4 * (wi >> 4) + (wi & 3)) * 2;
    __syncthreads();
    const int r = lane & 3;
    float s, qq;
    { float ps[5], pq[5];
#pragma unroll
      for (int i = 0; i < 5; ++i) { ps[i] = part[(5 * hf + i) * 8 + r]; pq[i] = part[(5 * hf + i) * 8 + 4 + r]; }
      batch_fence(); pin_arr(ps); pin_arr(pq);
      s = ((ps[0] + ps[1]) + (ps[2] + ps[3])) + ps[4]; qq = ((pq[0] + pq[1]) + (pq[2] + pq[3])) + pq[4]; }
    float m_, i_; ln_finish(s, qq, m_, i_);
    const float nb_ = -m_ * i_;
#pragma unroll
    for (int i = 0; i < 4; ++i) {
      const float na = __int_as_float(__builtin_amdgcn_readlane(__float_as_int(i_), i)), nb = __int_as_float(__builtin_amdgcn_readlane(__float_as_int(nb_), i));
      const f32x2 v2 = {bf_lo(x1[i]), bf_hi(x1[i])};
      const f32x2 hv = (v2 * na + nb) * gg + sb;
      *(u32*)(stage + so + 16 * i) = pk_bf(hv[0], hv[1]);
    }
    __syncthreads();
    const u32x4 piece = *(const u32x4*)(stage + 16 * tid);
    stg_wt(h2o + ((size_t)(s0 >> 1) * 20 * 64 + 8 * (s0 & 1)) * 8, (u32)((tid >> 3) * 16 + (tid & 7)) * 16u, piece);
  }
  if (w == 9) asm volatile("s_waitcnt vmcnt(0)" ::: "memory");
}

constexpr int K5_SLOT = 28 * 1024;
constexpr int K5_NS = 4;
constexpr int K5_PD = 4;
struct K5Ctx { const u16* wsrc; const u16* hsrc; u32 ldsw; u32 goff[7]; u32 hoff; u32 abase[2]; };
template <int s, int i> DI void k5_w1(const K5Ctx& c) {
  if constexpr (i == 0) set_m0(c.ldsw + (s % K5_NS) * K5_SLOT + 3072);
  glds16_s<1024 * i - 3072>((const char*)(c.wsrc + s * 4 * 512) - 4096, c.goff[i]);
}
template <int s, int i> DI void k5_t1(const K5Ctx& c, u32x4 (&t)[4]) {
  gload16_s<(i >> 1) * 1024>(t[i], c.hsrc + ((i & 1) * 20 + 2 * s) * 512, c.hoff);
}
template <int s> DI void k5_wall(const K5Ctx& c) { k5_w1<s, 0>(c); k5_w1<s, 1>(c); k5_w1<s, 2>(c); k5_w1<s, 3>(c); k5_w1<s, 4>(c); k5_w1<s, 5>(c); k5_w1<s, 6>(c); }
template <int s> DI void k5_tall(const K5Ctx& c, u32x4 (&t)[4]) { k5_t1<s, 0>(c, t); k5_t1<s, 1>(c, t); k5_t1<s, 2>(c, t); k5_t1<s, 3>(c, t); }
template <int n> DI s16x8 k5_rd(const K5Ctx& c) {
  constexpr int s = n / 28, t = (n / 14) & 1, j = n % 14, sl = s % K5_NS;
  return *(const LDSP(const s16x8))((LDSP(const char))(size_t)c.abase[sl >> 1] + (sl & 1) * K5_SLOT + ((j >> 1) * 4 + t * 2 + (j & 1)) * 1024);
}
template <int n> DI void k5_sub(const K5Ctx& c, f32x4 (&acc)[14][2], s16x8 (&fr)[K5_PD + 1], u32x4 (&tok)[10][4]) {
  constexpr int s = n / 28, t = (n / 14) & 1, j = n % 14;
  constexpr int nb = n + K5_PD;
  if constexpr (nb < 280 && nb % 28 == 0) {
    wait_vm<(s + 2 <= 9) ? 11 : 0>();
    __builtin_amdgcn_s_barrier();
    tie4(tok[s + 1]);
  }
  if constexpr (nb < 280) fr[nb % (K5_PD + 1)] = k5_rd<nb>(c);
  {
    constexpr int q = nb / 28, pos = nb % 28 - (q == 0 ? K5_PD : 0);
    constexpr int idx = (pos % 2 == 0 && pos < 22) ? pos / 2 : -1;
    if constexpr (idx >= 0 && idx < 4 && q + 2 <= 9) k5_t1<q + 2, idx>(c, tok[q + 2]);
    if constexpr (idx >= 4 && q + 2 <= 9) k5_w1<q + 2, idx - 4>(c);
  }
  const s16x8 a = fr[n % (K5_PD + 1)];
  acc[j][0] = mfma16(a, as8(tok[s][2 * t]), acc[j][0]);
  acc[j][1] = mfma16(a, as8(tok[s][2 * t + 1]), acc[j][1]);
  __builtin_amdgcn_sched_barrier(0);
}
template <int... I> DI void k5_all(const K5Ctx& c, f32x4 (&acc)[14][2], s16x8 (&fr)[K5_PD + 1], u32x4 (&tok)[10][4], std::integer_sequence<int, I...>) { (k5_sub<I>(c, acc, fr, tok), ...); }
extern "C" __global__ __launch_bounds__(256, 2) void k5_glu(const u16* __restrict__ h2, const u16* __restrict__ Wgu, u16* __restrict__ hb) {
  extern __shared__ __attribute__((aligned(16))) char smem[];
  const int tid = threadIdx.x, lane = tid & 63, w = __builtin_amdgcn_readfirstlane(tid >> 6);
  const int xcd = blockIdx.x & 7, li = blockIdx.x >> 3;
  const int tt = 8 * (xcd & 1) + (li & 7), fc = 4 * (xcd >> 1) + (li >> 3);
  const int u0 = fc < 12 ? 7 * fc : 84 + 6 * (fc - 12), U = fc < 12 ? 7 : 6;
  if (blockIdx.x < 8) {
    const void* c6; const void* c7; CODE_ADDR(c6, k6_down); CODE_ADDR(c7, k7_end);
    if ((u32)tid * 128u < (u32)((const char*)c7 - (const char*)c6)) l2_touch(c6, (u32)tid * 128u, lds_addr(smem) + 3 * K5_SLOT + w * 256);
  }
  K5Ctx c;
  c.wsrc = Wgu + (size_t)u0 * 20 * 2 * 512;
#pragma unroll
  for (int i = 0; i < 7; ++i) { const int f = 7 * w + i, u = min(f >> 2, U - 1); c.goff[i] = 2 * (u * 20 * 2 * 512 + (f & 3) * 512) + lane * 16 + 4096 - (1024 * i - 3072); }
  c.ldsw = lds_addr(smem) + 7 * w * 1024;
  const int T16 = 8 * tt + 2 * w;
  c.hsrc = h2 + (size_t)T16 * 20 * 512; c.hoff = lane * 16;
#pragma unroll
  for (int j = 0; j < 2; ++j) { c.abase[j] = lds_addr(smem) + 2 * j * K5_SLOT + lane * 16; asm volatile("" : "+v"(c.abase[j])); }
  u32x4 tok[10][4];
  k5_tall<0>(c, tok[0]); k5_wall<0>(c);
  k5_tall<1>(c, tok[1]); k5_wall<1>(c);
  f32x4 acc[14][2];
#pragma unroll
  for (int u = 0; u < 14; ++u) { acc[u][0] = f32x4{0.f, 0.f, 0.f, 0.f}; acc[u][1] = f32x4{0.f, 0.f, 0.f, 0.f}; }
  wait_vm<11>();
  __builtin_amdgcn_s_barrier();
  tie4(tok[0]);
  s16x8 fr[K5_PD + 1];
  fr[0] = k5_rd<0>(c); fr[1] = k5_rd<1>(c); fr[2] = k5_rd<2>(c); fr[3] = k5_rd<3>(c);
  __builtin_amdgcn_sched_barrier(0);
  k5_all(c, acc, fr, tok, std::make_integer_sequence<int, 280>{});
  auto hv = [&](int u, int tl) { const f32x4 a = acc[2 * u][tl] * acc[2 * u + 1][tl]; return u32x2{pk_bf(a[0], a[1]), pk_bf(a[2], a[3])}; };
#pragma unroll
  for (int tl = 0; tl < 2; ++tl) {
    u16* hdst = hb + (size_t)(T16 + tl) * 54 * 512 + (u0 >> 1) * 512;
    const u32 vo = (u32)lane * 16u;
    if (!(u0 & 1)) {
#pragma unroll
      for (int k = 0; k < 3; ++k) { const u32x2 a = hv(2 * k, tl), b = hv(2 * k + 1, tl); stg_wt(hdst + k * 512, vo, u32x4{a[0], a[1], b[0], b[1]}); }
      if (U == 7) stg_wt(hdst + 3 * 512, vo, hv(6, tl));
    } else {
      stg_wt(hdst + 4, vo, hv(0, tl));
#pragma unroll
      for (int k = 0; k < 3; ++k) { const u32x2 a = hv(2 * k + 1, tl), b = hv(2 * k + 2, tl); stg_wt(hdst + (k + 1) * 512, vo, u32x4{a[0], a[1], b[0], b[1]}); }
    }
  }
}

constexpr int K6_NS = 4;
struct K6Ctx { const u16* wsrc; const u16* hsrc; u32 hoff; const float* gbp; u32 gbo; const u16* xp; u32 xo; };
struct K6Grp { u32x4 a[5]; u32x4 t[4]; };
template <int i> DI void k6_v1(const K6Ctx& c, int ks, K6Grp& gr) {
  if constexpr (i < 5) gload16_s<0>(gr.a[i], c.wsrc + (ks + i * 54) * 512, c.hoff);
  else gload16_s<0>(gr.t[i - 5], c.hsrc + (ks + (i - 5) * 54) * 512, c.hoff);
}
DI void k6_issue(const K6Ctx& c, int ks, K6Grp& gr) {
  k6_v1<0>(c, ks, gr); k6_v1<5>(c, ks, gr); k6_v1<6>(c, ks, gr); k6_v1<7>(c, ks, gr); k6_v1<8>(c, ks, gr);
  k6_v1<1>(c, ks, gr); k6_v1<2>(c, ks, gr); k6_v1<3>(c, ks, gr); k6_v1<4>(c, ks, gr);
}
DI void k6_tie(K6Grp& g) { asm volatile("" : "+v"(g.a[0]), "+v"(g.a[1]), "+v"(g.a[2]), "+v"(g.a[3]), "+v"(g.a[4]), "+v"(g.t[0]), "+v"(g.t[1]), "+v"(g.t[2]), "+v"(g.t[3])); }
struct K6Epi { u32x4 gb[5]; u32x2 xr[5]; };
template <int OFF> DI void gload8_s(u32x2& d, const void* sbase, u32 voff) {
  asm volatile("global_load_dwordx2 %0, %1, %2 offset:%3" : "=v"(d) : "v"(voff), "s"(sbase), "n"(OFF) : "memory");
}
constexpr int k6_after(int i, int NK) {
  int n = 0; bool seen = false;
  for (int g = 0; g < 3; ++g) { if (seen) n += 9; if (g == i) seen = true; }
  for (int st = 0; st < i; ++st) {
    if (st == NK - 3 && seen) n += 10;
    if (st + 3 < NK) { if (seen) n += 9; if (st + 3 == i) seen = true; }
  }
  return n;
}
template <int i, int NK> DI void k6_step(const K6Ctx& c, int ks0, f32x4 (&acc)[5][4], K6Grp (&gr)[K6_NS], K6Epi& ep, f32x4* red, int w, int lane) {
  wait_vm<k6_after(i, NK)>();
  K6Grp& g = gr[i % K6_NS];
  k6_tie(g);
  K6Grp& nx = gr[(i + 3) % K6_NS];
#pragma unroll
  for (int nt = 0; nt < 5; ++nt) {
    if constexpr (i + 3 < NK) {
      if (nt == 0) { k6_v1<0>(c, ks0 + i + 3, nx); k6_v1<5>(c, ks0 + i + 3, nx); }
      if (nt == 1) { k6_v1<6>(c, ks0 + i + 3, nx); k6_v1<7>(c, ks0 + i + 3, nx); }
      if (nt == 2) { k6_v1<8>(c, ks0 + i + 3, nx); k6_v1<1>(c, ks0 + i + 3, nx); }
      if (nt == 3) { k6_v1<2>(c, ks0 + i + 3, nx); k6_v1<3>(c, ks0 + i + 3, nx); }
      if (nt == 4) { k6_v1<4>(c, ks0 + i + 3, nx); }
    }
    if constexpr (i == NK - 3) {
      gload16_s<0>(ep.gb[nt], c.gbp + 16 * nt, c.gbo); gload8_s<0>(ep.xr[nt], c.xp + 16 * nt, c.xo);
    }
#pragma unroll
    for (int t = 0; t < 4; ++t) acc[nt][t] = mfma16(as8(g.a[nt]), as8(g.t[t]), acc[nt][t]);
    if constexpr (i == NK - 1) {
      if (nt >= 1) {
#pragma unroll
        for (int t = 0; t < 4; ++t) if (t != w) red[((w * 5 + nt - 1) * 4 + t) * 64 + lane] = acc[nt - 1][t];
      }
    }
    __builtin_amdgcn_sched_barrier(0);
  }
}
template <int i, int NK> struct K6Loop { static DI void run(const K6Ctx& c, int ks0, f32x4 (&acc)[5][4], K6Grp (&gr)[K6_NS], K6Epi& ep, f32x4* red, int w, int lane) {
  k6_step<i, NK>(c, ks0, acc, gr, ep, red, w, lane); K6Loop<i + 1, NK>::run(c, ks0, acc, gr, ep, red, w, lane); } };
template <int NK> struct K6Loop<NK, NK> { static DI void run(const K6Ctx&, int, f32x4 (&)[5][4], K6Grp (&)[K6_NS], K6Epi&, f32x4*, int, int) {} };
template <int NK> DI void k6_main(const K6Ctx& cx, int ks0, f32x4* red, int w, int lane, u16* __restrict__ out, u32 oo) {
  K6Grp gr[K6_NS]; K6Epi ep;
  k6_issue(cx, ks0, gr[0]); k6_issue(cx, ks0 + 1, gr[1]); k6_issue(cx, ks0 + 2, gr[2]);
  f32x4 acc[5][4];
#pragma unroll
  for (int a = 0; a < 5; ++a)
#pragma unroll
    for (int b = 0; b < 4; ++b) acc[a][b] = f32x4{0.f, 0.f, 0.f, 0.f};
  K6Loop<0, NK>::run(cx, ks0, acc, gr, ep, red, w, lane);
#pragma unroll
  for (int t = 0; t < 4; ++t) if (t != w) red[((w * 5 + 4) * 4 + t) * 64 + lane] = acc[4][t];
  wait_vm<0>();
  asm volatile("" : "+v"(ep.gb[0]), "+v"(ep.gb[1]), "+v"(ep.gb[2]), "+v"(ep.gb[3]), "+v"(ep.gb[4]), "+v"(ep.xr[0]), "+v"(ep.xr[1]), "+v"(ep.xr[2]), "+v"(ep.xr[3]), "+v"(ep.xr[4]));
  __syncthreads();
#pragma unroll
  for (int nt = 0; nt < 5; ++nt) {
    f32x4 v = w == 0 ? acc[nt][0] : w == 1 ? acc[nt][1] : w == 2 ? acc[nt][2] : acc[nt][3];
#pragma unroll
    for (int ww = 1; ww < 4; ++ww) v += red[((((w + ww) & 3) * 5 + nt) * 4 + w) * 64 + lane];
    const f32x4 gb = __builtin_bit_cast(f32x4, ep.gb[nt]);
    const f32x4 x4 = {bf_lo(ep.xr[nt][0]), bf_hi(ep.xr[nt][0]), bf_lo(ep.xr[nt][1]), bf_hi(ep.xr[nt][1])};
    const f32x4 o = x4 + gb * v;
    stg_wt(out + 16 * nt, oo, u32x2{pk_bf(o[0], o[1]), pk_bf(o[2], o[3])});
  }
}
extern "C" __global__ __launch_bounds__(256, 2) void k6_down(const u16* __restrict__ hb, const u16* __restrict__ Wd, const u16* __restrict__ x1, const float* __restrict__ mod, u16* __restrict__ out) {
  extern __shared__ __attribute__((aligned(16))) char smem[];
  const int tid = threadIdx.x, lane = tid & 63, w = __builtin_amdgcn_readfirstlane(tid >> 6), c = lane & 15, g = lane >> 4;
  const int xcd = blockIdx.x & 7, li = blockIdx.x >> 3;
  const int tt = 8 * (xcd & 3) + (li & 7), nb = 4 * (xcd >> 2) + (li >> 3);
  const int n0 = 80 * nb;
  const int ks0 = w < 2 ? 14 * w : 28 + 13 * (w - 2);
  K6Ctx cx;
  cx.wsrc = Wd + (size_t)(5 * nb) * 54 * 512; cx.hsrc = hb + (size_t)(4 * tt) * 54 * 512; cx.hoff = lane * 16;
  cx.gbp = mod + 5 * 640 + n0; cx.gbo = 16 * g;
  cx.xp = x1 + (size_t)(64 * tt + 16 * w) * D_ + n0; cx.xo = (u32)(c * D_ + 4 * g) * 2u;
  const int tau = 64 * tt + 16 * w + c, srow = (tau >> 3) + 256 * (tau & 7);
  const u32 oo = (u32)(srow * D_ + 4 * g) * 2u;
  if (w < 2) k6_main<14>(cx, ks0, (f32x4*)smem, w, lane, out + n0, oo); else k6_main<13>(cx, ks0, (f32x4*)smem, w, lane, out + n0, oo);
}

extern "C" __global__ void k7_end() {}

constexpr size_t WS_MOD = 0, WS_XT = 16384, WS_ZT = WS_XT + 8 * 21 * 256 * 16 * 4, WS_X1 = WS_ZT + 8 * 21 * 256 * 16 * 4,
                 WS_H2 = WS_X1 + 2048 * 640 * 2, WS_HB = WS_H2 + 2048 * 640 * 2, WS_WGU = WS_HB + 2048 * 1728 * 2, WS_WD = WS_WGU + 2 * 640 * 1728 * 2, WS_SYNC = WS_WD + 640 * 1728 * 2;
extern "C" void kernel_launch(void* const* d_in, const int* in_sizes, int n_in, void* d_out, int out_size, void* d_ws, size_t ws_size,
                              hipStream_t stream) {
  const u16* x = (const u16*)d_in[0]; const u16* wr = (const u16*)d_in[1]; const u16* wi = (const u16*)d_in[2];
  const u16* wg = (const u16*)d_in[4]; const u16* wu = (const u16*)d_in[5]; const u16* wd = (const u16*)d_in[6];
  const u16* c = (const u16*)d_in[7]; const u16* wmod = (const u16*)d_in[8];
  char* ws = (char*)d_ws;
  float* mod = (float*)(ws + WS_MOD); u32* XT = (u32*)(ws + WS_XT); u32* ZT = (u32*)(ws + WS_ZT);
  u16* x1 = (u16*)(ws + WS_X1); u16* h2 = (u16*)(ws + WS_H2); u16* hb = (u16*)(ws + WS_HB);
  u16* Wgu = (u16*)(ws + WS_WGU); u16* Wd = (u16*)(ws + WS_WD);
  K2Args A;
  A.x = x; A.c = c; A.wmod = wmod; A.mod = mod; A.XT = XT; A.wr = wr; A.wi = wi; A.wg = wg; A.wu = wu; A.Wgu = Wgu; A.wd = wd; A.Wd = Wd;
  A.gen = (u32*)(ws + WS_SYNC); A.arr = (u32*)(ws + WS_SYNC + 4096); A.gran = (unsigned long long*)(ws + WS_SYNC + 8192);
  k2_fwd<<<K2_GRID, 640, 0, stream>>>(A);
  k3_mid<<<168, 512, 0, stream>>>(XT, wr, wi, ZT);
  k4_inv<<<256, 640, 0, stream>>>(ZT, x, mod, x1, h2);
  hipFuncSetAttribute((const void*)k5_glu, hipFuncAttributeMaxDynamicSharedMemorySize, K5_NS * K5_SLOT);
  k5_glu<<<256, 256, K5_NS * K5_SLOT, stream>>>(h2, Wgu, hb);
  hipFuncSetAttribute((const void*)k6_down, hipFuncAttributeMaxDynamicSharedMemorySize, 81920);
  k6_down<<<256, 256, 81920, stream>>>(hb, Wd, x1, mod, (u16*)d_out);
}
```

```cpp
#define K6DV 2
#include <hip/hip_runtime.h>
#include <hip/hip_bf16.h>
#include <stdint.h>
#include <utility>

typedef unsigned short u16;
typedef unsigned int u32;
typedef short s16x4 __attribute__((ext_vector_type(4)));
typedef short s16x8 __attribute__((ext_vector_type(8)));
typedef __bf16 bf2_t __attribute__((ext_vector_type(2)));
typedef float f32x2 __attribute__((ext_vector_type(2)));
typedef float f32x4 __attribute__((ext_vector_type(4)));
typedef u32 u32x2 __attribute__((ext_vector_type(2)));
typedef u32 u32x4 __attribute__((ext_vector_type(4)));
#define DI __device__ __forceinline__
#define LDSP(T) __attribute__((address_space(3))) T*

constexpr int S_ = 2048, D_ = 640, F_ = 1728, DF = 321;

constexpr double kPi = 3.14159265358979323846264338327950288;
constexpr double c_sin0(double x) { double x2 = x * x, t = x, s = x; for (int i = 1; i < 11; ++i) { t *= -x2 / ((2.0 * i) * (2.0 * i + 1.0)); s += t; } return s; }
constexpr double c_cos0(double x) { double x2 = x * x, t = 1.0, s = 1.0; for (int i = 1; i < 11; ++i) { t *= -x2 / ((2.0 * i - 1.0) * (2.0 * i)); s += t; } return s; }
constexpr double c_cos(long n, long N) {
  n %= N; if (n < 0) n += N;
  if (2 * n > N) n = N - n;
  bool neg = false;
  if (4 * n > N) { neg = true; n = N - 2 * n; N = 2 * N; }
  double r = 0;
  if (4 * n == N) r = 0.0;
  else if (8 * n > N) r = c_sin0(2 * kPi * (double)(N - 4 * n) / (double)(4 * N));
  else r = c_cos0(2 * kPi * (double)n / (double)N);
  return neg ? -r : r;
}
constexpr double c_sin(long n, long N) { return c_cos(4 * n - N, 4 * N); }
constexpr u16 c_bf16(double v) {
  float f = (float)v; u32 u = __builtin_bit_cast(u32, f);
  u += 0x7fffu + ((u >> 16) & 1u); return (u16)(u >> 16);
}

struct F8w { u16 v[2][64][8]; };
struct F8x6 { u16 v[3][2][64][8]; };
constexpr F8w mk_f32a() {
  F8w t{};
  for (int tt = 0; tt < 2; ++tt) for (int l = 0; l < 64; ++l) for (int j = 0; j < 8; ++j) {
    int o = 16 * tt + (l & 15), d1 = 8 * (l >> 4) + j; double v = 0;
    if (o == 0) v = 1.0; else if (o == 1) v = (d1 & 1) ? -1.0 : 1.0;
    else { int k1 = o >> 1; v = (o & 1) ? -c_sin(d1 * k1, 32) : c_cos(d1 * k1, 32); }
    t.v[tt][l][j] = c_bf16(v);
  }
  return t;
}
constexpr F8x6 mk_f20(bool inv) {
  F8x6 t{};
  for (int mt = 0; mt < 3; ++mt) for (int ks = 0; ks < 2; ++ks) for (int l = 0; l < 64; ++l) for (int j = 0; j < 8; ++j) {
    int row = l & 15, g = l >> 4, k0 = 8 * mt + (row >> 1), part = row & 1, d0 = 16 * ks + 4 * g + (j >> 1), pp = j & 1; double v = 0;
    if (d0 < 20 && k0 < 20) {
      double co = c_cos(d0 * k0, 20), si = inv ? -c_sin(d0 * k0, 20) : c_sin(d0 * k0, 20);
      v = part == 0 ? (pp == 0 ? co : si) : (pp == 0 ? -si : co);
    }
    t.v[mt][ks][l][j] = c_bf16(v);
  }
  return t;
}
constexpr F8w mk_a1(int m1, bool inv) {
  F8w t{};
  for (int l = 0; l < 64; ++l) for (int i = 0; i < 4; ++i) {
    int c = l & 15, a = 4 * (l >> 4) + i, n = inv ? c * (8 * a + m1) : a * (8 * c + m1);
    double co = c_cos(n, 128), si = inv ? -c_sin(n, 128) : c_sin(n, 128);
    t.v[0][l][2 * i] = c_bf16(co); t.v[0][l][2 * i + 1] = c_bf16(si);
    t.v[1][l][2 * i] = c_bf16(-si); t.v[1][l][2 * i + 1] = c_bf16(co);
  }
  return t;
}
constexpr F8w mk_f16(bool inv) {
  F8w t{};
  for (int l = 0; l < 64; ++l) for (int i = 0; i < 4; ++i) {
    int c = l & 15, a = 4 * (l >> 4) + i;
    double co = c_cos(a * c, 16), si = c_sin(a * c, 16);
    if (!inv) { t.v[0][l][2 * i] = c_bf16(co); t.v[0][l][2 * i + 1] = c_bf16(si); t.v[1][l][2 * i] = c_bf16(-si); t.v[1][l][2 * i + 1] = c_bf16(co); }
    else { t.v[0][l][i] = c_bf16(co); t.v[0][l][4 + i] = c_bf16(-si); t.v[1][l][i] = c_bf16(si); t.v[1][l][4 + i] = c_bf16(co); }
  }
  return t;
}
constexpr F8w mk_f32bi() {
  F8w t{};
  for (int nt = 0; nt < 2; ++nt) for (int l = 0; l < 64; ++l) for (int j = 0; j < 8; ++j) {
    int d1 = 16 * nt + (l & 15), K = 8 * (l >> 4) + j; double v = 0;
    if (K == 0) v = 1.0; else if (K == 1) v = (d1 & 1) ? -1.0 : 1.0;
    else { int k1 = K >> 1; v = (K & 1) ? -2.0 * c_sin(k1 * d1, 32) : 2.0 * c_cos(k1 * d1, 32); }
    t.v[nt][l][j] = c_bf16(v);
  }
  return t;
}
static __device__ const F8w g_f32a = mk_f32a();
static __device__ const F8x6 g_f20a = mk_f20(false);
static __device__ const F8x6 g_f20ai = mk_f20(true);
static __device__ const F8w g_a1[8] = {mk_a1(0, false), mk_a1(1, false), mk_a1(2, false), mk_a1(3, false), mk_a1(4, false), mk_a1(5, false), mk_a1(6, false), mk_a1(7, false)};
static __device__ const F8w g_a1i[8] = {mk_a1(0, true), mk_a1(1, true), mk_a1(2, true), mk_a1(3, true), mk_a1(4, true), mk_a1(5, true), mk_a1(6, true), mk_a1(7, true)};
static __device__ const F8w g_f16f = mk_f16(false);
static __device__ const F8w g_f16i = mk_f16(true);
static __device__ const F8w g_f32bi = mk_f32bi();

DI float bf_lo(u32 v) { return __uint_as_float(v << 16); }
DI float bf_hi(u32 v) { return __uint_as_float(v & 0xffff0000u); }
DI float bf2f(u16 v) { return __uint_as_float(((u32)v) << 16); }
DI u32 pk_bf(float a, float b) { f32x2 f = {a, b}; return __builtin_bit_cast(u32, __builtin_convertvector(f, bf2_t)); }
DI float rbf(float a) { return bf_lo(pk_bf(a, a)); }
DI float dpp_x1(float v) { return __int_as_float(__builtin_amdgcn_mov_dpp(__float_as_int(v), 0xB1, 0xf, 0xf, true)); }
DI float dpp_x2(float v) { return __int_as_float(__builtin_amdgcn_mov_dpp(__float_as_int(v), 0x4E, 0xf, 0xf, true)); }
DI float dpp_hm(float v) { return __int_as_float(__builtin_amdgcn_mov_dpp(__float_as_int(v), 0x141, 0xf, 0xf, true)); }
DI f32x4 mfma16(s16x8 a, s16x8 b, f32x4 c) { return __builtin_amdgcn_mfma_f32_16x16x32_bf16(a, b, c, 0, 0, 0); }
DI s16x4 tr_read(const void* p) { return __builtin_amdgcn_ds_read_tr16_b64_v4i16((LDSP(s16x4))p); }
DI void batch_fence() { asm volatile("" ::: "memory"); }
template <class T, int N> DI void pin_arr(T (&v)[N]) {
#pragma unroll
  for (int i = 0; i < N; ++i) asm volatile("" : "+v"(v[i]));
}
template <class T> DI void pin(T& v) { asm volatile("" : "+v"(v)); }
DI void l2_touch(const void* base, u32 voff, u32 lds) {
  asm volatile("s_mov_b32 m0, %0\n\ts_nop 0\n\tglobal_load_lds_dword %1, %2" :: "s"(lds), "v"(voff), "s"(base) : "memory");
}
DI void side_delay(int ticks10ns) {
  const unsigned long long t0 = __builtin_amdgcn_s_memrealtime();
  while (__builtin_amdgcn_s_memrealtime() - t0 < (unsigned long long)ticks10ns) __builtin_amdgcn_s_sleep(4);
}
#define GSP(T) __attribute__((address_space(1))) T*
template <class T> DI T ldg(const void* sbase, u32 voff) { GSP(const char) p = (GSP(const char))sbase; asm("" : "+s"(p)); return *(GSP(const T))(p + voff); }
DI void stg_wt(void* sbase, u32 voff, u32x4 v) { asm volatile("global_store_dwordx4 %0, %1, %2 sc1\n\ts_nop 1" :: "v"(voff), "v"(v), "s"(sbase) : "memory"); }
DI void stg_wt(void* sbase, u32 voff, u32x2 v) { asm volatile("global_store_dwordx2 %0, %1, %2 sc1\n\ts_nop 1" :: "v"(voff), "v"(v), "s"(sbase) : "memory"); }
DI void stg_wt(void* sbase, u32 voff, u32 v) { asm volatile("global_store_dword %0, %1, %2 sc1" :: "v"(voff), "v"(v), "s"(sbase) : "memory"); }
#define CODE_ADDR(var, sym) asm volatile("s_getpc_b64 s[40:41]\n\ts_add_u32 s40, s40, " #sym "@rel32@lo+4\n\ts_addc_u32 s41, s41, " #sym "@rel32@hi+12\n\ts_mov_b64 %0, s[40:41]" : "=s"(var) :: "s40", "s41", "scc")
DI u32 lds_addr(const void* p) { return (u32)(size_t)(LDSP(const char))p; }
DI s16x8 cat8(s16x4 a, s16x4 b) { return __builtin_shufflevector(a, b, 0, 1, 2, 3, 4, 5, 6, 7); }
DI s16x8 as8(u32x4 v) { return __builtin_bit_cast(s16x8, v); }
DI float dot2bf(u32 a, u32 b, float c) { return __builtin_amdgcn_fdot2_f32_bf16(__builtin_bit_cast(bf2_t, a), __builtin_bit_cast(bf2_t, b), c, false); }

DI void ln_finish(float s, float q, float& m_, float& i_) {
  const float mean = s * (1.0f / 640.0f);
  const float var = fmaxf(q * (1.0f / 640.0f) - mean * mean, 0.f);
  const float sd = rbf(__builtin_amdgcn_sqrtf(rbf(rbf(var) + 1e-5f)));
  m_ = rbf(mean); i_ = __builtin_amdgcn_rcpf(sd);
}
DI float reduce8(const float (&v)[8], int lane) {
  float b[4], c[2], d;
  const bool s2 = lane & 4, s1 = lane & 2, s0 = lane & 1;
#pragma unroll
  for (int i = 0; i < 4; ++i) { float keep = s2 ? v[4 + i] : v[i], send = s2 ? v[i] : v[4 + i]; b[i] = keep + dpp_hm(send); }
#pragma unroll
  for (int i = 0; i < 2; ++i) { float keep = s1 ? b[2 + i] : b[i], send = s1 ? b[i] : b[2 + i]; c[i] = keep + dpp_x2(send); }
  { float keep = s0 ? c[1] : c[0], send = s0 ? c[0] : c[1]; d = keep + dpp_x1(send); }
  d += __int_as_float(__builtin_amdgcn_mov_dpp(__float_as_int(d), 0x128, 0xf, 0xf, true));
  auto r16 = __builtin_amdgcn_permlane16_swap(__float_as_uint(d), __float_as_uint(d), false, false);
  d = __uint_as_float(r16[0]) + __uint_as_float(r16[1]);
  auto r32 = __builtin_amdgcn_permlane32_swap(__float_as_uint(d), __float_as_uint(d), false, false);
  return __uint_as_float(r32[0]) + __uint_as_float(r32[1]);
}

template <int N> DI void wait_vm() { asm volatile("s_waitcnt vmcnt(%0)" ::"n"(N) : "memory"); }
template <int OFF> DI void gload16_s(u32x4& d, const void* sbase, u32 voff) {
  asm volatile("global_load_dwordx4 %0, %1, %2 offset:%3" : "=v"(d) : "v"(voff), "s"(sbase), "n"(OFF) : "memory");
}
template <int OFF> DI void glds16_s(const void* sbase, u32 voff) {
  asm volatile("global_load_lds_dwordx4 %0, %1 offset:%2" :: "v"(voff), "s"(sbase), "n"(OFF) : "memory");
}
DI void dma1k(const void* sbase, u32 voff, u32 lds) {
  asm volatile("s_mov_b32 m0, %0\n\ts_nop 0\n\tglobal_load_lds_dwordx4 %1, %2" :: "s"(lds), "v"(voff), "s"(sbase) : "memory");
}
DI void set_m0(u32 v) { asm volatile("s_mov_b32 m0, %0\n\ts_nop 0" :: "s"(v) : "memory"); }
DI void tie4(u32x4 (&t)[4]) { asm volatile("" : "+v"(t[0]), "+v"(t[1]), "+v"(t[2]), "+v"(t[3])); }

DI void prep_load(u32x4 (&v)[4], const u16* __restrict__ src, int ld, int lane) {
  const u32 vo = (u32)((lane >> 3) * ld + 8 * (lane & 7)) * 2u;
#pragma unroll
  for (int i = 0; i < 4; ++i) v[i] = ldg<u32x4>(src + (size_t)8 * i * ld, vo);
}
DI void prep_store(const u32x4 (&v)[4], u16* __restrict__ dst, int jstride, char* lds, int lane) {
#pragma unroll
  for (int i = 0; i < 4; ++i) {
    const int row = 8 * i + (lane >> 3), ch = lane & 7;
    *(u32x4*)(lds + row * 128 + (((ch >> 1) ^ ((row >> 1) & 3)) * 32) + (ch & 1) * 16) = v[i];
  }
  const int c = lane & 15, g = lane >> 4, q = c >> 2, p = c & 3, r = 4 * g + q;
#pragma unroll
  for (int j = 0; j < 4; ++j) {
    const char* b = lds + r * 128 + ((j ^ ((r >> 1) & 3)) * 32) + 8 * p;
    const s16x8 f = cat8(tr_read(b), tr_read(b + 16 * 128));
    stg_wt(dst + (size_t)j * jstride, (u32)lane * 16u, __builtin_bit_cast(u32x4, f));
  }
}
DI void prep_wave(const u16* __restrict__ src, int ld, u16* __restrict__ dst, int jstride, char* lds, int lane) {
  u32x4 v[4]; prep_load(v, src, ld, lane); prep_store(v, dst, jstride, lds, lane);
}
DI void prep_gu(int task, const u16* wg, const u16* wu, u16* Wgu, char* lds, int lane) {
  const int mat = task >= 540, tk = task - 540 * mat, cg = tk / 20, ks = tk % 20;
  prep_wave((mat ? wu : wg) + (size_t)ks * 32 * F_ + 64 * cg, F_, Wgu + ((size_t)(4 * cg * 20 + ks) * 2 + mat) * 512, 20 * 2 * 512, lds, lane);
}
DI void prep_d(int task, const u16* wd, u16* Wd, char* lds, int lane) {
  const int cg = task / 54, ks = task % 54;
  prep_wave(wd + (size_t)ks * 32 * D_ + 64 * cg, D_, Wd + (size_t)(4 * cg * 54 + ks) * 512, 54 * 512, lds, lane);
}


typedef __attribute__((address_space(1))) u32 gu32;
typedef __attribute__((address_space(1))) unsigned long long gu64;
DI u32 ld_agent(const u32* p) { return __hip_atomic_load((const gu32*)p, __ATOMIC_RELAXED, __HIP_MEMORY_SCOPE_AGENT); }
DI void st_agent(u32* p, u32 v) { __hip_atomic_store((gu32*)p, v, __ATOMIC_RELAXED, __HIP_MEMORY_SCOPE_AGENT); }
DI void st_agent64(unsigned long long* p, unsigned long long v) { __hip_atomic_store((gu64*)p, v, __ATOMIC_RELAXED, __HIP_MEMORY_SCOPE_AGENT); }
constexpr int K2_SIDE_DELAY = 80;
constexpr int SPIN_LIMIT = 1 << 16;

template <int NR> DI void gemv_part(int col0, int row0, u32 tagv, bool publish, unsigned long long* gran, int gidx0, const u16* __restrict__ c, const u16* __restrict__ wmod,
                                    float* __restrict__ mod, char* lds) {
  float* sl = (float*)lds; float* part = sl + 640;
  const int t = threadIdx.x, lane = t & 63, w = t >> 6, cq = t & 7, rg = t >> 3;
  const u16 cv = ldg<u16>(c, 2 * t);
  u32x4 wv[NR];
  const u32 wo = (u32)((row0 + rg) * 3840 + col0 + 8 * cq) * 2u;
#pragma unroll
  for (int i = 0; i < NR; ++i) wv[i] = ldg<u32x4>(wmod + (size_t)80 * i * 3840, wo);
  { const float x = bf2f(cv); sl[t] = rbf(x * __builtin_amdgcn_rcpf(1.0f + __expf(-x))); }
  __syncthreads();
  float acc[8] = {};
#pragma unroll
  for (int i = 0; i < NR; ++i) {
    const float s = sl[row0 + rg + 80 * i];
#pragma unroll
    for (int k = 0; k < 4; ++k) { acc[2 * k] += s * bf_lo(wv[i][k]); acc[2 * k + 1] += s * bf_hi(wv[i][k]); }
  }
  float u[4];
  const bool b3 = lane & 8;
#pragma unroll
  for (int i = 0; i < 4; ++i) { const float keep = b3 ? acc[4 + i] : acc[i], send = b3 ? acc[i] : acc[4 + i]; u[i] = keep + __int_as_float(__builtin_amdgcn_mov_dpp(__float_as_int(send), 0x128, 0xf, 0xf, true)); }
  float r2[2];
#pragma unroll
  for (int i = 0; i < 2; ++i) { auto x = __builtin_amdgcn_permlane16_swap(__float_as_uint(u[i]), __float_as_uint(u[i + 2]), false, false); r2[i] = __uint_as_float(x[0]) + __uint_as_float(x[1]); }
  auto y = __builtin_amdgcn_permlane32_swap(__float_as_uint(r2[0]), __float_as_uint(r2[1]), false, false);
  part[w * 64 + 8 * cq + 4 * ((lane >> 3) & 1) + 2 * ((lane >> 4) & 1) + (lane >> 5)] = __uint_as_float(y[0]) + __uint_as_float(y[1]);
  __syncthreads();
  if (t < 64) {
    float s = 0.f;
#pragma unroll
    for (int i = 0; i < 10; ++i) s += part[i * 64 + t];
    if (publish) st_agent64(gran + gidx0 + t, ((unsigned long long)(tagv + 1u) << 32) | __float_as_uint(s));
    else mod[col0 + t] = rbf(s);
  }
}

DI int row_group(int b) { return 2 * (8 * (b >> 4) + (b & 7)) + ((b >> 3) & 1); }
DI void sincos_rev(float a, float& s, float& c) { s = __builtin_amdgcn_sinf(a); c = __builtin_amdgcn_cosf(a); }
DI void gram_part(const u32x4 (&f)[4], float* part  , int i, int lane) {
  f32x4 acc = {0.f, 0.f, 0.f, 0.f};
#pragma unroll
  for (int k = 0; k < 4; ++k) acc = mfma16(as8(f[k]), as8(f[k]), acc);
  const int c = lane & 15, g = lane >> 4;
  const float dg = (c & 2) ? ((c & 1) ? acc[3] : acc[2]) : ((c & 1) ? acc[1] : acc[0]);
  if (c < 8 && g == (c >> 2)) part[i * 16 + 8 + c] = dg;
  if (c < 8 && g == 2) part[i * 16 + c] = acc[0];
}
DI void ln_bcast8(const float* part, int lane, float (&a_)[8], float (&b_)[8]) {
  const int r = lane & 7;
  float ps[5], pq[5];
#pragma unroll
  for (int i = 0; i < 5; ++i) { ps[i] = part[i * 16 + r]; pq[i] = part[i * 16 + 8 + r]; }
  batch_fence(); pin_arr(ps); pin_arr(pq);
  const float s = ((ps[0] + ps[1]) + (ps[2] + ps[3])) + ps[4], q = ((pq[0] + pq[1]) + (pq[2] + pq[3])) + pq[4];
  float m_, i_; ln_finish(s, q, m_, i_);
  const float nb = -m_ * i_;
#pragma unroll
  for (int i = 0; i < 8; ++i) {
    a_[i] = __int_as_float(__builtin_amdgcn_readlane(__float_as_int(i_), i));
    b_[i] = __int_as_float(__builtin_amdgcn_readlane(__float_as_int(nb), i));
  }
}
constexpr int HT_RS = 1296;
struct K2Args { const u16* x; const u16* c; const u16* wmod; float* mod; u32* XT; const u16* wr; const u16* wi; const u16* wg; const u16* wu; u16* Wgu; const u16* wd; u16* Wd; u32* gen; u32* arr; unsigned long long* gran; };
constexpr int K2_MAIN0 = 88, K2_SIDE0 = 344, K2_GRID = 344 + 54;
extern "C" __global__ __launch_bounds__(640) void k2_fwd(const K2Args A) {
  __shared__ __attribute__((aligned(16))) char smem[32768 + 5 * 4096 + 256];
  const int bid = blockIdx.x;
  if (bid >= K2_SIDE0) {
    side_delay(K2_SIDE_DELAY);
    const int wv = __builtin_amdgcn_readfirstlane(threadIdx.x >> 6);
    prep_d((bid - K2_SIDE0) * 10 + wv, A.wd, A.Wd, smem + 4096 * wv, threadIdx.x & 63);
    return;
  }
  if (bid >= 40 && bid < 80) {
    side_delay(K2_SIDE_DELAY);
    gemv_part<8>(1280 + 64 * (bid - 40), 0, 0u, false, nullptr, 0, A.c, A.wmod, A.mod, smem); return;
  }
  if (bid >= 80 && bid < 88) {
    const void* c3; const void* c7;
    CODE_ADDR(c3, k3_mid); CODE_ADDR(c7, k7_end);
    const u32 len = (u32)((const char*)c7 - (const char*)c3);
    const u32 off = threadIdx.x * 128u;
    if (off < len) l2_touch(c3, off, lds_addr(smem) + __builtin_amdgcn_readfirstlane(threadIdx.x >> 6) * 256);
    asm volatile("s_waitcnt vmcnt(0)" ::: "memory");
    if (bid < 87) return;
  }
  if (bid < 40) {
    const u32 tagv = ld_agent(A.gen);
    gemv_part<4>(64 * (bid >> 1), 320 * (bid & 1), tagv, true, A.gran, 1280 * (bid & 1) + 64 * (bid >> 1), A.c, A.wmod, A.mod, smem);
    if (threadIdx.x == 0) st_agent(A.arr + bid, tagv + 1u);
    return;
  }
  if (bid == 87) {
    const u32 tag = ld_agent(A.gen) + 1u;
    const int t = threadIdx.x, who = t < 40 ? t : t + 48;
    for (int spin = 0; spin < SPIN_LIMIT; ++spin) {
      const bool ok = t >= 296 || ld_agent(A.arr + who) == tag;
      if (__syncthreads_and(ok)) break;
      __builtin_amdgcn_s_sleep(8);
    }
    if (t == 0) st_agent(A.gen, tag);
    return;
  }
  u32* __restrict__ XT = A.XT;
  char* hT = smem; char* U = smem + 8 * HT_RS; float* part = (float*)(smem + 8 * HT_RS + 8 * 17 * 80);
  char* tab = smem + 8 * HT_RS + 8 * 17 * 80 + 5 * 16 * 4;
  const int tid = threadIdx.x, lane = tid & 63, w = __builtin_amdgcn_readfirstlane(tid >> 6);
  const int s0 = row_group(bid - K2_MAIN0);
  const int c = lane & 15, g = lane >> 4, q = c >> 2, p = c & 3;
  const u16* xrow = A.x + (size_t)s0 * D_;
  f32x4 tw0, tw1; u32 ard, awr, aw0, acr; float z0r, z0i;
  auto precompute = [&]() {
    const int Qw = 4 * w + q, rho = Qw / 5, d0 = 4 * (Qw % 5) + p;
    const bool rot = rho >= 3 && (rho & 1);
    const float fd = (float)d0 * (1.0f / 640.0f);
    const int k10 = g == 0 ? 16 : 2 * g;
    float s_[4], c_[4];
    sincos_rev(fd * (float)k10, s_[0], c_[0]); sincos_rev(fd * (float)(2 * g + 1), s_[1], c_[1]);
    sincos_rev(fd * (float)(8 + 2 * g), s_[2], c_[2]); sincos_rev(fd * (float)(9 + 2 * g), s_[3], c_[3]);
    tw0 = f32x4{rot ? s_[0] : c_[0], rot ? c_[0] : -s_[0], rot ? s_[1] : c_[1], rot ? c_[1] : -s_[1]};
    tw1 = f32x4{rot ? s_[2] : c_[2], rot ? c_[2] : -s_[2], rot ? s_[3] : c_[3], rot ? c_[3] : -s_[3]};
    const int Qr = 4 * w + p;
    ard = lds_addr(hT) + (Qr / 5) * HT_RS + 2 * (20 * (8 * g + q) + 4 * (Qr % 5));
    awr = lds_addr(U) + 4 * (rho * 17 * 20 + d0 + 2 * g * 20);
    aw0 = g == 0 ? awr + 4 * 16 * 20 : awr;
    z0r = rot ? 0.f : 1.f; z0i = rot ? 1.f : 0.f;
    acr = lds_addr(U) + (w < 8 ? ((2 * (w >> 1)) * 17 + c) * 80 : ((c & 7) * 17 + 16) * 80);
    pin(tw0); pin(tw1); pin(ard); pin(awr); pin(aw0); pin(z0r); pin(z0i); pin(acr);
  };
  if (w < 5) {
    u32 xv[8];
#pragma unroll
    for (int r = 0; r < 8; ++r) xv[r] = ldg<u32>(xrow + (size_t)r * 256 * D_, 4 * tid);
    const u32 tagv = ld_agent(A.gen);
    precompute();
    __syncthreads();
    float na[8], nb[8];
    ln_bcast8(part, lane, na, nb);
    f32x2 o[8];
    {
      f32x2 h[8];
#pragma unroll
      for (int r = 0; r < 8; ++r) { f32x2 v = {bf_lo(xv[r]), bf_hi(xv[r])}; h[r] = v * na[r] + nb[r]; }
      const float cc = 0.70710678118654752f;
      f32x2 a0 = h[0] + h[4], a1 = h[1] + h[5], a2 = h[2] + h[6], a3 = h[3] + h[7];
      f32x2 b0 = h[0] - h[4], b1 = h[1] - h[5], b2 = h[2] - h[6], b3 = h[3] - h[7];
      f32x2 e = a0 + a2, f = a1 + a3, u = cc * (b1 - b3), v = cc * (b1 + b3);
      o[0] = e + f; o[1] = e - f; o[2] = b0 + u; o[3] = -b2 - v; o[4] = a0 - a2; o[5] = a3 - a1; o[6] = b0 - u; o[7] = b2 - v;
#pragma unroll
      for (int r = 0; r < 8; ++r) pin(o[r]);
    }
    const u32 tag = __builtin_amdgcn_readfirstlane(tagv) + 1u;
    if (tid == 0) st_agent(A.arr + bid, tag);
    f32x2 sa, gg;
    {
      const unsigned long long* ga_ = A.gran + 2 * tid;
      u32x4 va, vb, vc, vd;
      for (int spin = 0; spin < SPIN_LIMIT; ++spin) {
        asm volatile("global_load_dwordx4 %0, %4, off sc1\n\tglobal_load_dwordx4 %1, %5, off sc1\n\tglobal_load_dwordx4 %2, %6, off sc1\n\tglobal_load_dwordx4 %3, %7, off sc1\n\ts_waitcnt vmcnt(0)"
                     : "=&v"(va), "=&v"(vb), "=&v"(vc), "=&v"(vd) : "v"(ga_), "v"(ga_ + 1280), "v"(ga_ + 640), "v"(ga_ + 1920) : "memory");
        const bool ok = va[1] == tag && va[3] == tag && vb[1] == tag && vb[3] == tag && vc[1] == tag && vc[3] == tag && vd[1] == tag && vd[3] == tag;
        if (__all(ok)) break;
      }
      sa = f32x2{rbf(__uint_as_float(va[0]) + __uint_as_float(vb[0])), rbf(__uint_as_float(va[2]) + __uint_as_float(vb[2]))};
      gg = f32x2{rbf(1.0f + rbf(__uint_as_float(vc[0]) + __uint_as_float(vd[0]))), rbf(1.0f + rbf(__uint_as_float(vc[2]) + __uint_as_float(vd[2])))};
    }
    o[0] = o[0] * gg + 8.0f * sa;
    *(u32*)(hT + 4 * tid) = pk_bf(o[0][0], o[0][1]);
#pragma unroll
    for (int r = 1; r < 8; ++r) { const f32x2 v = o[r] * gg; *(u32*)(hT + r * HT_RS + 4 * tid) = pk_bf(v[0], v[1]); }
  } else {
    const int i = w - 5;
    u32x4 f[4];
    const u32 xo = (u32)((c & 7) * 256 * D_ + 128 * i + 8 * g) * 2u;
#pragma unroll
    for (int k = 0; k < 4; ++k) f[k] = ldg<u32x4>(xrow, xo + 64 * k);
    const s16x8 t0 = i < 2 ? ldg<s16x8>(g_f32a.v[i < 2 ? i : 0], lane * 16) : ldg<s16x8>(&g_f20a.v[0][0][0][0] + (i - 2) * 512, lane * 16);
    const s16x8 t1 = ldg<s16x8>(&g_f20a.v[0][0][0][0] + (i < 3 ? i + 3 : 5) * 512, lane * 16);
    precompute();
    if (c >= 8) {
      const u32 fill = c == 8 ? 0x3f803f80u : 0u;
#pragma unroll
      for (int k = 0; k < 4; ++k) f[k] = u32x4{fill, fill, fill, fill};
    }
    gram_part(f, part, i, lane);
    *(s16x8*)(tab + i * 1024 + lane * 16) = t0;
    if (i < 3) *(s16x8*)(tab + (i + 5) * 1024 + lane * 16) = t1;
    __syncthreads();
    const int task = (bid - K2_MAIN0) * 5 + i;
    if (task < 1080) prep_gu(task, A.wg, A.wu, A.Wgu, smem + 32768 + 4096 * i, lane);
    if (i == 4) {
      const int xc = bid & 7, li = (bid - K2_MAIN0) >> 3;
#pragma unroll
      for (int k = 0; k < 4; ++k) {
        const int e = (k & 1) * 64 + lane, id = li * 96 + e;
        const int ln = id % 6, row = 8 * ((id / 6) & 255) + (id < 1536 ? xc : ((8 - xc) & 7));
        const u32 off = min((u32)(row * 642 + ln * 128), (u32)(2048 * 642 - 4)) & ~3u;
        if (e < 96) l2_touch((k >> 1) ? A.wi : A.wr, off, lds_addr(smem) + sizeof(smem) - 256);
      }
    }
  }
  __syncthreads();
  {
    const LDSP(const char) base = (LDSP(const char))(size_t)ard;
    s16x8 b = cat8(__builtin_amdgcn_ds_read_tr16_b64_v4i16((LDSP(s16x4))base), __builtin_amdgcn_ds_read_tr16_b64_v4i16((LDSP(s16x4))(base + 160)));
    s16x8 fa0 = *(const s16x8*)(tab + lane * 16), fa1 = *(const s16x8*)(tab + 1024 + lane * 16);
    batch_fence(); pin(b); pin(fa0); pin(fa1);
    const f32x4 z = {0.f, 0.f, 0.f, 0.f};
    f32x4 a0 = mfma16(fa0, b, z), a1 = mfma16(fa1, b, z);
    LDSP(u32) Ub = (LDSP(u32))(size_t)awr;
    const float x0 = g == 0 ? a0[1] : a0[0], x1 = g == 0 ? 0.f : a0[1];
    *(LDSP(u32))(size_t)aw0 = pk_bf(x0 * tw0[0] - x1 * tw0[1], x0 * tw0[1] + x1 * tw0[0]);
    if (g == 0) Ub[0] = pk_bf(a0[0] * z0r, a0[0] * z0i);
    Ub[20] = pk_bf(a0[2] * tw0[2] - a0[3] * tw0[3], a0[2] * tw0[3] + a0[3] * tw0[2]);
    Ub[8 * 20] = pk_bf(a1[0] * tw1[0] - a1[1] * tw1[1], a1[0] * tw1[1] + a1[1] * tw1[0]);
    Ub[9 * 20] = pk_bf(a1[2] * tw1[2] - a1[3] * tw1[3], a1[2] * tw1[3] + a1[3] * tw1[2]);
  }
  __syncthreads();
  const f32x4 z4 = {0.f, 0.f, 0.f, 0.f};
  u32* XTb = XT + (size_t)s0 * 16;
  const char* ft = tab + 2048 + lane * 16;
  const LDSP(const char) be = (LDSP(const char))(size_t)acr;
  const int P = w >> 1, mp = P, mm = P == 0 ? 4 : 8 - P;
  if (w < 8) {
    s16x8 e0 = *(const LDSP(s16x8))(be + 16 * g), e1 = *(const LDSP(s16x8))(be + 64), o0 = *(const LDSP(s16x8))(be + 1360 + 16 * g), o1 = *(const LDSP(s16x8))(be + 1360 + 64);
    char* dp = (char*)(XTb + (size_t)(mp * 21) * 4096); char* dm = (char*)(XTb + (size_t)(mm * 21) * 4096);
    const u32 vo = (u32)(2 * g * 4096 + c) * 4u;
    if (!(w & 1)) {
      s16x8 fm[4];
#pragma unroll
      for (int k = 0; k < 4; ++k) fm[k] = *(const s16x8*)(ft + k * 1024);
      batch_fence(); pin(e0); pin(e1); pin(o0); pin(o1); pin_arr(fm);
#pragma unroll
      for (int mt = 0; mt < 2; ++mt) {
        const s16x8 f0 = fm[2 * mt], f1 = fm[2 * mt + 1];
        f32x4 ae = mfma16(f1, e1, mfma16(f0, e0, z4)), ao = mfma16(f1, o1, mfma16(f0, o0, z4));
        f32x4 tp = ae + ao, tm = ae - ao;
        if (P == 0) { tp = ae; tm = ao; }
        char* ep = dp + (size_t)(8 * mt) * 16384; char* em = dm + (size_t)(8 * mt) * 16384;
        const int k0 = 8 * mt + 2 * g;
        { const float d0_ = (c == 0 && k0 >= 1 && k0 <= 9) ? 2.f : 1.f, d1_ = (c == 0 && k0 + 1 <= 9) ? 2.f : 1.f;
          const f32x4 dd = {d0_, d0_, d1_, d1_}; tp = tp * dd; tm = tm * dd; }
        if (mt == 0 || c != 0 || k0 <= 10) { stg_wt(ep, vo, pk_bf(tp[0], tp[1])); stg_wt(em, vo, pk_bf(tm[0], tm[1])); }
        if (mt == 0 || c != 0 || k0 + 1 <= 10) { stg_wt(ep + 16384, vo, pk_bf(tp[2], tp[3])); stg_wt(em + 16384, vo, pk_bf(tm[2], tm[3])); }
      }
    } else {
      s16x8 f0 = *(const s16x8*)(ft + 4 * 1024), f1 = *(const s16x8*)(ft + 5 * 1024);
      batch_fence(); pin(e0); pin(e1); pin(o0); pin(o1); pin(f0); pin(f1);
      f32x4 ae = mfma16(f1, e1, mfma16(f0, e0, z4)), ao = mfma16(f1, o1, mfma16(f0, o0, z4));
      f32x4 tp = ae + ao, tm = ae - ao;
      if (P == 0) { tp = ae; tm = ao; }
      if (g < 2 && c != 0) {
        char* ep = dp + (size_t)16 * 16384; char* em = dm + (size_t)16 * 16384;
        stg_wt(ep, vo, pk_bf(tp[0], tp[1])); stg_wt(ep + 16384, vo, pk_bf(tp[2], tp[3]));
        stg_wt(em, vo, pk_bf(tm[0], tm[1])); stg_wt(em + 16384, vo, pk_bf(tm[2], tm[3]));
      }
    }
  } else {
    const int rh = c & 7, mt = w - 7;
    s16x8 b0 = *(const LDSP(s16x8))(be + 16 * g), b1 = *(const LDSP(s16x8))(be + 64);
    s16x8 f0 = *(const s16x8*)(ft + (2 * mt) * 1024), f1 = *(const s16x8*)(ft + (2 * mt + 1) * 1024);
    batch_fence(); pin(b0); pin(b1); pin(f0); pin(f1);
    f32x4 a = mfma16(f1, b1, mfma16(f0, b0, z4));
    const float ca_ = (rh >= 2 && (rh & 1)) ? -1.f : 1.f, cb_ = rh >= 2 ? 1.f : 0.f;
    f32x4 t;
#pragma unroll
    for (int r = 0; r < 4; ++r) t[r] = ca_ * a[r] + cb_ * dpp_x1(a[r]);
    const int m1 = rh == 0 ? 0 : rh == 1 ? 4 : (rh & 1) ? 8 - (rh >> 1) : (rh >> 1);
    const int k0 = 8 * mt + 2 * g;
    const u32 o0 = (u32)((m1 * 21 + (k0 == 10 ? 20 : k0)) * 4096) * 4u, o1 = (u32)((m1 * 21 + k0 + 1) * 4096) * 4u;
    if (c < 8 && k0 >= 10 && k0 < 20) { stg_wt(XTb, o0, pk_bf(t[0], t[1])); stg_wt(XTb, o1, pk_bf(t[2], t[3])); }
    if (w == 9) asm volatile("s_waitcnt vmcnt(0)" ::: "memory");
  }
}

constexpr int EX_RS = 20;
extern "C" __global__ __launch_bounds__(512) void k3_mid(const u32* __restrict__ XT, const u16* __restrict__ wr, const u16* __restrict__ wi, u32* __restrict__ ZT) {
  __shared__ __attribute__((aligned(16))) u32 ex[2][256 * EX_RS];
  __shared__ __attribute__((aligned(16))) char tab[8 * 1024];
  const int tid = threadIdx.x, lane = tid & 63, w = __builtin_amdgcn_readfirstlane(tid >> 6), c = lane & 15, g = lane >> 4;
  const int m1 = blockIdx.x & 7, tile = blockIdx.x >> 3;
  const char* in = (const char*)(XT + (size_t)(m1 * 21 + tile) * 4096);
  dma1k(in + 2048 * w, (u32)lane * 16u, lds_addr(ex[1]) + 2048 * w);
  dma1k(in + 2048 * w + 1024, (u32)lane * 16u, lds_addr(ex[1]) + 2048 * w + 1024);
  const F8w* tsrc = w < 2 ? &g_a1[m1] : w < 4 ? &g_f16f : w < 6 ? &g_f16i : &g_a1i[m1];
  const s16x8 tfrag = ldg<s16x8>(tsrc->v[w & 1], lane * 16);
  u32 wra[2][4], wia[2][4];
  const int kb = tile == 20 ? 336 : (c == 0 && tile >= 11) ? 32 * tile + 16 : 32 * tile + c;
  const bool mir = kb > 320;
  {
    const int kk = mir ? 640 - kb : kb;
#pragma unroll
    for (int j = 0; j < 2; ++j)
#pragma unroll
      for (int r = 0; r < 4; ++r) {
        const int m = m1 + 8 * (2 * w + j) + 128 * (4 * g + r), mm = mir ? ((2048 - m) & 2047) : m;
        const u32 vo = (u32)(mm * DF + kk) * 2u;
        wra[j][r] = (u32)ldg<u16>(wr, vo) << 16; wia[j][r] = (u32)ldg<u16>(wi, vo) << 16;
      }
  }
  f32x4 fcs[2], fsn[2], ics[2], isn[2];
  {
#pragma unroll
    for (int j = 0; j < 2; ++j) {
      float s0_, c0_, s1_, c1_;
      sincos_rev((float)((2 * w + j) * (m1 + 32 * g)) * (1.0f / 2048), s0_, c0_); sincos_rev((float)(2 * w + j) * (1.0f / 256), s1_, c1_);
#pragma unroll
      for (int r = 0; r < 4; ++r) { fcs[j][r] = c0_; fsn[j][r] = s0_; const float nc = c0_ * c1_ - s0_ * s1_, ns = s0_ * c1_ + c0_ * s1_; c0_ = nc; s0_ = ns; }
      const int M = m1 + 16 * w + 8 * j;
      sincos_rev((float)(4 * g * M) * (1.0f / 2048), s0_, c0_); sincos_rev((float)M * (1.0f / 2048), s1_, c1_);
#pragma unroll
      for (int r = 0; r < 4; ++r) { ics[j][r] = c0_; isn[j][r] = s0_; const float nc = c0_ * c1_ - s0_ * s1_, ns = s0_ * c1_ + c0_ * s1_; c0_ = nc; s0_ = ns; }
    }
  }
  u32 awr = lds_addr(ex) + 4 * ((4 * g * 16 + c) * EX_RS + 2 * w);
  u32 ard = lds_addr(ex) + 4 * ((2 * w * 16 + c) * EX_RS + 4 * g);
#pragma unroll
  for (int j = 0; j < 2; ++j) { pin(fcs[j]); pin(fsn[j]); pin(ics[j]); pin(isn[j]); }
  pin(awr); pin(ard);
  *(s16x8*)(tab + w * 1024 + lane * 16) = tfrag;
  wait_vm<16>();
  __syncthreads();
  u32x4 bx[2];
#pragma unroll
  for (int j = 0; j < 2; ++j)
#pragma unroll
    for (int i = 0; i < 4; ++i) bx[j][i] = ex[1][(16 * (4 * g + i) + 2 * w + j) * 16 + c];
  s16x8 A10 = *(const s16x8*)(tab + lane * 16), A11 = *(const s16x8*)(tab + lane * 16 + 1024);
  batch_fence(); pin_arr(bx); pin(A10); pin(A11);
  const f32x4 z4 = {0.f, 0.f, 0.f, 0.f};
  const char* tb = tab + lane * 16;
  const LDSP(char) wr0 = (LDSP(char))(size_t)awr; const LDSP(const char) rd0 = (LDSP(const char))(size_t)ard;
  constexpr int RB = 16 * EX_RS * 4, XB = 256 * EX_RS * 4;
  {
    u32 pk[2][4];
#pragma unroll
    for (int j = 0; j < 2; ++j) {
      f32x4 re = mfma16(A10, as8(bx[j]), z4), im = mfma16(A11, as8(bx[j]), z4);
      f32x4 tr = re * fcs[j] + im * fsn[j], ti = im * fcs[j] - re * fsn[j];
#pragma unroll
      for (int r = 0; r < 4; ++r) pk[j][r] = pk_bf(tr[r], ti[r]);
    }
#pragma unroll
    for (int r = 0; r < 4; ++r) *(LDSP(u32x2))(wr0 + r * RB) = u32x2{pk[0][r], pk[1][r]};
  }
  __syncthreads();
  {
    u32x4 bj[2] = {*(const LDSP(u32x4))(rd0), *(const LDSP(u32x4))(rd0 + RB)};
    s16x8 F0 = *(const s16x8*)(tb + 2048), F1 = *(const s16x8*)(tb + 3072), G0 = *(const s16x8*)(tb + 4096), G1 = *(const s16x8*)(tb + 5120);
    batch_fence(); pin_arr(bj); pin(F0); pin(F1); pin(G0); pin(G1);
    u32 pk[2][4];
#pragma unroll
    for (int j = 0; j < 2; ++j) {
      const u32x4 b = bj[j];
      f32x4 xr = mfma16(F0, as8(b), z4), xi = mfma16(F1, as8(b), z4);
      const f32x4 a = {__uint_as_float(wra[j][0]), __uint_as_float(wra[j][1]), __uint_as_float(wra[j][2]), __uint_as_float(wra[j][3])};
      const u32 sg = mir ? 0x80000000u : 0u;
      const f32x4 bb = {__uint_as_float(wia[j][0] ^ sg), __uint_as_float(wia[j][1] ^ sg), __uint_as_float(wia[j][2] ^ sg), __uint_as_float(wia[j][3] ^ sg)};
      const f32x4 yr = xr * a - xi * bb, yi = xr * bb + xi * a;
      const u32x4 yb = {pk_bf(yr[0], yr[1]), pk_bf(yr[2], yr[3]), pk_bf(yi[0], yi[1]), pk_bf(yi[2], yi[3])};
      f32x4 re = mfma16(G0, as8(yb), z4), im = mfma16(G1, as8(yb), z4);
      f32x4 tr = re * ics[j] - im * isn[j], ti = im * ics[j] + re * isn[j];
#pragma unroll
      for (int r = 0; r < 4; ++r) pk[j][r] = pk_bf(tr[r], ti[r]);
    }
#pragma unroll
    for (int r = 0; r < 4; ++r) *(LDSP(u32x2))(wr0 + XB + r * RB) = u32x2{pk[0][r], pk[1][r]};
  }
  __syncthreads();
  char* out = (char*)(ZT + (size_t)(2 * w) * 8 * 21 * 16 + (m1 * 21 + tile) * 16);
  const u32 vout = (u32)(64 * g * 10752 + c * 4);
  u32x4 bz[2] = {*(const LDSP(u32x4))(rd0 + XB), *(const LDSP(u32x4))(rd0 + XB + RB)};
  s16x8 I0 = *(const s16x8*)(tb + 6144), I1 = *(const s16x8*)(tb + 7168);
  batch_fence(); pin_arr(bz); pin(I0); pin(I1);
#pragma unroll
  for (int j = 0; j < 2; ++j) {
    const u32x4 b = bz[j];
    f32x4 zr = mfma16(I0, as8(b), z4), zi = mfma16(I1, as8(b), z4);
#pragma unroll
    for (int r = 0; r < 4; ++r) stg_wt(out + (16 * r + j) * 10752, vout, pk_bf(zr[r], zi[r]));
  }
}

constexpr int Y_RS = 640 + 4;
extern "C" __global__ __launch_bounds__(640) void k4_inv(const u32* __restrict__ ZT, const u16* __restrict__ x, const float* __restrict__ mod,
                                              u16* __restrict__ x1o, u16* __restrict__ h2o) {
  __shared__ __attribute__((aligned(16))) char smem[162 * 64 + 8 * Y_RS * 4 + 10 * 8 * 4 + 8 * 1024 + 10752 + 256];
  char* Vs = smem; float* Y = (float*)(Vs + 162 * 64); float* part = Y + 8 * Y_RS; char* tab = (char*)(part + 80);
  const int tid = threadIdx.x, lane = tid & 63, w = __builtin_amdgcn_readfirstlane(tid >> 6), c = lane & 15, g = lane >> 4;
  const int s0 = row_group(blockIdx.x);
  u32* Zs = (u32*)(tab + 8 * 1024);
  {
    const char* Zb = (const char*)(ZT + (size_t)s0 * 8 * 21 * 16);
    dma1k(Zb + 1024 * w, (u32)lane * 16u, lds_addr(Zs) + 1024 * w);
    if (w == 0 && lane < 32) dma1k(Zb + 10240, (u32)lane * 16u, lds_addr(Zs) + 10240);
  }
  const int rho = w < 8 ? w : (c & 7), P = rho >> 1, mA = rho == 1 ? 4 : P, mB = P == 0 ? 4 : 8 - P;
  const s16x8 tfrag = w < 6 ? ldg<s16x8>(&g_f20ai.v[0][0][0][0] + w * 512, lane * 16) : ldg<s16x8>(g_f32bi.v[(w - 6) & 1], lane * 16);
  const int hf = w >= 5, cp = tid - 320 * hf;
  u32 xv[4];
#pragma unroll
  for (int r = 0; r < 4; ++r) xv[r] = ldg<u32>(x + (size_t)(s0 + 256 * (4 * hf + r)) * D_, 4 * cp);
  const f32x2 ga = ldg<f32x2>(mod + 2 * 640, 8 * cp), sb = ldg<f32x2>(mod + 3 * 640, 8 * cp), cb = ldg<f32x2>(mod + 4 * 640, 8 * cp);
  f32x4 tw[3]; u32 aw[3];
  {
    const bool rot = rho >= 3 && (rho & 1);
    const float sc = w < 8 ? 1.0f / (2048.0f * 640.0f) : 2.0f / (2048.0f * 640.0f), fk = (float)(w < 8 ? c : 16) * (1.0f / 640.0f);
    float s0_, c0_, s1_, c1_, s8_, c8_;
    sincos_rev(fk * (float)(2 * g), s0_, c0_); sincos_rev(fk, s1_, c1_); sincos_rev(fk * 8.0f, s8_, c8_);
    float br = (rot ? -s0_ : c0_) * sc, bi = (rot ? c0_ : s0_) * sc;
#pragma unroll
    for (int mt = 0; mt < 3; ++mt) {
      tw[mt] = f32x4{br, bi, br * c1_ - bi * s1_, br * s1_ + bi * c1_};
      const float nr = br * c8_ - bi * s8_, ni = br * s8_ + bi * c8_; br = nr; bi = ni;
      const int d0 = 8 * mt + 2 * g;
      const bool ok = d0 < 20 && (w < 8 || c < 8);
      aw[mt] = lds_addr(Vs) + (ok ? (rho * 20 + d0) * 64 : 160 * 64) + (w < 8 ? 4 * c : 2);
      pin(tw[mt]); pin(aw[mt]);
    }
  }
  float be = rho < 2 ? 0.f : (rho & 1) ? -1.f : 1.f;
  u32 za0, za1, dbits, zdel = (u32)(mB - mA) * 21u * 64u;
  {
    const u32 zb = lds_addr(Zs) + mA * 21 * 64;
    if (w < 8) { za0 = zb + (64 * g + c) * 4; za1 = zb + 16 * 64 + c * 4; dbits = c == 0 ? (0xF0u | (g == 2 ? 8u : g == 3 ? 15u : 0u)) : 0u; }
    else {
      za0 = zb + (g == 3 ? 12 : 11 - 3) * 64; za1 = zb + 16 * 64; dbits = (g < 2 ? 0xFu : g == 2 ? 3u : 0u) | (g == 0 ? 0u : 0xF0u);
    }
  }
  const u32 z10 = lds_addr(Zs) + (mA * 21 + 20) * 64;
  pin(be); pin(za0); pin(za1); pin(dbits); pin(zdel);
  if (w == 9 && blockIdx.x < 8) {
    const void* c5; const void* c6; CODE_ADDR(c5, k5_glu); CODE_ADDR(c6, k6_down);
    const u32 len = (u32)((const char*)c6 - (const char*)c5);
    if (lane * 128u < len) l2_touch(c5, (u32)lane * 128u, lds_addr(smem) + sizeof(smem) - 256);
    if (lane * 128u + 8192u < len) l2_touch(c5, (u32)lane * 128u + 8192u, lds_addr(smem) + sizeof(smem) - 256);
  }
  if (w < 8) *(s16x8*)(tab + w * 1024 + lane * 16) = tfrag;
  wait_vm<4>();
  __syncthreads();
  if (w < 9) {
    u32 zz[8], zy[8]; s16x8 ft[6];
#pragma unroll
    for (int i = 0; i < 8; ++i) {
      u32 ad = (i < 4 ? za0 : za1) + 64 * (i & 3);
      if (i == 2) ad = (w == 8 && g == 2) ? z10 : ad;
      zz[i] = *(const LDSP(u32))(size_t)ad; zy[i] = *(const LDSP(u32))(size_t)(ad + zdel);
    }
#pragma unroll
    for (int k = 0; k < 6; ++k) ft[k] = *(const s16x8*)(tab + k * 1024 + lane * 16);
    batch_fence(); pin_arr(zz); pin_arr(zy); pin_arr(ft);
    u32x4 b0, b1;
#pragma unroll
    for (int i = 0; i < 8; ++i) {
      const u32 keep = ~(u32)__builtin_amdgcn_sbfe((int)dbits, i, 1);
      const u32 p_ = zz[i] & keep, q_ = zy[i] & keep;
      const f32x2 a = {bf_lo(p_), bf_hi(p_)}, b = {bf_lo(q_), bf_hi(q_)};
      const f32x2 v = a + be * b;
      if (i < 4) b0[i] = pk_bf(v[0], v[1]); else b1[i - 4] = pk_bf(v[0], v[1]);
    }
    f32x4 a[3];
#pragma unroll
    for (int mt = 0; mt < 3; ++mt) {
      const f32x4 z = {0.f, 0.f, 0.f, 0.f};
      a[mt] = mfma16(ft[2 * mt + 1], as8(b1), mfma16(ft[2 * mt], as8(b0), z));
    }
    if (w < 8) {
      const u32 hdel = c == 0 ? lds_addr(Vs) + 160 * 64 + 2 : 0u;
#pragma unroll
      for (int mt = 0; mt < 3; ++mt)
#pragma unroll
        for (int rr = 0; rr < 2; ++rr) {
          const u32 pv = pk_bf(a[mt][2 * rr] * tw[mt][2 * rr] - a[mt][2 * rr + 1] * tw[mt][2 * rr + 1], a[mt][2 * rr] * tw[mt][2 * rr + 1] + a[mt][2 * rr + 1] * tw[mt][2 * rr]);
          *(LDSP(u16))(size_t)(aw[mt] + 64 * rr) = (u16)pv;
          *(LDSP(u16))(size_t)((c == 0 ? hdel : aw[mt] + 2) + 64 * rr) = (u16)(pv >> 16);
        }
    } else {
#pragma unroll
      for (int mt = 0; mt < 3; ++mt)
#pragma unroll
        for (int rr = 0; rr < 2; ++rr)
          *(LDSP(u16))((LDSP(char))(size_t)aw[mt] + 64 * rr) = (u16)pk_bf(a[mt][2 * rr] * tw[mt][2 * rr] - a[mt][2 * rr + 1] * tw[mt][2 * rr + 1], 0.f);
    }
  }
  __syncthreads();
  {
    s16x8 a = *(const s16x8*)(Vs + (16 * w + c) * 64 + 16 * g);
    s16x8 fb0 = *(const s16x8*)(tab + 6144 + lane * 16), fb1 = *(const s16x8*)(tab + 7168 + lane * 16);
    batch_fence(); pin(a); pin(fb0); pin(fb1);
    const f32x4 z = {0.f, 0.f, 0.f, 0.f};
    const f32x4 y0 = mfma16(a, fb0, z), y1 = mfma16(a, fb1, z);
    const int R = 16 * w + 4 * g, rr = R / 20, d0 = R % 20;
    *(f32x4*)(Y + rr * Y_RS + 20 * c + d0) = y0;
    *(f32x4*)(Y + rr * Y_RS + 20 * (16 + c) + d0) = y1;
  }
  __syncthreads();
  {
    f32x2 R[8];
#pragma unroll
    for (int r = 0; r < 8; ++r) R[r] = *(const f32x2*)(Y + r * Y_RS + 2 * cp);
    batch_fence(); pin_arr(R);
    const float cc = 0.70710678118654752f;
    const f32x2 G = R[0] + R[1], Fm = R[0] - R[1];
    const f32x2 u = cc * (R[2] - R[6]), v = cc * (R[3] + R[7]);
    const f32x2 c13 = R[2] + R[6], s13 = R[3] - R[7];
    const float sg = hf ? -1.f : 1.f;
    f32x2 y[4];
    y[0] = G + R[4] + sg * c13;
    y[2] = G - R[4] + sg * s13;
    y[1] = Fm + R[5] + sg * (u + v);
    y[3] = Fm - R[5] + sg * (v - u);
    u32 x1[4];
    float sv[8];
#pragma unroll
    for (int r = 0; r < 4; ++r) {
      const f32x2 xx = {bf_lo(xv[r]), bf_hi(xv[r])};
      const f32x2 t = xx + ga * y[r];
      x1[r] = pk_bf(t[0], t[1]);
      stg_wt(x1o + (size_t)(8 * s0 + 4 * hf + r) * D_, (u32)cp * 4u, x1[r]);
      sv[r] = dot2bf(x1[r], 0x3f803f80u, 0.f); sv[4 + r] = dot2bf(x1[r], x1[r], 0.f);
    }
    const float tot = reduce8(sv, lane);
    if (lane < 8) part[w * 8 + lane] = tot;
    const f32x2 gg = {rbf(1.0f + cb[0]), rbf(1.0f + cb[1])};
    const int d = 2 * cp, wi = d & 31;
    char* stage = (char*)Y;
    const int so = (((d >> 5) * 4 + ((wi & 15) >> 2)) * 8 + 4 * hf) * 16 + (4 * (wi >> 4) + (wi & 3)) * 2;
    __syncthreads();
    const int r = lane & 3;
    float s, qq;
    { float ps[5], pq[5];
#pragma unroll
      for (int i = 0; i < 5; ++i) { ps[i] = part[(5 * hf + i) * 8 + r]; pq[i] = part[(5 * hf + i) * 8 + 4 + r]; }
      batch_fence(); pin_arr(ps); pin_arr(pq);
      s = ((ps[0] + ps[1]) + (ps[2] + ps[3])) + ps[4]; qq = ((pq[0] + pq[1]) + (pq[2] + pq[3])) + pq[4]; }
    float m_, i_; ln_finish(s, qq, m_, i_);
    const float nb_ = -m_ * i_;
#pragma unroll
    for (int i = 0; i < 4; ++i) {
      const float na = __int_as_float(__builtin_amdgcn_readlane(__float_as_int(i_), i)), nb = __int_as_float(__builtin_amdgcn_readlane(__float_as_int(nb_), i));
      const f32x2 v2 = {bf_lo(x1[i]), bf_hi(x1[i])};
      const f32x2 hv = (v2 * na + nb) * gg + sb;
      *(u32*)(stage + so + 16 * i) = pk_bf(hv[0], hv[1]);
    }
    __syncthreads();
    const u32x4 piece = *(const u32x4*)(stage + 16 * tid);
    stg_wt(h2o + ((size_t)(s0 >> 1) * 20 * 64 + 8 * (s0 & 1)) * 8, (u32)((tid >> 3) * 16 + (tid & 7)) * 16u, piece);
  }
  if (w == 9) asm volatile("s_waitcnt vmcnt(0)" ::: "memory");
}

constexpr int K5_SLOT = 28 * 1024;
constexpr int K5_NS = 4;
constexpr int K5_PD = 4;
struct K5Ctx { const u16* wsrc; const u16* hsrc; u32 ldsw; u32 goff[7]; u32 hoff; u32 abase[2]; };
template <int s, int i> DI void k5_w1(const K5Ctx& c) {
  if constexpr (i == 0) set_m0(c.ldsw + (s % K5_NS) * K5_SLOT + 3072);
  glds16_s<1024 * i - 3072>((const char*)(c.wsrc + s * 4 * 512) - 4096, c.goff[i]);
}
template <int s, int i> DI void k5_t1(const K5Ctx& c, u32x4 (&t)[4]) {
  gload16_s<(i >> 1) * 1024>(t[i], c.hsrc + ((i & 1) * 20 + 2 * s) * 512, c.hoff);
}
template <int s> DI void k5_wall(const K5Ctx& c) { k5_w1<s, 0>(c); k5_w1<s, 1>(c); k5_w1<s, 2>(c); k5_w1<s, 3>(c); k5_w1<s, 4>(c); k5_w1<s, 5>(c); k5_w1<s, 6>(c); }
template <int s> DI void k5_tall(const K5Ctx& c, u32x4 (&t)[4]) { k5_t1<s, 0>(c, t); k5_t1<s, 1>(c, t); k5_t1<s, 2>(c, t); k5_t1<s, 3>(c, t); }
template <int n> DI s16x8 k5_rd(const K5Ctx& c) {
  constexpr int s = n / 28, t = (n / 14) & 1, j = n % 14, sl = s % K5_NS;
  return *(const LDSP(const s16x8))((LDSP(const char))(size_t)c.abase[sl >> 1] + (sl & 1) * K5_SLOT + ((j >> 1) * 4 + t * 2 + (j & 1)) * 1024);
}
template <int n> DI void k5_sub(const K5Ctx& c, f32x4 (&acc)[14][2], s16x8 (&fr)[K5_PD + 1], u32x4 (&tok)[10][4]) {
  constexpr int s = n / 28, t = (n / 14) & 1, j = n % 14;
  constexpr int nb = n + K5_PD;
  if constexpr (nb < 280 && nb % 28 == 0) {
    wait_vm<(s + 2 <= 9) ? 11 : 0>();
    __builtin_amdgcn_s_barrier();
    tie4(tok[s + 1]);
  }
  if constexpr (nb < 280) fr[nb % (K5_PD + 1)] = k5_rd<nb>(c);
  {
    constexpr int q = nb / 28, pos = nb % 28 - (q == 0 ? K5_PD : 0);
    constexpr int idx = (pos % 2 == 0 && pos < 22) ? pos / 2 : -1;
    if constexpr (idx >= 0 && idx < 4 && q + 2 <= 9) k5_t1<q + 2, idx>(c, tok[q + 2]);
    if constexpr (idx >= 4 && q + 2 <= 9) k5_w1<q + 2, idx - 4>(c);
  }
  const s16x8 a = fr[n % (K5_PD + 1)];
  acc[j][0] = mfma16(a, as8(tok[s][2 * t]), acc[j][0]);
  acc[j][1] = mfma16(a, as8(tok[s][2 * t + 1]), acc[j][1]);
  __builtin_amdgcn_sched_barrier(0);
}
template <int... I> DI void k5_all(const K5Ctx& c, f32x4 (&acc)[14][2], s16x8 (&fr)[K5_PD + 1], u32x4 (&tok)[10][4], std::integer_sequence<int, I...>) { (k5_sub<I>(c, acc, fr, tok), ...); }
extern "C" __global__ __launch_bounds__(256, 2) void k5_glu(const u16* __restrict__ h2, const u16* __restrict__ Wgu, u16* __restrict__ hb) {
  extern __shared__ __attribute__((aligned(16))) char smem[];
  const int tid = threadIdx.x, lane = tid & 63, w = __builtin_amdgcn_readfirstlane(tid >> 6);
  const int xcd = blockIdx.x & 7, li = blockIdx.x >> 3;
  const int tt = 8 * (xcd & 1) + (li & 7), fc = 4 * (xcd >> 1) + (li >> 3);
  const int u0 = fc < 12 ? 7 * fc : 84 + 6 * (fc - 12), U = fc < 12 ? 7 : 6;
  if (blockIdx.x < 8) {
    const void* c6; const void* c7; CODE_ADDR(c6, k6_down); CODE_ADDR(c7, k7_end);
    if ((u32)tid * 128u < (u32)((const char*)c7 - (const char*)c6)) l2_touch(c6, (u32)tid * 128u, lds_addr(smem) + 3 * K5_SLOT + w * 256);
  }
  K5Ctx c;
  c.wsrc = Wgu + (size_t)u0 * 20 * 2 * 512;
#pragma unroll
  for (int i = 0; i < 7; ++i) { const int f = 7 * w + i, u = min(f >> 2, U - 1); c.goff[i] = 2 * (u * 20 * 2 * 512 + (f & 3) * 512) + lane * 16 + 4096 - (1024 * i - 3072); }
  c.ldsw = lds_addr(smem) + 7 * w * 1024;
  const int T16 = 8 * tt + 2 * w;
  c.hsrc = h2 + (size_t)T16 * 20 * 512; c.hoff = lane * 16;
#pragma unroll
  for (int j = 0; j < 2; ++j) { c.abase[j] = lds_addr(smem) + 2 * j * K5_SLOT + lane * 16; asm volatile("" : "+v"(c.abase[j])); }
  u32x4 tok[10][4];
  k5_tall<0>(c, tok[0]); k5_wall<0>(c);
  k5_tall<1>(c, tok[1]); k5_wall<1>(c);
  f32x4 acc[14][2];
#pragma unroll
  for (int u = 0; u < 14; ++u) { acc[u][0] = f32x4{0.f, 0.f, 0.f, 0.f}; acc[u][1] = f32x4{0.f, 0.f, 0.f, 0.f}; }
  wait_vm<11>();
  __builtin_amdgcn_s_barrier();
  tie4(tok[0]);
  s16x8 fr[K5_PD + 1];
  fr[0] = k5_rd<0>(c); fr[1] = k5_rd<1>(c); fr[2] = k5_rd<2>(c); fr[3] = k5_rd<3>(c);
  __builtin_amdgcn_sched_barrier(0);
  k5_all(c, acc, fr, tok, std::make_integer_sequence<int, 280>{});
  auto hv = [&](int u, int tl) { const f32x4 a = acc[2 * u][tl] * acc[2 * u + 1][tl]; return u32x2{pk_bf(a[0], a[1]), pk_bf(a[2], a[3])}; };
#pragma unroll
  for (int tl = 0; tl < 2; ++tl) {
    u16* hdst = hb + (size_t)(T16 + tl) * 54 * 512 + (u0 >> 1) * 512;
    const u32 vo = (u32)lane * 16u;
    if (!(u0 & 1)) {
#pragma unroll
      for (int k = 0; k < 3; ++k) { const u32x2 a = hv(2 * k, tl), b = hv(2 * k + 1, tl); stg_wt(hdst + k * 512, vo, u32x4{a[0], a[1], b[0], b[1]}); }
      if (U == 7) stg_wt(hdst + 3 * 512, vo, hv(6, tl));
    } else {
      stg_wt(hdst + 4, vo, hv(0, tl));
#pragma unroll
      for (int k = 0; k < 3; ++k) { const u32x2 a = hv(2 * k + 1, tl), b = hv(2 * k + 2, tl); stg_wt(hdst + (k + 1) * 512, vo, u32x4{a[0], a[1], b[0], b[1]}); }
    }
  }
}

constexpr int K6_D = K6DV, K6_NS = K6_D + 1;
struct K6Ctx { const u16* wsrc; const u16* hsrc; u32 hoff; const float* gbp; u32 gbo; const u16* xp; u32 xo; };
struct K6Grp { u32x4 a[5]; u32x4 t[4]; };
template <int i> DI void k6_v1(const K6Ctx& c, int ks, K6Grp& gr) {
  if constexpr (i < 5) gload16_s<0>(gr.a[i], c.wsrc + (ks + i * 54) * 512, c.hoff);
  else gload16_s<0>(gr.t[i - 5], c.hsrc + (ks + (i - 5) * 54) * 512, c.hoff);
}
DI void k6_issue(const K6Ctx& c, int ks, K6Grp& gr) {
  k6_v1<0>(c, ks, gr); k6_v1<5>(c, ks, gr); k6_v1<6>(c, ks, gr); k6_v1<7>(c, ks, gr); k6_v1<8>(c, ks, gr);
  k6_v1<1>(c, ks, gr); k6_v1<2>(c, ks, gr); k6_v1<3>(c, ks, gr); k6_v1<4>(c, ks, gr);
}
DI void k6_tie(K6Grp& g) { asm volatile("" : "+v"(g.a[0]), "+v"(g.a[1]), "+v"(g.a[2]), "+v"(g.a[3]), "+v"(g.a[4]), "+v"(g.t[0]), "+v"(g.t[1]), "+v"(g.t[2]), "+v"(g.t[3])); }
struct K6Epi { u32x4 gb[5]; u32x2 xr[5]; };
template <int OFF> DI void gload8_s(u32x2& d, const void* sbase, u32 voff) {
  asm volatile("global_load_dwordx2 %0, %1, %2 offset:%3" : "=v"(d) : "v"(voff), "s"(sbase), "n"(OFF) : "memory");
}
constexpr int k6_after(int i, int NK) {
  int n = 0; bool seen = false;
  for (int g = 0; g < K6_D; ++g) { if (seen) n += 9; if (g == i) seen = true; }
  for (int st = 0; st < i; ++st) {
    if (st == NK - K6_D && seen) n += 10;
    if (st + K6_D < NK) { if (seen) n += 9; if (st + K6_D == i) seen = true; }
  }
  return n;
}
template <int i, int NK> DI void k6_step(const K6Ctx& c, int ks0, f32x4 (&acc)[5][4], K6Grp (&gr)[K6_NS], K6Epi& ep, f32x4* red, int w, int lane) {
  wait_vm<k6_after(i, NK)>();
  K6Grp& g = gr[i % K6_NS];
  k6_tie(g);
  K6Grp& nx = gr[(i + K6_D) % K6_NS];
#pragma unroll
  for (int nt = 0; nt < 5; ++nt) {
    if constexpr (i + K6_D < NK) {
      if (nt == 0) { k6_v1<0>(c, ks0 + i + K6_D, nx); k6_v1<5>(c, ks0 + i + K6_D, nx); }
      if (nt == 1) { k6_v1<6>(c, ks0 + i + K6_D, nx); k6_v1<7>(c, ks0 + i + K6_D, nx); }
      if (nt == 2) { k6_v1<8>(c, ks0 + i + K6_D, nx); k6_v1<1>(c, ks0 + i + K6_D, nx); }
      if (nt == 3) { k6_v1<2>(c, ks0 + i + K6_D, nx); k6_v1<3>(c, ks0 + i + K6_D, nx); }
      if (nt == 4) { k6_v1<4>(c, ks0 + i + K6_D, nx); }
    }
    if constexpr (i == NK - K6_D) {
      gload16_s<0>(ep.gb[nt], c.gbp + 16 * nt, c.gbo); gload8_s<0>(ep.xr[nt], c.xp + 16 * nt, c.xo);
    }
#pragma unroll
    for (int t = 0; t < 4; ++t) acc[nt][t] = mfma16(as8(g.a[nt]), as8(g.t[t]), acc[nt][t]);
    if constexpr (i == NK - 1) {
      if (nt >= 1) {
#pragma unroll
        for (int t = 0; t < 4; ++t) if (t != w) red[((w * 5 + nt - 1) * 4 + t) * 64 + lane] = acc[nt - 1][t];
      }
    }
    __builtin_amdgcn_sched_barrier(0);
  }
}
template <int i, int NK> struct K6Loop { static DI void run(const K6Ctx& c, int ks0, f32x4 (&acc)[5][4], K6Grp (&gr)[K6_NS], K6Epi& ep, f32x4* red, int w, int lane) {
  k6_step<i, NK>(c, ks0, acc, gr, ep, red, w, lane); K6Loop<i + 1, NK>::run(c, ks0, acc, gr, ep, red, w, lane); } };
template <int NK> struct K6Loop<NK, NK> { static DI void run(const K6Ctx&, int, f32x4 (&)[5][4], K6Grp (&)[K6_NS], K6Epi&, f32x4*, int, int) {} };
template <int NK> DI void k6_main(const K6Ctx& cx, int ks0, f32x4* red, int w, int lane, u16* __restrict__ out, u32 oo) {
  K6Grp gr[K6_NS]; K6Epi ep;
#pragma unroll
  for (int g = 0; g < K6_D; ++g) k6_issue(cx, ks0 + g, gr[g]);
  f32x4 acc[5][4];
#pragma unroll
  for (int a = 0; a < 5; ++a)
#pragma unroll
    for (int b = 0; b < 4; ++b) acc[a][b] = f32x4{0.f, 0.f, 0.f, 0.f};
  K6Loop<0, NK>::run(cx, ks0, acc, gr, ep, red, w, lane);
#pragma unroll
  for (int t = 0; t < 4; ++t) if (t != w) red[((w * 5 + 4) * 4 + t) * 64 + lane] = acc[4][t];
  wait_vm<0>();
  asm volatile("" : "+v"(ep.gb[0]), "+v"(ep.gb[1]), "+v"(ep.gb[2]), "+v"(ep.gb[3]), "+v"(ep.gb[4]), "+v"(ep.xr[0]), "+v"(ep.xr[1]), "+v"(ep.xr[2]), "+v"(ep.xr[3]), "+v"(ep.xr[4]));
  __syncthreads();
#pragma unroll
  for (int nt = 0; nt < 5; ++nt) {
    f32x4 v = w == 0 ? acc[nt][0] : w == 1 ? acc[nt][1] : w == 2 ? acc[nt][2] : acc[nt][3];
#pragma unroll
    for (int ww = 1; ww < 4; ++ww) v += red[((((w + ww) & 3) * 5 + nt) * 4 + w) * 64 + lane];
    const f32x4 gb = __builtin_bit_cast(f32x4, ep.gb[nt]);
    const f32x4 x4 = {bf_lo(ep.xr[nt][0]), bf_hi(ep.xr[nt][0]), bf_lo(ep.xr[nt][1]), bf_hi(ep.xr[nt][1])};
    const f32x4 o = x4 + gb * v;
    stg_wt(out + 16 * nt, oo, u32x2{pk_bf(o[0], o[1]), pk_bf(o[2], o[3])});
  }
}
extern "C" __global__ __launch_bounds__(256, 2) void k6_down(const u16* __restrict__ hb, const u16* __restrict__ Wd, const u16* __restrict__ x1, const float* __restrict__ mod, u16* __restrict__ out) {
  extern __shared__ __attribute__((aligned(16))) char smem[];
  const int tid = threadIdx.x, lane = tid & 63, w = __builtin_amdgcn_readfirstlane(tid >> 6), c = lane & 15, g = lane >> 4;
  const int xcd = blockIdx.x & 7, li = blockIdx.x >> 3;
  const int tt = 8 * (xcd & 3) + (li & 7), nb = 4 * (xcd >> 2) + (li >> 3);
  const int n0 = 80 * nb;
  const int ks0 = w < 2 ? 14 * w : 28 + 13 * (w - 2);
  K6Ctx cx;
  cx.wsrc = Wd + (size_t)(5 * nb) * 54 * 512; cx.hsrc = hb + (size_t)(4 * tt) * 54 * 512; cx.hoff = lane * 16;
  cx.gbp = mod + 5 * 640 + n0; cx.gbo = 16 * g;
  cx.xp = x1 + (size_t)(64 * tt + 16 * w) * D_ + n0; cx.xo = (u32)(c * D_ + 4 * g) * 2u;
  const int tau = 64 * tt + 16 * w + c, srow = (tau >> 3) + 256 * (tau & 7);
  const u32 oo = (u32)(srow * D_ + 4 * g) * 2u;
  if (w < 2) k6_main<14>(cx, ks0, (f32x4*)smem, w, lane, out + n0, oo); else k6_main<13>(cx, ks0, (f32x4*)smem, w, lane, out + n0, oo);
}

extern "C" __global__ void k7_end() {}

constexpr size_t WS_MOD = 0, WS_XT = 16384, WS_ZT = WS_XT + 8 * 21 * 256 * 16 * 4, WS_X1 = WS_ZT + 8 * 21 * 256 * 16 * 4,
                 WS_H2 = WS_X1 + 2048 * 640 * 2, WS_HB = WS_H2 + 2048 * 640 * 2, WS_WGU = WS_HB + 2048 * 1728 * 2, WS_WD = WS_WGU + 2 * 640 * 1728 * 2, WS_SYNC = WS_WD + 640 * 1728 * 2;
extern "C" void kernel_launch(void* const* d_in, const int* in_sizes, int n_in, void* d_out, int out_size, void* d_ws, size_t ws_size,
                              hipStream_t stream) {
  const u16* x = (const u16*)d_in[0]; const u16* wr = (const u16*)d_in[1]; const u16* wi = (const u16*)d_in[2];
  const u16* wg = (const u16*)d_in[4]; const u16* wu = (const u16*)d_in[5]; const u16* wd = (const u16*)d_in[6];
  const u16* c = (const u16*)d_in[7]; const u16* wmod = (const u16*)d_in[8];
  char* ws = (char*)d_ws;
  float* mod = (float*)(ws + WS_MOD); u32* XT = (u32*)(ws + WS_XT); u32* ZT = (u32*)(ws + WS_ZT);
  u16* x1 = (u16*)(ws + WS_X1); u16* h2 = (u16*)(ws + WS_H2); u16* hb = (u16*)(ws + WS_HB);
  u16* Wgu = (u16*)(ws + WS_WGU); u16* Wd = (u16*)(ws + WS_WD);
  K2Args A;
  A.x = x; A.c = c; A.wmod = wmod; A.mod = mod; A.XT = XT; A.wr = wr; A.wi = wi; A.wg = wg; A.wu = wu; A.Wgu = Wgu; A.wd = wd; A.Wd = Wd;
  A.gen = (u32*)(ws + WS_SYNC); A.arr = (u32*)(ws + WS_SYNC + 4096); A.gran = (unsigned long long*)(ws + WS_SYNC + 8192);
  k2_fwd<<<K2_GRID, 640, 0, stream>>>(A);
  k3_mid<<<168, 512, 0, stream>>>(XT, wr, wi, ZT);
  k4_inv<<<256, 640, 0, stream>>>(ZT, x, mod, x1, h2);
  hipFuncSetAttribute((const void*)k5_glu, hipFuncAttributeMaxDynamicSharedMemorySize, K5_NS * K5_SLOT);
  k5_glu<<<256, 256, K5_NS * K5_SLOT, stream>>>(h2, Wgu, hb);
  hipFuncSetAttribute((const void*)k6_down, hipFuncAttributeMaxDynamicSharedMemorySize, 81920);
  k6_down<<<256, 256, 81920, stream>>>(hb, Wd, x1, mod, (u16*)d_out);
}
```

```cpp
#include <hip/hip_runtime.h>
#include <hip/hip_bf16.h>
#include <stdint.h>
#include <utility>

typedef unsigned short u16;
typedef unsigned int u32;
typedef short s16x4 __attribute__((ext_vector_type(4)));
typedef short s16x8 __attribute__((ext_vector_type(8)));
typedef __bf16 bf2_t __attribute__((ext_vector_type(2)));
typedef float f32x2 __attribute__((ext_vector_type(2)));
typedef float f32x4 __attribute__((ext_vector_type(4)));
typedef u32 u32x2 __attribute__((ext_vector_type(2)));
typedef u32 u32x4 __attribute__((ext_vector_type(4)));
#define DI __device__ __forceinline__
#define LDSP(T) __attribute__((address_space(3))) T*

constexpr int S_ = 2048, D_ = 640, F_ = 1728, DF = 321;

constexpr double kPi = 3.14159265358979323846264338327950288;
constexpr double c_sin0(double x) { double x2 = x * x, t = x, s = x; for (int i = 1; i < 11; ++i) { t *= -x2 / ((2.0 * i) * (2.0 * i + 1.0)); s += t; } return s; }
constexpr double c_cos0(double x) { double x2 = x * x, t = 1.0, s = 1.0; for (int i = 1; i < 11; ++i) { t *= -x2 / ((2.0 * i - 1.0) * (2.0 * i)); s += t; } return s; }
constexpr double c_cos(long n, long N) {
  n %= N; if (n < 0) n += N;
  if (2 * n > N) n = N - n;
  bool neg = false;
  if (4 * n > N) { neg = true; n = N - 2 * n; N = 2 * N; }
  double r = 0;
  if (4 * n == N) r = 0.0;
  else if (8 * n > N) r = c_sin0(2 * kPi * (double)(N - 4 * n) / (double)(4 * N));
  else r = c_cos0(2 * kPi * (double)n / (double)N);
  return neg ? -r : r;
}
constexpr double c_sin(long n, long N) { return c_cos(4 * n - N, 4 * N); }
constexpr u16 c_bf16(double v) {
  float f = (float)v; u32 u = __builtin_bit_cast(u32, f);
  u += 0x7fffu + ((u >> 16) & 1u); return (u16)(u >> 16);
}

struct F8w { u16 v[2][64][8]; };
struct F8x6 { u16 v[3][2][64][8]; };
constexpr F8w mk_f32a() {
  F8w t{};
  for (int tt = 0; tt < 2; ++tt) for (int l = 0; l < 64; ++l) for (int j = 0; j < 8; ++j) {
    int o = 16 * tt + (l & 15), d1 = 8 * (l >> 4) + j; double v = 0;
    if (o == 0) v = 1.0; else if (o == 1) v = (d1 & 1) ? -1.0 : 1.0;
    else { int k1 = o >> 1; v = (o & 1) ? -c_sin(d1 * k1, 32) : c_cos(d1 * k1, 32); }
    t.v[tt][l][j] = c_bf16(v);
  }
  return t;
}
constexpr F8x6 mk_f20(bool inv) {
  F8x6 t{};
  for (int mt = 0; mt < 3; ++mt) for (int ks = 0; ks < 2; ++ks) for (int l = 0; l < 64; ++l) for (int j = 0; j < 8; ++j) {
    int row = l & 15, g = l >> 4, k0 = 8 * mt + (row >> 1), part = row & 1, d0 = 16 * ks + 4 * g + (j >> 1), pp = j & 1; double v = 0;
    if (d0 < 20 && k0 < 20) {
      double co = c_cos(d0 * k0, 20), si = inv ? -c_sin(d0 * k0, 20) : c_sin(d0 * k0, 20);
      v = part == 0 ? (pp == 0 ? co : si) : (pp == 0 ? -si : co);
    }
    t.v[mt][ks][l][j] = c_bf16(v);
  }
  return t;
}
constexpr F8w mk_a1(int m1, bool inv) {
  F8w t{};
  for (int l = 0; l < 64; ++l) for (int i = 0; i < 4; ++i) {
    int c = l & 15, a = 4 * (l >> 4) + i, n = inv ? c * (8 * a + m1) : a * (8 * c + m1);
    double co = c_cos(n, 128), si = inv ? -c_sin(n, 128) : c_sin(n, 128);
    t.v[0][l][2 * i] = c_bf16(co); t.v[0][l][2 * i + 1] = c_bf16(si);
    t.v[1][l][2 * i] = c_bf16(-si); t.v[1][l][2 * i + 1] = c_bf16(co);
  }
  return t;
}
constexpr F8w mk_f16(bool inv) {
  F8w t{};
  for (int l = 0; l < 64; ++l) for (int i = 0; i < 4; ++i) {
    int c = l & 15, a = 4 * (l >> 4) + i;
    double co = c_cos(a * c, 16), si = c_sin(a * c, 16);
    if (!inv) { t.v[0][l][2 * i] = c_bf16(co); t.v[0][l][2 * i + 1] = c_bf16(si); t.v[1][l][2 * i] = c_bf16(-si); t.v[1][l][2 * i + 1] = c_bf16(co); }
    else { t.v[0][l][i] = c_bf16(co); t.v[0][l][4 + i] = c_bf16(-si); t.v[1][l][i] = c_bf16(si); t.v[1][l][4 + i] = c_bf16(co); }
  }
  return t;
}
constexpr F8w mk_f32bi() {
  F8w t{};
  for (int nt = 0; nt < 2; ++nt) for (int l = 0; l < 64; ++l) for (int j = 0; j < 8; ++j) {
    int d1 = 16 * nt + (l & 15), K = 8 * (l >> 4) + j; double v = 0;
    if (K == 0) v = 1.0; else if (K == 1) v = (d1 & 1) ? -1.0 : 1.0;
    else { int k1 = K >> 1; v = (K & 1) ? -2.0 * c_sin(k1 * d1, 32) : 2.0 * c_cos(k1 * d1, 32); }
    t.v[nt][l][j] = c_bf16(v);
  }
  return t;
}
static __device__ const F8w g_f32a = mk_f32a();
static __device__ const F8x6 g_f20a = mk_f20(false);
static __device__ const F8x6 g_f20ai = mk_f20(true);
static __device__ const F8w g_a1[8] = {mk_a1(0, false), mk_a1(1, false), mk_a1(2, false), mk_a1(3, false), mk_a1(4, false), mk_a1(5, false), mk_a1(6, false), mk_a1(7, false)};
static __device__ const F8w g_a1i[8] = {mk_a1(0, true), mk_a1(1, true), mk_a1(2, true), mk_a1(3, true), mk_a1(4, true), mk_a1(5, true), mk_a1(6, true), mk_a1(7, true)};
static __device__ const F8w g_f16f = mk_f16(false);
static __device__ const F8w g_f16i = mk_f16(true);
static __device__ const F8w g_f32bi = mk_f32bi();

DI float bf_lo(u32 v) { return __uint_as_float(v << 16); }
DI float bf_hi(u32 v) { return __uint_as_float(v & 0xffff0000u); }
DI float bf2f(u16 v) { return __uint_as_float(((u32)v) << 16); }
DI u32 pk_bf(float a, float b) { f32x2 f = {a, b}; return __builtin_bit_cast(u32, __builtin_convertvector(f, bf2_t)); }
DI float rbf(float a) { return bf_lo(pk_bf(a, a)); }
DI float dpp_x1(float v) { return __int_as_float(__builtin_amdgcn_mov_dpp(__float_as_int(v), 0xB1, 0xf, 0xf, true)); }
DI float dpp_x2(float v) { return __int_as_float(__builtin_amdgcn_mov_dpp(__float_as_int(v), 0x4E, 0xf, 0xf, true)); }
DI float dpp_hm(float v) { return __int_as_float(__builtin_amdgcn_mov_dpp(__float_as_int(v), 0x141, 0xf, 0xf, true)); }
DI f32x4 mfma16(s16x8 a, s16x8 b, f32x4 c) { return __builtin_amdgcn_mfma_f32_16x16x32_bf16(a, b, c, 0, 0, 0); }
DI s16x4 tr_read(const void* p) { return __builtin_amdgcn_ds_read_tr16_b64_v4i16((LDSP(s16x4))p); }
DI void batch_fence() { asm volatile("" ::: "memory"); }
template <class T, int N> DI void pin_arr(T (&v)[N]) {
#pragma unroll
  for (int i = 0; i < N; ++i) asm volatile("" : "+v"(v[i]));
}
template <class T> DI void pin(T& v) { asm volatile("" : "+v"(v)); }
DI void l2_touch(const void* base, u32 voff, u32 lds) {
  asm volatile("s_mov_b32 m0, %0\n\ts_nop 0\n\tglobal_load_lds_dword %1, %2" :: "s"(lds), "v"(voff), "s"(base) : "memory");
}
DI void side_delay(int ticks10ns) {
  const unsigned long long t0 = __builtin_amdgcn_s_memrealtime();
  while (__builtin_amdgcn_s_memrealtime() - t0 < (unsigned long long)ticks10ns) __builtin_amdgcn_s_sleep(4);
}
#define GSP(T) __attribute__((address_space(1))) T*
template <class T> DI T ldg(const void* sbase, u32 voff) { GSP(const char) p = (GSP(const char))sbase; asm("" : "+s"(p)); return *(GSP(const T))(p + voff); }
DI void stg_wt(void* sbase, u32 voff, u32x4 v) { asm volatile("global_store_dwordx4 %0, %1, %2 sc1\n\ts_nop 1" :: "v"(voff), "v"(v), "s"(sbase) : "memory"); }
DI void stg_wt(void* sbase, u32 voff, u32x2 v) { asm volatile("global_store_dwordx2 %0, %1, %2 sc1\n\ts_nop 1" :: "v"(voff), "v"(v), "s"(sbase) : "memory"); }
DI void stg_wt(void* sbase, u32 voff, u32 v) { asm volatile("global_store_dword %0, %1, %2 sc1" :: "v"(voff), "v"(v), "s"(sbase) : "memory"); }
#define CODE_ADDR(var, sym) asm volatile("s_getpc_b64 s[40:41]\n\ts_add_u32 s40, s40, " #sym "@rel32@lo+4\n\ts_addc_u32 s41, s41, " #sym "@rel32@hi+12\n\ts_mov_b64 %0, s[40:41]" : "=s"(var) :: "s40", "s41", "scc")
DI u32 lds_addr(const void* p) { return (u32)(size_t)(LDSP(const char))p; }
DI s16x8 cat8(s16x4 a, s16x4 b) { return __builtin_shufflevector(a, b, 0, 1, 2, 3, 4, 5, 6, 7); }
DI s16x8 as8(u32x4 v) { return __builtin_bit_cast(s16x8, v); }
DI float dot2bf(u32 a, u32 b, float c) { return __builtin_amdgcn_fdot2_f32_bf16(__builtin_bit_cast(bf2_t, a), __builtin_bit_cast(bf2_t, b), c, false); }

DI void ln_finish(float s, float q, float& m_, float& i_) {
  const float mean = s * (1.0f / 640.0f);
  const float var = fmaxf(q * (1.0f / 640.0f) - mean * mean, 0.f);
  const float sd = rbf(__builtin_amdgcn_sqrtf(rbf(rbf(var) + 1e-5f)));
  m_ = rbf(mean); i_ = __builtin_amdgcn_rcpf(sd);
}
DI float reduce8(const float (&v)[8], int lane) {
  float b[4], c[2], d;
  const bool s2 = lane & 4, s1 = lane & 2, s0 = lane & 1;
#pragma unroll
  for (int i = 0; i < 4; ++i) { float keep = s2 ? v[4 + i] : v[i], send = s2 ? v[i] : v[4 + i]; b[i] = keep + dpp_hm(send); }
#pragma unroll
  for (int i = 0; i < 2; ++i) { float keep = s1 ? b[2 + i] : b[i], send = s1 ? b[i] : b[2 + i]; c[i] = keep + dpp_x2(send); }
  { float keep = s0 ? c[1] : c[0], send = s0 ? c[0] : c[1]; d = keep + dpp_x1(send); }
  d += __int_as_float(__builtin_amdgcn_mov_dpp(__float_as_int(d), 0x128, 0xf, 0xf, true));
  auto r16 = __builtin_amdgcn_permlane16_swap(__float_as_uint(d), __float_as_uint(d), false, false);
  d = __uint_as_float(r16[0]) + __uint_as_float(r16[1]);
  auto r32 = __builtin_amdgcn_permlane32_swap(__float_as_uint(d), __float_as_uint(d), false, false);
  return __uint_as_float(r32[0]) + __uint_as_float(r32[1]);
}

template <int N> DI void wait_vm() { asm volatile("s_waitcnt vmcnt(%0)" ::"n"(N) : "memory"); }
template <int OFF> DI void gload16_s(u32x4& d, const void* sbase, u32 voff) {
  asm volatile("global_load_dwordx4 %0, %1, %2 offset:%3" : "=v"(d) : "v"(voff), "s"(sbase), "n"(OFF) : "memory");
}
template <int OFF> DI void glds16_s(const void* sbase, u32 voff) {
  asm volatile("global_load_lds_dwordx4 %0, %1 offset:%2" :: "v"(voff), "s"(sbase), "n"(OFF) : "memory");
}
DI void dma1k(const void* sbase, u32 voff, u32 lds) {
  asm volatile("s_mov_b32 m0, %0\n\ts_nop 0\n\tglobal_load_lds_dwordx4 %1, %2" :: "s"(lds), "v"(voff), "s"(sbase) : "memory");
}
DI void set_m0(u32 v) { asm volatile("s_mov_b32 m0, %0\n\ts_nop 0" :: "s"(v) : "memory"); }
DI void tie4(u32x4 (&t)[4]) { asm volatile("" : "+v"(t[0]), "+v"(t[1]), "+v"(t[2]), "+v"(t[3])); }

DI void prep_load(u32x4 (&v)[4], const u16* __restrict__ src, int ld, int lane) {
  const u32 vo = (u32)((lane >> 3) * ld + 8 * (lane & 7)) * 2u;
#pragma unroll
  for (int i = 0; i < 4; ++i) v[i] = ldg<u32x4>(src + (size_t)8 * i * ld, vo);
}
DI void prep_store(const u32x4 (&v)[4], u16* __restrict__ dst, int jstride, char* lds, int lane) {
#pragma unroll
  for (int i = 0; i < 4; ++i) {
    const int row = 8 * i + (lane >> 3), ch = lane & 7;
    *(u32x4*)(lds + row * 128 + (((ch >> 1) ^ ((row >> 1) & 3)) * 32) + (ch & 1) * 16) = v[i];
  }
  const int c = lane & 15, g = lane >> 4, q = c >> 2, p = c & 3, r = 4 * g + q;
#pragma unroll
  for (int j = 0; j < 4; ++j) {
    const char* b = lds + r * 128 + ((j ^ ((r >> 1) & 3)) * 32) + 8 * p;
    const s16x8 f = cat8(tr_read(b), tr_read(b + 16 * 128));
    stg_wt(dst + (size_t)j * jstride, (u32)lane * 16u, __builtin_bit_cast(u32x4, f));
  }
}
DI void prep_wave(const u16* __restrict__ src, int ld, u16* __restrict__ dst, int jstride, char* lds, int lane) {
  u32x4 v[4]; prep_load(v, src, ld, lane); prep_store(v, dst, jstride, lds, lane);
}
DI void prep_gu(int task, const u16* wg, const u16* wu, u16* Wgu, char* lds, int lane) {
  const int mat = task >= 540, tk = task - 540 * mat, cg = tk / 20, ks = tk % 20;
  prep_wave((mat ? wu : wg) + (size_t)ks * 32 * F_ + 64 * cg, F_, Wgu + ((size_t)(4 * cg * 20 + ks) * 2 + mat) * 512, 20 * 2 * 512, lds, lane);
}
DI void prep_d(int task, const u16* wd, u16* Wd, char* lds, int lane) {
  const int cg = task / 54, ks = task % 54;
  prep_wave(wd + (size_t)ks * 32 * D_ + 64 * cg, D_, Wd + (size_t)(4 * cg * 54 + ks) * 512, 54 * 512, lds, lane);
}


typedef __attribute__((address_space(1))) u32 gu32;
typedef __attribute__((address_space(1))) unsigned long long gu64;
DI u32 ld_agent(const u32* p) { return __hip_atomic_load((const gu32*)p, __ATOMIC_RELAXED, __HIP_MEMORY_SCOPE_AGENT); }
DI void st_agent(u32* p, u32 v) { __hip_atomic_store((gu32*)p, v, __ATOMIC_RELAXED, __HIP_MEMORY_SCOPE_AGENT); }
DI void st_agent64(unsigned long long* p, unsigned long long v) { __hip_atomic_store((gu64*)p, v, __ATOMIC_RELAXED, __HIP_MEMORY_SCOPE_AGENT); }
constexpr int K2_SIDE_DELAY = 80;
constexpr int SPIN_LIMIT = 1 << 16;

template <int NR> DI void gemv_part(int col0, int row0, u32 tagv, bool publish, unsigned long long* gran, int gidx0, const u16* __restrict__ c, const u16* __restrict__ wmod,
                                    float* __restrict__ mod, char* lds) {
  float* sl = (float*)lds; float* part = sl + 640;
  const int t = threadIdx.x, lane = t & 63, w = t >> 6, cq = t & 7, rg = t >> 3;
  const u16 cv = ldg<u16>(c, 2 * t);
  u32x4 wv[NR];
  const u32 wo = (u32)((row0 + rg) * 3840 + col0 + 8 * cq) * 2u;
#pragma unroll
  for (int i = 0; i < NR; ++i) wv[i] = ldg<u32x4>(wmod + (size_t)80 * i * 3840, wo);
  { const float x = bf2f(cv); sl[t] = rbf(x * __builtin_amdgcn_rcpf(1.0f + __expf(-x))); }
  __syncthreads();
  float acc[8] = {};
#pragma unroll
  for (int i = 0; i < NR; ++i) {
    const float s = sl[row0 + rg + 80 * i];
#pragma unroll
    for (int k = 0; k < 4; ++k) { acc[2 * k] += s * bf_lo(wv[i][k]); acc[2 * k + 1] += s * bf_hi(wv[i][k]); }
  }
  float u[4];
  const bool b3 = lane & 8;
#pragma unroll
  for (int i = 0; i < 4; ++i) { const float keep = b3 ? acc[4 + i] : acc[i], send = b3 ? acc[i] : acc[4 + i]; u[i] = keep + __int_as_float(__builtin_amdgcn_mov_dpp(__float_as_int(send), 0x128, 0xf, 0xf, true)); }
  float r2[2];
#pragma unroll
  for (int i = 0; i < 2; ++i) { auto x = __builtin_amdgcn_permlane16_swap(__float_as_uint(u[i]), __float_as_uint(u[i + 2]), false, false); r2[i] = __uint_as_float(x[0]) + __uint_as_float(x[1]); }
  auto y = __builtin_amdgcn_permlane32_swap(__float_as_uint(r2[0]), __float_as_uint(r2[1]), false, false);
  part[w * 64 + 8 * cq + 4 * ((lane >> 3) & 1) + 2 * ((lane >> 4) & 1) + (lane >> 5)] = __uint_as_float(y[0]) + __uint_as_float(y[1]);
  __syncthreads();
  if (t < 64) {
    float s = 0.f;
#pragma unroll
    for (int i = 0; i < 10; ++i) s += part[i * 64 + t];
    if (publish) st_agent64(gran + gidx0 + t, ((unsigned long long)(tagv + 1u) << 32) | __float_as_uint(s));
    else mod[col0 + t] = rbf(s);
  }
}

DI int row_group(int b) { return 2 * (8 * (b >> 4) + (b & 7)) + ((b >> 3) & 1); }
DI void sincos_rev(float a, float& s, float& c) { s = __builtin_amdgcn_sinf(a); c = __builtin_amdgcn_cosf(a); }
DI void gram_part(const u32x4 (&f)[4], float* part  , int i, int lane) {
  f32x4 acc = {0.f, 0.f, 0.f, 0.f};
#pragma unroll
  for (int k = 0; k < 4; ++k) acc = mfma16(as8(f[k]), as8(f[k]), acc);
  const int c = lane & 15, g = lane >> 4;
  const float dg = (c & 2) ? ((c & 1) ? acc[3] : acc[2]) : ((c & 1) ? acc[1] : acc[0]);
  if (c < 8 && g == (c >> 2)) part[i * 16 + 8 + c] = dg;
  if (c < 8 && g == 2) part[i * 16 + c] = acc[0];
}
DI void ln_bcast8(const float* part, int lane, float (&a_)[8], float (&b_)[8]) {
  const int r = lane & 7;
  float ps[5], pq[5];
#pragma unroll
  for (int i = 0; i < 5; ++i) { ps[i] = part[i * 16 + r]; pq[i] = part[i * 16 + 8 + r]; }
  batch_fence(); pin_arr(ps); pin_arr(pq);
  const float s = ((ps[0] + ps[1]) + (ps[2] + ps[3])) + ps[4], q = ((pq[0] + pq[1]) + (pq[2] + pq[3])) + pq[4];
  float m_, i_; ln_finish(s, q, m_, i_);
  const float nb = -m_ * i_;
#pragma unroll
  for (int i = 0; i < 8; ++i) {
    a_[i] = __int_as_float(__builtin_amdgcn_readlane(__float_as_int(i_), i));
    b_[i] = __int_as_float(__builtin_amdgcn_readlane(__float_as_int(nb), i));
  }
}
constexpr int HT_RS = 1296;
struct K2Args { const u16* x; const u16* c; const u16* wmod; float* mod; u32* XT; const u16* wr; const u16* wi; const u16* wg; const u16* wu; u16* Wgu; u32* gen; u32* arr; unsigned long long* gran; };
constexpr int K2_MAIN0 = 88, K2_GRID = 344;
extern "C" __global__ __launch_bounds__(640) void k2_fwd(const K2Args A) {
  __shared__ __attribute__((aligned(16))) char smem[32768 + 5 * 4096 + 256];
  const int bid = blockIdx.x;
  if (bid >= 40 && bid < 80) {
    side_delay(K2_SIDE_DELAY);
    gemv_part<8>(1280 + 64 * (bid - 40), 0, 0u, false, nullptr, 0, A.c, A.wmod, A.mod, smem); return;
  }
  if (bid >= 80 && bid < 88) {
    const void* c3; const void* c7;
    CODE_ADDR(c3, k3_mid); CODE_ADDR(c7, k7_end);
    const u32 len = (u32)((const char*)c7 - (const char*)c3);
    const u32 off = threadIdx.x * 128u;
    if (off < len) l2_touch(c3, off, lds_addr(smem) + __builtin_amdgcn_readfirstlane(threadIdx.x >> 6) * 256);
    asm volatile("s_waitcnt vmcnt(0)" ::: "memory");
    if (bid < 87) return;
  }
  if (bid < 40) {
    const u32 tagv = ld_agent(A.gen);
    gemv_part<4>(64 * (bid >> 1), 320 * (bid & 1), tagv, true, A.gran, 1280 * (bid & 1) + 64 * (bid >> 1), A.c, A.wmod, A.mod, smem);
    if (threadIdx.x == 0) st_agent(A.arr + bid, tagv + 1u);
    return;
  }
  if (bid == 87) {
    const u32 tag = ld_agent(A.gen) + 1u;
    const int t = threadIdx.x, who = t < 40 ? t : t + 48;
    for (int spin = 0; spin < SPIN_LIMIT; ++spin) {
      const bool ok = t >= 296 || ld_agent(A.arr + who) == tag;
      if (__syncthreads_and(ok)) break;
      __builtin_amdgcn_s_sleep(8);
    }
    if (t == 0) st_agent(A.gen, tag);
    return;
  }
  u32* __restrict__ XT = A.XT;
  char* hT = smem; char* U = smem + 8 * HT_RS; float* part = (float*)(smem + 8 * HT_RS + 8 * 17 * 80);
  char* tab = smem + 8 * HT_RS + 8 * 17 * 80 + 5 * 16 * 4;
  const int tid = threadIdx.x, lane = tid & 63, w = __builtin_amdgcn_readfirstlane(tid >> 6);
  const int s0 = row_group(bid - K2_MAIN0);
  const int c = lane & 15, g = lane >> 4, q = c >> 2, p = c & 3;
  const u16* xrow = A.x + (size_t)s0 * D_;
  f32x4 tw0, tw1; u32 ard, awr, aw0, acr; float z0r, z0i;
  auto precompute = [&]() {
    const int Qw = 4 * w + q, rho = Qw / 5, d0 = 4 * (Qw % 5) + p;
    const bool rot = rho >= 3 && (rho & 1);
    const float fd = (float)d0 * (1.0f / 640.0f);
    const int k10 = g == 0 ? 16 : 2 * g;
    float s_[4], c_[4];
    sincos_rev(fd * (float)k10, s_[0], c_[0]); sincos_rev(fd * (float)(2 * g + 1), s_[1], c_[1]);
    sincos_rev(fd * (float)(8 + 2 * g), s_[2], c_[2]); sincos_rev(fd * (float)(9 + 2 * g), s_[3], c_[3]);
    tw0 = f32x4{rot ? s_[0] : c_[0], rot ? c_[0] : -s_[0], rot ? s_[1] : c_[1], rot ? c_[1] : -s_[1]};
    tw1 = f32x4{rot ? s_[2] : c_[2], rot ? c_[2] : -s_[2], rot ? s_[3] : c_[3], rot ? c_[3] : -s_[3]};
    const int Qr = 4 * w + p;
    ard = lds_addr(hT) + (Qr / 5) * HT_RS + 2 * (20 * (8 * g + q) + 4 * (Qr % 5));
    awr = lds_addr(U) + 4 * (rho * 17 * 20 + d0 + 2 * g * 20);
    aw0 = g == 0 ? awr + 4 * 16 * 20 : awr;
    z0r = rot ? 0.f : 1.f; z0i = rot ? 1.f : 0.f;
    acr = lds_addr(U) + (w < 8 ? ((2 * (w >> 1)) * 17 + c) * 80 : ((c & 7) * 17 + 16) * 80);
    pin(tw0); pin(tw1); pin(ard); pin(awr); pin(aw0); pin(z0r); pin(z0i); pin(acr);
  };
  if (w < 5) {
    u32 xv[8];
#pragma unroll
    for (int r = 0; r < 8; ++r) xv[r] = ldg<u32>(xrow + (size_t)r * 256 * D_, 4 * tid);
    const u32 tagv = ld_agent(A.gen);
    precompute();
    __syncthreads();
    float na[8], nb[8];
    ln_bcast8(part, lane, na, nb);
    f32x2 o[8];
    {
      f32x2 h[8];
#pragma unroll
      for (int r = 0; r < 8; ++r) { f32x2 v = {bf_lo(xv[r]), bf_hi(xv[r])}; h[r] = v * na[r] + nb[r]; }
      const float cc = 0.70710678118654752f;
      f32x2 a0 = h[0] + h[4], a1 = h[1] + h[5], a2 = h[2] + h[6], a3 = h[3] + h[7];
      f32x2 b0 = h[0] - h[4], b1 = h[1] - h[5], b2 = h[2] - h[6], b3 = h[3] - h[7];
      f32x2 e = a0 + a2, f = a1 + a3, u = cc * (b1 - b3), v = cc * (b1 + b3);
      o[0] = e + f; o[1] = e - f; o[2] = b0 + u; o[3] = -b2 - v; o[4] = a0 - a2; o[5] = a3 - a1; o[6] = b0 - u; o[7] = b2 - v;
#pragma unroll
      for (int r = 0; r < 8; ++r) pin(o[r]);
    }
    const u32 tag = __builtin_amdgcn_readfirstlane(tagv) + 1u;
    if (tid == 0) st_agent(A.arr + bid, tag);
    f32x2 sa, gg;
    {
      const unsigned long long* ga_ = A.gran + 2 * tid;
      u32x4 va, vb, vc, vd;
      for (int spin = 0; spin < SPIN_LIMIT; ++spin) {
        asm volatile("global_load_dwordx4 %0, %4, off sc1\n\tglobal_load_dwordx4 %1, %5, off sc1\n\tglobal_load_dwordx4 %2, %6, off sc1\n\tglobal_load_dwordx4 %3, %7, off sc1\n\ts_waitcnt vmcnt(0)"
                     : "=&v"(va), "=&v"(vb), "=&v"(vc), "=&v"(vd) : "v"(ga_), "v"(ga_ + 1280), "v"(ga_ + 640), "v"(ga_ + 1920) : "memory");
        const bool ok = va[1] == tag && va[3] == tag && vb[1] == tag && vb[3] == tag && vc[1] == tag && vc[3] == tag && vd[1] == tag && vd[3] == tag;
        if (__all(ok)) break;
      }
      sa = f32x2{rbf(__uint_as_float(va[0]) + __uint_as_float(vb[0])), rbf(__uint_as_float(va[2]) + __uint_as_float(vb[2]))};
      gg = f32x2{rbf(1.0f + rbf(__uint_as_float(vc[0]) + __uint_as_float(vd[0]))), rbf(1.0f + rbf(__uint_as_float(vc[2]) + __uint_as_float(vd[2])))};
    }
    o[0] = o[0] * gg + 8.0f * sa;
    *(u32*)(hT + 4 * tid) = pk_bf(o[0][0], o[0][1]);
#pragma unroll
    for (int r = 1; r < 8; ++r) { const f32x2 v = o[r] * gg; *(u32*)(hT + r * HT_RS + 4 * tid) = pk_bf(v[0], v[1]); }
  } else {
    const int i = w - 5;
    u32x4 f[4];
    const u32 xo = (u32)((c & 7) * 256 * D_ + 128 * i + 8 * g) * 2u;
#pragma unroll
    for (int k = 0; k < 4; ++k) f[k] = ldg<u32x4>(xrow, xo + 64 * k);
    const s16x8 t0 = i < 2 ? ldg<s16x8>(g_f32a.v[i < 2 ? i : 0], lane * 16) : ldg<s16x8>(&g_f20a.v[0][0][0][0] + (i - 2) * 512, lane * 16);
    const s16x8 t1 = ldg<s16x8>(&g_f20a.v[0][0][0][0] + (i < 3 ? i + 3 : 5) * 512, lane * 16);
    precompute();
    if (c >= 8) {
      const u32 fill = c == 8 ? 0x3f803f80u : 0u;
#pragma unroll
      for (int k = 0; k < 4; ++k) f[k] = u32x4{fill, fill, fill, fill};
    }
    gram_part(f, part, i, lane);
    *(s16x8*)(tab + i * 1024 + lane * 16) = t0;
    if (i < 3) *(s16x8*)(tab + (i + 5) * 1024 + lane * 16) = t1;
    __syncthreads();
    const int task = (bid - K2_MAIN0) * 5 + i;
    if (task < 1080) prep_gu(task, A.wg, A.wu, A.Wgu, smem + 32768 + 4096 * i, lane);
    if (i == 4) {
      const int xc = bid & 7, li = (bid - K2_MAIN0) >> 3;
#pragma unroll
      for (int k = 0; k < 4; ++k) {
        const int e = (k & 1) * 64 + lane, id = li * 96 + e;
        const int ln = id % 6, row = 8 * ((id / 6) & 255) + (id < 1536 ? xc : ((8 - xc) & 7));
        const u32 off = min((u32)(row * 642 + ln * 128), (u32)(2048 * 642 - 4)) & ~3u;
        if (e < 96) l2_touch((k >> 1) ? A.wi : A.wr, off, lds_addr(smem) + sizeof(smem) - 256);
      }
    }
  }
  __syncthreads();
  {
    const LDSP(const char) base = (LDSP(const char))(size_t)ard;
    s16x8 b = cat8(__builtin_amdgcn_ds_read_tr16_b64_v4i16((LDSP(s16x4))base), __builtin_amdgcn_ds_read_tr16_b64_v4i16((LDSP(s16x4))(base + 160)));
    s16x8 fa0 = *(const s16x8*)(tab + lane * 16), fa1 = *(const s16x8*)(tab + 1024 + lane * 16);
    batch_fence(); pin(b); pin(fa0); pin(fa1);
    const f32x4 z = {0.f, 0.f, 0.f, 0.f};
    f32x4 a0 = mfma16(fa0, b, z), a1 = mfma16(fa1, b, z);
    LDSP(u32) Ub = (LDSP(u32))(size_t)awr;
    const float x0 = g == 0 ? a0[1] : a0[0], x1 = g == 0 ? 0.f : a0[1];
    *(LDSP(u32))(size_t)aw0 = pk_bf(x0 * tw0[0] - x1 * tw0[1], x0 * tw0[1] + x1 * tw0[0]);
    if (g == 0) Ub[0] = pk_bf(a0[0] * z0r, a0[0] * z0i);
    Ub[20] = pk_bf(a0[2] * tw0[2] - a0[3] * tw0[3], a0[2] * tw0[3] + a0[3] * tw0[2]);
    Ub[8 * 20] = pk_bf(a1[0] * tw1[0] - a1[1] * tw1[1], a1[0] * tw1[1] + a1[1] * tw1[0]);
    Ub[9 * 20] = pk_bf(a1[2] * tw1[2] - a1[3] * tw1[3], a1[2] * tw1[3] + a1[3] * tw1[2]);
  }
  __syncthreads();
  const f32x4 z4 = {0.f, 0.f, 0.f, 0.f};
  u32* XTb = XT + (size_t)s0 * 16;
  const char* ft = tab + 2048 + lane * 16;
  const LDSP(const char) be = (LDSP(const char))(size_t)acr;
  const int P = w >> 1, mp = P, mm = P == 0 ? 4 : 8 - P;
  if (w < 8) {
    s16x8 e0 = *(const LDSP(s16x8))(be + 16 * g), e1 = *(const LDSP(s16x8))(be + 64), o0 = *(const LDSP(s16x8))(be + 1360 + 16 * g), o1 = *(const LDSP(s16x8))(be + 1360 + 64);
    char* dp = (char*)(XTb + (size_t)(mp * 21) * 4096); char* dm = (char*)(XTb + (size_t)(mm * 21) * 4096);
    const u32 vo = (u32)(2 * g * 4096 + c) * 4u;
    if (!(w & 1)) {
      s16x8 fm[4];
#pragma unroll
      for (int k = 0; k < 4; ++k) fm[k] = *(const s16x8*)(ft + k * 1024);
      batch_fence(); pin(e0); pin(e1); pin(o0); pin(o1); pin_arr(fm);
#pragma unroll
      for (int mt = 0; mt < 2; ++mt) {
        const s16x8 f0 = fm[2 * mt], f1 = fm[2 * mt + 1];
        f32x4 ae = mfma16(f1, e1, mfma16(f0, e0, z4)), ao = mfma16(f1, o1, mfma16(f0, o0, z4));
        f32x4 tp = ae + ao, tm = ae - ao;
        if (P == 0) { tp = ae; tm = ao; }
        char* ep = dp + (size_t)(8 * mt) * 16384; char* em = dm + (size_t)(8 * mt) * 16384;
        const int k0 = 8 * mt + 2 * g;
        { const float d0_ = (c == 0 && k0 >= 1 && k0 <= 9) ? 2.f : 1.f, d1_ = (c == 0 && k0 + 1 <= 9) ? 2.f : 1.f;
          const f32x4 dd = {d0_, d0_, d1_, d1_}; tp = tp * dd; tm = tm * dd; }
        if (mt == 0 || c != 0 || k0 <= 10) { stg_wt(ep, vo, pk_bf(tp[0], tp[1])); stg_wt(em, vo, pk_bf(tm[0], tm[1])); }
        if (mt == 0 || c != 0 || k0 + 1 <= 10) { stg_wt(ep + 16384, vo, pk_bf(tp[2], tp[3])); stg_wt(em + 16384, vo, pk_bf(tm[2], tm[3])); }
      }
    } else {
      s16x8 f0 = *(const s16x8*)(ft + 4 * 1024), f1 = *(const s16x8*)(ft + 5 * 1024);
      batch_fence(); pin(e0); pin(e1); pin(o0); pin(o1); pin(f0); pin(f1);
      f32x4 ae = mfma16(f1, e1, mfma16(f0, e0, z4)), ao = mfma16(f1, o1, mfma16(f0, o0, z4));
      f32x4 tp = ae + ao, tm = ae - ao;
      if (P == 0) { tp = ae; tm = ao; }
      if (g < 2 && c != 0) {
        char* ep = dp + (size_t)16 * 16384; char* em = dm + (size_t)16 * 16384;
        stg_wt(ep, vo, pk_bf(tp[0], tp[1])); stg_wt(ep + 16384, vo, pk_bf(tp[2], tp[3]));
        stg_wt(em, vo, pk_bf(tm[0], tm[1])); stg_wt(em + 16384, vo, pk_bf(tm[2], tm[3]));
      }
    }
  } else {
    const int rh = c & 7, mt = w - 7;
    s16x8 b0 = *(const LDSP(s16x8))(be + 16 * g), b1 = *(const LDSP(s16x8))(be + 64);
    s16x8 f0 = *(const s16x8*)(ft + (2 * mt) * 1024), f1 = *(const s16x8*)(ft + (2 * mt + 1) * 1024);
    batch_fence(); pin(b0); pin(b1); pin(f0); pin(f1);
    f32x4 a = mfma16(f1, b1, mfma16(f0, b0, z4));
    const float ca_ = (rh >= 2 && (rh & 1)) ? -1.f : 1.f, cb_ = rh >= 2 ? 1.f : 0.f;
    f32x4 t;
#pragma unroll
    for (int r = 0; r < 4; ++r) t[r] = ca_ * a[r] + cb_ * dpp_x1(a[r]);
    const int m1 = rh == 0 ? 0 : rh == 1 ? 4 : (rh & 1) ? 8 - (rh >> 1) : (rh >> 1);
    const int k0 = 8 * mt + 2 * g;
    const u32 o0 = (u32)((m1 * 21 + (k0 == 10 ? 20 : k0)) * 4096) * 4u, o1 = (u32)((m1 * 21 + k0 + 1) * 4096) * 4u;
    if (c < 8 && k0 >= 10 && k0 < 20) { stg_wt(XTb, o0, pk_bf(t[0], t[1])); stg_wt(XTb, o1, pk_bf(t[2], t[3])); }
    if (w == 9) asm volatile("s_waitcnt vmcnt(0)" ::: "memory");
  }
}

constexpr int EX_RS = 20;
extern "C" __global__ __launch_bounds__(512) void k3_mid(const u32* __restrict__ XT, const u16* __restrict__ wr, const u16* __restrict__ wi, u32* __restrict__ ZT) {
  __shared__ __attribute__((aligned(16))) u32 ex[2][256 * EX_RS];
  __shared__ __attribute__((aligned(16))) char tab[8 * 1024];
  const int tid = threadIdx.x, lane = tid & 63, w = __builtin_amdgcn_readfirstlane(tid >> 6), c = lane & 15, g = lane >> 4;
  const int m1 = blockIdx.x & 7, tile = blockIdx.x >> 3;
  const char* in = (const char*)(XT + (size_t)(m1 * 21 + tile) * 4096);
  dma1k(in + 2048 * w, (u32)lane * 16u, lds_addr(ex[1]) + 2048 * w);
  dma1k(in + 2048 * w + 1024, (u32)lane * 16u, lds_addr(ex[1]) + 2048 * w + 1024);
  const F8w* tsrc = w < 2 ? &g_a1[m1] : w < 4 ? &g_f16f : w < 6 ? &g_f16i : &g_a1i[m1];
  const s16x8 tfrag = ldg<s16x8>(tsrc->v[w & 1], lane * 16);
  u32 wra[2][4], wia[2][4];
  const int kb = tile == 20 ? 336 : (c == 0 && tile >= 11) ? 32 * tile + 16 : 32 * tile + c;
  const bool mir = kb > 320;
  {
    const int kk = mir ? 640 - kb : kb;
#pragma unroll
    for (int j = 0; j < 2; ++j)
#pragma unroll
      for (int r = 0; r < 4; ++r) {
        const int m = m1 + 8 * (2 * w + j) + 128 * (4 * g + r), mm = mir ? ((2048 - m) & 2047) : m;
        const u32 vo = (u32)(mm * DF + kk) * 2u;
        wra[j][r] = (u32)ldg<u16>(wr, vo) << 16; wia[j][r] = (u32)ldg<u16>(wi, vo) << 16;
      }
  }
  f32x4 fcs[2], fsn[2], ics[2], isn[2];
  {
#pragma unroll
    for (int j = 0; j < 2; ++j) {
      float s0_, c0_, s1_, c1_;
      sincos_rev((float)((2 * w + j) * (m1 + 32 * g)) * (1.0f / 2048), s0_, c0_); sincos_rev((float)(2 * w + j) * (1.0f / 256), s1_, c1_);
#pragma unroll
      for (int r = 0; r < 4; ++r) { fcs[j][r] = c0_; fsn[j][r] = s0_; const float nc = c0_ * c1_ - s0_ * s1_, ns = s0_ * c1_ + c0_ * s1_; c0_ = nc; s0_ = ns; }
      const int M = m1 + 16 * w + 8 * j;
      sincos_rev((float)(4 * g * M) * (1.0f / 2048), s0_, c0_); sincos_rev((float)M * (1.0f / 2048), s1_, c1_);
#pragma unroll
      for (int r = 0; r < 4; ++r) { ics[j][r] = c0_; isn[j][r] = s0_; const float nc = c0_ * c1_ - s0_ * s1_, ns = s0_ * c1_ + c0_ * s1_; c0_ = nc; s0_ = ns; }
    }
  }
  u32 awr = lds_addr(ex) + 4 * ((4 * g * 16 + c) * EX_RS + 2 * w);
  u32 ard = lds_addr(ex) + 4 * ((2 * w * 16 + c) * EX_RS + 4 * g);
#pragma unroll
  for (int j = 0; j < 2; ++j) { pin(fcs[j]); pin(fsn[j]); pin(ics[j]); pin(isn[j]); }
  pin(awr); pin(ard);
  *(s16x8*)(tab + w * 1024 + lane * 16) = tfrag;
  wait_vm<16>();
  __syncthreads();
  u32x4 bx[2];
#pragma unroll
  for (int j = 0; j < 2; ++j)
#pragma unroll
    for (int i = 0; i < 4; ++i) bx[j][i] = ex[1][(16 * (4 * g + i) + 2 * w + j) * 16 + c];
  s16x8 A10 = *(const s16x8*)(tab + lane * 16), A11 = *(const s16x8*)(tab + lane * 16 + 1024);
  batch_fence(); pin_arr(bx); pin(A10); pin(A11);
  const f32x4 z4 = {0.f, 0.f, 0.f, 0.f};
  const char* tb = tab + lane * 16;
  const LDSP(char) wr0 = (LDSP(char))(size_t)awr; const LDSP(const char) rd0 = (LDSP(const char))(size_t)ard;
  constexpr int RB = 16 * EX_RS * 4, XB = 256 * EX_RS * 4;
  {
    u32 pk[2][4];
#pragma unroll
    for (int j = 0; j < 2; ++j) {
      f32x4 re = mfma16(A10, as8(bx[j]), z4), im = mfma16(A11, as8(bx[j]), z4);
      f32x4 tr = re * fcs[j] + im * fsn[j], ti = im * fcs[j] - re * fsn[j];
#pragma unroll
      for (int r = 0; r < 4; ++r) pk[j][r] = pk_bf(tr[r], ti[r]);
    }
#pragma unroll
    for (int r = 0; r < 4; ++r) *(LDSP(u32x2))(wr0 + r * RB) = u32x2{pk[0][r], pk[1][r]};
  }
  __syncthreads();
  {
    u32x4 bj[2] = {*(const LDSP(u32x4))(rd0), *(const LDSP(u32x4))(rd0 + RB)};
    s16x8 F0 = *(const s16x8*)(tb + 2048), F1 = *(const s16x8*)(tb + 3072), G0 = *(const s16x8*)(tb + 4096), G1 = *(const s16x8*)(tb + 5120);
    batch_fence(); pin_arr(bj); pin(F0); pin(F1); pin(G0); pin(G1);
    u32 pk[2][4];
#pragma unroll
    for (int j = 0; j < 2; ++j) {
      const u32x4 b = bj[j];
      f32x4 xr = mfma16(F0, as8(b), z4), xi = mfma16(F1, as8(b), z4);
      const f32x4 a = {__uint_as_float(wra[j][0]), __uint_as_float(wra[j][1]), __uint_as_float(wra[j][2]), __uint_as_float(wra[j][3])};
      const u32 sg = mir ? 0x80000000u : 0u;
      const f32x4 bb = {__uint_as_float(wia[j][0] ^ sg), __uint_as_float(wia[j][1] ^ sg), __uint_as_float(wia[j][2] ^ sg), __uint_as_float(wia[j][3] ^ sg)};
      const f32x4 yr = xr * a - xi * bb, yi = xr * bb + xi * a;
      const u32x4 yb = {pk_bf(yr[0], yr[1]), pk_bf(yr[2], yr[3]), pk_bf(yi[0], yi[1]), pk_bf(yi[2], yi[3])};
      f32x4 re = mfma16(G0, as8(yb), z4), im = mfma16(G1, as8(yb), z4);
      f32x4 tr = re * ics[j] - im * isn[j], ti = im * ics[j] + re * isn[j];
#pragma unroll
      for (int r = 0; r < 4; ++r) pk[j][r] = pk_bf(tr[r], ti[r]);
    }
#pragma unroll
    for (int r = 0; r < 4; ++r) *(LDSP(u32x2))(wr0 + XB + r * RB) = u32x2{pk[0][r], pk[1][r]};
  }
  __syncthreads();
  char* out = (char*)(ZT + (size_t)(2 * w) * 8 * 21 * 16 + (m1 * 21 + tile) * 16);
  const u32 vout = (u32)(64 * g * 10752 + c * 4);
  u32x4 bz[2] = {*(const LDSP(u32x4))(rd0 + XB), *(const LDSP(u32x4))(rd0 + XB + RB)};
  s16x8 I0 = *(const s16x8*)(tb + 6144), I1 = *(const s16x8*)(tb + 7168);
  batch_fence(); pin_arr(bz); pin(I0); pin(I1);
#pragma unroll
  for (int j = 0; j < 2; ++j) {
    const u32x4 b = bz[j];
    f32x4 zr = mfma16(I0, as8(b), z4), zi = mfma16(I1, as8(b), z4);
#pragma unroll
    for (int r = 0; r < 4; ++r) stg_wt(out + (16 * r + j) * 10752, vout, pk_bf(zr[r], zi[r]));
  }
}

constexpr int Y_RS = 640 + 4;
extern "C" __global__ __launch_bounds__(640) void k4_inv(const u32* __restrict__ ZT, const u16* __restrict__ x, const float* __restrict__ mod,
                                              u16* __restrict__ x1o, u16* __restrict__ h2o, const u16* __restrict__ wd, u16* __restrict__ Wd) {
  __shared__ __attribute__((aligned(16))) char smem[162 * 64 + 8 * Y_RS * 4 + 10 * 8 * 4 + 8 * 1024 + 10752 + 12288 + 256];
  char* Vs = smem; float* Y = (float*)(Vs + 162 * 64); float* part = Y + 8 * Y_RS; char* tab = (char*)(part + 80);
  const int tid = threadIdx.x, lane = tid & 63, w = __builtin_amdgcn_readfirstlane(tid >> 6), c = lane & 15, g = lane >> 4;
  const int s0 = row_group(blockIdx.x);
  u32* Zs = (u32*)(tab + 8 * 1024);
  {
    const char* Zb = (const char*)(ZT + (size_t)s0 * 8 * 21 * 16);
    if (w < 9) dma1k(Zb + 1024 * w, (u32)lane * 16u, lds_addr(Zs) + 1024 * w);
    if (w == 8) dma1k(Zb + 9216, (u32)lane * 16u, lds_addr(Zs) + 9216);
    if (w == 0 && lane < 32) dma1k(Zb + 10240, (u32)lane * 16u, lds_addr(Zs) + 10240);
  }
  const int rho = w < 8 ? w : (c & 7), P = rho >> 1, mA = rho == 1 ? 4 : P, mB = P == 0 ? 4 : 8 - P;
  const s16x8 tfrag = w < 6 ? ldg<s16x8>(&g_f20ai.v[0][0][0][0] + w * 512, lane * 16) : ldg<s16x8>(g_f32bi.v[(w - 6) & 1], lane * 16);
  const int hf = w >= 5, cp = tid - 320 * hf;
  u32 xv[4];
#pragma unroll
  for (int r = 0; r < 4; ++r) xv[r] = ldg<u32>(x + (size_t)(s0 + 256 * (4 * hf + r)) * D_, 4 * cp);
  const f32x2 ga = ldg<f32x2>(mod + 2 * 640, 8 * cp), sb = ldg<f32x2>(mod + 3 * 640, 8 * cp), cb = ldg<f32x2>(mod + 4 * 640, 8 * cp);
  f32x4 tw[3]; u32 aw[3];
  {
    const bool rot = rho >= 3 && (rho & 1);
    const float sc = w < 8 ? 1.0f / (2048.0f * 640.0f) : 2.0f / (2048.0f * 640.0f), fk = (float)(w < 8 ? c : 16) * (1.0f / 640.0f);
    float s0_, c0_, s1_, c1_, s8_, c8_;
    sincos_rev(fk * (float)(2 * g), s0_, c0_); sincos_rev(fk, s1_, c1_); sincos_rev(fk * 8.0f, s8_, c8_);
    float br = (rot ? -s0_ : c0_) * sc, bi = (rot ? c0_ : s0_) * sc;
#pragma unroll
    for (int mt = 0; mt < 3; ++mt) {
      tw[mt] = f32x4{br, bi, br * c1_ - bi * s1_, br * s1_ + bi * c1_};
      const float nr = br * c8_ - bi * s8_, ni = br * s8_ + bi * c8_; br = nr; bi = ni;
      const int d0 = 8 * mt + 2 * g;
      const bool ok = d0 < 20 && (w < 8 || c < 8);
      aw[mt] = lds_addr(Vs) + (ok ? (rho * 20 + d0) * 64 : 160 * 64) + (w < 8 ? 4 * c : 2);
      pin(tw[mt]); pin(aw[mt]);
    }
  }
  float be = rho < 2 ? 0.f : (rho & 1) ? -1.f : 1.f;
  u32 za0, za1, dbits, zdel = (u32)(mB - mA) * 21u * 64u;
  {
    const u32 zb = lds_addr(Zs) + mA * 21 * 64;
    if (w < 8) { za0 = zb + (64 * g + c) * 4; za1 = zb + 16 * 64 + c * 4; dbits = c == 0 ? (0xF0u | (g == 2 ? 8u : g == 3 ? 15u : 0u)) : 0u; }
    else {
      za0 = zb + (g == 3 ? 12 : 11 - 3) * 64; za1 = zb + 16 * 64; dbits = (g < 2 ? 0xFu : g == 2 ? 3u : 0u) | (g == 0 ? 0u : 0xF0u);
    }
  }
  const u32 z10 = lds_addr(Zs) + (mA * 21 + 20) * 64;
  pin(be); pin(za0); pin(za1); pin(dbits); pin(zdel);
  u32x4 pv0[4], pv1[4], pv2[4];
  const int pt0 = blockIdx.x, pt1 = blockIdx.x + 256, pt2 = min((int)blockIdx.x + 512, 539);
  if (w == 9) {
    prep_load(pv0, wd + (size_t)(pt0 % 54) * 32 * D_ + 64 * (pt0 / 54), D_, lane);
    prep_load(pv1, wd + (size_t)(pt1 % 54) * 32 * D_ + 64 * (pt1 / 54), D_, lane);
    if (blockIdx.x + 512 < 540) prep_load(pv2, wd + (size_t)(pt2 % 54) * 32 * D_ + 64 * (pt2 / 54), D_, lane);
  }
  if (w == 9 && blockIdx.x < 8) {
    const void* c5; const void* c6; CODE_ADDR(c5, k5_glu); CODE_ADDR(c6, k6_down);
    const u32 len = (u32)((const char*)c6 - (const char*)c5);
    if (lane * 128u < len) l2_touch(c5, (u32)lane * 128u, lds_addr(smem) + sizeof(smem) - 256);
    if (lane * 128u + 8192u < len) l2_touch(c5, (u32)lane * 128u + 8192u, lds_addr(smem) + sizeof(smem) - 256);
  }
  if (w < 8) *(s16x8*)(tab + w * 1024 + lane * 16) = tfrag;
  if (w < 9) wait_vm<4>();
  __syncthreads();
  if (w < 9) {
    u32 zz[8], zy[8]; s16x8 ft[6];
#pragma unroll
    for (int i = 0; i < 8; ++i) {
      u32 ad = (i < 4 ? za0 : za1) + 64 * (i & 3);
      if (i == 2) ad = (w == 8 && g == 2) ? z10 : ad;
      zz[i] = *(const LDSP(u32))(size_t)ad; zy[i] = *(const LDSP(u32))(size_t)(ad + zdel);
    }
#pragma unroll
    for (int k = 0; k < 6; ++k) ft[k] = *(const s16x8*)(tab + k * 1024 + lane * 16);
    batch_fence(); pin_arr(zz); pin_arr(zy); pin_arr(ft);
    u32x4 b0, b1;
#pragma unroll
    for (int i = 0; i < 8; ++i) {
      const u32 keep = ~(u32)__builtin_amdgcn_sbfe((int)dbits, i, 1);
      const u32 p_ = zz[i] & keep, q_ = zy[i] & keep;
      const f32x2 a = {bf_lo(p_), bf_hi(p_)}, b = {bf_lo(q_), bf_hi(q_)};
      const f32x2 v = a + be * b;
      if (i < 4) b0[i] = pk_bf(v[0], v[1]); else b1[i - 4] = pk_bf(v[0], v[1]);
    }
    f32x4 a[3];
#pragma unroll
    for (int mt = 0; mt < 3; ++mt) {
      const f32x4 z = {0.f, 0.f, 0.f, 0.f};
      a[mt] = mfma16(ft[2 * mt + 1], as8(b1), mfma16(ft[2 * mt], as8(b0), z));
    }
    if (w < 8) {
      const u32 hdel = c == 0 ? lds_addr(Vs) + 160 * 64 + 2 : 0u;
#pragma unroll
      for (int mt = 0; mt < 3; ++mt)
#pragma unroll
        for (int rr = 0; rr < 2; ++rr) {
          const u32 pv = pk_bf(a[mt][2 * rr] * tw[mt][2 * rr] - a[mt][2 * rr + 1] * tw[mt][2 * rr + 1], a[mt][2 * rr] * tw[mt][2 * rr + 1] + a[mt][2 * rr + 1] * tw[mt][2 * rr]);
          *(LDSP(u16))(size_t)(aw[mt] + 64 * rr) = (u16)pv;
          *(LDSP(u16))(size_t)((c == 0 ? hdel : aw[mt] + 2) + 64 * rr) = (u16)(pv >> 16);
        }
    } else {
#pragma unroll
      for (int mt = 0; mt < 3; ++mt)
#pragma unroll
        for (int rr = 0; rr < 2; ++rr)
          *(LDSP(u16))((LDSP(char))(size_t)aw[mt] + 64 * rr) = (u16)pk_bf(a[mt][2 * rr] * tw[mt][2 * rr] - a[mt][2 * rr + 1] * tw[mt][2 * rr + 1], 0.f);
    }
  } else {
    char* pl = (char*)Zs + 10752;
    prep_store(pv0, Wd + (size_t)(4 * (pt0 / 54) * 54 + pt0 % 54) * 512, 54 * 512, pl, lane);
    prep_store(pv1, Wd + (size_t)(4 * (pt1 / 54) * 54 + pt1 % 54) * 512, 54 * 512, pl + 4096, lane);
    if (blockIdx.x + 512 < 540) prep_store(pv2, Wd + (size_t)(4 * (pt2 / 54) * 54 + pt2 % 54) * 512, 54 * 512, pl + 8192, lane);
  }
  __syncthreads();
  {
    s16x8 a = *(const s16x8*)(Vs + (16 * w + c) * 64 + 16 * g);
    s16x8 fb0 = *(const s16x8*)(tab + 6144 + lane * 16), fb1 = *(const s16x8*)(tab + 7168 + lane * 16);
    batch_fence(); pin(a); pin(fb0); pin(fb1);
    const f32x4 z = {0.f, 0.f, 0.f, 0.f};
    const f32x4 y0 = mfma16(a, fb0, z), y1 = mfma16(a, fb1, z);
    const int R = 16 * w + 4 * g, rr = R / 20, d0 = R % 20;
    *(f32x4*)(Y + rr * Y_RS + 20 * c + d0) = y0;
    *(f32x4*)(Y + rr * Y_RS + 20 * (16 + c) + d0) = y1;
  }
  __syncthreads();
  {
    f32x2 R[8];
#pragma unroll
    for (int r = 0; r < 8; ++r) R[r] = *(const f32x2*)(Y + r * Y_RS + 2 * cp);
    batch_fence(); pin_arr(R);
    const float cc = 0.70710678118654752f;
    const f32x2 G = R[0] + R[1], Fm = R[0] - R[1];
    const f32x2 u = cc * (R[2] - R[6]), v = cc * (R[3] + R[7]);
    const f32x2 c13 = R[2] + R[6], s13 = R[3] - R[7];
    const float sg = hf ? -1.f : 1.f;
    f32x2 y[4];
    y[0] = G + R[4] + sg * c13;
    y[2] = G - R[4] + sg * s13;
    y[1] = Fm + R[5] + sg * (u + v);
    y[3] = Fm - R[5] + sg * (v - u);
    u32 x1[4];
    float sv[8];
#pragma unroll
    for (int r = 0; r < 4; ++r) {
      const f32x2 xx = {bf_lo(xv[r]), bf_hi(xv[r])};
      const f32x2 t = xx + ga * y[r];
      x1[r] = pk_bf(t[0], t[1]);
      stg_wt(x1o + (size_t)(8 * s0 + 4 * hf + r) * D_, (u32)cp * 4u, x1[r]);
      sv[r] = dot2bf(x1[r], 0x3f803f80u, 0.f); sv[4 + r] = dot2bf(x1[r], x1[r], 0.f);
    }
    const float tot = reduce8(sv, lane);
    if (lane < 8) part[w * 8 + lane] = tot;
    const f32x2 gg = {rbf(1.0f + cb[0]), rbf(1.0f + cb[1])};
    const int d = 2 * cp, wi = d & 31;
    char* stage = (char*)Y;
    const int so = (((d >> 5) * 4 + ((wi & 15) >> 2)) * 8 + 4 * hf) * 16 + (4 * (wi >> 4) + (wi & 3)) * 2;
    __syncthreads();
    const int r = lane & 3;
    float s, qq;
    { float ps[5], pq[5];
#pragma unroll
      for (int i = 0; i < 5; ++i) { ps[i] = part[(5 * hf + i) * 8 + r]; pq[i] = part[(5 * hf + i) * 8 + 4 + r]; }
      batch_fence(); pin_arr(ps); pin_arr(pq);
      s = ((ps[0] + ps[1]) + (ps[2] + ps[3])) + ps[4]; qq = ((pq[0] + pq[1]) + (pq[2] + pq[3])) + pq[4]; }
    float m_, i_; ln_finish(s, qq, m_, i_);
    const float nb_ = -m_ * i_;
#pragma unroll
    for (int i = 0; i < 4; ++i) {
      const float na = __int_as_float(__builtin_amdgcn_readlane(__float_as_int(i_), i)), nb = __int_as_float(__builtin_amdgcn_readlane(__float_as_int(nb_), i));
      const f32x2 v2 = {bf_lo(x1[i]), bf_hi(x1[i])};
      const f32x2 hv = (v2 * na + nb) * gg + sb;
      *(u32*)(stage + so + 16 * i) = pk_bf(hv[0], hv[1]);
    }
    __syncthreads();
    const u32x4 piece = *(const u32x4*)(stage + 16 * tid);
    stg_wt(h2o + ((size_t)(s0 >> 1) * 20 * 64 + 8 * (s0 & 1)) * 8, (u32)((tid >> 3) * 16 + (tid & 7)) * 16u, piece);
  }
  if (w == 9) asm volatile("s_waitcnt vmcnt(0)" ::: "memory");
}

constexpr int K5_SLOT = 28 * 1024;
constexpr int K5_NS = 4;
constexpr int K5_PD = 4;
struct K5Ctx { const u16* wsrc; const u16* hsrc; u32 ldsw; u32 goff[7]; u32 hoff; u32 abase[2]; };
template <int s, int i> DI void k5_w1(const K5Ctx& c) {
  if constexpr (i == 0) set_m0(c.ldsw + (s % K5_NS) * K5_SLOT + 3072);
  glds16_s<1024 * i - 3072>((const char*)(c.wsrc + s * 4 * 512) - 4096, c.goff[i]);
}
template <int s, int i> DI void k5_t1(const K5Ctx& c, u32x4 (&t)[4]) {
  gload16_s<(i >> 1) * 1024>(t[i], c.hsrc + ((i & 1) * 20 + 2 * s) * 512, c.hoff);
}
template <int s> DI void k5_wall(const K5Ctx& c) { k5_w1<s, 0>(c); k5_w1<s, 1>(c); k5_w1<s, 2>(c); k5_w1<s, 3>(c); k5_w1<s, 4>(c); k5_w1<s, 5>(c); k5_w1<s, 6>(c); }
template <int s> DI void k5_tall(const K5Ctx& c, u32x4 (&t)[4]) { k5_t1<s, 0>(c, t); k5_t1<s, 1>(c, t); k5_t1<s, 2>(c, t); k5_t1<s, 3>(c, t); }
template <int n> DI s16x8 k5_rd(const K5Ctx& c) {
  constexpr int s = n / 28, t = (n / 14) & 1, j = n % 14, sl = s % K5_NS;
  return *(const LDSP(const s16x8))((LDSP(const char))(size_t)c.abase[sl >> 1] + (sl & 1) * K5_SLOT + ((j >> 1) * 4 + t * 2 + (j & 1)) * 1024);
}
template <int n> DI void k5_sub(const K5Ctx& c, f32x4 (&acc)[14][2], s16x8 (&fr)[K5_PD + 1], u32x4 (&tok)[10][4]) {
  constexpr int s = n / 28, t = (n / 14) & 1, j = n % 14;
  constexpr int nb = n + K5_PD;
  if constexpr (nb < 280 && nb % 28 == 0) {
    wait_vm<(s + 2 <= 9) ? 11 : 0>();
    __builtin_amdgcn_s_barrier();
    tie4(tok[s + 1]);
  }
  if constexpr (nb < 280) fr[nb % (K5_PD + 1)] = k5_rd<nb>(c);
  {
    constexpr int q = nb / 28, pos = nb % 28 - (q == 0 ? K5_PD : 0);
    constexpr int idx = (pos % 2 == 0 && pos < 22) ? pos / 2 : -1;
    if constexpr (idx >= 0 && idx < 4 && q + 2 <= 9) k5_t1<q + 2, idx>(c, tok[q + 2]);
    if constexpr (idx >= 4 && q + 2 <= 9) k5_w1<q + 2, idx - 4>(c);
  }
  const s16x8 a = fr[n % (K5_PD + 1)];
  acc[j][0] = mfma16(a, as8(tok[s][2 * t]), acc[j][0]);
  acc[j][1] = mfma16(a, as8(tok[s][2 * t + 1]), acc[j][1]);
  __builtin_amdgcn_sched_barrier(0);
}
template <int... I> DI void k5_all(const K5Ctx& c, f32x4 (&acc)[14][2], s16x8 (&fr)[K5_PD + 1], u32x4 (&tok)[10][4], std::integer_sequence<int, I...>) { (k5_sub<I>(c, acc, fr, tok), ...); }
extern "C" __global__ __launch_bounds__(256, 2) void k5_glu(const u16* __restrict__ h2, const u16* __restrict__ Wgu, u16* __restrict__ hb) {
  extern __shared__ __attribute__((aligned(16))) char smem[];
  const int tid = threadIdx.x, lane = tid & 63, w = __builtin_amdgcn_readfirstlane(tid >> 6);
  const int xcd = blockIdx.x & 7, li = blockIdx.x >> 3;
  const int tt = 8 * (xcd & 1) + (li & 7), fc = 4 * (xcd >> 1) + (li >> 3);
  const int u0 = fc < 12 ? 7 * fc : 84 + 6 * (fc - 12), U = fc < 12 ? 7 : 6;
  if (blockIdx.x < 8) {
    const void* c6; const void* c7; CODE_ADDR(c6, k6_down); CODE_ADDR(c7, k7_end);
    if ((u32)tid * 128u < (u32)((const char*)c7 - (const char*)c6)) l2_touch(c6, (u32)tid * 128u, lds_addr(smem) + 3 * K5_SLOT + w * 256);
  }
  K5Ctx c;
  c.wsrc = Wgu + (size_t)u0 * 20 * 2 * 512;
#pragma unroll
  for (int i = 0; i < 7; ++i) { const int f = 7 * w + i, u = min(f >> 2, U - 1); c.goff[i] = 2 * (u * 20 * 2 * 512 + (f & 3) * 512) + lane * 16 + 4096 - (1024 * i - 3072); }
  c.ldsw = lds_addr(smem) + 7 * w * 1024;
  const int T16 = 8 * tt + 2 * w;
  c.hsrc = h2 + (size_t)T16 * 20 * 512; c.hoff = lane * 16;
#pragma unroll
  for (int j = 0; j < 2; ++j) { c.abase[j] = lds_addr(smem) + 2 * j * K5_SLOT + lane * 16; asm volatile("" : "+v"(c.abase[j])); }
  u32x4 tok[10][4];
  k5_tall<0>(c, tok[0]); k5_wall<0>(c);
  k5_tall<1>(c, tok[1]); k5_wall<1>(c);
  f32x4 acc[14][2];
#pragma unroll
  for (int u = 0; u < 14; ++u) { acc[u][0] = f32x4{0.f, 0.f, 0.f, 0.f}; acc[u][1] = f32x4{0.f, 0.f, 0.f, 0.f}; }
  wait_vm<11>();
  __builtin_amdgcn_s_barrier();
  tie4(tok[0]);
  s16x8 fr[K5_PD + 1];
  fr[0] = k5_rd<0>(c); fr[1] = k5_rd<1>(c); fr[2] = k5_rd<2>(c); fr[3] = k5_rd<3>(c);
  __builtin_amdgcn_sched_barrier(0);
  k5_all(c, acc, fr, tok, std::make_integer_sequence<int, 280>{});
  auto hv = [&](int u, int tl) { const f32x4 a = acc[2 * u][tl] * acc[2 * u + 1][tl]; return u32x2{pk_bf(a[0], a[1]), pk_bf(a[2], a[3])}; };
#pragma unroll
  for (int tl = 0; tl < 2; ++tl) {
    u16* hdst = hb + (size_t)(T16 + tl) * 54 * 512 + (u0 >> 1) * 512;
    const u32 vo = (u32)lane * 16u;
    if (!(u0 & 1)) {
#pragma unroll
      for (int k = 0; k < 3; ++k) { const u32x2 a = hv(2 * k, tl), b = hv(2 * k + 1, tl); stg_wt(hdst + k * 512, vo, u32x4{a[0], a[1], b[0], b[1]}); }
      if (U == 7) stg_wt(hdst + 3 * 512, vo, hv(6, tl));
    } else {
      stg_wt(hdst + 4, vo, hv(0, tl));
#pragma unroll
      for (int k = 0; k < 3; ++k) { const u32x2 a = hv(2 * k + 1, tl), b = hv(2 * k + 2, tl); stg_wt(hdst + (k + 1) * 512, vo, u32x4{a[0], a[1], b[0], b[1]}); }
    }
  }
}

constexpr int K6_NS = 4;
struct K6Ctx { const u16* wsrc; const u16* hsrc; u32 hoff; const float* gbp; u32 gbo; const u16* xp; u32 xo; };
struct K6Grp { u32x4 a[5]; u32x4 t[4]; };
template <int i> DI void k6_v1(const K6Ctx& c, int ks, K6Grp& gr) {
  if constexpr (i < 5) gload16_s<0>(gr.a[i], c.wsrc + (ks + i * 54) * 512, c.hoff);
  else gload16_s<0>(gr.t[i - 5], c.hsrc + (ks + (i - 5) * 54) * 512, c.hoff);
}
DI void k6_issue(const K6Ctx& c, int ks, K6Grp& gr) {
  k6_v1<0>(c, ks, gr); k6_v1<5>(c, ks, gr); k6_v1<6>(c, ks, gr); k6_v1<7>(c, ks, gr); k6_v1<8>(c, ks, gr);
  k6_v1<1>(c, ks, gr); k6_v1<2>(c, ks, gr); k6_v1<3>(c, ks, gr); k6_v1<4>(c, ks, gr);
}
DI void k6_tie(K6Grp& g) { asm volatile("" : "+v"(g.a[0]), "+v"(g.a[1]), "+v"(g.a[2]), "+v"(g.a[3]), "+v"(g.a[4]), "+v"(g.t[0]), "+v"(g.t[1]), "+v"(g.t[2]), "+v"(g.t[3])); }
struct K6Epi { u32x4 gb[5]; u32x2 xr[5]; };
template <int OFF> DI void gload8_s(u32x2& d, const void* sbase, u32 voff) {
  asm volatile("global_load_dwordx2 %0, %1, %2 offset:%3" : "=v"(d) : "v"(voff), "s"(sbase), "n"(OFF) : "memory");
}
constexpr int k6_after(int i, int NK) {
  int n = 0; bool seen = false;
  for (int g = 0; g < 3; ++g) { if (seen) n += 9; if (g == i) seen = true; }
  for (int st = 0; st < i; ++st) {
    if (st == NK - 3 && seen) n += 10;
    if (st + 3 < NK) { if (seen) n += 9; if (st + 3 == i) seen = true; }
  }
  return n;
}
template <int i, int NK> DI void k6_step(const K6Ctx& c, int ks0, f32x4 (&acc)[5][4], K6Grp (&gr)[K6_NS], K6Epi& ep, f32x4* red, int w, int lane) {
  wait_vm<k6_after(i, NK)>();
  K6Grp& g = gr[i % K6_NS];
  k6_tie(g);
  K6Grp& nx = gr[(i + 3) % K6_NS];
#pragma unroll
  for (int nt = 0; nt < 5; ++nt) {
    if constexpr (i + 3 < NK) {
      if (nt == 0) { k6_v1<0>(c, ks0 + i + 3, nx); k6_v1<5>(c, ks0 + i + 3, nx); }
      if (nt == 1) { k6_v1<6>(c, ks0 + i + 3, nx); k6_v1<7>(c, ks0 + i + 3, nx); }
      if (nt == 2) { k6_v1<8>(c, ks0 + i + 3, nx); k6_v1<1>(c, ks0 + i + 3, nx); }
      if (nt == 3) { k6_v1<2>(c, ks0 + i + 3, nx); k6_v1<3>(c, ks0 + i + 3, nx); }
      if (nt == 4) { k6_v1<4>(c, ks0 + i + 3, nx); }
    }
    if constexpr (i == NK - 3) {
      gload16_s<0>(ep.gb[nt], c.gbp + 16 * nt, c.gbo); gload8_s<0>(ep.xr[nt], c.xp + 16 * nt, c.xo);
    }
#pragma unroll
    for (int t = 0; t < 4; ++t) acc[nt][t] = mfma16(as8(g.a[nt]), as8(g.t[t]), acc[nt][t]);
    if constexpr (i == NK - 1) {
      if (nt >= 1) {
#pragma unroll
        for (int t = 0; t < 4; ++t) if (t != w) red[((w * 5 + nt - 1) * 4 + t) * 64 + lane] = acc[nt - 1][t];
      }
    }
    __builtin_amdgcn_sched_barrier(0);
  }
}
template <int i, int NK> struct K6Loop { static DI void run(const K6Ctx& c, int ks0, f32x4 (&acc)[5][4], K6Grp (&gr)[K6_NS], K6Epi& ep, f32x4* red, int w, int lane) {
  k6_step<i, NK>(c, ks0, acc, gr, ep, red, w, lane); K6Loop<i + 1, NK>::run(c, ks0, acc, gr, ep, red, w, lane); } };
template <int NK> struct K6Loop<NK, NK> { static DI void run(const K6Ctx&, int, f32x4 (&)[5][4], K6Grp (&)[K6_NS], K6Epi&, f32x4*, int, int) {} };
template <int NK> DI void k6_main(const K6Ctx& cx, int ks0, f32x4* red, int w, int lane, u16* __restrict__ out, u32 oo) {
  K6Grp gr[K6_NS]; K6Epi ep;
  k6_issue(cx, ks0, gr[0]); k6_issue(cx, ks0 + 1, gr[1]); k6_issue(cx, ks0 + 2, gr[2]);
  f32x4 acc[5][4];
#pragma unroll
  for (int a = 0; a < 5; ++a)
#pragma unroll
    for (int b = 0; b < 4; ++b) acc[a][b] = f32x4{0.f, 0.f, 0.f, 0.f};
  K6Loop<0, NK>::run(cx, ks0, acc, gr, ep, red, w, lane);
#pragma unroll
  for (int t = 0; t < 4; ++t) if (t != w) red[((w * 5 + 4) * 4 + t) * 64 + lane] = acc[4][t];
  wait_vm<0>();
  asm volatile("" : "+v"(ep.gb[0]), "+v"(ep.gb[1]), "+v"(ep.gb[2]), "+v"(ep.gb[3]), "+v"(ep.gb[4]), "+v"(ep.xr[0]), "+v"(ep.xr[1]), "+v"(ep.xr[2]), "+v"(ep.xr[3]), "+v"(ep.xr[4]));
  __syncthreads();
#pragma unroll
  for (int nt = 0; nt < 5; ++nt) {
    f32x4 v = w == 0 ? acc[nt][0] : w == 1 ? acc[nt][1] : w == 2 ? acc[nt][2] : acc[nt][3];
#pragma unroll
    for (int ww = 1; ww < 4; ++ww) v += red[((((w + ww) & 3) * 5 + nt) * 4 + w) * 64 + lane];
    const f32x4 gb = __builtin_bit_cast(f32x4, ep.gb[nt]);
    const f32x4 x4 = {bf_lo(ep.xr[nt][0]), bf_hi(ep.xr[nt][0]), bf_lo(ep.xr[nt][1]), bf_hi(ep.xr[nt][1])};
    const f32x4 o = x4 + gb * v;
    stg_wt(out + 16 * nt, oo, u32x2{pk_bf(o[0], o[1]), pk_bf(o[2], o[3])});
  }
}
extern "C" __global__ __launch_bounds__(256, 2) void k6_down(const u16* __restrict__ hb, const u16* __restrict__ Wd, const u16* __restrict__ x1, const float* __restrict__ mod, u16* __restrict__ out) {
  extern __shared__ __attribute__((aligned(16))) char smem[];
  const int tid = threadIdx.x, lane = tid & 63, w = __builtin_amdgcn_readfirstlane(tid >> 6), c = lane & 15, g = lane >> 4;
  const int xcd = blockIdx.x & 7, li = blockIdx.x >> 3;
  const int tt = 8 * (xcd & 3) + (li & 7), nb = 4 * (xcd >> 2) + (li >> 3);
  const int n0 = 80 * nb;
  const int ks0 = w < 2 ? 14 * w : 28 + 13 * (w - 2);
  K6Ctx cx;
  cx.wsrc = Wd + (size_t)(5 * nb) * 54 * 512; cx.hsrc = hb + (size_t)(4 * tt) * 54 * 512; cx.hoff = lane * 16;
  cx.gbp = mod + 5 * 640 + n0; cx.gbo = 16 * g;
  cx.xp = x1 + (size_t)(64 * tt + 16 * w) * D_ + n0; cx.xo = (u32)(c * D_ + 4 * g) * 2u;
  const int tau = 64 * tt + 16 * w + c, srow = (tau >> 3) + 256 * (tau & 7);
  const u32 oo = (u32)(srow * D_ + 4 * g) * 2u;
  if (w < 2) k6_main<14>(cx, ks0, (f32x4*)smem, w, lane, out + n0, oo); else k6_main<13>(cx, ks0, (f32x4*)smem, w, lane, out + n0, oo);
}

extern "C" __global__ void k7_end() {}

constexpr size_t WS_MOD = 0, WS_XT = 16384, WS_ZT = WS_XT + 8 * 21 * 256 * 16 * 4, WS_X1 = WS_ZT + 8 * 21 * 256 * 16 * 4,
                 WS_H2 = WS_X1 + 2048 * 640 * 2, WS_HB = WS_H2 + 2048 * 640 * 2, WS_WGU = WS_HB + 2048 * 1728 * 2, WS_WD = WS_WGU + 2 * 640 * 1728 * 2, WS_SYNC = WS_WD + 640 * 1728 * 2;
extern "C" void kernel_launch(void* const* d_in, const int* in_sizes, int n_in, void* d_out, int out_size, void* d_ws, size_t ws_size,
                              hipStream_t stream) {
  const u16* x = (const u16*)d_in[0]; const u16* wr = (const u16*)d_in[1]; const u16* wi = (const u16*)d_in[2];
  const u16* wg = (const u16*)d_in[4]; const u16* wu = (const u16*)d_in[5]; const u16* wd = (const u16*)d_in[6];
  const u16* c = (const u16*)d_in[7]; const u16* wmod = (const u16*)d_in[8];
  char* ws = (char*)d_ws;
  float* mod = (float*)(ws + WS_MOD); u32* XT = (u32*)(ws + WS_XT); u32* ZT = (u32*)(ws + WS_ZT);
  u16* x1 = (u16*)(ws + WS_X1); u16* h2 = (u16*)(ws + WS_H2); u16* hb = (u16*)(ws + WS_HB);
  u16* Wgu = (u16*)(ws + WS_WGU); u16* Wd = (u16*)(ws + WS_WD);
  K2Args A;
  A.x = x; A.c = c; A.wmod = wmod; A.mod = mod; A.XT = XT; A.wr = wr; A.wi = wi; A.wg = wg; A.wu = wu; A.Wgu = Wgu;
  A.gen = (u32*)(ws + WS_SYNC); A.arr = (u32*)(ws + WS_SYNC + 4096); A.gran = (unsigned long long*)(ws + WS_SYNC + 8192);
  k2_fwd<<<K2_GRID, 640, 0, stream>>>(A);
  k3_mid<<<168, 512, 0, stream>>>(XT, wr, wi, ZT);
  k4_inv<<<256, 640, 0, stream>>>(ZT, x, mod, x1, h2, wd, Wd);
  hipFuncSetAttribute((const void*)k5_glu, hipFuncAttributeMaxDynamicSharedMemorySize, K5_NS * K5_SLOT);
  k5_glu<<<256, 256, K5_NS * K5_SLOT, stream>>>(h2, Wgu, hb);
  hipFuncSetAttribute((const void*)k6_down, hipFuncAttributeMaxDynamicSharedMemorySize, 81920);
  k6_down<<<256, 256, 81920, stream>>>(hb, Wd, x1, mod, (u16*)d_out);
}
```

```cpp
#include <hip/hip_runtime.h>
#include <hip/hip_bf16.h>
#include <stdint.h>
#include <utility>

typedef unsigned short u16;
typedef unsigned int u32;
typedef short s16x4 __attribute__((ext_vector_type(4)));
typedef short s16x8 __attribute__((ext_vector_type(8)));
typedef __bf16 bf2_t __attribute__((ext_vector_type(2)));
typedef float f32x2 __attribute__((ext_vector_type(2)));
typedef float f32x4 __attribute__((ext_vector_type(4)));
typedef u32 u32x2 __attribute__((ext_vector_type(2)));
typedef u32 u32x4 __attribute__((ext_vector_type(4)));
#define DI __device__ __forceinline__
#define LDSP(T) __attribute__((address_space(3))) T*

constexpr int S_ = 2048, D_ = 640, F_ = 1728, DF = 321;

constexpr double kPi = 3.14159265358979323846264338327950288;
constexpr double c_sin0(double x) { double x2 = x * x, t = x, s = x; for (int i = 1; i < 11; ++i) { t *= -x2 / ((2.0 * i) * (2.0 * i + 1.0)); s += t; } return s; }
constexpr double c_cos0(double x) { double x2 = x * x, t = 1.0, s = 1.0; for (int i = 1; i < 11; ++i) { t *= -x2 / ((2.0 * i - 1.0) * (2.0 * i)); s += t; } return s; }
constexpr double c_cos(long n, long N) {
  n %= N; if (n < 0) n += N;
  if (2 * n > N) n = N - n;
  bool neg = false;
  if (4 * n > N) { neg = true; n = N - 2 * n; N = 2 * N; }
  double r = 0;
  if (4 * n == N) r = 0.0;
  else if (8 * n > N) r = c_sin0(2 * kPi * (double)(N - 4 * n) / (double)(4 * N));
  else r = c_cos0(2 * kPi * (double)n / (double)N);
  return neg ? -r : r;
}
constexpr double c_sin(long n, long N) { return c_cos(4 * n - N, 4 * N); }
constexpr u16 c_bf16(double v) {
  float f = (float)v; u32 u = __builtin_bit_cast(u32, f);
  u += 0x7fffu + ((u >> 16) & 1u); return (u16)(u >> 16);
}

struct F8w { u16 v[2][64][8]; };
struct F8x6 { u16 v[3][2][64][8]; };
constexpr F8w mk_f32a() {
  F8w t{};
  for (int tt = 0; tt < 2; ++tt) for (int l = 0; l < 64; ++l) for (int j = 0; j < 8; ++j) {
    int o = 16 * tt + (l & 15), d1 = 8 * (l >> 4) + j; double v = 0;
    if (o == 0) v = 1.0; else if (o == 1) v = (d1 & 1) ? -1.0 : 1.0;
    else { int k1 = o >> 1; v = (o & 1) ? -c_sin(d1 * k1, 32) : c_cos(d1 * k1, 32); }
    t.v[tt][l][j] = c_bf16(v);
  }
  return t;
}
constexpr F8x6 mk_f20(bool inv) {
  F8x6 t{};
  for (int mt = 0; mt < 3; ++mt) for (int ks = 0; ks < 2; ++ks) for (int l = 0; l < 64; ++l) for (int j = 0; j < 8; ++j) {
    int row = l & 15, g = l >> 4, k0 = 8 * mt + (row >> 1), part = row & 1, d0 = 16 * ks + 4 * g + (j >> 1), pp = j & 1; double v = 0;
    if (d0 < 20 && k0 < 20) {
      double co = c_cos(d0 * k0, 20), si = inv ? -c_sin(d0 * k0, 20) : c_sin(d0 * k0, 20);
      v = part == 0 ? (pp == 0 ? co : si) : (pp == 0 ? -si : co);
    }
    t.v[mt][ks][l][j] = c_bf16(v);
  }
  return t;
}
constexpr F8w mk_a1(int m1, bool inv) {
  F8w t{};
  for (int l = 0; l < 64; ++l) for (int i = 0; i < 4; ++i) {
    int c = l & 15, a = 4 * (l >> 4) + i, n = inv ? c * (8 * a + m1) : a * (8 * c + m1);
    double co = c_cos(n, 128), si = inv ? -c_sin(n, 128) : c_sin(n, 128);
    t.v[0][l][2 * i] = c_bf16(co); t.v[0][l][2 * i + 1] = c_bf16(si);
    t.v[1][l][2 * i] = c_bf16(-si); t.v[1][l][2 * i + 1] = c_bf16(co);
  }
  return t;
}
constexpr F8w mk_f16(bool inv) {
  F8w t{};
  for (int l = 0; l < 64; ++l) for (int i = 0; i < 4; ++i) {
    int c = l & 15, a = 4 * (l >> 4) + i;
    double co = c_cos(a * c, 16), si = c_sin(a * c, 16);
    if (!inv) { t.v[0][l][2 * i] = c_bf16(co); t.v[0][l][2 * i + 1] = c_bf16(si); t.v[1][l][2 * i] = c_bf16(-si); t.v[1][l][2 * i + 1] = c_bf16(co); }
    else { t.v[0][l][i] = c_bf16(co); t.v[0][l][4 + i] = c_bf16(-si); t.v[1][l][i] = c_bf16(si); t.v[1][l][4 + i] = c_bf16(co); }
  }
  return t;
}
constexpr F8w mk_f32bi() {
  F8w t{};
  for (int nt = 0; nt < 2; ++nt) for (int l = 0; l < 64; ++l) for (int j = 0; j < 8; ++j) {
    int d1 = 16 * nt + (l & 15), K = 8 * (l >> 4) + j; double v = 0;
    if (K == 0) v = 1.0; else if (K == 1) v = (d1 & 1) ? -1.0 : 1.0;
    else { int k1 = K >> 1; v = (K & 1) ? -2.0 * c_sin(k1 * d1, 32) : 2.0 * c_cos(k1 * d1, 32); }
    t.v[nt][l][j] = c_bf16(v);
  }
  return t;
}
static __device__ const F8w g_f32a = mk_f32a();
static __device__ const F8x6 g_f20a = mk_f20(false);
static __device__ const F8x6 g_f20ai = mk_f20(true);
static __device__ const F8w g_a1[8] = {mk_a1(0, false), mk_a1(1, false), mk_a1(2, false), mk_a1(3, false), mk_a1(4, false), mk_a1(5, false), mk_a1(6, false), mk_a1(7, false)};
static __device__ const F8w g_a1i[8] = {mk_a1(0, true), mk_a1(1, true), mk_a1(2, true), mk_a1(3, true), mk_a1(4, true), mk_a1(5, true), mk_a1(6, true), mk_a1(7, true)};
static __device__ const F8w g_f16f = mk_f16(false);
static __device__ const F8w g_f16i = mk_f16(true);
static __device__ const F8w g_f32bi = mk_f32bi();

DI float bf_lo(u32 v) { return __uint_as_float(v << 16); }
DI float bf_hi(u32 v) { return __uint_as_float(v & 0xffff0000u); }
DI float bf2f(u16 v) { return __uint_as_float(((u32)v) << 16); }
DI u32 pk_bf(float a, float b) { f32x2 f = {a, b}; return __builtin_bit_cast(u32, __builtin_convertvector(f, bf2_t)); }
DI float rbf(float a) { return bf_lo(pk_bf(a, a)); }
DI float dpp_x1(float v) { return __int_as_float(__builtin_amdgcn_mov_dpp(__float_as_int(v), 0xB1, 0xf, 0xf, true)); }
DI float dpp_x2(float v) { return __int_as_float(__builtin_amdgcn_mov_dpp(__float_as_int(v), 0x4E, 0xf, 0xf, true)); }
DI float dpp_hm(float v) { return __int_as_float(__builtin_amdgcn_mov_dpp(__float_as_int(v), 0x141, 0xf, 0xf, true)); }
DI f32x4 mfma16(s16x8 a, s16x8 b, f32x4 c) { return __builtin_amdgcn_mfma_f32_16x16x32_bf16(a, b, c, 0, 0, 0); }
DI s16x4 tr_read(const void* p) { return __builtin_amdgcn_ds_read_tr16_b64_v4i16((LDSP(s16x4))p); }
DI void batch_fence() { asm volatile("" ::: "memory"); }
template <class T, int N> DI void pin_arr(T (&v)[N]) {
#pragma unroll
  for (int i = 0; i < N; ++i) asm volatile("" : "+v"(v[i]));
}
template <class T> DI void pin(T& v) { asm volatile("" : "+v"(v)); }
DI void l2_touch(const void* base, u32 voff, u32 lds) {
  asm volatile("s_mov_b32 m0, %0\n\ts_nop 0\n\tglobal_load_lds_dword %1, %2" :: "s"(lds), "v"(voff), "s"(base) : "memory");
}
DI void side_delay(int ticks10ns) {
  const unsigned long long t0 = __builtin_amdgcn_s_memrealtime();
  while (__builtin_amdgcn_s_memrealtime() - t0 < (unsigned long long)ticks10ns) __builtin_amdgcn_s_sleep(4);
}
#define GSP(T) __attribute__((address_space(1))) T*
template <class T> DI T ldg(const void* sbase, u32 voff) { GSP(const char) p = (GSP(const char))sbase; asm("" : "+s"(p)); return *(GSP(const T))(p + voff); }
DI void stg_wt(void* sbase, u32 voff, u32x4 v) { asm volatile("global_store_dwordx4 %0, %1, %2 sc1\n\ts_nop 1" :: "v"(voff), "v"(v), "s"(sbase) : "memory"); }
DI void stg_wt(void* sbase, u32 voff, u32x2 v) { asm volatile("global_store_dwordx2 %0, %1, %2 sc1\n\ts_nop 1" :: "v"(voff), "v"(v), "s"(sbase) : "memory"); }
DI void stg_wt(void* sbase, u32 voff, u32 v) { asm volatile("global_store_dword %0, %1, %2 sc1" :: "v"(voff), "v"(v), "s"(sbase) : "memory"); }
#define CODE_ADDR(var, sym) asm volatile("s_getpc_b64 s[40:41]\n\ts_add_u32 s40, s40, " #sym "@rel32@lo+4\n\ts_addc_u32 s41, s41, " #sym "@rel32@hi+12\n\ts_mov_b64 %0, s[40:41]" : "=s"(var) :: "s40", "s41", "scc")
DI u32 lds_addr(const void* p) { return (u32)(size_t)(LDSP(const char))p; }
DI s16x8 cat8(s16x4 a, s16x4 b) { return __builtin_shufflevector(a, b, 0, 1, 2, 3, 4, 5, 6, 7); }
DI s16x8 as8(u32x4 v) { return __builtin_bit_cast(s16x8, v); }
DI float dot2bf(u32 a, u32 b, float c) { return __builtin_amdgcn_fdot2_f32_bf16(__builtin_bit_cast(bf2_t, a), __builtin_bit_cast(bf2_t, b), c, false); }

DI void ln_finish(float s, float q, float& m_, float& i_) {
  const float mean = s * (1.0f / 640.0f);
  const float var = fmaxf(q * (1.0f / 640.0f) - mean * mean, 0.f);
  const float sd = rbf(__builtin_amdgcn_sqrtf(rbf(rbf(var) + 1e-5f)));
  m_ = rbf(mean); i_ = __builtin_amdgcn_rcpf(sd);
}
DI float reduce8(const float (&v)[8], int lane) {
  float b[4], c[2], d;
  const bool s2 = lane & 4, s1 = lane & 2, s0 = lane & 1;
#pragma unroll
  for (int i = 0; i < 4; ++i) { float keep = s2 ? v[4 + i] : v[i], send = s2 ? v[i] : v[4 + i]; b[i] = keep + dpp_hm(send); }
#pragma unroll
  for (int i = 0; i < 2; ++i) { float keep = s1 ? b[2 + i] : b[i], send = s1 ? b[i] : b[2 + i]; c[i] = keep + dpp_x2(send); }
  { float keep = s0 ? c[1] : c[0], send = s0 ? c[0] : c[1]; d = keep + dpp_x1(send); }
  d += __int_as_float(__builtin_amdgcn_mov_dpp(__float_as_int(d), 0x128, 0xf, 0xf, true));
  auto r16 = __builtin_amdgcn_permlane16_swap(__float_as_uint(d), __float_as_uint(d), false, false);
  d = __uint_as_float(r16[0]) + __uint_as_float(r16[1]);
  auto r32 = __builtin_amdgcn_permlane32_swap(__float_as_uint(d), __float_as_uint(d), false, false);
  return __uint_as_float(r32[0]) + __uint_as_float(r32[1]);
}

template <int N> DI void wait_vm() { asm volatile("s_waitcnt vmcnt(%0)" ::"n"(N) : "memory"); }
template <int OFF> DI void gload16_s(u32x4& d, const void* sbase, u32 voff) {
  asm volatile("global_load_dwordx4 %0, %1, %2 offset:%3" : "=v"(d) : "v"(voff), "s"(sbase), "n"(OFF) : "memory");
}
template <int OFF> DI void glds16_s(const void* sbase, u32 voff) {
  asm volatile("global_load_lds_dwordx4 %0, %1 offset:%2" :: "v"(voff), "s"(sbase), "n"(OFF) : "memory");
}
DI void dma1k(const void* sbase, u32 voff, u32 lds) {
  asm volatile("s_mov_b32 m0, %0\n\ts_nop 0\n\tglobal_load_lds_dwordx4 %1, %2" :: "s"(lds), "v"(voff), "s"(sbase) : "memory");
}
DI void set_m0(u32 v) { asm volatile("s_mov_b32 m0, %0\n\ts_nop 0" :: "s"(v) : "memory"); }
DI void tie4(u32x4 (&t)[4]) { asm volatile("" : "+v"(t[0]), "+v"(t[1]), "+v"(t[2]), "+v"(t[3])); }

DI void prep_load(u32x4 (&v)[4], const u16* __restrict__ src, int ld, int lane) {
  const u32 vo = (u32)((lane >> 3) * ld + 8 * (lane & 7)) * 2u;
#pragma unroll
  for (int i = 0; i < 4; ++i) v[i] = ldg<u32x4>(src + (size_t)8 * i * ld, vo);
}
DI void prep_store(const u32x4 (&v)[4], u16* __restrict__ dst, int jstride, char* lds, int lane) {
#pragma unroll
  for (int i = 0; i < 4; ++i) {
    const int row = 8 * i + (lane >> 3), ch = lane & 7;
    *(u32x4*)(lds + row * 128 + (((ch >> 1) ^ ((row >> 1) & 3)) * 32) + (ch & 1) * 16) = v[i];
  }
  const int c = lane & 15, g = lane >> 4, q = c >> 2, p = c & 3, r = 4 * g + q;
#pragma unroll
  for (int j = 0; j < 4; ++j) {
    const char* b = lds + r * 128 + ((j ^ ((r >> 1) & 3)) * 32) + 8 * p;
    const s16x8 f = cat8(tr_read(b), tr_read(b + 16 * 128));
    stg_wt(dst + (size_t)j * jstride, (u32)lane * 16u, __builtin_bit_cast(u32x4, f));
  }
}
DI void prep_wave(const u16* __restrict__ src, int ld, u16* __restrict__ dst, int jstride, char* lds, int lane) {
  u32x4 v[4]; prep_load(v, src, ld, lane); prep_store(v, dst, jstride, lds, lane);
}
DI void prep_gu(int task, const u16* wg, const u16* wu, u16* Wgu, char* lds, int lane) {
  const int mat = task >= 540, tk = task - 540 * mat, cg = tk / 20, ks = tk % 20;
  prep_wave((mat ? wu : wg) + (size_t)ks * 32 * F_ + 64 * cg, F_, Wgu + ((size_t)(4 * cg * 20 + ks) * 2 + mat) * 512, 20 * 2 * 512, lds, lane);
}
DI void prep_d(int task, const u16* wd, u16* Wd, char* lds, int lane) {
  const int cg = task / 54, ks = task % 54;
  prep_wave(wd + (size_t)ks * 32 * D_ + 64 * cg, D_, Wd + (size_t)(4 * cg * 54 + ks) * 512, 54 * 512, lds, lane);
}


typedef __attribute__((address_space(1))) u32 gu32;
typedef __attribute__((address_space(1))) unsigned long long gu64;
DI u32 ld_agent(const u32* p) { return __hip_atomic_load((const gu32*)p, __ATOMIC_RELAXED, __HIP_MEMORY_SCOPE_AGENT); }
DI void st_agent(u32* p, u32 v) { __hip_atomic_store((gu32*)p, v, __ATOMIC_RELAXED, __HIP_MEMORY_SCOPE_AGENT); }
DI void st_agent64(unsigned long long* p, unsigned long long v) { __hip_atomic_store((gu64*)p, v, __ATOMIC_RELAXED, __HIP_MEMORY_SCOPE_AGENT); }
constexpr int K2_SIDE_DELAY = 80;
constexpr int SPIN_LIMIT = 1 << 16;

template <int NR> DI void gemv_part(int col0, int row0, u32 tagv, bool publish, unsigned long long* gran, int gidx0, const u16* __restrict__ c, const u16* __restrict__ wmod,
                                    float* __restrict__ mod, char* lds) {
  float* sl = (float*)lds; float* part = sl + 640;
  const int t = threadIdx.x, lane = t & 63, w = t >> 6, cq = t & 7, rg = t >> 3;
  const u16 cv = ldg<u16>(c, 2 * t);
  u32x4 wv[NR];
  const u32 wo = (u32)((row0 + rg) * 3840 + col0 + 8 * cq) * 2u;
#pragma unroll
  for (int i = 0; i < NR; ++i) wv[i] = ldg<u32x4>(wmod + (size_t)80 * i * 3840, wo);
  { const float x = bf2f(cv); sl[t] = rbf(x * __builtin_amdgcn_rcpf(1.0f + __expf(-x))); }
  __syncthreads();
  float acc[8] = {};
#pragma unroll
  for (int i = 0; i < NR; ++i) {
    const float s = sl[row0 + rg + 80 * i];
#pragma unroll
    for (int k = 0; k < 4; ++k) { acc[2 * k] += s * bf_lo(wv[i][k]); acc[2 * k + 1] += s * bf_hi(wv[i][k]); }
  }
  float u[4];
  const bool b3 = lane & 8;
#pragma unroll
  for (int i = 0; i < 4; ++i) { const float keep = b3 ? acc[4 + i] : acc[i], send = b3 ? acc[i] : acc[4 + i]; u[i] = keep + __int_as_float(__builtin_amdgcn_mov_dpp(__float_as_int(send), 0x128, 0xf, 0xf, true)); }
  float r2[2];
#pragma unroll
  for (int i = 0; i < 2; ++i) { auto x = __builtin_amdgcn_permlane16_swap(__float_as_uint(u[i]), __float_as_uint(u[i + 2]), false, false); r2[i] = __uint_as_float(x[0]) + __uint_as_float(x[1]); }
  auto y = __builtin_amdgcn_permlane32_swap(__float_as_uint(r2[0]), __float_as_uint(r2[1]), false, false);
  part[w * 64 + 8 * cq + 4 * ((lane >> 3) & 1) + 2 * ((lane >> 4) & 1) + (lane >> 5)] = __uint_as_float(y[0]) + __uint_as_float(y[1]);
  __syncthreads();
  if (t < 64) {
    float s = 0.f;
#pragma unroll
    for (int i = 0; i < 10; ++i) s += part[i * 64 + t];
    if (publish) st_agent64(gran + gidx0 + t, ((unsigned long long)(tagv + 1u) << 32) | __float_as_uint(s));
    else mod[col0 + t] = rbf(s);
  }
}

DI int row_group(int b) { return 2 * (8 * (b >> 4) + (b & 7)) + ((b >> 3) & 1); }
DI void sincos_rev(float a, float& s, float& c) { s = __builtin_amdgcn_sinf(a); c = __builtin_amdgcn_cosf(a); }
DI void gram_part(const u32x4 (&f)[4], float* part  , int i, int lane) {
  f32x4 acc = {0.f, 0.f, 0.f, 0.f};
#pragma unroll
  for (int k = 0; k < 4; ++k) acc = mfma16(as8(f[k]), as8(f[k]), acc);
  const int c = lane & 15, g = lane >> 4;
  const float dg = (c & 2) ? ((c & 1) ? acc[3] : acc[2]) : ((c & 1) ? acc[1] : acc[0]);
  if (c < 8 && g == (c >> 2)) part[i * 16 + 8 + c] = dg;
  if (c < 8 && g == 2) part[i * 16 + c] = acc[0];
}
DI void ln_bcast8(const float* part, int lane, float (&a_)[8], float (&b_)[8]) {
  const int r = lane & 7;
  float ps[5], pq[5];
#pragma unroll
  for (int i = 0; i < 5; ++i) { ps[i] = part[i * 16 + r]; pq[i] = part[i * 16 + 8 + r]; }
  batch_fence(); pin_arr(ps); pin_arr(pq);
  const float s = ((ps[0] + ps[1]) + (ps[2] + ps[3])) + ps[4], q = ((pq[0] + pq[1]) + (pq[2] + pq[3])) + pq[4];
  float m_, i_; ln_finish(s, q, m_, i_);
  const float nb = -m_ * i_;
#pragma unroll
  for (int i = 0; i < 8; ++i) {
    a_[i] = __int_as_float(__builtin_amdgcn_readlane(__float_as_int(i_), i));
    b_[i] = __int_as_float(__builtin_amdgcn_readlane(__float_as_int(nb), i));
  }
}
constexpr int HT_RS = 1296;
struct K2Args { const u16* x; const u16* c; const u16* wmod; float* mod; u32* XT; const u16* wr; const u16* wi; const u16* wg; const u16* wu; u16* Wgu; u32* gen; u32* arr; unsigned long long* gran; };
constexpr int K2_MAIN0 = 88, K2_GRID = 344;
extern "C" __global__ __launch_bounds__(640) void k2_fwd(const K2Args A) {
  __shared__ __attribute__((aligned(16))) char smem[32768 + 5 * 4096 + 256];
  const int bid = blockIdx.x;
  if (bid >= 40 && bid < 80) {
    side_delay(K2_SIDE_DELAY);
    gemv_part<8>(1280 + 64 * (bid - 40), 0, 0u, false, nullptr, 0, A.c, A.wmod, A.mod, smem); return;
  }
  if (bid >= 80 && bid < 88) {
    const void* c3; const void* c7;
    CODE_ADDR(c3, k3_mid); CODE_ADDR(c7, k7_end);
    const u32 len = min((u32)((const char*)c7 - (const char*)c3), 65536u) * ((const char*)c7 > (const char*)c3);
    const u32 off = threadIdx.x * 128u;
    if (off < len) l2_touch(c3, off, lds_addr(smem) + __builtin_amdgcn_readfirstlane(threadIdx.x >> 6) * 256);
    asm volatile("s_waitcnt vmcnt(0)" ::: "memory");
    if (bid < 87) return;
  }
  if (bid < 40) {
    const u32 tagv = ld_agent(A.gen);
    gemv_part<4>(64 * (bid >> 1), 320 * (bid & 1), tagv, true, A.gran, 1280 * (bid & 1) + 64 * (bid >> 1), A.c, A.wmod, A.mod, smem);
    if (threadIdx.x == 0) st_agent(A.arr + bid, tagv + 1u);
    return;
  }
  if (bid == 87) {
    const u32 tag = ld_agent(A.gen) + 1u;
    const int t = threadIdx.x, who = t < 40 ? t : t + 48;
    for (int spin = 0; spin < SPIN_LIMIT; ++spin) {
      const bool ok = t >= 296 || ld_agent(A.arr + who) == tag;
      if (__syncthreads_and(ok)) break;
      __builtin_amdgcn_s_sleep(8);
    }
    if (t == 0) st_agent(A.gen, tag);
    return;
  }
  u32* __restrict__ XT = A.XT;
  char* hT = smem; char* U = smem + 8 * HT_RS; float* part = (float*)(smem + 8 * HT_RS + 8 * 17 * 80);
  char* tab = smem + 8 * HT_RS + 8 * 17 * 80 + 5 * 16 * 4;
  const int tid = threadIdx.x, lane = tid & 63, w = __builtin_amdgcn_readfirstlane(tid >> 6);
  const int s0 = row_group(bid - K2_MAIN0);
  const int c = lane & 15, g = lane >> 4, q = c >> 2, p = c & 3;
  const u16* xrow = A.x + (size_t)s0 * D_;
  f32x4 tw0, tw1; u32 ard, awr, aw0, acr; float z0r, z0i;
  auto precompute = [&]() {
    const int Qw = 4 * w + q, rho = Qw / 5, d0 = 4 * (Qw % 5) + p;
    const bool rot = rho >= 3 && (rho & 1);
    const float fd = (float)d0 * (1.0f / 640.0f);
    const int k10 = g == 0 ? 16 : 2 * g;
    float s_[4], c_[4];
    sincos_rev(fd * (float)k10, s_[0], c_[0]); sincos_rev(fd * (float)(2 * g + 1), s_[1], c_[1]);
    sincos_rev(fd * (float)(8 + 2 * g), s_[2], c_[2]); sincos_rev(fd * (float)(9 + 2 * g), s_[3], c_[3]);
    tw0 = f32x4{rot ? s_[0] : c_[0], rot ? c_[0] : -s_[0], rot ? s_[1] : c_[1], rot ? c_[1] : -s_[1]};
    tw1 = f32x4{rot ? s_[2] : c_[2], rot ? c_[2] : -s_[2], rot ? s_[3] : c_[3], rot ? c_[3] : -s_[3]};
    const int Qr = 4 * w + p;
    ard = lds_addr(hT) + (Qr / 5) * HT_RS + 2 * (20 * (8 * g + q) + 4 * (Qr % 5));
    awr = lds_addr(U) + 4 * (rho * 17 * 20 + d0 + 2 * g * 20);
    aw0 = g == 0 ? awr + 4 * 16 * 20 : awr;
    z0r = rot ? 0.f : 1.f; z0i = rot ? 1.f : 0.f;
    acr = lds_addr(U) + (w < 8 ? ((2 * (w >> 1)) * 17 + c) * 80 : ((c & 7) * 17 + 16) * 80);
    pin(tw0); pin(tw1); pin(ard); pin(awr); pin(aw0); pin(z0r); pin(z0i); pin(acr);
  };
  if (w < 5) {
    u32 xv[8];
#pragma unroll
    for (int r = 0; r < 8; ++r) xv[r] = ldg<u32>(xrow + (size_t)r * 256 * D_, 4 * tid);
    const u32 tagv = ld_agent(A.gen);
    precompute();
    __syncthreads();
    float na[8], nb[8];
    ln_bcast8(part, lane, na, nb);
    f32x2 o[8];
    {
      f32x2 h[8];
#pragma unroll
      for (int r = 0; r < 8; ++r) { f32x2 v = {bf_lo(xv[r]), bf_hi(xv[r])}; h[r] = v * na[r] + nb[r]; }
      const float cc = 0.70710678118654752f;
      f32x2 a0 = h[0] + h[4], a1 = h[1] + h[5], a2 = h[2] + h[6], a3 = h[3] + h[7];
      f32x2 b0 = h[0] - h[4], b1 = h[1] - h[5], b2 = h[2] - h[6], b3 = h[3] - h[7];
      f32x2 e = a0 + a2, f = a1 + a3, u = cc * (b1 - b3), v = cc * (b1 + b3);
      o[0] = e + f; o[1] = e - f; o[2] = b0 + u; o[3] = -b2 - v; o[4] = a0 - a2; o[5] = a3 - a1; o[6] = b0 - u; o[7] = b2 - v;
#pragma unroll
      for (int r = 0; r < 8; ++r) pin(o[r]);
    }
    const u32 tag = __builtin_amdgcn_readfirstlane(tagv) + 1u;
    if (tid == 0) st_agent(A.arr + bid, tag);
    f32x2 sa, gg;
    {
      const unsigned long long* ga_ = A.gran + 2 * tid;
      u32x4 va, vb, vc, vd;
      for (int spin = 0; spin < SPIN_LIMIT; ++spin) {
        asm volatile("global_load_dwordx4 %0, %4, off sc1\n\tglobal_load_dwordx4 %1, %5, off sc1\n\tglobal_load_dwordx4 %2, %6, off sc1\n\tglobal_load_dwordx4 %3, %7, off sc1\n\ts_waitcnt vmcnt(0)"
                     : "=&v"(va), "=&v"(vb), "=&v"(vc), "=&v"(vd) : "v"(ga_), "v"(ga_ + 1280), "v"(ga_ + 640), "v"(ga_ + 1920) : "memory");
        const bool ok = va[1] == tag && va[3] == tag && vb[1] == tag && vb[3] == tag && vc[1] == tag && vc[3] == tag && vd[1] == tag && vd[3] == tag;
        if (__all(ok)) break;
      }
      sa = f32x2{rbf(__uint_as_float(va[0]) + __uint_as_float(vb[0])), rbf(__uint_as_float(va[2]) + __uint_as_float(vb[2]))};
      gg = f32x2{rbf(1.0f + rbf(__uint_as_float(vc[0]) + __uint_as_float(vd[0]))), rbf(1.0f + rbf(__uint_as_float(vc[2]) + __uint_as_float(vd[2])))};
    }
    o[0] = o[0] * gg + 8.0f * sa;
    *(u32*)(hT + 4 * tid) = pk_bf(o[0][0], o[0][1]);
#pragma unroll
    for (int r = 1; r < 8; ++r) { const f32x2 v = o[r] * gg; *(u32*)(hT + r * HT_RS + 4 * tid) = pk_bf(v[0], v[1]); }
  } else {
    const int i = w - 5;
    u32x4 f[4];
    const u32 xo = (u32)((c & 7) * 256 * D_ + 128 * i + 8 * g) * 2u;
#pragma unroll
    for (int k = 0; k < 4; ++k) f[k] = ldg<u32x4>(xrow, xo + 64 * k);
    const s16x8 t0 = i < 2 ? ldg<s16x8>(g_f32a.v[i < 2 ? i : 0], lane * 16) : ldg<s16x8>(&g_f20a.v[0][0][0][0] + (i - 2) * 512, lane * 16);
    const s16x8 t1 = ldg<s16x8>(&g_f20a.v[0][0][0][0] + (i < 3 ? i + 3 : 5) * 512, lane * 16);
    precompute();
    if (c >= 8) {
      const u32 fill = c == 8 ? 0x3f803f80u : 0u;
#pragma unroll
      for (int k = 0; k < 4; ++k) f[k] = u32x4{fill, fill, fill, fill};
    }
    gram_part(f, part, i, lane);
    *(s16x8*)(tab + i * 1024 + lane * 16) = t0;
    if (i < 3) *(s16x8*)(tab + (i + 5) * 1024 + lane * 16) = t1;
    __syncthreads();
    const int task = (bid - K2_MAIN0) * 5 + i;
    if (task < 1080) prep_gu(task, A.wg, A.wu, A.Wgu, smem + 32768 + 4096 * i, lane);
    if (i == 4) {
      const int xc = bid & 7, li = (bid - K2_MAIN0) >> 3;
#pragma unroll
      for (int k = 0; k < 4; ++k) {
        const int e = (k & 1) * 64 + lane, id = li * 96 + e;
        const int ln = id % 6, row = 8 * ((id / 6) & 255) + (id < 1536 ? xc : ((8 - xc) & 7));
        const u32 off = min((u32)(row * 642 + ln * 128), (u32)(2048 * 642 - 4)) & ~3u;
        if (e < 96) l2_touch((k >> 1) ? A.wi : A.wr, off, lds_addr(smem) + sizeof(smem) - 256);
      }
    }
  }
  __syncthreads();
  {
    LDSP(const char) base = (LDSP(const char))(size_t)ard;
    s16x8 b = cat8(__builtin_amdgcn_ds_read_tr16_b64_v4i16((LDSP(s16x4))base), __builtin_amdgcn_ds_read_tr16_b64_v4i16((LDSP(s16x4))(base + 160)));
    s16x8 fa0 = *(const s16x8*)(tab + lane * 16), fa1 = *(const s16x8*)(tab + 1024 + lane * 16);
    batch_fence(); pin(b); pin(fa0); pin(fa1);
    const f32x4 z = {0.f, 0.f, 0.f, 0.f};
    f32x4 a0 = mfma16(fa0, b, z), a1 = mfma16(fa1, b, z);
    LDSP(u32) Ub = (LDSP(u32))(size_t)awr;
    const float x0 = g == 0 ? a0[1] : a0[0], x1 = g == 0 ? 0.f : a0[1];
    *(LDSP(u32))(size_t)aw0 = pk_bf(x0 * tw0[0] - x1 * tw0[1], x0 * tw0[1] + x1 * tw0[0]);
    if (g == 0) Ub[0] = pk_bf(a0[0] * z0r, a0[0] * z0i);
    Ub[20] = pk_bf(a0[2] * tw0[2] - a0[3] * tw0[3], a0[2] * tw0[3] + a0[3] * tw0[2]);
    Ub[8 * 20] = pk_bf(a1[0] * tw1[0] - a1[1] * tw1[1], a1[0] * tw1[1] + a1[1] * tw1[0]);
    Ub[9 * 20] = pk_bf(a1[2] * tw1[2] - a1[3] * tw1[3], a1[2] * tw1[3] + a1[3] * tw1[2]);
  }
  __syncthreads();
  const f32x4 z4 = {0.f, 0.f, 0.f, 0.f};
  u32* XTb = XT + (size_t)s0 * 16;
  const char* ft = tab + 2048 + lane * 16;
  LDSP(const char) be = (LDSP(const char))(size_t)acr;
  const int P = w >> 1, mp = P, mm = P == 0 ? 4 : 8 - P;
  if (w < 8) {
    s16x8 e0 = *(LDSP(const s16x8))(be + 16 * g), e1 = *(LDSP(const s16x8))(be + 64), o0 = *(LDSP(const s16x8))(be + 1360 + 16 * g), o1 = *(LDSP(const s16x8))(be + 1360 + 64);
    char* dp = (char*)(XTb + (size_t)(mp * 21) * 4096); char* dm = (char*)(XTb + (size_t)(mm * 21) * 4096);
    const u32 vo = (u32)(2 * g * 4096 + c) * 4u;
    if (!(w & 1)) {
      s16x8 fm[4];
#pragma unroll
      for (int k = 0; k < 4; ++k) fm[k] = *(const s16x8*)(ft + k * 1024);
      batch_fence(); pin(e0); pin(e1); pin(o0); pin(o1); pin_arr(fm);
#pragma unroll
      for (int mt = 0; mt < 2; ++mt) {
        const s16x8 f0 = fm[2 * mt], f1 = fm[2 * mt + 1];
        f32x4 ae = mfma16(f1, e1, mfma16(f0, e0, z4)), ao = mfma16(f1, o1, mfma16(f0, o0, z4));
        f32x4 tp = ae + ao, tm = ae - ao;
        if (P == 0) { tp = ae; tm = ao; }
        char* ep = dp + (size_t)(8 * mt) * 16384; char* em = dm + (size_t)(8 * mt) * 16384;
        const int k0 = 8 * mt + 2 * g;
        { const float d0_ = (c == 0 && k0 >= 1 && k0 <= 9) ? 2.f : 1.f, d1_ = (c == 0 && k0 + 1 <= 9) ? 2.f : 1.f;
          const f32x4 dd = {d0_, d0_, d1_, d1_}; tp = tp * dd; tm = tm * dd; }
        if (mt == 0 || c != 0 || k0 <= 10) { stg_wt(ep, vo, pk_bf(tp[0], tp[1])); stg_wt(em, vo, pk_bf(tm[0], tm[1])); }
        if (mt == 0 || c != 0 || k0 + 1 <= 10) { stg_wt(ep + 16384, vo, pk_bf(tp[2], tp[3])); stg_wt(em + 16384, vo, pk_bf(tm[2], tm[3])); }
      }
    } else {
      s16x8 f0 = *(const s16x8*)(ft + 4 * 1024), f1 = *(const s16x8*)(ft + 5 * 1024);
      batch_fence(); pin(e0); pin(e1); pin(o0); pin(o1); pin(f0); pin(f1);
      f32x4 ae = mfma16(f1, e1, mfma16(f0, e0, z4)), ao = mfma16(f1, o1, mfma16(f0, o0, z4));
      f32x4 tp = ae + ao, tm = ae - ao;
      if (P == 0) { tp = ae; tm = ao; }
      if (g < 2 && c != 0) {
        char* ep = dp + (size_t)16 * 16384; char* em = dm + (size_t)16 * 16384;
        stg_wt(ep, vo, pk_bf(tp[0], tp[1])); stg_wt(ep + 16384, vo, pk_bf(tp[2], tp[3]));
        stg_wt(em, vo, pk_bf(tm[0], tm[1])); stg_wt(em + 16384, vo, pk_bf(tm[2], tm[3]));
      }
    }
  } else {
    const int rh = c & 7, mt = w - 7;
    s16x8 b0 = *(LDSP(const s16x8))(be + 16 * g), b1 = *(LDSP(const s16x8))(be + 64);
    s16x8 f0 = *(const s16x8*)(ft + (2 * mt) * 1024), f1 = *(const s16x8*)(ft + (2 * mt + 1) * 1024);
    batch_fence(); pin(b0); pin(b1); pin(f0); pin(f1);
    f32x4 a = mfma16(f1, b1, mfma16(f0, b0, z4));
    const float ca_ = (rh >= 2 && (rh & 1)) ? -1.f : 1.f, cb_ = rh >= 2 ? 1.f : 0.f;
    f32x4 t;
#pragma unroll
    for (int r = 0; r < 4; ++r) t[r] = ca_ * a[r] + cb_ * dpp_x1(a[r]);
    const int m1 = rh == 0 ? 0 : rh == 1 ? 4 : (rh & 1) ? 8 - (rh >> 1) : (rh >> 1);
    const int k0 = 8 * mt + 2 * g;
    const u32 o0 = (u32)((m1 * 21 + (k0 == 10 ? 20 : k0)) * 4096) * 4u, o1 = (u32)((m1 * 21 + k0 + 1) * 4096) * 4u;
    if (c < 8 && k0 >= 10 && k0 < 20) { stg_wt(XTb, o0, pk_bf(t[0], t[1])); stg_wt(XTb, o1, pk_bf(t[2], t[3])); }
    if (w == 9) asm volatile("s_waitcnt vmcnt(0)" ::: "memory");
  }
}

constexpr int EX_RS = 20;
extern "C" __global__ __launch_bounds__(512) void k3_mid(const u32* __restrict__ XT, const u16* __restrict__ wr, const u16* __restrict__ wi, u32* __restrict__ ZT) {
  __shared__ __attribute__((aligned(16))) u32 ex[2][256 * EX_RS];
  __shared__ __attribute__((aligned(16))) char tab[8 * 1024];
  const int tid = threadIdx.x, lane = tid & 63, w = __builtin_amdgcn_readfirstlane(tid >> 6), c = lane & 15, g = lane >> 4;
  const int m1 = blockIdx.x & 7, tile = blockIdx.x >> 3;
  const char* in = (const char*)(XT + (size_t)(m1 * 21 + tile) * 4096);
  dma1k(in + 2048 * w, (u32)lane * 16u, lds_addr(ex[1]) + 2048 * w);
  dma1k(in + 2048 * w + 1024, (u32)lane * 16u, lds_addr(ex[1]) + 2048 * w + 1024);
  const F8w* tsrc = w < 2 ? &g_a1[m1] : w < 4 ? &g_f16f : w < 6 ? &g_f16i : &g_a1i[m1];
  const s16x8 tfrag = ldg<s16x8>(tsrc->v[w & 1], lane * 16);
  u32 wra[2][4], wia[2][4];
  const int kb = tile == 20 ? 336 : (c == 0 && tile >= 11) ? 32 * tile + 16 : 32 * tile + c;
  const bool mir = kb > 320;
  {
    const int kk = mir ? 640 - kb : kb;
#pragma unroll
    for (int j = 0; j < 2; ++j)
#pragma unroll
      for (int r = 0; r < 4; ++r) {
        const int m = m1 + 8 * (2 * w + j) + 128 * (4 * g + r), mm = mir ? ((2048 - m) & 2047) : m;
        const u32 vo = (u32)(mm * DF + kk) * 2u;
        wra[j][r] = (u32)ldg<u16>(wr, vo) << 16; wia[j][r] = (u32)ldg<u16>(wi, vo) << 16;
      }
  }
  f32x4 fcs[2], fsn[2], ics[2], isn[2];
  {
#pragma unroll
    for (int j = 0; j < 2; ++j) {
      float s0_, c0_, s1_, c1_;
      sincos_rev((float)((2 * w + j) * (m1 + 32 * g)) * (1.0f / 2048), s0_, c0_); sincos_rev((float)(2 * w + j) * (1.0f / 256), s1_, c1_);
#pragma unroll
      for (int r = 0; r < 4; ++r) { fcs[j][r] = c0_; fsn[j][r] = s0_; const float nc = c0_ * c1_ - s0_ * s1_, ns = s0_ * c1_ + c0_ * s1_; c0_ = nc; s0_ = ns; }
      const int M = m1 + 16 * w + 8 * j;
      sincos_rev((float)(4 * g * M) * (1.0f / 2048), s0_, c0_); sincos_rev((float)M * (1.0f / 2048), s1_, c1_);
#pragma unroll
      for (int r = 0; r < 4; ++r) { ics[j][r] = c0_; isn[j][r] = s0_; const float nc = c0_ * c1_ - s0_ * s1_, ns = s0_ * c1_ + c0_ * s1_; c0_ = nc; s0_ = ns; }
    }
  }
  u32 awr = lds_addr(ex) + 4 * ((4 * g * 16 + c) * EX_RS + 2 * w);
  u32 ard = lds_addr(ex) + 4 * ((2 * w * 16 + c) * EX_RS + 4 * g);
#pragma unroll
  for (int j = 0; j < 2; ++j) { pin(fcs[j]); pin(fsn[j]); pin(ics[j]); pin(isn[j]); }
  pin(awr); pin(ard);
  *(s16x8*)(tab + w * 1024 + lane * 16) = tfrag;
  wait_vm<16>();
  __syncthreads();
  u32x4 bx[2];
#pragma unroll
  for (int j = 0; j < 2; ++j)
#pragma unroll
    for (int i = 0; i < 4; ++i) bx[j][i] = ex[1][(16 * (4 * g + i) + 2 * w + j) * 16 + c];
  s16x8 A10 = *(const s16x8*)(tab + lane * 16), A11 = *(const s16x8*)(tab + lane * 16 + 1024);
  batch_fence(); pin_arr(bx); pin(A10); pin(A11);
  const f32x4 z4 = {0.f, 0.f, 0.f, 0.f};
  const char* tb = tab + lane * 16;
  LDSP(char) wr0 = (LDSP(char))(size_t)awr; LDSP(const char) rd0 = (LDSP(const char))(size_t)ard;
  constexpr int RB = 16 * EX_RS * 4, XB = 256 * EX_RS * 4;
  {
    u32 pk[2][4];
#pragma unroll
    for (int j = 0; j < 2; ++j) {
      f32x4 re = mfma16(A10, as8(bx[j]), z4), im = mfma16(A11, as8(bx[j]), z4);
      f32x4 tr = re * fcs[j] + im * fsn[j], ti = im * fcs[j] - re * fsn[j];
#pragma unroll
      for (int r = 0; r < 4; ++r) pk[j][r] = pk_bf(tr[r], ti[r]);
    }
#pragma unroll
    for (int r = 0; r < 4; ++r) *(LDSP(u32x2))(wr0 + r * RB) = u32x2{pk[0][r], pk[1][r]};
  }
  __syncthreads();
  {
    u32x4 bj[2] = {*(LDSP(const u32x4))(rd0), *(LDSP(const u32x4))(rd0 + RB)};
    s16x8 F0 = *(const s16x8*)(tb + 2048), F1 = *(const s16x8*)(tb + 3072), G0 = *(const s16x8*)(tb + 4096), G1 = *(const s16x8*)(tb + 5120);
    batch_fence(); pin_arr(bj); pin(F0); pin(F1); pin(G0); pin(G1);
    u32 pk[2][4];
#pragma unroll
    for (int j = 0; j < 2; ++j) {
      const u32x4 b = bj[j];
      f32x4 xr = mfma16(F0, as8(b), z4), xi = mfma16(F1, as8(b), z4);
      const f32x4 a = {__uint_as_float(wra[j][0]), __uint_as_float(wra[j][1]), __uint_as_float(wra[j][2]), __uint_as_float(wra[j][3])};
      const u32 sg = mir ? 0x80000000u : 0u;
      const f32x4 bb = {__uint_as_float(wia[j][0] ^ sg), __uint_as_float(wia[j][1] ^ sg), __uint_as_float(wia[j][2] ^ sg), __uint_as_float(wia[j][3] ^ sg)};
      const f32x4 yr = xr * a - xi * bb, yi = xr * bb + xi * a;
      const u32x4 yb = {pk_bf(yr[0], yr[1]), pk_bf(yr[2], yr[3]), pk_bf(yi[0], yi[1]), pk_bf(yi[2], yi[3])};
      f32x4 re = mfma16(G0, as8(yb), z4), im = mfma16(G1, as8(yb), z4);
      f32x4 tr = re * ics[j] - im * isn[j], ti = im * ics[j] + re * isn[j];
#pragma unroll
      for (int r = 0; r < 4; ++r) pk[j][r] = pk_bf(tr[r], ti[r]);
    }
#pragma unroll
    for (int r = 0; r < 4; ++r) *(LDSP(u32x2))(wr0 + XB + r * RB) = u32x2{pk[0][r], pk[1][r]};
  }
  __syncthreads();
  char* out = (char*)(ZT + (size_t)(2 * w) * 8 * 21 * 16 + (m1 * 21 + tile) * 16);
  const u32 vout = (u32)(64 * g * 10752 + c * 4);
  u32x4 bz[2] = {*(LDSP(const u32x4))(rd0 + XB), *(LDSP(const u32x4))(rd0 + XB + RB)};
  s16x8 I0 = *(const s16x8*)(tb + 6144), I1 = *(const s16x8*)(tb + 7168);
  batch_fence(); pin_arr(bz); pin(I0); pin(I1);
#pragma unroll
  for (int j = 0; j < 2; ++j) {
    const u32x4 b = bz[j];
    f32x4 zr = mfma16(I0, as8(b), z4), zi = mfma16(I1, as8(b), z4);
#pragma unroll
    for (int r = 0; r < 4; ++r) stg_wt(out + (16 * r + j) * 10752, vout, pk_bf(zr[r], zi[r]));
  }
}

constexpr int Y_RS = 640 + 4;
extern "C" __global__ __launch_bounds__(640) void k4_inv(const u32* __restrict__ ZT, const u16* __restrict__ x, const float* __restrict__ mod,
                                              u16* __restrict__ x1o, u16* __restrict__ h2o, const u16* __restrict__ wd, u16* __restrict__ Wd) {
  __shared__ __attribute__((aligned(16))) char smem[162 * 64 + 8 * Y_RS * 4 + 10 * 8 * 4 + 8 * 1024 + 10752 + 12288 + 256];
  char* Vs = smem; float* Y = (float*)(Vs + 162 * 64); float* part = Y + 8 * Y_RS; char* tab = (char*)(part + 80);
  const int tid = threadIdx.x, lane = tid & 63, w = __builtin_amdgcn_readfirstlane(tid >> 6), c = lane & 15, g = lane >> 4;
  const int s0 = row_group(blockIdx.x);
  u32* Zs = (u32*)(tab + 8 * 1024);
  {
    const char* Zb = (const char*)(ZT + (size_t)s0 * 8 * 21 * 16);
    if (w < 9) dma1k(Zb + 1024 * w, (u32)lane * 16u, lds_addr(Zs) + 1024 * w);
    if (w == 8) dma1k(Zb + 9216, (u32)lane * 16u, lds_addr(Zs) + 9216);
    if (w == 0 && lane < 32) dma1k(Zb + 10240, (u32)lane * 16u, lds_addr(Zs) + 10240);
  }
  const int rho = w < 8 ? w : (c & 7), P = rho >> 1, mA = rho == 1 ? 4 : P, mB = P == 0 ? 4 : 8 - P;
  const s16x8 tfrag = w < 6 ? ldg<s16x8>(&g_f20ai.v[0][0][0][0] + w * 512, lane * 16) : ldg<s16x8>(g_f32bi.v[(w - 6) & 1], lane * 16);
  const int hf = w >= 5, cp = tid - 320 * hf;
  u32 xv[4];
#pragma unroll
  for (int r = 0; r < 4; ++r) xv[r] = ldg<u32>(x + (size_t)(s0 + 256 * (4 * hf + r)) * D_, 4 * cp);
  const f32x2 ga = ldg<f32x2>(mod + 2 * 640, 8 * cp), sb = ldg<f32x2>(mod + 3 * 640, 8 * cp), cb = ldg<f32x2>(mod + 4 * 640, 8 * cp);
  f32x4 tw[3]; u32 aw[3];
  {
    const bool rot = rho >= 3 && (rho & 1);
    const float sc = w < 8 ? 1.0f / (2048.0f * 640.0f) : 2.0f / (2048.0f * 640.0f), fk = (float)(w < 8 ? c : 16) * (1.0f / 640.0f);
    float s0_, c0_, s1_, c1_, s8_, c8_;
    sincos_rev(fk * (float)(2 * g), s0_, c0_); sincos_rev(fk, s1_, c1_); sincos_rev(fk * 8.0f, s8_, c8_);
    float br = (rot ? -s0_ : c0_) * sc, bi = (rot ? c0_ : s0_) * sc;
#pragma unroll
    for (int mt = 0; mt < 3; ++mt) {
      tw[mt] = f32x4{br, bi, br * c1_ - bi * s1_, br * s1_ + bi * c1_};
      const float nr = br * c8_ - bi * s8_, ni = br * s8_ + bi * c8_; br = nr; bi = ni;
      const int d0 = 8 * mt + 2 * g;
      const bool ok = d0 < 20 && (w < 8 || c < 8);
      aw[mt] = lds_addr(Vs) + (ok ? (rho * 20 + d0) * 64 : 160 * 64) + (w < 8 ? 4 * c : 2);
      pin(tw[mt]); pin(aw[mt]);
    }
  }
  float be = rho < 2 ? 0.f : (rho & 1) ? -1.f : 1.f;
  u32 za0, za1, dbits, zdel = (u32)(mB - mA) * 21u * 64u;
  {
    const u32 zb = lds_addr(Zs) + mA * 21 * 64;
    if (w < 8) { za0 = zb + (64 * g + c) * 4; za1 = zb + 16 * 64 + c * 4; dbits = c == 0 ? (0xF0u | (g == 2 ? 8u : g == 3 ? 15u : 0u)) : 0u; }
    else {
      za0 = zb + (g == 3 ? 12 : 11 - 3) * 64; za1 = zb + 16 * 64; dbits = (g < 2 ? 0xFu : g == 2 ? 3u : 0u) | (g == 0 ? 0u : 0xF0u);
    }
  }
  const u32 z10 = lds_addr(Zs) + (mA * 21 + 20) * 64;
  pin(be); pin(za0); pin(za1); pin(dbits); pin(zdel);
  u32x4 pv0[4], pv1[4], pv2[4];
  const int pt0 = blockIdx.x, pt1 = blockIdx.x + 256, pt2 = min((int)blockIdx.x + 512, 539);
  if (w == 9) {
    prep_load(pv0, wd + (size_t)(pt0 % 54) * 32 * D_ + 64 * (pt0 / 54), D_, lane);
    prep_load(pv1, wd + (size_t)(pt1 % 54) * 32 * D_ + 64 * (pt1 / 54), D_, lane);
    if (blockIdx.x + 512 < 540) prep_load(pv2, wd + (size_t)(pt2 % 54) * 32 * D_ + 64 * (pt2 / 54), D_, lane);
  }
  if (w == 9 && blockIdx.x < 8) {
    const void* c5; const void* c6; CODE_ADDR(c5, k5_glu); CODE_ADDR(c6, k6_down);
    const u32 len = min((u32)((const char*)c6 - (const char*)c5), 16384u) * ((const char*)c6 > (const char*)c5);
    if (lane * 128u < len) l2_touch(c5, (u32)lane * 128u, lds_addr(smem) + sizeof(smem) - 256);
    if (lane * 128u + 8192u < len) l2_touch(c5, (u32)lane * 128u + 8192u, lds_addr(smem) + sizeof(smem) - 256);
  }
  if (w < 8) *(s16x8*)(tab + w * 1024 + lane * 16) = tfrag;
  if (w < 9) wait_vm<4>();
  __syncthreads();
  if (w < 9) {
    u32 zz[8], zy[8]; s16x8 ft[6];
#pragma unroll
    for (int i = 0; i < 8; ++i) {
      u32 ad = (i < 4 ? za0 : za1) + 64 * (i & 3);
      if (i == 2) ad = (w == 8 && g == 2) ? z10 : ad;
      zz[i] = *(LDSP(const u32))(size_t)ad; zy[i] = *(LDSP(const u32))(size_t)(ad + zdel);
    }
#pragma unroll
    for (int k = 0; k < 6; ++k) ft[k] = *(const s16x8*)(tab + k * 1024 + lane * 16);
    batch_fence(); pin_arr(zz); pin_arr(zy); pin_arr(ft);
    u32x4 b0, b1;
#pragma unroll
    for (int i = 0; i < 8; ++i) {
      const u32 keep = ~(u32)__builtin_amdgcn_sbfe((int)dbits, i, 1);
      const u32 p_ = zz[i] & keep, q_ = zy[i] & keep;
      const f32x2 a = {bf_lo(p_), bf_hi(p_)}, b = {bf_lo(q_), bf_hi(q_)};
      const f32x2 v = a + be * b;
      if (i < 4) b0[i] = pk_bf(v[0], v[1]); else b1[i - 4] = pk_bf(v[0], v[1]);
    }
    f32x4 a[3];
#pragma unroll
    for (int mt = 0; mt < 3; ++mt) {
      const f32x4 z = {0.f, 0.f, 0.f, 0.f};
      a[mt] = mfma16(ft[2 * mt + 1], as8(b1), mfma16(ft[2 * mt], as8(b0), z));
    }
    if (w < 8) {
      const u32 hdel = c == 0 ? lds_addr(Vs) + 160 * 64 + 2 : 0u;
#pragma unroll
      for (int mt = 0; mt < 3; ++mt)
#pragma unroll
        for (int rr = 0; rr < 2; ++rr) {
          const u32 pv = pk_bf(a[mt][2 * rr] * tw[mt][2 * rr] - a[mt][2 * rr + 1] * tw[mt][2 * rr + 1], a[mt][2 * rr] * tw[mt][2 * rr + 1] + a[mt][2 * rr + 1] * tw[mt][2 * rr]);
          *(LDSP(u16))(size_t)(aw[mt] + 64 * rr) = (u16)pv;
          *(LDSP(u16))(size_t)((c == 0 ? hdel : aw[mt] + 2) + 64 * rr) = (u16)(pv >> 16);
        }
    } else {
#pragma unroll
      for (int mt = 0; mt < 3; ++mt)
#pragma unroll
        for (int rr = 0; rr < 2; ++rr)
          *(LDSP(u16))((LDSP(char))(size_t)aw[mt] + 64 * rr) = (u16)pk_bf(a[mt][2 * rr] * tw[mt][2 * rr] - a[mt][2 * rr + 1] * tw[mt][2 * rr + 1], 0.f);
    }
  } else {
    char* pl = (char*)Zs + 10752;
    prep_store(pv0, Wd + (size_t)(4 * (pt0 / 54) * 54 + pt0 % 54) * 512, 54 * 512, pl, lane);
    prep_store(pv1, Wd + (size_t)(4 * (pt1 / 54) * 54 + pt1 % 54) * 512, 54 * 512, pl + 4096, lane);
    if (blockIdx.x + 512 < 540) prep_store(pv2, Wd + (size_t)(4 * (pt2 / 54) * 54 + pt2 % 54) * 512, 54 * 512, pl + 8192, lane);
  }
  __syncthreads();
  {
    s16x8 a = *(const s16x8*)(Vs + (16 * w + c) * 64 + 16 * g);
    s16x8 fb0 = *(const s16x8*)(tab + 6144 + lane * 16), fb1 = *(const s16x8*)(tab + 7168 + lane * 16);
    batch_fence(); pin(a); pin(fb0); pin(fb1);
    const f32x4 z = {0.f, 0.f, 0.f, 0.f};
    const f32x4 y0 = mfma16(a, fb0, z), y1 = mfma16(a, fb1, z);
    const int R = 16 * w + 4 * g, rr = R / 20, d0 = R % 20;
    *(f32x4*)(Y + rr * Y_RS + 20 * c + d0) = y0;
    *(f32x4*)(Y + rr * Y_RS + 20 * (16 + c) + d0) = y1;
  }
  __syncthreads();
  {
    f32x2 R[8];
#pragma unroll
    for (int r = 0; r < 8; ++r) R[r] = *(const f32x2*)(Y + r * Y_RS + 2 * cp);
    batch_fence(); pin_arr(R);
    const float cc = 0.70710678118654752f;
    const f32x2 G = R[0] + R[1], Fm = R[0] - R[1];
    const f32x2 u = cc * (R[2] - R[6]), v = cc * (R[3] + R[7]);
    const f32x2 c13 = R[2] + R[6], s13 = R[3] - R[7];
    const float sg = hf ? -1.f : 1.f;
    f32x2 y[4];
    y[0] = G + R[4] + sg * c13;
    y[2] = G - R[4] + sg * s13;
    y[1] = Fm + R[5] + sg * (u + v);
    y[3] = Fm - R[5] + sg * (v - u);
    u32 x1[4];
    float sv[8];
#pragma unroll
    for (int r = 0; r < 4; ++r) {
      const f32x2 xx = {bf_lo(xv[r]), bf_hi(xv[r])};
      const f32x2 t = xx + ga * y[r];
      x1[r] = pk_bf(t[0], t[1]);
      stg_wt(x1o + (size_t)(8 * s0 + 4 * hf + r) * D_, (u32)cp * 4u, x1[r]);
      sv[r] = dot2bf(x1[r], 0x3f803f80u, 0.f); sv[4 + r] = dot2bf(x1[r], x1[r], 0.f);
    }
    const float tot = reduce8(sv, lane);
    if (lane < 8) part[w * 8 + lane] = tot;
    const f32x2 gg = {rbf(1.0f + cb[0]), rbf(1.0f + cb[1])};
    const int d = 2 * cp, wi = d & 31;
    char* stage = (char*)Y;
    const int so = (((d >> 5) * 4 + ((wi & 15) >> 2)) * 8 + 4 * hf) * 16 + (4 * (wi >> 4) + (wi & 3)) * 2;
    __syncthreads();
    const int r = lane & 3;
    float s, qq;
    { float ps[5], pq[5];
#pragma unroll
      for (int i = 0; i < 5; ++i) { ps[i] = part[(5 * hf + i) * 8 + r]; pq[i] = part[(5 * hf + i) * 8 + 4 + r]; }
      batch_fence(); pin_arr(ps); pin_arr(pq);
      s = ((ps[0] + ps[1]) + (ps[2] + ps[3])) + ps[4]; qq = ((pq[0] + pq[1]) + (pq[2] + pq[3])) + pq[4]; }
    float m_, i_; ln_finish(s, qq, m_, i_);
    const float nb_ = -m_ * i_;
#pragma unroll
    for (int i = 0; i < 4; ++i) {
      const float na = __int_as_float(__builtin_amdgcn_readlane(__float_as_int(i_), i)), nb = __int_as_float(__builtin_amdgcn_readlane(__float_as_int(nb_), i));
      const f32x2 v2 = {bf_lo(x1[i]), bf_hi(x1[i])};
      const f32x2 hv = (v2 * na + nb) * gg + sb;
      *(u32*)(stage + so + 16 * i) = pk_bf(hv[0], hv[1]);
    }
    __syncthreads();
    const u32x4 piece = *(const u32x4*)(stage + 16 * tid);
    stg_wt(h2o + ((size_t)(s0 >> 1) * 20 * 64 + 8 * (s0 & 1)) * 8, (u32)((tid >> 3) * 16 + (tid & 7)) * 16u, piece);
  }
  if (w == 9) asm volatile("s_waitcnt vmcnt(0)" ::: "memory");
}

constexpr int K5_SLOT = 28 * 1024;
constexpr int K5_NS = 4;
constexpr int K5_PD = 4;
struct K5Ctx { const u16* wsrc; const u16* hsrc; u32 ldsw; u32 goff[7]; u32 hoff; u32 abase[2]; };
template <int s, int i> DI void k5_w1(const K5Ctx& c) {
  if constexpr (i == 0) set_m0(c.ldsw + (s % K5_NS) * K5_SLOT + 3072);
  glds16_s<1024 * i - 3072>((const char*)(c.wsrc + s * 4 * 512) - 4096, c.goff[i]);
}
template <int s, int i> DI void k5_t1(const K5Ctx& c, u32x4 (&t)[4]) {
  gload16_s<(i >> 1) * 1024>(t[i], c.hsrc + ((i & 1) * 20 + 2 * s) * 512, c.hoff);
}
template <int s> DI void k5_wall(const K5Ctx& c) { k5_w1<s, 0>(c); k5_w1<s, 1>(c); k5_w1<s, 2>(c); k5_w1<s, 3>(c); k5_w1<s, 4>(c); k5_w1<s, 5>(c); k5_w1<s, 6>(c); }
template <int s> DI void k5_tall(const K5Ctx& c, u32x4 (&t)[4]) { k5_t1<s, 0>(c, t); k5_t1<s, 1>(c, t); k5_t1<s, 2>(c, t); k5_t1<s, 3>(c, t); }
template <int n> DI s16x8 k5_rd(const K5Ctx& c) {
  constexpr int s = n / 28, t = (n / 14) & 1, j = n % 14, sl = s % K5_NS;
  return *(LDSP(const s16x8))((LDSP(const char))(size_t)c.abase[sl >> 1] + (sl & 1) * K5_SLOT + ((j >> 1) * 4 + t * 2 + (j & 1)) * 1024);
}
template <int n> DI void k5_sub(const K5Ctx& c, f32x4 (&acc)[14][2], s16x8 (&fr)[K5_PD + 1], u32x4 (&tok)[10][4]) {
  constexpr int s = n / 28, t = (n / 14) & 1, j = n % 14;
  constexpr int nb = n + K5_PD;
  if constexpr (nb < 280 && nb % 28 == 0) {
    wait_vm<(s + 2 <= 9) ? 11 : 0>();
    __builtin_amdgcn_s_barrier();
    tie4(tok[s + 1]);
  }
  if constexpr (nb < 280) fr[nb % (K5_PD + 1)] = k5_rd<nb>(c);
  {
    constexpr int q = nb / 28, pos = nb % 28 - (q == 0 ? K5_PD : 0);
    constexpr int idx = (pos % 2 == 0 && pos < 22) ? pos / 2 : -1;
    if constexpr (idx >= 0 && idx < 4 && q + 2 <= 9) k5_t1<q + 2, idx>(c, tok[q + 2]);
    if constexpr (idx >= 4 && q + 2 <= 9) k5_w1<q + 2, idx - 4>(c);
  }
  const s16x8 a = fr[n % (K5_PD + 1)];
  acc[j][0] = mfma16(a, as8(tok[s][2 * t]), acc[j][0]);
  acc[j][1] = mfma16(a, as8(tok[s][2 * t + 1]), acc[j][1]);
  __builtin_amdgcn_sched_barrier(0);
}
template <int... I> DI void k5_all(const K5Ctx& c, f32x4 (&acc)[14][2], s16x8 (&fr)[K5_PD + 1], u32x4 (&tok)[10][4], std::integer_sequence<int, I...>) { (k5_sub<I>(c, acc, fr, tok), ...); }
extern "C" __global__ __launch_bounds__(256, 2) void k5_glu(const u16* __restrict__ h2, const u16* __restrict__ Wgu, u16* __restrict__ hb) {
  extern __shared__ __attribute__((aligned(16))) char smem[];
  const int tid = threadIdx.x, lane = tid & 63, w = __builtin_amdgcn_readfirstlane(tid >> 6);
  const int xcd = blockIdx.x & 7, li = blockIdx.x >> 3;
  const int tt = 8 * (xcd & 1) + (li & 7), fc = 4 * (xcd >> 1) + (li >> 3);
  const int u0 = fc < 12 ? 7 * fc : 84 + 6 * (fc - 12), U = fc < 12 ? 7 : 6;
  if (blockIdx.x < 8) {
    const void* c6; const void* c7; CODE_ADDR(c6, k6_down); CODE_ADDR(c7, k7_end);
    if ((const char*)c7 > (const char*)c6 && (u32)tid * 128u < (u32)((const char*)c7 - (const char*)c6)) l2_touch(c6, (u32)tid * 128u, lds_addr(smem) + 3 * K5_SLOT + w * 256);
  }
  K5Ctx c;
  c.wsrc = Wgu + (size_t)u0 * 20 * 2 * 512;
#pragma unroll
  for (int i = 0; i < 7; ++i) { const int f = 7 * w + i, u = min(f >> 2, U - 1); c.goff[i] = 2 * (u * 20 * 2 * 512 + (f & 3) * 512) + lane * 16 + 4096 - (1024 * i - 3072); }
  c.ldsw = lds_addr(smem) + 7 * w * 1024;
  const int T16 = 8 * tt + 2 * w;
  c.hsrc = h2 + (size_t)T16 * 20 * 512; c.hoff = lane * 16;
#pragma unroll
  for (int j = 0; j < 2; ++j) { c.abase[j] = lds_addr(smem) + 2 * j * K5_SLOT + lane * 16; asm volatile("" : "+v"(c.abase[j])); }
  u32x4 tok[10][4];
  k5_tall<0>(c, tok[0]); k5_wall<0>(c);
  k5_tall<1>(c, tok[1]); k5_wall<1>(c);
  f32x4 acc[14][2];
#pragma unroll
  for (int u = 0; u < 14; ++u) { acc[u][0] = f32x4{0.f, 0.f, 0.f, 0.f}; acc[u][1] = f32x4{0.f, 0.f, 0.f, 0.f}; }
  wait_vm<11>();
  __builtin_amdgcn_s_barrier();
  tie4(tok[0]);
  s16x8 fr[K5_PD + 1];
  fr[0] = k5_rd<0>(c); fr[1] = k5_rd<1>(c); fr[2] = k5_rd<2>(c); fr[3] = k5_rd<3>(c);
  __builtin_amdgcn_sched_barrier(0);
  k5_all(c, acc, fr, tok, std::make_integer_sequence<int, 280>{});
  auto hv = [&](int u, int tl) { const f32x4 a = acc[2 * u][tl] * acc[2 * u + 1][tl]; return u32x2{pk_bf(a[0], a[1]), pk_bf(a[2], a[3])}; };
#pragma unroll
  for (int tl = 0; tl < 2; ++tl) {
    u16* hdst = hb + (size_t)(T16 + tl) * 54 * 512 + (u0 >> 1) * 512;
    const u32 vo = (u32)lane * 16u;
    if (!(u0 & 1)) {
#pragma unroll
      for (int k = 0; k < 3; ++k) { const u32x2 a = hv(2 * k, tl), b = hv(2 * k + 1, tl); stg_wt(hdst + k * 512, vo, u32x4{a[0], a[1], b[0], b[1]}); }
      if (U == 7) stg_wt(hdst + 3 * 512, vo, hv(6, tl));
    } else {
      stg_wt(hdst + 4, vo, hv(0, tl));
#pragma unroll
      for (int k = 0; k < 3; ++k) { const u32x2 a = hv(2 * k + 1, tl), b = hv(2 * k + 2, tl); stg_wt(hdst + (k + 1) * 512, vo, u32x4{a[0], a[1], b[0], b[1]}); }
    }
  }
}

constexpr int K6_NS = 4;
struct K6Ctx { const u16* wsrc; const u16* hsrc; u32 hoff; const float* gbp; u32 gbo; const u16* xp; u32 xo; };
struct K6Grp { u32x4 a[5]; u32x4 t[4]; };
template <int i> DI void k6_v1(const K6Ctx& c, int ks, K6Grp& gr) {
  if constexpr (i < 5) gload16_s<0>(gr.a[i], c.wsrc + (ks + i * 54) * 512, c.hoff);
  else gload16_s<0>(gr.t[i - 5], c.hsrc + (ks + (i - 5) * 54) * 512, c.hoff);
}
DI void k6_issue(const K6Ctx& c, int ks, K6Grp& gr) {
  k6_v1<0>(c, ks, gr); k6_v1<5>(c, ks, gr); k6_v1<6>(c, ks, gr); k6_v1<7>(c, ks, gr); k6_v1<8>(c, ks, gr);
  k6_v1<1>(c, ks, gr); k6_v1<2>(c, ks, gr); k6_v1<3>(c, ks, gr); k6_v1<4>(c, ks, gr);
}
DI void k6_tie(K6Grp& g) { asm volatile("" : "+v"(g.a[0]), "+v"(g.a[1]), "+v"(g.a[2]), "+v"(g.a[3]), "+v"(g.a[4]), "+v"(g.t[0]), "+v"(g.t[1]), "+v"(g.t[2]), "+v"(g.t[3])); }
struct K6Epi { u32x4 gb[5]; u32x2 xr[5]; };
template <int OFF> DI void gload8_s(u32x2& d, const void* sbase, u32 voff) {
  asm volatile("global_load_dwordx2 %0, %1, %2 offset:%3" : "=v"(d) : "v"(voff), "s"(sbase), "n"(OFF) : "memory");
}
constexpr int k6_after(int i, int NK) {
  int n = 0; bool seen = false;
  for (int g = 0; g < 3; ++g) { if (seen) n += 9; if (g == i) seen = true; }
  for (int st = 0; st < i; ++st) {
    if (st == NK - 3 && seen) n += 10;
    if (st + 3 < NK) { if (seen) n += 9; if (st + 3 == i) seen = true; }
  }
  return n;
}
template <int i, int NK> DI void k6_step(const K6Ctx& c, int ks0, f32x4 (&acc)[5][4], K6Grp (&gr)[K6_NS], K6Epi& ep, f32x4* red, int w, int lane) {
  wait_vm<k6_after(i, NK)>();
  K6Grp& g = gr[i % K6_NS];
  k6_tie(g);
  K6Grp& nx = gr[(i + 3) % K6_NS];
#pragma unroll
  for (int nt = 0; nt < 5; ++nt) {
    if constexpr (i + 3 < NK) {
      if (nt == 0) { k6_v1<0>(c, ks0 + i + 3, nx); k6_v1<5>(c, ks0 + i + 3, nx); }
      if (nt == 1) { k6_v1<6>(c, ks0 + i + 3, nx); k6_v1<7>(c, ks0 + i + 3, nx); }
      if (nt == 2) { k6_v1<8>(c, ks0 + i + 3, nx); k6_v1<1>(c, ks0 + i + 3, nx); }
      if (nt == 3) { k6_v1<2>(c, ks0 + i + 3, nx); k6_v1<3>(c, ks0 + i + 3, nx); }
      if (nt == 4) { k6_v1<4>(c, ks0 + i + 3, nx); }
    }
    if constexpr (i == NK - 3) {
      gload16_s<0>(ep.gb[nt], c.gbp + 16 * nt, c.gbo); gload8_s<0>(ep.xr[nt], c.xp + 16 * nt, c.xo);
    }
#pragma unroll
    for (int t = 0; t < 4; ++t) acc[nt][t] = mfma16(as8(g.a[nt]), as8(g.t[t]), acc[nt][t]);
    if constexpr (i == NK - 1) {
      if (nt >= 1) {
#pragma unroll
        for (int t = 0; t < 4; ++t) if (t != w) red[((w * 5 + nt - 1) * 4 + t) * 64 + lane] = acc[nt - 1][t];
      }
    }
    __builtin_amdgcn_sched_barrier(0);
  }
}
template <int i, int NK> struct K6Loop { static DI void run(const K6Ctx& c, int ks0, f32x4 (&acc)[5][4], K6Grp (&gr)[K6_NS], K6Epi& ep, f32x4* red, int w, int lane) {
  k6_step<i, NK>(c, ks0, acc, gr, ep, red, w, lane); K6Loop<i + 1, NK>::run(c, ks0, acc, gr, ep, red, w, lane); } };
template <int NK> struct K6Loop<NK, NK> { static DI void run(const K6Ctx&, int, f32x4 (&)[5][4], K6Grp (&)[K6_NS], K6Epi&, f32x4*, int, int) {} };
template <int NK> DI void k6_main(const K6Ctx& cx, int ks0, f32x4* red, int w, int lane, u16* __restrict__ out, u32 oo) {
  K6Grp gr[K6_NS]; K6Epi ep;
  k6_issue(cx, ks0, gr[0]); k6_issue(cx, ks0 + 1, gr[1]); k6_issue(cx, ks0 + 2, gr[2]);
  f32x4 acc[5][4];
#pragma unroll
  for (int a = 0; a < 5; ++a)
#pragma unroll
    for (int b = 0; b < 4; ++b) acc[a][b] = f32x4{0.f, 0.f, 0.f, 0.f};
  K6Loop<0, NK>::run(cx, ks0, acc, gr, ep, red, w, lane);
#pragma unroll
  for (int t = 0; t < 4; ++t) if (t != w) red[((w * 5 + 4) * 4 + t) * 64 + lane] = acc[4][t];
  wait_vm<0>();
  asm volatile("" : "+v"(ep.gb[0]), "+v"(ep.gb[1]), "+v"(ep.gb[2]), "+v"(ep.gb[3]), "+v"(ep.gb[4]), "+v"(ep.xr[0]), "+v"(ep.xr[1]), "+v"(ep.xr[2]), "+v"(ep.xr[3]), "+v"(ep.xr[4]));
  __syncthreads();
#pragma unroll
  for (int nt = 0; nt < 5; ++nt) {
    f32x4 v = w == 0 ? acc[nt][0] : w == 1 ? acc[nt][1] : w == 2 ? acc[nt][2] : acc[nt][3];
#pragma unroll
    for (int ww = 1; ww < 4; ++ww) v += red[((((w + ww) & 3) * 5 + nt) * 4 + w) * 64 + lane];
    const f32x4 gb = __builtin_bit_cast(f32x4, ep.gb[nt]);
    const f32x4 x4 = {bf_lo(ep.xr[nt][0]), bf_hi(ep.xr[nt][0]), bf_lo(ep.xr[nt][1]), bf_hi(ep.xr[nt][1])};
    const f32x4 o = x4 + gb * v;
    stg_wt(out + 16 * nt, oo, u32x2{pk_bf(o[0], o[1]), pk_bf(o[2], o[3])});
  }
}
extern "C" __global__ __launch_bounds__(256, 2) void k6_down(const u16* __restrict__ hb, const u16* __restrict__ Wd, const u16* __restrict__ x1, const float* __restrict__ mod, u16* __restrict__ out) {
  extern __shared__ __attribute__((aligned(16))) char smem[];
  const int tid = threadIdx.x, lane = tid & 63, w = __builtin_amdgcn_readfirstlane(tid >> 6), c = lane & 15, g = lane >> 4;
  const int xcd = blockIdx.x & 7, li = blockIdx.x >> 3;
  const int tt = 8 * (xcd & 3) + (li & 7), nb = 4 * (xcd >> 2) + (li >> 3);
  const int n0 = 80 * nb;
  const int ks0 = w < 2 ? 14 * w : 28 + 13 * (w - 2);
  K6Ctx cx;
  cx.wsrc = Wd + (size_t)(5 * nb) * 54 * 512; cx.hsrc = hb + (size_t)(4 * tt) * 54 * 512; cx.hoff = lane * 16;
  cx.gbp = mod + 5 * 640 + n0; cx.gbo = 16 * g;
  cx.xp = x1 + (size_t)(64 * tt + 16 * w) * D_ + n0; cx.xo = (u32)(c * D_ + 4 * g) * 2u;
  const int tau = 64 * tt + 16 * w + c, srow = (tau >> 3) + 256 * (tau & 7);
  const u32 oo = (u32)(srow * D_ + 4 * g) * 2u;
  if (w < 2) k6_main<14>(cx, ks0, (f32x4*)smem, w, lane, out + n0, oo); else k6_main<13>(cx, ks0, (f32x4*)smem, w, lane, out + n0, oo);
}

extern "C" __global__ void k7_end() {}

constexpr size_t WS_MOD = 0, WS_XT = 16384, WS_ZT = WS_XT + 8 * 21 * 256 * 16 * 4, WS_X1 = WS_ZT + 8 * 21 * 256 * 16 * 4,
                 WS_H2 = WS_X1 + 2048 * 640 * 2, WS_HB = WS_H2 + 2048 * 640 * 2, WS_WGU = WS_HB + 2048 * 1728 * 2, WS_WD = WS_WGU + 2 * 640 * 1728 * 2, WS_SYNC = WS_WD + 640 * 1728 * 2;
extern "C" void kernel_launch(void* const* d_in, const int* in_sizes, int n_in, void* d_out, int out_size, void* d_ws, size_t ws_size,
                              hipStream_t stream) {
  const u16* x = (const u16*)d_in[0]; const u16* wr = (const u16*)d_in[1]; const u16* wi = (const u16*)d_in[2];
  const u16* wg = (const u16*)d_in[4]; const u16* wu = (const u16*)d_in[5]; const u16* wd = (const u16*)d_in[6];
  const u16* c = (const u16*)d_in[7]; const u16* wmod = (const u16*)d_in[8];
  char* ws = (char*)d_ws;
  float* mod = (float*)(ws + WS_MOD); u32* XT = (u32*)(ws + WS_XT); u32* ZT = (u32*)(ws + WS_ZT);
  u16* x1 = (u16*)(ws + WS_X1); u16* h2 = (u16*)(ws + WS_H2); u16* hb = (u16*)(ws + WS_HB);
  u16* Wgu = (u16*)(ws + WS_WGU); u16* Wd = (u16*)(ws + WS_WD);
  K2Args A;
  A.x = x; A.c = c; A.wmod = wmod; A.mod = mod; A.XT = XT; A.wr = wr; A.wi = wi; A.wg = wg; A.wu = wu; A.Wgu = Wgu;
  A.gen = (u32*)(ws + WS_SYNC); A.arr = (u32*)(ws + WS_SYNC + 4096); A.gran = (unsigned long long*)(ws + WS_SYNC + 8192);
  k2_fwd<<<K2_GRID, 640, 0, stream>>>(A);
  k3_mid<<<168, 512, 0, stream>>>(XT, wr, wi, ZT);
  k4_inv<<<256, 640, 0, stream>>>(ZT, x, mod, x1, h2, wd, Wd);
  (void)hipFuncSetAttribute((const void*)k5_glu, hipFuncAttributeMaxDynamicSharedMemorySize, K5_NS * K5_SLOT);
  k5_glu<<<256, 256, K5_NS * K5_SLOT, stream>>>(h2, Wgu, hb);
  (void)hipFuncSetAttribute((const void*)k6_down, hipFuncAttributeMaxDynamicSharedMemorySize, 81920);
  k6_down<<<256, 256, 81920, stream>>>(hb, Wd, x1, mod, (u16*)d_out);
}
```

```cpp
#include <hip/hip_runtime.h>
#include <hip/hip_bf16.h>
#include <stdint.h>
#include <utility>

typedef unsigned short u16;
typedef unsigned int u32;
typedef short s16x4 __attribute__((ext_vector_type(4)));
typedef short s16x8 __attribute__((ext_vector_type(8)));
typedef __bf16 bf2_t __attribute__((ext_vector_type(2)));
typedef float f32x2 __attribute__((ext_vector_type(2)));
typedef float f32x4 __attribute__((ext_vector_type(4)));
typedef u32 u32x2 __attribute__((ext_vector_type(2)));
typedef u32 u32x4 __attribute__((ext_vector_type(4)));
#define DI __device__ __forceinline__
#define LDSP(T) __attribute__((address_space(3))) T*

constexpr int S_ = 2048, D_ = 640, F_ = 1728, DF = 321;

constexpr double kPi = 3.14159265358979323846264338327950288;
constexpr double c_sin0(double x) { double x2 = x * x, t = x, s = x; for (int i = 1; i < 11; ++i) { t *= -x2 / ((2.0 * i) * (2.0 * i + 1.0)); s += t; } return s; }
constexpr double c_cos0(double x) { double x2 = x * x, t = 1.0, s = 1.0; for (int i = 1; i < 11; ++i) { t *= -x2 / ((2.0 * i - 1.0) * (2.0 * i)); s += t; } return s; }
constexpr double c_cos(long n, long N) {
  n %= N; if (n < 0) n += N;
  if (2 * n > N) n = N - n;
  bool neg = false;
  if (4 * n > N) { neg = true; n = N - 2 * n; N = 2 * N; }
  double r = 0;
  if (4 * n == N) r = 0.0;
  else if (8 * n > N) r = c_sin0(2 * kPi * (double)(N - 4 * n) / (double)(4 * N));
  else r = c_cos0(2 * kPi * (double)n / (double)N);
  return neg ? -r : r;
}
constexpr double c_sin(long n, long N) { return c_cos(4 * n - N, 4 * N); }
constexpr u16 c_bf16(double v) {
  float f = (float)v; u32 u = __builtin_bit_cast(u32, f);
  u += 0x7fffu + ((u >> 16) & 1u); return (u16)(u >> 16);
}

struct F8w { u16 v[2][64][8]; };
struct F8x6 { u16 v[3][2][64][8]; };
constexpr F8w mk_f32a() {
  F8w t{};
  for (int tt = 0; tt < 2; ++tt) for (int l = 0; l < 64; ++l) for (int j = 0; j < 8; ++j) {
    int o = 16 * tt + (l & 15), d1 = 8 * (l >> 4) + j; double v = 0;
    if (o == 0) v = 1.0; else if (o == 1) v = (d1 & 1) ? -1.0 : 1.0;
    else { int k1 = o >> 1; v = (o & 1) ? -c_sin(d1 * k1, 32) : c_cos(d1 * k1, 32); }
    t.v[tt][l][j] = c_bf16(v);
  }
  return t;
}
constexpr F8x6 mk_f20(bool inv) {
  F8x6 t{};
  for (int mt = 0; mt < 3; ++mt) for (int ks = 0; ks < 2; ++ks) for (int l = 0; l < 64; ++l) for (int j = 0; j < 8; ++j) {
    int row = l & 15, g = l >> 4, k0 = 8 * mt + (row >> 1), part = row & 1, d0 = 16 * ks + 4 * g + (j >> 1), pp = j & 1; double v = 0;
    if (d0 < 20 && k0 < 20) {
      double co = c_cos(d0 * k0, 20), si = inv ? -c_sin(d0 * k0, 20) : c_sin(d0 * k0, 20);
      v = part == 0 ? (pp == 0 ? co : si) : (pp == 0 ? -si : co);
    }
    t.v[mt][ks][l][j] = c_bf16(v);
  }
  return t;
}
constexpr F8w mk_a1(int m1, bool inv) {
  F8w t{};
  for (int l = 0; l < 64; ++l) for (int i = 0; i < 4; ++i) {
    int c = l & 15, a = 4 * (l >> 4) + i, n = inv ? c * (8 * a + m1) : a * (8 * c + m1);
    double co = c_cos(n, 128), si = inv ? -c_sin(n, 128) : c_sin(n, 128);
    t.v[0][l][2 * i] = c_bf16(co); t.v[0][l][2 * i + 1] = c_bf16(si);
    t.v[1][l][2 * i] = c_bf16(-si); t.v[1][l][2 * i + 1] = c_bf16(co);
  }
  return t;
}
constexpr F8w mk_f16(bool inv) {
  F8w t{};
  for (int l = 0; l < 64; ++l) for (int i = 0; i < 4; ++i) {
    int c = l & 15, a = 4 * (l >> 4) + i;
    double co = c_cos(a * c, 16), si = c_sin(a * c, 16);
    if (!inv) { t.v[0][l][2 * i] = c_bf16(co); t.v[0][l][2 * i + 1] = c_bf16(si); t.v[1][l][2 * i] = c_bf16(-si); t.v[1][l][2 * i + 1] = c_bf16(co); }
    else { t.v[0][l][i] = c_bf16(co); t.v[0][l][4 + i] = c_bf16(-si); t.v[1][l][i] = c_bf16(si); t.v[1][l][4 + i] = c_bf16(co); }
  }
  return t;
}
constexpr F8w mk_f32bi() {
  F8w t{};
  for (int nt = 0; nt < 2; ++nt) for (int l = 0; l < 64; ++l) for (int j = 0; j < 8; ++j) {
    int d1 = 16 * nt + (l & 15), K = 8 * (l >> 4) + j; double v = 0;
    if (K == 0) v = 1.0; else if (K == 1) v = (d1 & 1) ? -1.0 : 1.0;
    else { int k1 = K >> 1; v = (K & 1) ? -2.0 * c_sin(k1 * d1, 32) : 2.0 * c_cos(k1 * d1, 32); }
    t.v[nt][l][j] = c_bf16(v);
  }
  return t;
}
static __device__ const F8w g_f32a = mk_f32a();
static __device__ const F8x6 g_f20a = mk_f20(false);
static __device__ const F8x6 g_f20ai = mk_f20(true);
static __device__ const F8w g_a1[8] = {mk_a1(0, false), mk_a1(1, false), mk_a1(2, false), mk_a1(3, false), mk_a1(4, false), mk_a1(5, false), mk_a1(6, false), mk_a1(7, false)};
static __device__ const F8w g_a1i[8] = {mk_a1(0, true), mk_a1(1, true), mk_a1(2, true), mk_a1(3, true), mk_a1(4, true), mk_a1(5, true), mk_a1(6, true), mk_a1(7, true)};
static __device__ const F8w g_f16f = mk_f16(false);
static __device__ const F8w g_f16i = mk_f16(true);
static __device__ const F8w g_f32bi = mk_f32bi();

DI float bf_lo(u32 v) { return __uint_as_float(v << 16); }
DI float bf_hi(u32 v) { return __uint_as_float(v & 0xffff0000u); }
DI float bf2f(u16 v) { return __uint_as_float(((u32)v) << 16); }
DI u32 pk_bf(float a, float b) { f32x2 f = {a, b}; return __builtin_bit_cast(u32, __builtin_convertvector(f, bf2_t)); }
DI float rbf(float a) { return bf_lo(pk_bf(a, a)); }
DI float dpp_x1(float v) { return __int_as_float(__builtin_amdgcn_mov_dpp(__float_as_int(v), 0xB1, 0xf, 0xf, true)); }
DI float dpp_x2(float v) { return __int_as_float(__builtin_amdgcn_mov_dpp(__float_as_int(v), 0x4E, 0xf, 0xf, true)); }
DI float dpp_hm(float v) { return __int_as_float(__builtin_amdgcn_mov_dpp(__float_as_int(v), 0x141, 0xf, 0xf, true)); }
DI f32x4 mfma16(s16x8 a, s16x8 b, f32x4 c) { return __builtin_amdgcn_mfma_f32_16x16x32_bf16(a, b, c, 0, 0, 0); }
DI s16x4 tr_read(const void* p) { return __builtin_amdgcn_ds_read_tr16_b64_v4i16((LDSP(s16x4))p); }
DI void batch_fence() { asm volatile("" ::: "memory"); }
template <class T, int N> DI void pin_arr(T (&v)[N]) {
#pragma unroll
  for (int i = 0; i < N; ++i) asm volatile("" : "+v"(v[i]));
}
template <class T> DI void pin(T& v) { asm volatile("" : "+v"(v)); }
DI void l2_touch(const void* base, u32 voff, u32 lds) {
  asm volatile("s_mov_b32 m0, %0\n\ts_nop 0\n\tglobal_load_lds_dword %1, %2" :: "s"(lds), "v"(voff), "s"(base) : "memory");
}
DI void side_delay(int ticks10ns) {
  const unsigned long long t0 = __builtin_amdgcn_s_memrealtime();
  while (__builtin_amdgcn_s_memrealtime() - t0 < (unsigned long long)ticks10ns) __builtin_amdgcn_s_sleep(4);
}
#define GSP(T) __attribute__((address_space(1))) T*
template <class T> DI T ldg(const void* sbase, u32 voff) { GSP(const char) p = (GSP(const char))sbase; asm("" : "+s"(p)); return *(GSP(const T))(p + voff); }
DI void stg_wt(void* sbase, u32 voff, u32x4 v) { asm volatile("global_store_dwordx4 %0, %1, %2 sc1\n\ts_nop 1" :: "v"(voff), "v"(v), "s"(sbase) : "memory"); }
DI void stg_wt(void* sbase, u32 voff, u32x2 v) { asm volatile("global_store_dwordx2 %0, %1, %2 sc1\n\ts_nop 1" :: "v"(voff), "v"(v), "s"(sbase) : "memory"); }
DI void stg_wt(void* sbase, u32 voff, u32 v) { asm volatile("global_store_dword %0, %1, %2 sc1" :: "v"(voff), "v"(v), "s"(sbase) : "memory"); }
#define CODE_ADDR(var, sym) asm volatile("s_getpc_b64 s[40:41]\n\ts_add_u32 s40, s40, " #sym "@rel32@lo+4\n\ts_addc_u32 s41, s41, " #sym "@rel32@hi+12\n\ts_mov_b64 %0, s[40:41]" : "=s"(var) :: "s40", "s41", "scc")
DI u32 lds_addr(const void* p) { return (u32)(size_t)(LDSP(const char))p; }
DI s16x8 cat8(s16x4 a, s16x4 b) { return __builtin_shufflevector(a, b, 0, 1, 2, 3, 4, 5, 6, 7); }
DI s16x8 as8(u32x4 v) { return __builtin_bit_cast(s16x8, v); }
DI float dot2bf(u32 a, u32 b, float c) { return __builtin_amdgcn_fdot2_f32_bf16(__builtin_bit_cast(bf2_t, a), __builtin_bit_cast(bf2_t, b), c, false); }

DI void ln_finish(float s, float q, float& m_, float& i_) {
  const float mean = s * (1.0f / 640.0f);
  const float var = fmaxf(q * (1.0f / 640.0f) - mean * mean, 0.f);
  const float sd = rbf(__builtin_amdgcn_sqrtf(rbf(rbf(var) + 1e-5f)));
  m_ = rbf(mean); i_ = __builtin_amdgcn_rcpf(sd);
}
DI float reduce8(const float (&v)[8], int lane) {
  float b[4], c[2], d;
  const bool s2 = lane & 4, s1 = lane & 2, s0 = lane & 1;
#pragma unroll
  for (int i = 0; i < 4; ++i) { float keep = s2 ? v[4 + i] : v[i], send = s2 ? v[i] : v[4 + i]; b[i] = keep + dpp_hm(send); }
#pragma unroll
  for (int i = 0; i < 2; ++i) { float keep = s1 ? b[2 + i] : b[i], send = s1 ? b[i] : b[2 + i]; c[i] = keep + dpp_x2(send); }
  { float keep = s0 ? c[1] : c[0], send = s0 ? c[0] : c[1]; d = keep + dpp_x1(send); }
  d += __int_as_float(__builtin_amdgcn_mov_dpp(__float_as_int(d), 0x128, 0xf, 0xf, true));
  auto r16 = __builtin_amdgcn_permlane16_swap(__float_as_uint(d), __float_as_uint(d), false, false);
  d = __uint_as_float(r16[0]) + __uint_as_float(r16[1]);
  auto r32 = __builtin_amdgcn_permlane32_swap(__float_as_uint(d), __float_as_uint(d), false, false);
  return __uint_as_float(r32[0]) + __uint_as_float(r32[1]);
}

template <int N> DI void wait_vm() { asm volatile("s_waitcnt vmcnt(%0)" ::"n"(N) : "memory"); }
template <int OFF> DI void gload16_s(u32x4& d, const void* sbase, u32 voff) {
  asm volatile("global_load_dwordx4 %0, %1, %2 offset:%3" : "=v"(d) : "v"(voff), "s"(sbase), "n"(OFF) : "memory");
}
template <int OFF> DI void glds16_s(const void* sbase, u32 voff) {
  asm volatile("global_load_lds_dwordx4 %0, %1 offset:%2" :: "v"(voff), "s"(sbase), "n"(OFF) : "memory");
}
DI void dma1k(const void* sbase, u32 voff, u32 lds) {
  asm volatile("s_mov_b32 m0, %0\n\ts_nop 0\n\tglobal_load_lds_dwordx4 %1, %2" :: "s"(lds), "v"(voff), "s"(sbase) : "memory");
}
DI void set_m0(u32 v) { asm volatile("s_mov_b32 m0, %0\n\ts_nop 0" :: "s"(v) : "memory"); }
DI void tie4(u32x4 (&t)[4]) { asm volatile("" : "+v"(t[0]), "+v"(t[1]), "+v"(t[2]), "+v"(t[3])); }

DI void prep_load(u32x4 (&v)[4], const u16* __restrict__ src, int ld, int lane) {
  const u32 vo = (u32)((lane >> 3) * ld + 8 * (lane & 7)) * 2u;
#pragma unroll
  for (int i = 0; i < 4; ++i) v[i] = ldg<u32x4>(src + (size_t)8 * i * ld, vo);
}
DI void prep_store(const u32x4 (&v)[4], u16* __restrict__ dst, int jstride, char* lds, int lane) {
#pragma unroll
  for (int i = 0; i < 4; ++i) {
    const int row = 8 * i + (lane >> 3), ch = lane & 7;
    *(u32x4*)(lds + row * 128 + (((ch >> 1) ^ ((row >> 1) & 3)) * 32) + (ch & 1) * 16) = v[i];
  }
  const int c = lane & 15, g = lane >> 4, q = c >> 2, p = c & 3, r = 4 * g + q;
#pragma unroll
  for (int j = 0; j < 4; ++j) {
    const char* b = lds + r * 128 + ((j ^ ((r >> 1) & 3)) * 32) + 8 * p;
    const s16x8 f = cat8(tr_read(b), tr_read(b + 16 * 128));
    stg_wt(dst + (size_t)j * jstride, (u32)lane * 16u, __builtin_bit_cast(u32x4, f));
  }
}
DI void prep_wave(const u16* __restrict__ src, int ld, u16* __restrict__ dst, int jstride, char* lds, int lane) {
  u32x4 v[4]; prep_load(v, src, ld, lane); prep_store(v, dst, jstride, lds, lane);
}
DI void prep_gu(int task, const u16* wg, const u16* wu, u16* Wgu, char* lds, int lane) {
  const int mat = task >= 540, tk = task - 540 * mat, cg = tk / 20, ks = tk % 20;
  prep_wave((mat ? wu : wg) + (size_t)ks * 32 * F_ + 64 * cg, F_, Wgu + ((size_t)(4 * cg * 20 + ks) * 2 + mat) * 512, 20 * 2 * 512, lds, lane);
}
DI void prep_d(int task, const u16* wd, u16* Wd, char* lds, int lane) {
  const int cg = task / 54, ks = task % 54;
  prep_wave(wd + (size_t)ks * 32 * D_ + 64 * cg, D_, Wd + (size_t)(4 * cg * 54 + ks) * 512, 54 * 512, lds, lane);
}


typedef __attribute__((address_space(1))) u32 gu32;
typedef __attribute__((address_space(1))) unsigned long long gu64;
DI u32 ld_agent(const u32* p) { return __hip_atomic_load((const gu32*)p, __ATOMIC_RELAXED, __HIP_MEMORY_SCOPE_AGENT); }
DI void st_agent(u32* p, u32 v) { __hip_atomic_store((gu32*)p, v, __ATOMIC_RELAXED, __HIP_MEMORY_SCOPE_AGENT); }
DI void st_agent64(unsigned long long* p, unsigned long long v) { __hip_atomic_store((gu64*)p, v, __ATOMIC_RELAXED, __HIP_MEMORY_SCOPE_AGENT); }
constexpr int K2_SIDE_DELAY = 80;
constexpr int SPIN_LIMIT = 1 << 16;

template <int NR> DI void gemv_part(int col0, int row0, u32 tagv, bool publish, unsigned long long* gran, int gidx0, const u16* __restrict__ c, const u16* __restrict__ wmod,
                                    float* __restrict__ mod, char* lds) {
  float* sl = (float*)lds; float* part = sl + 640;
  const int t = threadIdx.x, lane = t & 63, w = t >> 6, cq = t & 7, rg = t >> 3;
  const u16 cv = ldg<u16>(c, 2 * t);
  u32x4 wv[NR];
  const u32 wo = (u32)((row0 + rg) * 3840 + col0 + 8 * cq) * 2u;
#pragma unroll
  for (int i = 0; i < NR; ++i) wv[i] = ldg<u32x4>(wmod + (size_t)80 * i * 3840, wo);
  { const float x = bf2f(cv); sl[t] = rbf(x * __builtin_amdgcn_rcpf(1.0f + __expf(-x))); }
  __syncthreads();
  float acc[8] = {};
#pragma unroll
  for (int i = 0; i < NR; ++i) {
    const float s = sl[row0 + rg + 80 * i];
#pragma unroll
    for (int k = 0; k < 4; ++k) { acc[2 * k] += s * bf_lo(wv[i][k]); acc[2 * k + 1] += s * bf_hi(wv[i][k]); }
  }
  float u[4];
  const bool b3 = lane & 8;
#pragma unroll
  for (int i = 0; i < 4; ++i) { const float keep = b3 ? acc[4 + i] : acc[i], send = b3 ? acc[i] : acc[4 + i]; u[i] = keep + __int_as_float(__builtin_amdgcn_mov_dpp(__float_as_int(send), 0x128, 0xf, 0xf, true)); }
  float r2[2];
#pragma unroll
  for (int i = 0; i < 2; ++i) { auto x = __builtin_amdgcn_permlane16_swap(__float_as_uint(u[i]), __float_as_uint(u[i + 2]), false, false); r2[i] = __uint_as_float(x[0]) + __uint_as_float(x[1]); }
  auto y = __builtin_amdgcn_permlane32_swap(__float_as_uint(r2[0]), __float_as_uint(r2[1]), false, false);
  part[w * 64 + 8 * cq + 4 * ((lane >> 3) & 1) + 2 * ((lane >> 4) & 1) + (lane >> 5)] = __uint_as_float(y[0]) + __uint_as_float(y[1]);
  __syncthreads();
  if (t < 64) {
    float s = 0.f;
#pragma unroll
    for (int i = 0; i < 10; ++i) s += part[i * 64 + t];
    if (publish) st_agent64(gran + gidx0 + t, ((unsigned long long)(tagv + 1u) << 32) | __float_as_uint(s));
    else mod[col0 + t] = rbf(s);
  }
}

DI int row_group(int b) { return 2 * (8 * (b >> 4) + (b & 7)) + ((b >> 3) & 1); }
DI void sincos_rev(float a, float& s, float& c) { s = __builtin_amdgcn_sinf(a); c = __builtin_amdgcn_cosf(a); }
DI void gram_part(const u32x4 (&f)[4], float* part  , int i, int lane) {
  f32x4 acc = {0.f, 0.f, 0.f, 0.f};
#pragma unroll
  for (int k = 0; k < 4; ++k) acc = mfma16(as8(f[k]), as8(f[k]), acc);
  const int c = lane & 15, g = lane >> 4;
  const float dg = (c & 2) ? ((c & 1) ? acc[3] : acc[2]) : ((c & 1) ? acc[1] : acc[0]);
  if (c < 8 && g == (c >> 2)) part[i * 16 + 8 + c] = dg;
  if (c < 8 && g == 2) part[i * 16 + c] = acc[0];
}
DI void ln_bcast8(const float* part, int lane, float (&a_)[8], float (&b_)[8]) {
  const int r = lane & 7;
  float ps[5], pq[5];
#pragma unroll
  for (int i = 0; i < 5; ++i) { ps[i] = part[i * 16 + r]; pq[i] = part[i * 16 + 8 + r]; }
  batch_fence(); pin_arr(ps); pin_arr(pq);
  const float s = ((ps[0] + ps[1]) + (ps[2] + ps[3])) + ps[4], q = ((pq[0] + pq[1]) + (pq[2] + pq[3])) + pq[4];
  float m_, i_; ln_finish(s, q, m_, i_);
  const float nb = -m_ * i_;
#pragma unroll
  for (int i = 0; i < 8; ++i) {
    a_[i] = __int_as_float(__builtin_amdgcn_readlane(__float_as_int(i_), i));
    b_[i] = __int_as_float(__builtin_amdgcn_readlane(__float_as_int(nb), i));
  }
}
constexpr int HT_RS = 1296;
struct K2Args { const u16* x; const u16* c; const u16* wmod; float* mod; u32* XT; const u16* wr; const u16* wi; const u16* wg; const u16* wu; u16* Wgu; const u16* wd; u16* Wd; u32* gen; u32* arr; unsigned long long* gran; };
constexpr int K2_MAIN0 = 88, K2_SIDE0 = 344, K2_GRID = 344 + 54;
extern "C" __global__ __launch_bounds__(640) void k2_fwd(const K2Args A) {
  __shared__ __attribute__((aligned(16))) char smem[32768 + 5 * 4096 + 256];
  const int bid = blockIdx.x;
  if (bid >= K2_SIDE0) {
    side_delay(K2_SIDE_DELAY);
    const int wv = __builtin_amdgcn_readfirstlane(threadIdx.x >> 6);
    prep_d((bid - K2_SIDE0) * 10 + wv, A.wd, A.Wd, smem + 4096 * wv, threadIdx.x & 63);
    return;
  }
  if (bid >= 40 && bid < 80) {
    side_delay(K2_SIDE_DELAY);
    gemv_part<8>(1280 + 64 * (bid - 40), 0, 0u, false, nullptr, 0, A.c, A.wmod, A.mod, smem); return;
  }
  if (bid >= 80 && bid < 88) {
    const void* c3; const void* c7;
    CODE_ADDR(c3, k3_mid); CODE_ADDR(c7, k7_end);
    const u32 len = min((u32)((const char*)c7 - (const char*)c3), 65536u) * ((const char*)c7 > (const char*)c3);
    const u32 off = threadIdx.x * 128u;
    if (off < len) l2_touch(c3, off, lds_addr(smem) + __builtin_amdgcn_readfirstlane(threadIdx.x >> 6) * 256);
    asm volatile("s_waitcnt vmcnt(0)" ::: "memory");
    if (bid < 87) return;
  }
  if (bid < 40) {
    const u32 tagv = ld_agent(A.gen);
    gemv_part<4>(64 * (bid >> 1), 320 * (bid & 1), tagv, true, A.gran, 1280 * (bid & 1) + 64 * (bid >> 1), A.c, A.wmod, A.mod, smem);
    if (threadIdx.x == 0) st_agent(A.arr + bid, tagv + 1u);
    return;
  }
  if (bid == 87) {
    const u32 tag = ld_agent(A.gen) + 1u;
    const int t = threadIdx.x, who = t < 40 ? t : t + 48;
    for (int spin = 0; spin < SPIN_LIMIT; ++spin) {
      const bool ok = t >= 296 || ld_agent(A.arr + who) == tag;
      if (__syncthreads_and(ok)) break;
      __builtin_amdgcn_s_sleep(8);
    }
    if (t == 0) st_agent(A.gen, tag);
    return;
  }
  u32* __restrict__ XT = A.XT;
  char* hT = smem; char* U = smem + 8 * HT_RS; float* part = (float*)(smem + 8 * HT_RS + 8 * 17 * 80);
  char* tab = smem + 8 * HT_RS + 8 * 17 * 80 + 5 * 16 * 4;
  const int tid = threadIdx.x, lane = tid & 63, w = __builtin_amdgcn_readfirstlane(tid >> 6);
  const int s0 = row_group(bid - K2_MAIN0);
  const int c = lane & 15, g = lane >> 4, q = c >> 2, p = c & 3;
  const u16* xrow = A.x + (size_t)s0 * D_;
  f32x4 tw0, tw1; u32 ard, awr, aw0, acr; float z0r, z0i;
  auto precompute = [&]() {
    const int Qw = 4 * w + q, rho = Qw / 5, d0 = 4 * (Qw % 5) + p;
    const bool rot = rho >= 3 && (rho & 1);
    const float fd = (float)d0 * (1.0f / 640.0f);
    const int k10 = g == 0 ? 16 : 2 * g;
    float s_[4], c_[4];
    sincos_rev(fd * (float)k10, s_[0], c_[0]); sincos_rev(fd * (float)(2 * g + 1), s_[1], c_[1]);
    sincos_rev(fd * (float)(8 + 2 * g), s_[2], c_[2]); sincos_rev(fd * (float)(9 + 2 * g), s_[3], c_[3]);
    tw0 = f32x4{rot ? s_[0] : c_[0], rot ? c_[0] : -s_[0], rot ? s_[1] : c_[1], rot ? c_[1] : -s_[1]};
    tw1 = f32x4{rot ? s_[2] : c_[2], rot ? c_[2] : -s_[2], rot ? s_[3] : c_[3], rot ? c_[3] : -s_[3]};
    const int Qr = 4 * w + p;
    ard = lds_addr(hT) + (Qr / 5) * HT_RS + 2 * (20 * (8 * g + q) + 4 * (Qr % 5));
    awr = lds_addr(U) + 4 * (rho * 17 * 20 + d0 + 2 * g * 20);
    aw0 = g == 0 ? awr + 4 * 16 * 20 : awr;
    z0r = rot ? 0.f : 1.f; z0i = rot ? 1.f : 0.f;
    acr = lds_addr(U) + (w < 8 ? ((2 * (w >> 1)) * 17 + c) * 80 : ((c & 7) * 17 + 16) * 80);
    pin(tw0); pin(tw1); pin(ard); pin(awr); pin(aw0); pin(z0r); pin(z0i); pin(acr);
  };
  if (w < 5) {
    u32 xv[8];
#pragma unroll
    for (int r = 0; r < 8; ++r) xv[r] = ldg<u32>(xrow + (size_t)r * 256 * D_, 4 * tid);
    const u32 tagv = ld_agent(A.gen);
    precompute();
    __syncthreads();
    float na[8], nb[8];
    ln_bcast8(part, lane, na, nb);
    f32x2 o[8];
    {
      f32x2 h[8];
#pragma unroll
      for (int r = 0; r < 8; ++r) { f32x2 v = {bf_lo(xv[r]), bf_hi(xv[r])}; h[r] = v * na[r] + nb[r]; }
      const float cc = 0.70710678118654752f;
      f32x2 a0 = h[0] + h[4], a1 = h[1] + h[5], a2 = h[2] + h[6], a3 = h[3] + h[7];
      f32x2 b0 = h[0] - h[4], b1 = h[1] - h[5], b2 = h[2] - h[6], b3 = h[3] - h[7];
      f32x2 e = a0 + a2, f = a1 + a3, u = cc * (b1 - b3), v = cc * (b1 + b3);
      o[0] = e + f; o[1] = e - f; o[2] = b0 + u; o[3] = -b2 - v; o[4] = a0 - a2; o[5] = a3 - a1; o[6] = b0 - u; o[7] = b2 - v;
#pragma unroll
      for (int r = 0; r < 8; ++r) pin(o[r]);
    }
    const u32 tag = __builtin_amdgcn_readfirstlane(tagv) + 1u;
    if (tid == 0) st_agent(A.arr + bid, tag);
    f32x2 sa, gg;
    {
      const unsigned long long* ga_ = A.gran + 2 * tid;
      u32x4 va, vb, vc, vd;
      for (int spin = 0; spin < SPIN_LIMIT; ++spin) {
        asm volatile("global_load_dwordx4 %0, %4, off sc1\n\tglobal_load_dwordx4 %1, %5, off sc1\n\tglobal_load_dwordx4 %2, %6, off sc1\n\tglobal_load_dwordx4 %3, %7, off sc1\n\ts_waitcnt vmcnt(0)"
                     : "=&v"(va), "=&v"(vb), "=&v"(vc), "=&v"(vd) : "v"(ga_), "v"(ga_ + 1280), "v"(ga_ + 640), "v"(ga_ + 1920) : "memory");
        const bool ok = va[1] == tag && va[3] == tag && vb[1] == tag && vb[3] == tag && vc[1] == tag && vc[3] == tag && vd[1] == tag && vd[3] == tag;
        if (__all(ok)) break;
      }
      sa = f32x2{rbf(__uint_as_float(va[0]) + __uint_as_float(vb[0])), rbf(__uint_as_float(va[2]) + __uint_as_float(vb[2]))};
      gg = f32x2{rbf(1.0f + rbf(__uint_as_float(vc[0]) + __uint_as_float(vd[0]))), rbf(1.0f + rbf(__uint_as_float(vc[2]) + __uint_as_float(vd[2])))};
    }
    o[0] = o[0] * gg + 8.0f * sa;
    *(u32*)(hT + 4 * tid) = pk_bf(o[0][0], o[0][1]);
#pragma unroll
    for (int r = 1; r < 8; ++r) { const f32x2 v = o[r] * gg; *(u32*)(hT + r * HT_RS + 4 * tid) = pk_bf(v[0], v[1]); }
  } else {
    const int i = w - 5;
    u32x4 f[4];
    const u32 xo = (u32)((c & 7) * 256 * D_ + 128 * i + 8 * g) * 2u;
#pragma unroll
    for (int k = 0; k < 4; ++k) f[k] = ldg<u32x4>(xrow, xo + 64 * k);
    const s16x8 t0 = i < 2 ? ldg<s16x8>(g_f32a.v[i < 2 ? i : 0], lane * 16) : ldg<s16x8>(&g_f20a.v[0][0][0][0] + (i - 2) * 512, lane * 16);
    const s16x8 t1 = ldg<s16x8>(&g_f20a.v[0][0][0][0] + (i < 3 ? i + 3 : 5) * 512, lane * 16);
    precompute();
    if (c >= 8) {
      const u32 fill = c == 8 ? 0x3f803f80u : 0u;
#pragma unroll
      for (int k = 0; k < 4; ++k) f[k] = u32x4{fill, fill, fill, fill};
    }
    gram_part(f, part, i, lane);
    *(s16x8*)(tab + i * 1024 + lane * 16) = t0;
    if (i < 3) *(s16x8*)(tab + (i + 5) * 1024 + lane * 16) = t1;
    __syncthreads();
    const int task = (bid - K2_MAIN0) * 5 + i;
    if (task < 1080) prep_gu(task, A.wg, A.wu, A.Wgu, smem + 32768 + 4096 * i, lane);
    if (i == 4) {
      const int xc = bid & 7, li = (bid - K2_MAIN0) >> 3;
#pragma unroll
      for (int k = 0; k < 4; ++k) {
        const int e = (k & 1) * 64 + lane, id = li * 96 + e;
        const int ln = id % 6, row = 8 * ((id / 6) & 255) + (id < 1536 ? xc : ((8 - xc) & 7));
        const u32 off = min((u32)(row * 642 + ln * 128), (u32)(2048 * 642 - 4)) & ~3u;
        if (e < 96) l2_touch((k >> 1) ? A.wi : A.wr, off, lds_addr(smem) + sizeof(smem) - 256);
      }
    }
  }
  __syncthreads();
  {
    LDSP(const char) base = (LDSP(const char))(size_t)ard;
    s16x8 b = cat8(__builtin_amdgcn_ds_read_tr16_b64_v4i16((LDSP(s16x4))base), __builtin_amdgcn_ds_read_tr16_b64_v4i16((LDSP(s16x4))(base + 160)));
    s16x8 fa0 = *(const s16x8*)(tab + lane * 16), fa1 = *(const s16x8*)(tab + 1024 + lane * 16);
    batch_fence(); pin(b); pin(fa0); pin(fa1);
    const f32x4 z = {0.f, 0.f, 0.f, 0.f};
    f32x4 a0 = mfma16(fa0, b, z), a1 = mfma16(fa1, b, z);
    LDSP(u32) Ub = (LDSP(u32))(size_t)awr;
    const float x0 = g == 0 ? a0[1] : a0[0], x1 = g == 0 ? 0.f : a0[1];
    *(LDSP(u32))(size_t)aw0 = pk_bf(x0 * tw0[0] - x1 * tw0[1], x0 * tw0[1] + x1 * tw0[0]);
    if (g == 0) Ub[0] = pk_bf(a0[0] * z0r, a0[0] * z0i);
    Ub[20] = pk_bf(a0[2] * tw0[2] - a0[3] * tw0[3], a0[2] * tw0[3] + a0[3] * tw0[2]);
    Ub[8 * 20] = pk_bf(a1[0] * tw1[0] - a1[1] * tw1[1], a1[0] * tw1[1] + a1[1] * tw1[0]);
    Ub[9 * 20] = pk_bf(a1[2] * tw1[2] - a1[3] * tw1[3], a1[2] * tw1[3] + a1[3] * tw1[2]);
  }
  __syncthreads();
  const f32x4 z4 = {0.f, 0.f, 0.f, 0.f};
  u32* XTb = XT + (size_t)s0 * 16;
  const char* ft = tab + 2048 + lane * 16;
  LDSP(const char) be = (LDSP(const char))(size_t)acr;
  const int P = w >> 1, mp = P, mm = P == 0 ? 4 : 8 - P;
  if (w < 8) {
    s16x8 e0 = *(LDSP(const s16x8))(be + 16 * g), e1 = *(LDSP(const s16x8))(be + 64), o0 = *(LDSP(const s16x8))(be + 1360 + 16 * g), o1 = *(LDSP(const s16x8))(be + 1360 + 64);
    char* dp = (char*)(XTb + (size_t)(mp * 21) * 4096); char* dm = (char*)(XTb + (size_t)(mm * 21) * 4096);
    const u32 vo = (u32)(2 * g * 4096 + c) * 4u;
    if (!(w & 1)) {
      s16x8 fm[4];
#pragma unroll
      for (int k = 0; k < 4; ++k) fm[k] = *(const s16x8*)(ft + k * 1024);
      batch_fence(); pin(e0); pin(e1); pin(o0); pin(o1); pin_arr(fm);
#pragma unroll
      for (int mt = 0; mt < 2; ++mt) {
        const s16x8 f0 = fm[2 * mt], f1 = fm[2 * mt + 1];
        f32x4 ae = mfma16(f1, e1, mfma16(f0, e0, z4)), ao = mfma16(f1, o1, mfma16(f0, o0, z4));
        f32x4 tp = ae + ao, tm = ae - ao;
        if (P == 0) { tp = ae; tm = ao; }
        char* ep = dp + (size_t)(8 * mt) * 16384; char* em = dm + (size_t)(8 * mt) * 16384;
        const int k0 = 8 * mt + 2 * g;
        { const float d0_ = (c == 0 && k0 >= 1 && k0 <= 9) ? 2.f : 1.f, d1_ = (c == 0 && k0 + 1 <= 9) ? 2.f : 1.f;
          const f32x4 dd = {d0_, d0_, d1_, d1_}; tp = tp * dd; tm = tm * dd; }
        if (mt == 0 || c != 0 || k0 <= 10) { stg_wt(ep, vo, pk_bf(tp[0], tp[1])); stg_wt(em, vo, pk_bf(tm[0], tm[1])); }
        if (mt == 0 || c != 0 || k0 + 1 <= 10) { stg_wt(ep + 16384, vo, pk_bf(tp[2], tp[3])); stg_wt(em + 16384, vo, pk_bf(tm[2], tm[3])); }
      }
    } else {
      s16x8 f0 = *(const s16x8*)(ft + 4 * 1024), f1 = *(const s16x8*)(ft + 5 * 1024);
      batch_fence(); pin(e0); pin(e1); pin(o0); pin(o1); pin(f0); pin(f1);
      f32x4 ae = mfma16(f1, e1, mfma16(f0, e0, z4)), ao = mfma16(f1, o1, mfma16(f0, o0, z4));
      f32x4 tp = ae + ao, tm = ae - ao;
      if (P == 0) { tp = ae; tm = ao; }
      if (g < 2 && c != 0) {
        char* ep = dp + (size_t)16 * 16384; char* em = dm + (size_t)16 * 16384;
        stg_wt(ep, vo, pk_bf(tp[0], tp[1])); stg_wt(ep + 16384, vo, pk_bf(tp[2], tp[3]));
        stg_wt(em, vo, pk_bf(tm[0], tm[1])); stg_wt(em + 16384, vo, pk_bf(tm[2], tm[3]));
      }
    }
  } else {
    const int rh = c & 7, mt = w - 7;
    s16x8 b0 = *(LDSP(const s16x8))(be + 16 * g), b1 = *(LDSP(const s16x8))(be + 64);
    s16x8 f0 = *(const s16x8*)(ft + (2 * mt) * 1024), f1 = *(const s16x8*)(ft + (2 * mt + 1) * 1024);
    batch_fence(); pin(b0); pin(b1); pin(f0); pin(f1);
    f32x4 a = mfma16(f1, b1, mfma16(f0, b0, z4));
    const float ca_ = (rh >= 2 && (rh & 1)) ? -1.f : 1.f, cb_ = rh >= 2 ? 1.f : 0.f;
    f32x4 t;
#pragma unroll
    for (int r = 0; r < 4; ++r) t[r] = ca_ * a[r] + cb_ * dpp_x1(a[r]);
    const int m1 = rh == 0 ? 0 : rh == 1 ? 4 : (rh & 1) ? 8 - (rh >> 1) : (rh >> 1);
    const int k0 = 8 * mt + 2 * g;
    const u32 o0 = (u32)((m1 * 21 + (k0 == 10 ? 20 : k0)) * 4096) * 4u, o1 = (u32)((m1 * 21 + k0 + 1) * 4096) * 4u;
    if (c < 8 && k0 >= 10 && k0 < 20) { stg_wt(XTb, o0, pk_bf(t[0], t[1])); stg_wt(XTb, o1, pk_bf(t[2], t[3])); }
    if (w == 9) asm volatile("s_waitcnt vmcnt(0)" ::: "memory");
  }
}

constexpr int EX_RS = 20;
extern "C" __global__ __launch_bounds__(512) void k3_mid(const u32* __restrict__ XT, const u16* __restrict__ wr, const u16* __restrict__ wi, u32* __restrict__ ZT) {
  __shared__ __attribute__((aligned(16))) u32 ex[2][256 * EX_RS];
  __shared__ __attribute__((aligned(16))) char tab[8 * 1024];
  const int tid = threadIdx.x, lane = tid & 63, w = __builtin_amdgcn_readfirstlane(tid >> 6), c = lane & 15, g = lane >> 4;
  const int m1 = blockIdx.x & 7, tile = blockIdx.x >> 3;
  const char* in = (const char*)(XT + (size_t)(m1 * 21 + tile) * 4096);
  dma1k(in + 2048 * w, (u32)lane * 16u, lds_addr(ex[1]) + 2048 * w);
  dma1k(in + 2048 * w + 1024, (u32)lane * 16u, lds_addr(ex[1]) + 2048 * w + 1024);
  const F8w* tsrc = w < 2 ? &g_a1[m1] : w < 4 ? &g_f16f : w < 6 ? &g_f16i : &g_a1i[m1];
  const s16x8 tfrag = ldg<s16x8>(tsrc->v[w & 1], lane * 16);
  u32 wra[2][4], wia[2][4];
  const int kb = tile == 20 ? 336 : (c == 0 && tile >= 11) ? 32 * tile + 16 : 32 * tile + c;
  const bool mir = kb > 320;
  {
    const int kk = mir ? 640 - kb : kb;
#pragma unroll
    for (int j = 0; j < 2; ++j)
#pragma unroll
      for (int r = 0; r < 4; ++r) {
        const int m = m1 + 8 * (2 * w + j) + 128 * (4 * g + r), mm = mir ? ((2048 - m) & 2047) : m;
        const u32 vo = (u32)(mm * DF + kk) * 2u;
        wra[j][r] = (u32)ldg<u16>(wr, vo) << 16; wia[j][r] = (u32)ldg<u16>(wi, vo) << 16;
      }
  }
  f32x4 fcs[2], fsn[2], ics[2], isn[2];
  {
#pragma unroll
    for (int j = 0; j < 2; ++j) {
      float s0_, c0_, s1_, c1_;
      sincos_rev((float)((2 * w + j) * (m1 + 32 * g)) * (1.0f / 2048), s0_, c0_); sincos_rev((float)(2 * w + j) * (1.0f / 256), s1_, c1_);
#pragma unroll
      for (int r = 0; r < 4; ++r) { fcs[j][r] = c0_; fsn[j][r] = s0_; const float nc = c0_ * c1_ - s0_ * s1_, ns = s0_ * c1_ + c0_ * s1_; c0_ = nc; s0_ = ns; }
      const int M = m1 + 16 * w + 8 * j;
      sincos_rev((float)(4 * g * M) * (1.0f / 2048), s0_, c0_); sincos_rev((float)M * (1.0f / 2048), s1_, c1_);
#pragma unroll
      for (int r = 0; r < 4; ++r) { ics[j][r] = c0_; isn[j][r] = s0_; const float nc = c0_ * c1_ - s0_ * s1_, ns = s0_ * c1_ + c0_ * s1_; c0_ = nc; s0_ = ns; }
    }
  }
  u32 awr = lds_addr(ex) + 4 * ((4 * g * 16 + c) * EX_RS + 2 * w);
  u32 ard = lds_addr(ex) + 4 * ((2 * w * 16 + c) * EX_RS + 4 * g);
#pragma unroll
  for (int j = 0; j < 2; ++j) { pin(fcs[j]); pin(fsn[j]); pin(ics[j]); pin(isn[j]); }
  pin(awr); pin(ard);
  *(s16x8*)(tab + w * 1024 + lane * 16) = tfrag;
  wait_vm<16>();
  __syncthreads();
  u32x4 bx[2];
#pragma unroll
  for (int j = 0; j < 2; ++j)
#pragma unroll
    for (int i = 0; i < 4; ++i) bx[j][i] = ex[1][(16 * (4 * g + i) + 2 * w + j) * 16 + c];
  s16x8 A10 = *(const s16x8*)(tab + lane * 16), A11 = *(const s16x8*)(tab + lane * 16 + 1024);
  batch_fence(); pin_arr(bx); pin(A10); pin(A11);
  const f32x4 z4 = {0.f, 0.f, 0.f, 0.f};
  const char* tb = tab + lane * 16;
  LDSP(char) wr0 = (LDSP(char))(size_t)awr; LDSP(const char) rd0 = (LDSP(const char))(size_t)ard;
  constexpr int RB = 16 * EX_RS * 4, XB = 256 * EX_RS * 4;
  {
    u32 pk[2][4];
#pragma unroll
    for (int j = 0; j < 2; ++j) {
      f32x4 re = mfma16(A10, as8(bx[j]), z4), im = mfma16(A11, as8(bx[j]), z4);
      f32x4 tr = re * fcs[j] + im * fsn[j], ti = im * fcs[j] - re * fsn[j];
#pragma unroll
      for (int r = 0; r < 4; ++r) pk[j][r] = pk_bf(tr[r], ti[r]);
    }
#pragma unroll
    for (int r = 0; r < 4; ++r) *(LDSP(u32x2))(wr0 + r * RB) = u32x2{pk[0][r], pk[1][r]};
  }
  __syncthreads();
  {
    u32x4 bj[2] = {*(LDSP(const u32x4))(rd0), *(LDSP(const u32x4))(rd0 + RB)};
    s16x8 F0 = *(const s16x8*)(tb + 2048), F1 = *(const s16x8*)(tb + 3072), G0 = *(const s16x8*)(tb + 4096), G1 = *(const s16x8*)(tb + 5120);
    batch_fence(); pin_arr(bj); pin(F0); pin(F1); pin(G0); pin(G1);
    u32 pk[2][4];
#pragma unroll
    for (int j = 0; j < 2; ++j) {
      const u32x4 b = bj[j];
      f32x4 xr = mfma16(F0, as8(b), z4), xi = mfma16(F1, as8(b), z4);
      const f32x4 a = {__uint_as_float(wra[j][0]), __uint_as_float(wra[j][1]), __uint_as_float(wra[j][2]), __uint_as_float(wra[j][3])};
      const u32 sg = mir ? 0x80000000u : 0u;
      const f32x4 bb = {__uint_as_float(wia[j][0] ^ sg), __uint_as_float(wia[j][1] ^ sg), __uint_as_float(wia[j][2] ^ sg), __uint_as_float(wia[j][3] ^ sg)};
      const f32x4 yr = xr * a - xi * bb, yi = xr * bb + xi * a;
      const u32x4 yb = {pk_bf(yr[0], yr[1]), pk_bf(yr[2], yr[3]), pk_bf(yi[0], yi[1]), pk_bf(yi[2], yi[3])};
      f32x4 re = mfma16(G0, as8(yb), z4), im = mfma16(G1, as8(yb), z4);
      f32x4 tr = re * ics[j] - im * isn[j], ti = im * ics[j] + re * isn[j];
#pragma unroll
      for (int r = 0; r < 4; ++r) pk[j][r] = pk_bf(tr[r], ti[r]);
    }
#pragma unroll
    for (int r = 0; r < 4; ++r) *(LDSP(u32x2))(wr0 + XB + r * RB) = u32x2{pk[0][r], pk[1][r]};
  }
  __syncthreads();
  char* out = (char*)(ZT + (size_t)(2 * w) * 8 * 21 * 16 + (m1 * 21 + tile) * 16);
  const u32 vout = (u32)(64 * g * 10752 + c * 4);
  u32x4 bz[2] = {*(LDSP(const u32x4))(rd0 + XB), *(LDSP(const u32x4))(rd0 + XB + RB)};
  s16x8 I0 = *(const s16x8*)(tb + 6144), I1 = *(const s16x8*)(tb + 7168);
  batch_fence(); pin_arr(bz); pin(I0); pin(I1);
#pragma unroll
  for (int j = 0; j < 2; ++j) {
    const u32x4 b = bz[j];
    f32x4 zr = mfma16(I0, as8(b), z4), zi = mfma16(I1, as8(b), z4);
#pragma unroll
    for (int r = 0; r < 4; ++r) stg_wt(out + (16 * r + j) * 10752, vout, pk_bf(zr[r], zi[r]));
  }
}

constexpr int Y_RS = 640 + 4;
extern "C" __global__ __launch_bounds__(640) void k4_inv(const u32* __restrict__ ZT, const u16* __restrict__ x, const float* __restrict__ mod,
                                              u16* __restrict__ x1o, u16* __restrict__ h2o) {
  __shared__ __attribute__((aligned(16))) char smem[162 * 64 + 8 * Y_RS * 4 + 10 * 8 * 4 + 8 * 1024 + 10752 + 256];
  char* Vs = smem; float* Y = (float*)(Vs + 162 * 64); float* part = Y + 8 * Y_RS; char* tab = (char*)(part + 80);
  const int tid = threadIdx.x, lane = tid & 63, w = __builtin_amdgcn_readfirstlane(tid >> 6), c = lane & 15, g = lane >> 4;
  const int s0 = row_group(blockIdx.x);
  u32* Zs = (u32*)(tab + 8 * 1024);
  {
    const char* Zb = (const char*)(ZT + (size_t)s0 * 8 * 21 * 16);
    dma1k(Zb + 1024 * w, (u32)lane * 16u, lds_addr(Zs) + 1024 * w);
    if (w == 0 && lane < 32) dma1k(Zb + 10240, (u32)lane * 16u, lds_addr(Zs) + 10240);
  }
  const int rho = w < 8 ? w : (c & 7), P = rho >> 1, mA = rho == 1 ? 4 : P, mB = P == 0 ? 4 : 8 - P;
  const s16x8 tfrag = w < 6 ? ldg<s16x8>(&g_f20ai.v[0][0][0][0] + w * 512, lane * 16) : ldg<s16x8>(g_f32bi.v[(w - 6) & 1], lane * 16);
  const int hf = w >= 5, cp = tid - 320 * hf;
  u32 xv[4];
#pragma unroll
  for (int r = 0; r < 4; ++r) xv[r] = ldg<u32>(x + (size_t)(s0 + 256 * (4 * hf + r)) * D_, 4 * cp);
  const f32x2 ga = ldg<f32x2>(mod + 2 * 640, 8 * cp), sb = ldg<f32x2>(mod + 3 * 640, 8 * cp), cb = ldg<f32x2>(mod + 4 * 640, 8 * cp);
  f32x4 tw[3]; u32 aw[3];
  {
    const bool rot = rho >= 3 && (rho & 1);
    const float sc = w < 8 ? 1.0f / (2048.0f * 640.0f) : 2.0f / (2048.0f * 640.0f), fk = (float)(w < 8 ? c : 16) * (1.0f / 640.0f);
    float s0_, c0_, s1_, c1_, s8_, c8_;
    sincos_rev(fk * (float)(2 * g), s0_, c0_); sincos_rev(fk, s1_, c1_); sincos_rev(fk * 8.0f, s8_, c8_);
    float br = (rot ? -s0_ : c0_) * sc, bi = (rot ? c0_ : s0_) * sc;
#pragma unroll
    for (int mt = 0; mt < 3; ++mt) {
      tw[mt] = f32x4{br, bi, br * c1_ - bi * s1_, br * s1_ + bi * c1_};
      const float nr = br * c8_ - bi * s8_, ni = br * s8_ + bi * c8_; br = nr; bi = ni;
      const int d0 = 8 * mt + 2 * g;
      const bool ok = d0 < 20 && (w < 8 || c < 8);
      aw[mt] = lds_addr(Vs) + (ok ? (rho * 20 + d0) * 64 : 160 * 64) + (w < 8 ? 4 * c : 2);
      pin(tw[mt]); pin(aw[mt]);
    }
  }
  u32 bsg = (rho >= 2 && (rho & 1)) ? 0x80008000u : 0u, bon = rho < 2 ? 0u : 0xffffffffu;
  u32 za0, za1, dbits, zdel = (u32)(mB - mA) * 21u * 64u;
  {
    const u32 zb = lds_addr(Zs) + mA * 21 * 64;
    if (w < 8) { za0 = zb + (64 * g + c) * 4; za1 = zb + 16 * 64 + c * 4; dbits = c == 0 ? (0xF0u | (g == 2 ? 8u : g == 3 ? 15u : 0u)) : 0u; }
    else {
      za0 = zb + (g == 3 ? 12 : 11 - 3) * 64; za1 = zb + 16 * 64; dbits = (g < 2 ? 0xFu : g == 2 ? 3u : 0u) | (g == 0 ? 0u : 0xF0u);
    }
  }
  const u32 z10 = lds_addr(Zs) + (mA * 21 + 20) * 64;
  pin(bsg); pin(bon); pin(za0); pin(za1); pin(dbits); pin(zdel);
  if (w == 9 && blockIdx.x < 8) {
    const void* c5; const void* c6; CODE_ADDR(c5, k5_glu); CODE_ADDR(c6, k6_down);
    const u32 len = min((u32)((const char*)c6 - (const char*)c5), 16384u) * ((const char*)c6 > (const char*)c5);
    if (lane * 128u < len) l2_touch(c5, (u32)lane * 128u, lds_addr(smem) + sizeof(smem) - 256);
    if (lane * 128u + 8192u < len) l2_touch(c5, (u32)lane * 128u + 8192u, lds_addr(smem) + sizeof(smem) - 256);
  }
  if (w < 8) *(s16x8*)(tab + w * 1024 + lane * 16) = tfrag;
  wait_vm<4>();
  __syncthreads();
  if (w < 9) {
    u32 zz[8], zy[8]; s16x8 ft[6];
#pragma unroll
    for (int i = 0; i < 8; ++i) {
      u32 ad = (i < 4 ? za0 : za1) + 64 * (i & 3);
      if (i == 2) ad = (w == 8 && g == 2) ? z10 : ad;
      zz[i] = *(LDSP(const u32))(size_t)ad; zy[i] = *(LDSP(const u32))(size_t)(ad + zdel);
    }
#pragma unroll
    for (int k = 0; k < 6; ++k) ft[k] = *(const s16x8*)(tab + k * 1024 + lane * 16);
    batch_fence(); pin_arr(zz); pin_arr(zy); pin_arr(ft);
    u32x4 a0, a1, b0, b1;
#pragma unroll
    for (int i = 0; i < 8; ++i) {
      const u32 keep = ~(u32)__builtin_amdgcn_sbfe((int)dbits, i, 1);
      const u32 p_ = zz[i] & keep, q_ = (zy[i] ^ bsg) & keep & bon;
      if (i < 4) { a0[i] = p_; b0[i] = q_; } else { a1[i - 4] = p_; b1[i - 4] = q_; }
    }
    f32x4 a[3];
#pragma unroll
    for (int mt = 0; mt < 3; ++mt) {
      const f32x4 z = {0.f, 0.f, 0.f, 0.f};
      a[mt] = mfma16(ft[2 * mt + 1], as8(a1), mfma16(ft[2 * mt], as8(a0), z));
      if (w >= 2) a[mt] = mfma16(ft[2 * mt + 1], as8(b1), mfma16(ft[2 * mt], as8(b0), a[mt]));
    }
    if (w < 8) {
      const u32 hdel = c == 0 ? lds_addr(Vs) + 160 * 64 + 2 : 0u;
#pragma unroll
      for (int mt = 0; mt < 3; ++mt)
#pragma unroll
        for (int rr = 0; rr < 2; ++rr) {
          const u32 pv = pk_bf(a[mt][2 * rr] * tw[mt][2 * rr] - a[mt][2 * rr + 1] * tw[mt][2 * rr + 1], a[mt][2 * rr] * tw[mt][2 * rr + 1] + a[mt][2 * rr + 1] * tw[mt][2 * rr]);
          *(LDSP(u16))(size_t)(aw[mt] + 64 * rr) = (u16)pv;
          *(LDSP(u16))(size_t)((c == 0 ? hdel : aw[mt] + 2) + 64 * rr) = (u16)(pv >> 16);
        }
    } else {
#pragma unroll
      for (int mt = 0; mt < 3; ++mt)
#pragma unroll
        for (int rr = 0; rr < 2; ++rr)
          *(LDSP(u16))((LDSP(char))(size_t)aw[mt] + 64 * rr) = (u16)pk_bf(a[mt][2 * rr] * tw[mt][2 * rr] - a[mt][2 * rr + 1] * tw[mt][2 * rr + 1], 0.f);
    }
  }
  __syncthreads();
  {
    s16x8 a = *(const s16x8*)(Vs + (16 * w + c) * 64 + 16 * g);
    s16x8 fb0 = *(const s16x8*)(tab + 6144 + lane * 16), fb1 = *(const s16x8*)(tab + 7168 + lane * 16);
    batch_fence(); pin(a); pin(fb0); pin(fb1);
    const f32x4 z = {0.f, 0.f, 0.f, 0.f};
    const f32x4 y0 = mfma16(a, fb0, z), y1 = mfma16(a, fb1, z);
    const int R = 16 * w + 4 * g, rr = R / 20, d0 = R % 20;
    *(f32x4*)(Y + rr * Y_RS + 20 * c + d0) = y0;
    *(f32x4*)(Y + rr * Y_RS + 20 * (16 + c) + d0) = y1;
  }
  __syncthreads();
  {
    f32x2 R[8];
#pragma unroll
    for (int r = 0; r < 8; ++r) R[r] = *(const f32x2*)(Y + r * Y_RS + 2 * cp);
    batch_fence(); pin_arr(R);
    const float cc = 0.70710678118654752f;
    const f32x2 G = R[0] + R[1], Fm = R[0] - R[1];
    const f32x2 u = cc * (R[2] - R[6]), v = cc * (R[3] + R[7]);
    const f32x2 c13 = R[2] + R[6], s13 = R[3] - R[7];
    const float sg = hf ? -1.f : 1.f;
    f32x2 y[4];
    y[0] = G + R[4] + sg * c13;
    y[2] = G - R[4] + sg * s13;
    y[1] = Fm + R[5] + sg * (u + v);
    y[3] = Fm - R[5] + sg * (v - u);
    u32 x1[4];
    float sv[8];
#pragma unroll
    for (int r = 0; r < 4; ++r) {
      const f32x2 xx = {bf_lo(xv[r]), bf_hi(xv[r])};
      const f32x2 t = xx + ga * y[r];
      x1[r] = pk_bf(t[0], t[1]);
      stg_wt(x1o + (size_t)(8 * s0 + 4 * hf + r) * D_, (u32)cp * 4u, x1[r]);
      sv[r] = dot2bf(x1[r], 0x3f803f80u, 0.f); sv[4 + r] = dot2bf(x1[r], x1[r], 0.f);
    }
    const float tot = reduce8(sv, lane);
    if (lane < 8) part[w * 8 + lane] = tot;
    const f32x2 gg = {rbf(1.0f + cb[0]), rbf(1.0f + cb[1])};
    const int d = 2 * cp, wi = d & 31;
    char* stage = (char*)Y;
    const int so = (((d >> 5) * 4 + ((wi & 15) >> 2)) * 8 + 4 * hf) * 16 + (4 * (wi >> 4) + (wi & 3)) * 2;
    __syncthreads();
    const int r = lane & 3;
    float s, qq;
    { float ps[5], pq[5];
#pragma unroll
      for (int i = 0; i < 5; ++i) { ps[i] = part[(5 * hf + i) * 8 + r]; pq[i] = part[(5 * hf + i) * 8 + 4 + r]; }
      batch_fence(); pin_arr(ps); pin_arr(pq);
      s = ((ps[0] + ps[1]) + (ps[2] + ps[3])) + ps[4]; qq = ((pq[0] + pq[1]) + (pq[2] + pq[3])) + pq[4]; }
    float m_, i_; ln_finish(s, qq, m_, i_);
    const float nb_ = -m_ * i_;
#pragma unroll
    for (int i = 0; i < 4; ++i) {
      const float na = __int_as_float(__builtin_amdgcn_readlane(__float_as_int(i_), i)), nb = __int_as_float(__builtin_amdgcn_readlane(__float_as_int(nb_), i));
      const f32x2 v2 = {bf_lo(x1[i]), bf_hi(x1[i])};
      const f32x2 hv = (v2 * na + nb) * gg + sb;
      *(u32*)(stage + so + 16 * i) = pk_bf(hv[0], hv[1]);
    }
    __syncthreads();
    const u32x4 piece = *(const u32x4*)(stage + 16 * tid);
    stg_wt(h2o + ((size_t)(s0 >> 1) * 20 * 64 + 8 * (s0 & 1)) * 8, (u32)((tid >> 3) * 16 + (tid & 7)) * 16u, piece);
  }
  if (w == 9) asm volatile("s_waitcnt vmcnt(0)" ::: "memory");
}

constexpr int K5_SLOT = 28 * 1024;
constexpr int K5_NS = 4;
constexpr int K5_PD = 4;
struct K5Ctx { const u16* wsrc; const u16* hsrc; u32 ldsw; u32 goff[7]; u32 hoff; u32 abase[2]; };
template <int s, int i> DI void k5_w1(const K5Ctx& c) {
  if constexpr (i == 0) set_m0(c.ldsw + (s % K5_NS) * K5_SLOT + 3072);
  glds16_s<1024 * i - 3072>((const char*)(c.wsrc + s * 4 * 512) - 4096, c.goff[i]);
}
template <int s, int i> DI void k5_t1(const K5Ctx& c, u32x4 (&t)[4]) {
  gload16_s<(i >> 1) * 1024>(t[i], c.hsrc + ((i & 1) * 20 + 2 * s) * 512, c.hoff);
}
template <int s> DI void k5_wall(const K5Ctx& c) { k5_w1<s, 0>(c); k5_w1<s, 1>(c); k5_w1<s, 2>(c); k5_w1<s, 3>(c); k5_w1<s, 4>(c); k5_w1<s, 5>(c); k5_w1<s, 6>(c); }
template <int s> DI void k5_tall(const K5Ctx& c, u32x4 (&t)[4]) { k5_t1<s, 0>(c, t); k5_t1<s, 1>(c, t); k5_t1<s, 2>(c, t); k5_t1<s, 3>(c, t); }
template <int n> DI s16x8 k5_rd(const K5Ctx& c) {
  constexpr int s = n / 28, t = (n / 14) & 1, j = n % 14, sl = s % K5_NS;
  return *(LDSP(const s16x8))((LDSP(const char))(size_t)c.abase[sl >> 1] + (sl & 1) * K5_SLOT + ((j >> 1) * 4 + t * 2 + (j & 1)) * 1024);
}
template <int n> DI void k5_sub(const K5Ctx& c, f32x4 (&acc)[14][2], s16x8 (&fr)[K5_PD + 1], u32x4 (&tok)[10][4]) {
  constexpr int s = n / 28, t = (n / 14) & 1, j = n % 14;
  constexpr int nb = n + K5_PD;
  if constexpr (nb < 280 && nb % 28 == 0) {
    wait_vm<(s + 2 <= 9) ? 11 : 0>();
    __builtin_amdgcn_s_barrier();
    tie4(tok[s + 1]);
  }
  if constexpr (nb < 280) fr[nb % (K5_PD + 1)] = k5_rd<nb>(c);
  {
    constexpr int q = nb / 28, pos = nb % 28 - (q == 0 ? K5_PD : 0);
    constexpr int idx = (pos % 2 == 0 && pos < 22) ? pos / 2 : -1;
    if constexpr (idx >= 0 && idx < 4 && q + 2 <= 9) k5_t1<q + 2, idx>(c, tok[q + 2]);
    if constexpr (idx >= 4 && q + 2 <= 9) k5_w1<q + 2, idx - 4>(c);
  }
  const s16x8 a = fr[n % (K5_PD + 1)];
  acc[j][0] = mfma16(a, as8(tok[s][2 * t]), acc[j][0]);
  acc[j][1] = mfma16(a, as8(tok[s][2 * t + 1]), acc[j][1]);
  __builtin_amdgcn_sched_barrier(0);
}
template <int... I> DI void k5_all(const K5Ctx& c, f32x4 (&acc)[14][2], s16x8 (&fr)[K5_PD + 1], u32x4 (&tok)[10][4], std::integer_sequence<int, I...>) { (k5_sub<I>(c, acc, fr, tok), ...); }
extern "C" __global__ __launch_bounds__(256, 2) void k5_glu(const u16* __restrict__ h2, const u16* __restrict__ Wgu, u16* __restrict__ hb) {
  extern __shared__ __attribute__((aligned(16))) char smem[];
  const int tid = threadIdx.x, lane = tid & 63, w = __builtin_amdgcn_readfirstlane(tid >> 6);
  const int xcd = blockIdx.x & 7, li = blockIdx.x >> 3;
  const int tt = 8 * (xcd & 1) + (li & 7), fc = 4 * (xcd >> 1) + (li >> 3);
  const int u0 = fc < 12 ? 7 * fc : 84 + 6 * (fc - 12), U = fc < 12 ? 7 : 6;
  if (blockIdx.x < 8) {
    const void* c6; const void* c7; CODE_ADDR(c6, k6_down); CODE_ADDR(c7, k7_end);
    if ((const char*)c7 > (const char*)c6 && (u32)tid * 128u < (u32)((const char*)c7 - (const char*)c6)) l2_touch(c6, (u32)tid * 128u, lds_addr(smem) + 3 * K5_SLOT + w * 256);
  }
  K5Ctx c;
  c.wsrc = Wgu + (size_t)u0 * 20 * 2 * 512;
#pragma unroll
  for (int i = 0; i < 7; ++i) { const int f = 7 * w + i, u = min(f >> 2, U - 1); c.goff[i] = 2 * (u * 20 * 2 * 512 + (f & 3) * 512) + lane * 16 + 4096 - (1024 * i - 3072); }
  c.ldsw = lds_addr(smem) + 7 * w * 1024;
  const int T16 = 8 * tt + 2 * w;
  c.hsrc = h2 + (size_t)T16 * 20 * 512; c.hoff = lane * 16;
#pragma unroll
  for (int j = 0; j < 2; ++j) { c.abase[j] = lds_addr(smem) + 2 * j * K5_SLOT + lane * 16; asm volatile("" : "+v"(c.abase[j])); }
  u32x4 tok[10][4];
  k5_tall<0>(c, tok[0]); k5_wall<0>(c);
  k5_tall<1>(c, tok[1]); k5_wall<1>(c);
  f32x4 acc[14][2];
#pragma unroll
  for (int u = 0; u < 14; ++u) { acc[u][0] = f32x4{0.f, 0.f, 0.f, 0.f}; acc[u][1] = f32x4{0.f, 0.f, 0.f, 0.f}; }
  wait_vm<11>();
  __builtin_amdgcn_s_barrier();
  tie4(tok[0]);
  s16x8 fr[K5_PD + 1];
  fr[0] = k5_rd<0>(c); fr[1] = k5_rd<1>(c); fr[2] = k5_rd<2>(c); fr[3] = k5_rd<3>(c);
  __builtin_amdgcn_sched_barrier(0);
  k5_all(c, acc, fr, tok, std::make_integer_sequence<int, 280>{});
  auto hv = [&](int u, int tl) { const f32x4 a = acc[2 * u][tl] * acc[2 * u + 1][tl]; return u32x2{pk_bf(a[0], a[1]), pk_bf(a[2], a[3])}; };
#pragma unroll
  for (int tl = 0; tl < 2; ++tl) {
    u16* hdst = hb + (size_t)(T16 + tl) * 54 * 512 + (u0 >> 1) * 512;
    const u32 vo = (u32)lane * 16u;
    if (!(u0 & 1)) {
#pragma unroll
      for (int k = 0; k < 3; ++k) { const u32x2 a = hv(2 * k, tl), b = hv(2 * k + 1, tl); stg_wt(hdst + k * 512, vo, u32x4{a[0], a[1], b[0], b[1]}); }
      if (U == 7) stg_wt(hdst + 3 * 512, vo, hv(6, tl));
    } else {
      stg_wt(hdst + 4, vo, hv(0, tl));
#pragma unroll
      for (int k = 0; k < 3; ++k) { const u32x2 a = hv(2 * k + 1, tl), b = hv(2 * k + 2, tl); stg_wt(hdst + (k + 1) * 512, vo, u32x4{a[0], a[1], b[0], b[1]}); }
    }
  }
}

constexpr int K6_NS = 4;
struct K6Ctx { const u16* wsrc; const u16* hsrc; u32 hoff; const float* gbp; u32 gbo; const u16* xp; u32 xo; };
struct K6Grp { u32x4 a[5]; u32x4 t[4]; };
template <int i> DI void k6_v1(const K6Ctx& c, int ks, K6Grp& gr) {
  if constexpr (i < 5) gload16_s<0>(gr.a[i], c.wsrc + (ks + i * 54) * 512, c.hoff);
  else gload16_s<0>(gr.t[i - 5], c.hsrc + (ks + (i - 5) * 54) * 512, c.hoff);
}
DI void k6_issue(const K6Ctx& c, int ks, K6Grp& gr) {
  k6_v1<0>(c, ks, gr); k6_v1<5>(c, ks, gr); k6_v1<6>(c, ks, gr); k6_v1<7>(c, ks, gr); k6_v1<8>(c, ks, gr);
  k6_v1<1>(c, ks, gr); k6_v1<2>(c, ks, gr); k6_v1<3>(c, ks, gr); k6_v1<4>(c, ks, gr);
}
DI void k6_tie(K6Grp& g) { asm volatile("" : "+v"(g.a[0]), "+v"(g.a[1]), "+v"(g.a[2]), "+v"(g.a[3]), "+v"(g.a[4]), "+v"(g.t[0]), "+v"(g.t[1]), "+v"(g.t[2]), "+v"(g.t[3])); }
struct K6Epi { u32x4 gb[5]; u32x2 xr[5]; };
template <int OFF> DI void gload8_s(u32x2& d, const void* sbase, u32 voff) {
  asm volatile("global_load_dwordx2 %0, %1, %2 offset:%3" : "=v"(d) : "v"(voff), "s"(sbase), "n"(OFF) : "memory");
}
constexpr int k6_after(int i, int NK) {
  int n = 0; bool seen = false;
  for (int g = 0; g < 3; ++g) { if (seen) n += 9; if (g == i) seen = true; }
  for (int st = 0; st < i; ++st) {
    if (st == NK - 3 && seen) n += 10;
    if (st + 3 < NK) { if (seen) n += 9; if (st + 3 == i) seen = true; }
  }
  return n;
}
template <int i, int NK> DI void k6_step(const K6Ctx& c, int ks0, f32x4 (&acc)[5][4], K6Grp (&gr)[K6_NS], K6Epi& ep, f32x4* red, int w, int lane) {
  wait_vm<k6_after(i, NK)>();
  K6Grp& g = gr[i % K6_NS];
  k6_tie(g);
  K6Grp& nx = gr[(i + 3) % K6_NS];
#pragma unroll
  for (int nt = 0; nt < 5; ++nt) {
    if constexpr (i + 3 < NK) {
      if (nt == 0) { k6_v1<0>(c, ks0 + i + 3, nx); k6_v1<5>(c, ks0 + i + 3, nx); }
      if (nt == 1) { k6_v1<6>(c, ks0 + i + 3, nx); k6_v1<7>(c, ks0 + i + 3, nx); }
      if (nt == 2) { k6_v1<8>(c, ks0 + i + 3, nx); k6_v1<1>(c, ks0 + i + 3, nx); }
      if (nt == 3) { k6_v1<2>(c, ks0 + i + 3, nx); k6_v1<3>(c, ks0 + i + 3, nx); }
      if (nt == 4) { k6_v1<4>(c, ks0 + i + 3, nx); }
    }
    if constexpr (i == NK - 3) {
      gload16_s<0>(ep.gb[nt], c.gbp + 16 * nt, c.gbo); gload8_s<0>(ep.xr[nt], c.xp + 16 * nt, c.xo);
    }
#pragma unroll
    for (int t = 0; t < 4; ++t) acc[nt][t] = mfma16(as8(g.a[nt]), as8(g.t[t]), acc[nt][t]);
    if constexpr (i == NK - 1) {
      if (nt >= 1) {
#pragma unroll
        for (int t = 0; t < 4; ++t) if (t != w) red[((w * 5 + nt - 1) * 4 + t) * 64 + lane] = acc[nt - 1][t];
      }
    }
    __builtin_amdgcn_sched_barrier(0);
  }
}
template <int i, int NK> struct K6Loop { static DI void run(const K6Ctx& c, int ks0, f32x4 (&acc)[5][4], K6Grp (&gr)[K6_NS], K6Epi& ep, f32x4* red, int w, int lane) {
  k6_step<i, NK>(c, ks0, acc, gr, ep, red, w, lane); K6Loop<i + 1, NK>::run(c, ks0, acc, gr, ep, red, w, lane); } };
template <int NK> struct K6Loop<NK, NK> { static DI void run(const K6Ctx&, int, f32x4 (&)[5][4], K6Grp (&)[K6_NS], K6Epi&, f32x4*, int, int) {} };
template <int NK> DI void k6_main(const K6Ctx& cx, int ks0, f32x4* red, int w, int lane, u16* __restrict__ out, u32 oo) {
  K6Grp gr[K6_NS]; K6Epi ep;
  k6_issue(cx, ks0, gr[0]); k6_issue(cx, ks0 + 1, gr[1]); k6_issue(cx, ks0 + 2, gr[2]);
  f32x4 acc[5][4];
#pragma unroll
  for (int a = 0; a < 5; ++a)
#pragma unroll
    for (int b = 0; b < 4; ++b) acc[a][b] = f32x4{0.f, 0.f, 0.f, 0.f};
  K6Loop<0, NK>::run(cx, ks0, acc, gr, ep, red, w, lane);
#pragma unroll
  for (int t = 0; t < 4; ++t) if (t != w) red[((w * 5 + 4) * 4 + t) * 64 + lane] = acc[4][t];
  wait_vm<0>();
  asm volatile("" : "+v"(ep.gb[0]), "+v"(ep.gb[1]), "+v"(ep.gb[2]), "+v"(ep.gb[3]), "+v"(ep.gb[4]), "+v"(ep.xr[0]), "+v"(ep.xr[1]), "+v"(ep.xr[2]), "+v"(ep.xr[3]), "+v"(ep.xr[4]));
  __syncthreads();
#pragma unroll
  for (int nt = 0; nt < 5; ++nt) {
    f32x4 v = w == 0 ? acc[nt][0] : w == 1 ? acc[nt][1] : w == 2 ? acc[nt][2] : acc[nt][3];
#pragma unroll
    for (int ww = 1; ww < 4; ++ww) v += red[((((w + ww) & 3) * 5 + nt) * 4 + w) * 64 + lane];
    const f32x4 gb = __builtin_bit_cast(f32x4, ep.gb[nt]);
    const f32x4 x4 = {bf_lo(ep.xr[nt][0]), bf_hi(ep.xr[nt][0]), bf_lo(ep.xr[nt][1]), bf_hi(ep.xr[nt][1])};
    const f32x4 o = x4 + gb * v;
    stg_wt(out + 16 * nt, oo, u32x2{pk_bf(o[0], o[1]), pk_bf(o[2], o[3])});
  }
}
extern "C" __global__ __launch_bounds__(256, 2) void k6_down(const u16* __restrict__ hb, const u16* __restrict__ Wd, const u16* __restrict__ x1, const float* __restrict__ mod, u16* __restrict__ out) {
  extern __shared__ __attribute__((aligned(16))) char smem[];
  const int tid = threadIdx.x, lane = tid & 63, w = __builtin_amdgcn_readfirstlane(tid >> 6), c = lane & 15, g = lane >> 4;
  const int xcd = blockIdx.x & 7, li = blockIdx.x >> 3;
  const int tt = 8 * (xcd & 3) + (li & 7), nb = 4 * (xcd >> 2) + (li >> 3);
  const int n0 = 80 * nb;
  const int ks0 = w < 2 ? 14 * w : 28 + 13 * (w - 2);
  K6Ctx cx;
  cx.wsrc = Wd + (size_t)(5 * nb) * 54 * 512; cx.hsrc = hb + (size_t)(4 * tt) * 54 * 512; cx.hoff = lane * 16;
  cx.gbp = mod + 5 * 640 + n0; cx.gbo = 16 * g;
  cx.xp = x1 + (size_t)(64 * tt + 16 * w) * D_ + n0; cx.xo = (u32)(c * D_ + 4 * g) * 2u;
  const int tau = 64 * tt + 16 * w + c, srow = (tau >> 3) + 256 * (tau & 7);
  const u32 oo = (u32)(srow * D_ + 4 * g) * 2u;
  if (w < 2) k6_main<14>(cx, ks0, (f32x4*)smem, w, lane, out + n0, oo); else k6_main<13>(cx, ks0, (f32x4*)smem, w, lane, out + n0, oo);
}

extern "C" __global__ void k7_end() {}

constexpr size_t WS_MOD = 0, WS_XT = 16384, WS_ZT = WS_XT + 8 * 21 * 256 * 16 * 4, WS_X1 = WS_ZT + 8 * 21 * 256 * 16 * 4,
                 WS_H2 = WS_X1 + 2048 * 640 * 2, WS_HB = WS_H2 + 2048 * 640 * 2, WS_WGU = WS_HB + 2048 * 1728 * 2, WS_WD = WS_WGU + 2 * 640 * 1728 * 2, WS_SYNC = WS_WD + 640 * 1728 * 2;
extern "C" void kernel_launch(void* const* d_in, const int* in_sizes, int n_in, void* d_out, int out_size, void* d_ws, size_t ws_size,
                              hipStream_t stream) {
  const u16* x = (const u16*)d_in[0]; const u16* wr = (const u16*)d_in[1]; const u16* wi = (const u16*)d_in[2];
  const u16* wg = (const u16*)d_in[4]; const u16* wu = (const u16*)d_in[5]; const u16* wd = (const u16*)d_in[6];
  const u16* c = (const u16*)d_in[7]; const u16* wmod = (const u16*)d_in[8];
  char* ws = (char*)d_ws;
  float* mod = (float*)(ws + WS_MOD); u32* XT = (u32*)(ws + WS_XT); u32* ZT = (u32*)(ws + WS_ZT);
  u16* x1 = (u16*)(ws + WS_X1); u16* h2 = (u16*)(ws + WS_H2); u16* hb = (u16*)(ws + WS_HB);
  u16* Wgu = (u16*)(ws + WS_WGU); u16* Wd = (u16*)(ws + WS_WD);
  K2Args A;
  A.x = x; A.c = c; A.wmod = wmod; A.mod = mod; A.XT = XT; A.wr = wr; A.wi = wi; A.wg = wg; A.wu = wu; A.Wgu = Wgu; A.wd = wd; A.Wd = Wd;
  A.gen = (u32*)(ws + WS_SYNC); A.arr = (u32*)(ws + WS_SYNC + 4096); A.gran = (unsigned long long*)(ws + WS_SYNC + 8192);
  k2_fwd<<<K2_GRID, 640, 0, stream>>>(A);
  k3_mid<<<168, 512, 0, stream>>>(XT, wr, wi, ZT);
  k4_inv<<<256, 640, 0, stream>>>(ZT, x, mod, x1, h2);
  (void)hipFuncSetAttribute((const void*)k5_glu, hipFuncAttributeMaxDynamicSharedMemorySize, K5_NS * K5_SLOT);
  k5_glu<<<256, 256, K5_NS * K5_SLOT, stream>>>(h2, Wgu, hb);
  (void)hipFuncSetAttribute((const void*)k6_down, hipFuncAttributeMaxDynamicSharedMemorySize, 81920);
  k6_down<<<256, 256, 81920, stream>>>(hb, Wd, x1, mod, (u16*)d_out);
}
```
